```python
import torch

def _softmax(x, axis=-1):
    return torch.softmax(x, dim=axis)
def _gelu(x):
    return torch.nn.functional.gelu(x, approximate="tanh")
def _silu(x):
    return torch.nn.functional.silu(x)
def _sigmoid(x):
    return torch.sigmoid(x)
def _layernorm(x, axis=-1, eps=1e-5):
    m = x.mean(dim=axis, keepdim=True)
    v = x.var(dim=axis, keepdim=True, unbiased=False)
    return (x - m) / torch.sqrt(v + eps)
def _scatter_add(tgt, idx, src, axis=0):
    return tgt.clone().index_add_(axis, idx, src)
def _segment_sum(src, seg, n):
    out = torch.zeros((n,) + src.shape[1:], dtype=src.dtype)
    return out.index_add_(0, seg, src)
def _pad(x, pw):
    flat = tuple(v for pair in reversed(pw) for v in pair)
    return torch.nn.functional.pad(x, flat)

kernel_name = 'standalone__mha_block_cached__near_ridge'

def setup_inputs():

    _w = (lambda a: (lambda t: t.to(torch.bfloat16) if t.is_floating_point() else t)(torch.as_tensor(a)))
    B, Sq, D = 32, 160, 640
    H, Sk, Dh = 32, 512, 128
    return {
        "x": _w((lambda *s: torch.randn(*s, dtype=torch.bfloat16) * 0.8)(B, Sq, D)),
        "k_cache": _w((lambda *s: torch.randn(*s, dtype=torch.bfloat16) * 0.8)(B, H, Sk, Dh)),
        "v_cache": _w((lambda *s: torch.randn(*s, dtype=torch.bfloat16) * 0.05)(B, H, Sk, Dh)),
        "wq": _w((lambda *s: torch.randn(*s, dtype=torch.bfloat16) * 0.8)(D, H * Dh) * (D ** -0.5)),
        "wo": _w((lambda *s: torch.randn(*s, dtype=torch.bfloat16) * 0.05)(H * Dh, D) * ((H * Dh) ** -0.5)),
    }

def reference(x, k_cache, v_cache, wq, wo):
    B, Sq, D = x.shape
    H, Dh = 32, 128
    q = (lambda x, *d: x.permute(*d))((x @ wq).reshape(B, Sq, H, Dh), 0, 2, 1, 3)
    scores = torch.einsum("bhqd,bhkd->bhqk", q, k_cache) * (Dh ** -0.5)
    p = _softmax(scores, axis=-1)
    o = torch.einsum("bhqk,bhkd->bhqd", p, v_cache)
    return (lambda x, *d: x.permute(*d))(o, 0, 2, 1, 3).reshape(B, Sq, H * Dh) @ wo
```

```cpp
#include <hip/hip_runtime.h>
#include <hip/hip_bf16.h>
#include <stdint.h>

using bf16x8 = __attribute__((ext_vector_type(8))) short;
using s16x4  = __attribute__((ext_vector_type(4))) short;
using f32x16 = __attribute__((ext_vector_type(16))) float;
using u32x4  = __attribute__((ext_vector_type(4))) unsigned;
typedef short v4i16_t __attribute__((ext_vector_type(4)));
typedef __attribute__((address_space(3))) const char* lds_cptr;
#define DI __device__ __forceinline__
#define MFMA(a, b, c) __builtin_amdgcn_mfma_f32_32x32x16_bf16((a), (b), (c), 0, 0, 0)

constexpr int B_ = 32, SQ = 160, DM = 640, H_ = 32, SK = 512, DH = 128, HD = H_ * DH;

DI unsigned cvtpk(float lo, float hi) { unsigned r; asm("v_cvt_pk_bf16_f32 %0, %1, %2" : "=v"(r) : "v"(lo), "v"(hi)); return r; }
DI s16x4 vtr(lds_cptr p) { return __builtin_bit_cast(s16x4, __builtin_amdgcn_ds_read_tr16_b64_v4i16((__attribute__((address_space(3))) v4i16_t*)p)); }
DI unsigned off_a(unsigned row, unsigned ch) { return 2048u * (row >> 3) + 512u * (ch >> 2) + 64u * (row & 7) + 16u * ((ch & 3) ^ ((row >> 2) & 3)); }
DI bf16x8 pack8(const f32x16& x, int s) {
  u32x4 p = {cvtpk(x[8 * s], x[8 * s + 1]), cvtpk(x[8 * s + 2], x[8 * s + 3]), cvtpk(x[8 * s + 4], x[8 * s + 5]), cvtpk(x[8 * s + 6], x[8 * s + 7])};
  return __builtin_bit_cast(bf16x8, p);
}
template <int NT> DI void load_tile(char* lds, const uint16_t* g, int stride) {
  for (int c = threadIdx.x; c < 512; c += NT) { int row = c >> 4, ch = c & 15;
    *(u32x4*)(lds + off_a(row, ch)) = *(const u32x4*)(g + (size_t)row * stride + ch * 8); }
}
DI bf16x8 tr_frag(const char* tile, int lane, int t, int krow0, int krow1, bool swp) {
  int blk = (lane >> 4) & 1, q = (lane & 15) >> 2, p = lane & 3;
  if (swp) p = ((p & 1) << 1) | (p >> 1);
  int ch = 4 * t + 2 * blk + (p >> 1);
  s16x4 lo = vtr((lds_cptr)tile + off_a(krow0 + q, ch) + 8 * (p & 1));
  s16x4 hi = vtr((lds_cptr)tile + off_a(krow1 + q, ch) + 8 * (p & 1));
  return __builtin_shufflevector(lo, hi, 0, 1, 2, 3, 4, 5, 6, 7);
}

__global__ __launch_bounds__(320) void attn_kernel(const uint16_t* __restrict__ x, const uint16_t* __restrict__ kc, const uint16_t* __restrict__ vc,
                                                   const uint16_t* __restrict__ wq, uint16_t* __restrict__ o) {
  __shared__ __attribute__((aligned(16))) char lds[2 * 8192];
  const int bh = blockIdx.x, b = bh >> 5, h = bh & 31;
  const int lane = threadIdx.x & 63, w = threadIdx.x >> 6, r = lane & 31, hh = lane >> 5;
  f32x16 acc[4] = {};
  const uint16_t* xr = x + ((size_t)b * SQ + w * 32 + r) * DM + hh * 8;
  for (int kc0 = 0; kc0 < DM; kc0 += 32) {
    __syncthreads();
    load_tile<320>(lds, wq + (size_t)kc0 * HD + h * DH, HD);
    __syncthreads();
#pragma unroll
    for (int s = 0; s < 2; ++s) {
      bf16x8 xb = *(const bf16x8*)(xr + kc0 + s * 16);
#pragma unroll
      for (int t = 0; t < 4; ++t) { bf16x8 a = tr_frag(lds, lane, t, 16 * s + 8 * hh, 16 * s + 8 * hh + 4, true); acc[t] = MFMA(a, xb, acc[t]); }
    }
  }
  const float C = 0.088388347648318440f * 1.4426950408889634f;
  bf16x8 qf[8];
#pragma unroll
  for (int t = 0; t < 4; ++t) {
#pragma unroll
    for (int i = 0; i < 16; ++i) acc[t][i] *= C;
    qf[2 * t] = pack8(acc[t], 0); qf[2 * t + 1] = pack8(acc[t], 1);
  }
  f32x16 oacc[4] = {};
  float m = -1e30f, l = 0.f;
  const uint16_t* kp = kc + (size_t)bh * SK * DH; const uint16_t* vp = vc + (size_t)bh * SK * DH;
  for (int k0 = 0; k0 < SK; k0 += 32) {
    __syncthreads();
    load_tile<320>(lds, kp + (size_t)k0 * DH, DH);
    load_tile<320>(lds + 8192, vp + (size_t)k0 * DH, DH);
    __syncthreads();
    f32x16 s = {};
#pragma unroll
    for (int d = 0; d < 8; ++d) { bf16x8 a = *(const bf16x8*)(lds + off_a(r, 2 * d + hh)); s = MFMA(a, qf[d], s); }
    float mx = s[0];
#pragma unroll
    for (int i = 1; i < 16; ++i) mx = fmaxf(mx, s[i]);
    mx = fmaxf(mx, __shfl_xor(mx, 32));
    float mn = fmaxf(m, mx), alpha = __builtin_amdgcn_exp2f(m - mn); m = mn;
    float ps = 0.f;
#pragma unroll
    for (int i = 0; i < 16; ++i) { s[i] = __builtin_amdgcn_exp2f(s[i] - mn); ps += s[i]; }
    l = l * alpha + ps;
#pragma unroll
    for (int t = 0; t < 4; ++t)
#pragma unroll
      for (int i = 0; i < 16; ++i) oacc[t][i] *= alpha;
#pragma unroll
    for (int u = 0; u < 2; ++u) {
      bf16x8 pb = pack8(s, u);
#pragma unroll
      for (int t = 0; t < 4; ++t) { bf16x8 a = tr_frag(lds + 8192, lane, t, 16 * u + 4 * hh, 16 * u + 8 + 4 * hh, false); oacc[t] = MFMA(a, pb, oacc[t]); }
    }
  }
  l += __shfl_xor(l, 32);
  const float il = 1.f / l;
  uint16_t* op = o + ((size_t)b * SQ + w * 32 + r) * HD + h * DH + 4 * hh;
#pragma unroll
  for (int t = 0; t < 4; ++t)
#pragma unroll
    for (int g = 0; g < 4; ++g) {
      uint2 v; v.x = cvtpk(oacc[t][4 * g] * il, oacc[t][4 * g + 1] * il); v.y = cvtpk(oacc[t][4 * g + 2] * il, oacc[t][4 * g + 3] * il);
      *(uint2*)(op + 32 * t + 8 * g) = v;
    }
}

__global__ __launch_bounds__(256) void oproj_kernel(const uint16_t* __restrict__ o, const uint16_t* __restrict__ wo, uint16_t* __restrict__ out) {
  __shared__ __attribute__((aligned(16))) char lds[2 * 8192];
  const int mb = blockIdx.x / 5, nb = blockIdx.x % 5;
  const int lane = threadIdx.x & 63, w = threadIdx.x >> 6, r = lane & 31, hh = lane >> 5;
  f32x16 acc[4] = {};
  const uint16_t* orow = o + ((size_t)mb * 128 + w * 32 + r) * HD + hh * 8;
  for (int k0 = 0; k0 < HD; k0 += 64) {
    __syncthreads();
    load_tile<256>(lds, wo + (size_t)k0 * DM + nb * 128, DM);
    load_tile<256>(lds + 8192, wo + (size_t)(k0 + 32) * DM + nb * 128, DM);
    __syncthreads();
#pragma unroll
    for (int s = 0; s < 4; ++s) {
      bf16x8 xb = *(const bf16x8*)(orow + k0 + s * 16);
      const char* tl = lds + (s >> 1) * 8192; int sr = (s & 1) * 16;
#pragma unroll
      for (int t = 0; t < 4; ++t) { bf16x8 a = tr_frag(tl, lane, t, sr + 8 * hh, sr + 8 * hh + 4, false); acc[t] = MFMA(a, xb, acc[t]); }
    }
  }
  uint16_t* op = out + ((size_t)mb * 128 + w * 32 + r) * DM + nb * 128 + 4 * hh;
#pragma unroll
  for (int t = 0; t < 4; ++t)
#pragma unroll
    for (int g = 0; g < 4; ++g) {
      uint2 v; v.x = cvtpk(acc[t][4 * g], acc[t][4 * g + 1]); v.y = cvtpk(acc[t][4 * g + 2], acc[t][4 * g + 3]);
      *(uint2*)(op + 32 * t + 8 * g) = v;
    }
}

extern "C" void kernel_launch(void* const* d_in, const int* in_sizes, int n_in, void* d_out, int out_size, void* d_ws, size_t ws_size, hipStream_t stream) {
  const uint16_t* x = (const uint16_t*)d_in[0]; const uint16_t* kc = (const uint16_t*)d_in[1]; const uint16_t* vc = (const uint16_t*)d_in[2];
  const uint16_t* wq = (const uint16_t*)d_in[3]; const uint16_t* wo = (const uint16_t*)d_in[4];
  uint16_t* o = (uint16_t*)d_ws;
  attn_kernel<<<B_ * H_, 320, 0, stream>>>(x, kc, vc, wq, o);
  oproj_kernel<<<(B_ * SQ / 128) * 5, 256, 0, stream>>>(o, wo, (uint16_t*)d_out);
}
```

```cpp
#include <hip/hip_runtime.h>
#include <hip/hip_bf16.h>
#include <stdint.h>

using u32x4  = __attribute__((ext_vector_type(4))) unsigned;
using u32x2  = __attribute__((ext_vector_type(2))) unsigned;
#define DI __device__ __forceinline__
#define TYPE_Y (hg >> 2)
constexpr unsigned WS_FLAGS = 0;

DI float b2f(unsigned short v) { return __uint_as_float((unsigned)v << 16); }
DI unsigned cvtpk(float lo, float hi) { unsigned r; asm("v_cvt_pk_bf16_f32 %0, %1, %2" : "=v"(r) : "v"(lo), "v"(hi)); return r; }
DI float rbf(float f) { return __uint_as_float(cvtpk(f, 0.f) << 16); }
typedef __attribute__((address_space(1))) unsigned gu32;
typedef __attribute__((address_space(1))) unsigned long long gu64;
#define RLX_AGENT __ATOMIC_RELAXED, __HIP_MEMORY_SCOPE_AGENT

DI void slow_rows(const unsigned short* __restrict__ x, const unsigned short* __restrict__ kc, const unsigned short* __restrict__ vc,
                                       const unsigned short* __restrict__ wq, const unsigned short* __restrict__ wo, unsigned short* __restrict__ out,
                                       unsigned b, unsigned hg, bool all_rows, unsigned t, float* lds) {
  float* sq = lds; float* sc = lds + 4096; float* red = lds + 4608;
  for (unsigned r = 0; r < 160u; ++r) {
    const bool mine = r / 20u == hg;
    if (!mine && !all_rows) continue;
    const unsigned row = b * 160u + r;
    for (unsigned n = t; n < 4096u; n += 256u) { float s = 0.f; for (unsigned k = 0; k < 640u; ++k) s += b2f(x[(size_t)row * 640u + k]) * b2f(wq[(size_t)k * 4096u + n]); sq[n] = rbf(s); }
    __syncthreads();
    for (unsigned h = 0; h < 32u; ++h) {
      const unsigned short* kp = kc + (size_t)(b * 32u + h) * 65536u; const unsigned short* vp = vc + (size_t)(b * 32u + h) * 65536u;
      float mx = -__builtin_inff();
      for (unsigned k = t; k < 512u; k += 256u) { float s = 0.f; for (unsigned d = 0; d < 128u; ++d) s += sq[h * 128u + d] * b2f(kp[k * 128u + d]); s *= 0.08838834764831845f; sc[k] = s; mx = fmaxf(mx, s); }
      red[t] = mx; __syncthreads();
      for (unsigned o = 128u; o; o >>= 1) { if (t < o) red[t] = fmaxf(red[t], red[t + o]); __syncthreads(); }
      mx = red[0]; __syncthreads();
      float l = 0.f;
      for (unsigned k = t; k < 512u; k += 256u) { const float e = __expf(sc[k] - mx); sc[k] = e; l += e; }
      red[t] = l; __syncthreads();
      for (unsigned o = 128u; o; o >>= 1) { if (t < o) red[t] += red[t + o]; __syncthreads(); }
      l = red[0];
      if (t < 128u) { float o = 0.f; for (unsigned k = 0; k < 512u; ++k) o += sc[k] * b2f(vp[k * 128u + t]); sq[h * 128u + t] = rbf(o / l); }
      __syncthreads();
    }
    for (unsigned n = t; n < 640u; n += 256u) {
      if (!mine && (n >> 6) != hg && !((n >> 5) == 16u + 2u * (hg >> 2) + ((hg >> 1) & 1u) && r / 80u == (hg & 1u))) continue;
      float s = 0.f; for (unsigned k = 0; k < 4096u; ++k) s += sq[k] * b2f(wo[(size_t)k * 640u + n]); out[(size_t)row * 640u + n] = (unsigned short)(cvtpk(s, 0.f) & 0xffffu); }
    __syncthreads();
  }
}

__global__ __launch_bounds__(256, 1) void fused_mha(const char* __restrict__ x, const char* __restrict__ kc, const char* __restrict__ vc,
                                                    const char* __restrict__ wq, const char* __restrict__ wo, char* __restrict__ ws, unsigned short* __restrict__ out) {
  extern __shared__ __attribute__((aligned(16))) char lds[];
  typedef unsigned u32x8 __attribute__((ext_vector_type(8)));
  u32x8 ka8; u32x4 ka4;
  asm volatile("s_load_dwordx8 %0, %2, 0\n\ts_load_dwordx4 %1, %2, 32" : "={s[64:71]}"(ka8), "={s[72:75]}"(ka4) : "s"(__builtin_amdgcn_kernarg_segment_ptr()));
  __builtin_amdgcn_sched_barrier(0);
  const unsigned bid = blockIdx.x;
  const unsigned xg = bid & 7u, r_ = bid >> 3;
  const unsigned b = (xg >> 1) * 8u + (r_ >> 2), hg = (xg & 1u) * 4u + (r_ & 3u);
  const unsigned wv = __builtin_amdgcn_readfirstlane(threadIdx.x >> 6);
  const unsigned L = threadIdx.x & 63u, c = L & 15u, g = L >> 4, qq = c >> 2, p = L & 3u;
  const unsigned hsel = wv >> 1, qh = wv & 1u;
  auto PI = [](unsigned i) { return (0x78u >> (2u * i)) & 3u; };
  unsigned vKr = 8192u * hsel + 2048u * qq + 64u * p + 16u * (g ^ PI(qq));
  unsigned vV0 = 8192u * hsel + 2048u * g + 64u * qq + 16u * ((p >> 1) ^ (2u * (g & 1u))) + 8u * (p & 1u);
  unsigned vV1 = vV0 ^ 32u;
  const unsigned r4 = (L >> 2) & 3u, s4 = L >> 4, sl = L & 3u, r8 = (L >> 2) & 7u, sub = L >> 5;
  unsigned gK = wv * 2048u + r4 * 256u + 16u * (4u * s4 + (sl ^ PI(wv)));
  unsigned gV = wv * 2048u + r4 * 256u + 16u * (4u * s4 + (sl ^ (2u * (wv & 1u))));
  unsigned gW = (8u * wv + r4) * 8192u + 16u * (4u * s4 + sl);
  unsigned gX = (8u * wv + r8) * 1280u + 16u * (4u * sub + (sl ^ PI((2u * wv + (r8 >> 2)) & 3u)));
  unsigned gWo = (8u * wv + r8) * 1280u + 16u * (4u * sub + (sl ^ (2u * ((r8 >> 2) & 1u))));
  unsigned vW0 = 8224u * hsel + 2056u * g + 64u * qq + 16u * p;
  unsigned vXr = 1024u * (10u * qh + (c >> 3)) + 64u * (c & 7u) + 16u * (g ^ PI(qq));
  unsigned vWo = 1024u * (g >> 1) + 512u * hsel + 64u * (4u * (g & 1u) + qq) + 16u * ((p >> 1) ^ (2u * (g & 1u))) + 8u * (p & 1u) + 16384u * hsel;
  unsigned tXs = L < 50u ? (L % 10u) * 1280u + (L / 10u) * 128u : 0x40000000u;
  unsigned tWs = L < 40u ? ((L >> 3) * 64u + ((L >> 2) & 1u)) * 8192u + (L & 3u) * 128u : 0x40000000u;
  unsigned tL = L * 128u;
  unsigned ty = TYPE_Y ? 2u : 0u; asm volatile("" : "+s"(ty));
  unsigned dum = 0, tmp = 0, stat;
  asm volatile(
"s_lshl_b32 s48, %[wv], 11\n"
"s_mul_i32 s54, %[wv], 2056\n"
"s_lshl_b32 s49, %[wv], 10\n"
"s_and_b32 s90, %[rr], 3\n"
"s_mul_i32 s90, s90, 40\n"
"s_mul_i32 s59, %[wv], 10\n"
"s_add_u32 s90, s90, s59\n"
"s_mul_i32 s90, s90, 1280\n"
"s_lshr_b32 s91, %[rr], 2\n"
"s_lshl_b32 s91, s91, 2\n"
"s_add_u32 s91, s91, %[wv]\n"
"s_lshl_b32 s91, s91, 14\n"
"s_getpc_b64 s[76:77]\n"
"L_beg_%=:\n"
"s_and_b32 s77, s77, 0xffff\n"
"s_mov_b32 s78, L_end_%=-L_beg_%=\n"
"s_mov_b32 s79, 0x20000\n"
"s_lshl_b32 s58, %[rr], 2\n"
"s_add_u32 s58, s58, %[wv]\n"
"s_lshl_b32 s58, s58, 10\n"
"s_mov_b32 exec_lo, 255\n"
"s_mov_b32 exec_hi, 0\n"
"buffer_load_dword v254, v253, s[76:79], s58 offen\n"
"s_mov_b64 exec, -1\n"
"s_mul_i32 s58, %[bb], 204800\n"
"s_lshl_b32 s57, %[hg], 10\n"
"s_lshl_b32 s56, %[ty], 8\n"
"s_add_u32 s57, s57, s56\n"
"s_waitcnt lgkmcnt(0)\n"
"s_add_u32 s84, s64, s58\n"
"s_addc_u32 s85, s65, 0\n"
"s_and_b32 s85, s85, 65535\n"
"s_mov_b32 s87, 131072\n"
"s_mov_b32 s86, 204800\n"
"s_add_u32 s36, s70, s57\n"
"s_addc_u32 s37, s71, 0\n"
"s_and_b32 s37, s37, 65535\n"
"s_mov_b32 s39, 131072\n"
"s_sub_u32 s38, 5242880, s57\n"
"s_add_u32 s52, s90, 0\n"
"s_mov_b32 exec_lo, 1073741823\n"
"s_mov_b32 exec_hi, 0\n"
"buffer_load_dword v254, v251, s[84:87], s52 offen\n"
"s_mov_b64 exec, -1\n"
"s_add_u32 s52, s91, 0\n"
"s_mov_b32 exec_lo, 16777215\n"
"s_mov_b32 exec_hi, 0\n"
"buffer_load_dword v254, v252, s[36:39], s52 offen\n"
"s_mov_b64 exec, -1\n"
"s_lshl_b32 s58, %[bb], 8\n"
"s_add_u32 s58, s58, %[hg]\n"
"s_lshl_b32 s58, s58, 3\n"
"v_mov_b32 v2, s58\n"
"s_mov_b64 s[80:81], s[74:75]\n"
"global_load_dwordx2 v[4:5], v2, s[80:81] sc1\n"
"s_add_u32 m0, s49, 32896\n"
"s_mov_b32 s52, 0\n"
"buffer_load_dwordx4 v246, s[84:87], s52 offen lds\n"
"s_add_u32 m0, s49, 36992\n"
"s_mov_b32 s52, 40960\n"
"buffer_load_dwordx4 v246, s[84:87], s52 offen lds\n"
"s_add_u32 m0, s49, 41088\n"
"s_mov_b32 s52, 81920\n"
"buffer_load_dwordx4 v246, s[84:87], s52 offen lds\n"
"s_add_u32 m0, s49, 45184\n"
"s_mov_b32 s52, 122880\n"
"buffer_load_dwordx4 v246, s[84:87], s52 offen lds\n"
"s_add_u32 m0, s49, 49280\n"
"s_mov_b32 s52, 163840\n"
"buffer_load_dwordx4 v246, s[84:87], s52 offen lds\n"
"s_add_u32 m0, s54, 0\n"
"s_mov_b32 s52, 0\n"
"buffer_load_dwordx4 v245, s[36:39], s52 offen lds\n"
"s_add_u32 m0, s54, 1024\n"
"s_mov_b32 s52, 32768\n"
"buffer_load_dwordx4 v245, s[36:39], s52 offen lds\n"
"s_add_u32 m0, s54, 8224\n"
"s_mov_b32 s52, 256\n"
"buffer_load_dwordx4 v245, s[36:39], s52 offen lds\n"
"s_add_u32 m0, s54, 9248\n"
"s_mov_b32 s52, 33024\n"
"buffer_load_dwordx4 v245, s[36:39], s52 offen lds\n"
"s_add_u32 m0, s54, 16448\n"
"s_mov_b32 s52, 262144\n"
"buffer_load_dwordx4 v245, s[36:39], s52 offen lds\n"
"s_add_u32 m0, s54, 17472\n"
"s_mov_b32 s52, 294912\n"
"buffer_load_dwordx4 v245, s[36:39], s52 offen lds\n"
"s_add_u32 m0, s54, 24672\n"
"s_mov_b32 s52, 262400\n"
"buffer_load_dwordx4 v245, s[36:39], s52 offen lds\n"
"s_add_u32 m0, s54, 25696\n"
"s_mov_b32 s52, 295168\n"
"buffer_load_dwordx4 v245, s[36:39], s52 offen lds\n"
"s_lshl_b32 s58, %[bb], 3\n"
"s_add_u32 s58, s58, %[hg]\n"
"s_lshl_b32 s58, s58, 19\n"
"s_add_u32 s40, s66, s58\n"
"s_addc_u32 s41, s67, 0\n"
"s_and_b32 s41, s41, 65535\n"
"s_mov_b32 s43, 131072\n"
"s_mov_b32 s42, 524288\n"
"s_add_u32 s60, s68, s58\n"
"s_addc_u32 s61, s69, 0\n"
"s_and_b32 s61, s61, 65535\n"
"s_mov_b32 s63, 131072\n"
"s_mov_b32 s62, 524288\n"
"s_mul_i32 s58, %[bb], 1493248\n"
"s_add_u32 s58, s58, 262144\n"
"s_add_u32 s44, s74, s58\n"
"s_addc_u32 s45, s75, 0\n"
"s_and_b32 s45, s45, 65535\n"
"s_mov_b32 s47, 131072\n"
"s_mov_b32 s46, 1493248\n"
"s_mul_i32 s58, %[hg], 655360\n"
"s_add_u32 s64, s72, s58\n"
"s_addc_u32 s65, s73, 0\n"
"s_and_b32 s65, s65, 65535\n"
"s_mov_b32 s67, 131072\n"
"s_mov_b32 s66, 655360\n"
"s_lshl_b32 s92, %[bb], 2\n"
"s_add_u32 s92, s92, %[wv]\n"
"s_mul_i32 s92, s92, 5120\n"
"s_mul_i32 s93, %[wv], 20480\n"
"s_add_u32 s93, s93, 32768\n"
"s_cmp_eq_u32 %[wv], 3\n"
"s_cselect_b32 s93, 139520, s93\n"
"s_xor_b32 s59, %[wv], 2\n"
"s_mul_i32 s94, s59, 20480\n"
"s_add_u32 s94, s94, 32768\n"
"s_cmp_eq_u32 s59, 3\n"
"s_cselect_b32 s94, 139520, s94\n"
"s_and_b32 s95, %[rr], 15\n"
"s_lshl_b32 s95, s95, 13\n"
"s_mov_b64 s[68:69], s[84:85]\n"
"s_mov_b64 s[70:71], s[86:87]\n"
"s_mov_b64 s[76:77], s[64:65]\n"
"s_mov_b32 s78, 655360\n"
"s_mov_b32 s79, 131072\n"
"s_add_u32 s72, s36, 512\n"
"s_addc_u32 s73, s37, 0\n"
"s_sub_u32 s74, s38, 512\n"
"s_mov_b32 s75, 131072\n"
"s_sub_u32 s96, s36, 512\n"
"s_subb_u32 s97, s37, 0\n"
"s_add_u32 s98, s38, 512\n"
"s_mov_b32 s99, 131072\n"
"s_cmp_eq_u32 %[ty], 0\n"
"s_mov_b32 s70, 0\n"
"s_cselect_b32 s74, s74, 0\n"
"s_cselect_b32 s98, 0, s98\n"
"s_sub_u32 s58, 8, %[hg]\n"
"s_mul_i32 s58, s58, 655360\n"
"s_cmp_eq_u32 %[ty], 0\n"
"s_cselect_b32 s78, s58, 0\n"
"s_mov_b32 s50, 1040356334\n"
"s_mov_b32 s53, 1400897536\n"
"s_mov_b64 s[58:59], 0\n"
"s_mov_b32 s51, %[ty]\n"
"v_mov_b32 v0, 1065369472\n"
"v_accvgpr_write_b32 a164, v0\n"
"v_accvgpr_write_b32 a165, v0\n"
"v_accvgpr_write_b32 a166, v0\n"
"v_accvgpr_write_b32 a167, v0\n"
"v_add_u32 v224, 0, v247\n"
"v_add_u32 v230, 32896, v248\n"
"v_add_u32 v225, 53376, v247\n"
"v_add_u32 v231, 86272, v248\n"
"v_add_u32 v226, 106752, v247\n"
"v_add_u32 v232, 139648, v248\n"
"s_add_u32 m0, s49, 86272\n"
"s_mov_b32 s52, 128\n"
"buffer_load_dwordx4 v246, s[84:87], s52 offen lds\n"
"s_add_u32 m0, s49, 90368\n"
"s_mov_b32 s52, 41088\n"
"buffer_load_dwordx4 v246, s[84:87], s52 offen lds\n"
"s_add_u32 m0, s49, 94464\n"
"s_mov_b32 s52, 82048\n"
"buffer_load_dwordx4 v246, s[84:87], s52 offen lds\n"
"s_add_u32 m0, s49, 98560\n"
"s_mov_b32 s52, 123008\n"
"buffer_load_dwordx4 v246, s[84:87], s52 offen lds\n"
"s_add_u32 m0, s49, 102656\n"
"s_mov_b32 s52, 163968\n"
"buffer_load_dwordx4 v246, s[84:87], s52 offen lds\n"
"s_add_u32 m0, s54, 53376\n"
"s_mov_b32 s52, 524288\n"
"buffer_load_dwordx4 v245, s[36:39], s52 offen lds\n"
"s_add_u32 m0, s54, 54400\n"
"s_mov_b32 s52, 557056\n"
"buffer_load_dwordx4 v245, s[36:39], s52 offen lds\n"
"s_add_u32 m0, s54, 61600\n"
"s_mov_b32 s52, 524544\n"
"buffer_load_dwordx4 v245, s[36:39], s52 offen lds\n"
"s_add_u32 m0, s54, 62624\n"
"s_mov_b32 s52, 557312\n"
"buffer_load_dwordx4 v245, s[36:39], s52 offen lds\n"
"s_add_u32 m0, s54, 69824\n"
"s_mov_b32 s52, 786432\n"
"buffer_load_dwordx4 v245, s[36:39], s52 offen lds\n"
"s_add_u32 m0, s54, 70848\n"
"s_mov_b32 s52, 819200\n"
"buffer_load_dwordx4 v245, s[36:39], s52 offen lds\n"
"s_add_u32 m0, s54, 78048\n"
"s_mov_b32 s52, 786688\n"
"buffer_load_dwordx4 v245, s[36:39], s52 offen lds\n"
"s_add_u32 m0, s54, 79072\n"
"s_mov_b32 s52, 819456\n"
"buffer_load_dwordx4 v245, s[36:39], s52 offen lds\n"
"s_add_u32 s52, s90, 0\n"
"s_mov_b32 exec_lo, 3221225472\n"
"s_mov_b32 exec_hi, 262143\n"
"buffer_load_dword v254, v251, s[84:87], s52 offen\n"
"s_mov_b64 exec, -1\n"
"s_add_u32 s52, s91, 0\n"
"s_mov_b32 exec_lo, 4278190080\n"
"s_mov_b32 exec_hi, 255\n"
"buffer_load_dword v254, v252, s[36:39], s52 offen\n"
"s_mov_b64 exec, -1\n"
"s_waitcnt vmcnt(15) lgkmcnt(0)\n"
"s_barrier\n"
"ds_read_b128 a[32:35], v230 offset:0\n"
"ds_read_b128 a[36:39], v230 offset:2048\n"
"ds_read_b128 a[40:43], v230 offset:4096\n"
"ds_read_b128 a[44:47], v230 offset:6144\n"
"ds_read_b128 a[48:51], v230 offset:8192\n"
"ds_read_b64_tr_b16 a[0:1], v224 offset:0\n"
"ds_read_b64_tr_b16 a[2:3], v224 offset:1024\n"
"ds_read_b64_tr_b16 a[4:5], v224 offset:8\n"
"ds_read_b64_tr_b16 a[6:7], v224 offset:1032\n"
"ds_read_b64_tr_b16 a[8:9], v224 offset:256\n"
"ds_read_b64_tr_b16 a[10:11], v224 offset:1280\n"
"ds_read_b64_tr_b16 a[12:13], v224 offset:264\n"
"ds_read_b64_tr_b16 a[14:15], v224 offset:1288\n"
"ds_read_b64_tr_b16 a[16:17], v224 offset:512\n"
"ds_read_b64_tr_b16 a[18:19], v224 offset:1536\n"
"ds_read_b64_tr_b16 a[20:21], v224 offset:520\n"
"ds_read_b64_tr_b16 a[22:23], v224 offset:1544\n"
"ds_read_b64_tr_b16 a[24:25], v224 offset:768\n"
"ds_read_b64_tr_b16 a[26:27], v224 offset:1792\n"
"ds_read_b64_tr_b16 a[28:29], v224 offset:776\n"
"ds_read_b64_tr_b16 a[30:31], v224 offset:1800\n"
"v_add_co_u32 v4, vcc, 1, v4\n"
"s_nop 1\n"
"v_addc_co_u32 v5, vcc, 0, v5, vcc\n"
"global_store_dwordx2 v2, v[4:5], s[80:81] sc1\n"
"s_mov_b32 s81, s95\n"
"s_waitcnt lgkmcnt(0)\n"
"v_mfma_f32_16x16x32_bf16 v[0:3], a[0:3], a[32:35], 0\n"
"ds_read_b128 v[192:195], v230 offset:512\n"
"s_add_u32 m0, s49, 139648\n"
"v_mfma_f32_16x16x32_bf16 v[4:7], a[0:3], a[36:39], 0\n"
"ds_read_b128 v[196:199], v230 offset:2560\n"
"s_mov_b32 s52, 256\n"
"v_mfma_f32_16x16x32_bf16 v[8:11], a[0:3], a[40:43], 0\n"
"buffer_load_dwordx4 v246, s[84:87], s52 offen lds\n"
"v_mfma_f32_16x16x32_bf16 v[12:15], a[0:3], a[44:47], 0\n"
"ds_read_b128 v[200:203], v230 offset:4608\n"
"v_mfma_f32_16x16x32_bf16 v[16:19], a[0:3], a[48:51], 0\n"
"ds_read_b128 v[204:207], v230 offset:6656\n"
"s_add_u32 m0, s49, 143744\n"
"v_mfma_f32_16x16x32_bf16 v[20:23], a[4:7], a[32:35], 0\n"
"s_mov_b32 s52, 41216\n"
"buffer_load_dwordx4 v246, s[84:87], s52 offen lds\n"
"v_mfma_f32_16x16x32_bf16 v[24:27], a[4:7], a[36:39], 0\n"
"ds_read_b128 v[208:211], v230 offset:8704\n"
"v_mfma_f32_16x16x32_bf16 v[28:31], a[4:7], a[40:43], 0\n"
"ds_read_b64_tr_b16 v[160:161], v224 offset:16448\n"
"v_mfma_f32_16x16x32_bf16 v[32:35], a[4:7], a[44:47], 0\n"
"s_add_u32 m0, s49, 147840\n"
"s_mov_b32 s52, 82176\n"
"v_mfma_f32_16x16x32_bf16 v[36:39], a[4:7], a[48:51], 0\n"
"ds_read_b64_tr_b16 v[162:163], v224 offset:17472\n"
"v_mfma_f32_16x16x32_bf16 v[40:43], a[8:11], a[32:35], 0\n"
"buffer_load_dwordx4 v246, s[84:87], s52 offen lds\n"
"v_mfma_f32_16x16x32_bf16 v[44:47], a[8:11], a[36:39], 0\n"
"ds_read_b64_tr_b16 v[164:165], v224 offset:16456\n"
"v_mfma_f32_16x16x32_bf16 v[48:51], a[8:11], a[40:43], 0\n"
"ds_read_b64_tr_b16 v[166:167], v224 offset:17480\n"
"s_add_u32 m0, s49, 151936\n"
"v_mfma_f32_16x16x32_bf16 v[52:55], a[8:11], a[44:47], 0\n"
"s_mov_b32 s52, 123136\n"
"buffer_load_dwordx4 v246, s[84:87], s52 offen lds\n"
"v_mfma_f32_16x16x32_bf16 v[56:59], a[8:11], a[48:51], 0\n"
"ds_read_b64_tr_b16 v[168:169], v224 offset:16704\n"
"v_mfma_f32_16x16x32_bf16 v[60:63], a[12:15], a[32:35], 0\n"
"ds_read_b64_tr_b16 v[170:171], v224 offset:17728\n"
"v_mfma_f32_16x16x32_bf16 v[64:67], a[12:15], a[36:39], 0\n"
"s_add_u32 m0, s49, 156032\n"
"s_mov_b32 s52, 164096\n"
"v_mfma_f32_16x16x32_bf16 v[68:71], a[12:15], a[40:43], 0\n"
"ds_read_b64_tr_b16 v[172:173], v224 offset:16712\n"
"v_mfma_f32_16x16x32_bf16 v[72:75], a[12:15], a[44:47], 0\n"
"ds_read_b64_tr_b16 v[174:175], v224 offset:17736\n"
"v_mfma_f32_16x16x32_bf16 v[76:79], a[12:15], a[48:51], 0\n"
"buffer_load_dwordx4 v246, s[84:87], s52 offen lds\n"
"v_mfma_f32_16x16x32_bf16 v[80:83], a[16:19], a[32:35], 0\n"
"ds_read_b64_tr_b16 v[176:177], v224 offset:16960\n"
"s_add_u32 m0, s54, 106752\n"
"v_mfma_f32_16x16x32_bf16 v[84:87], a[16:19], a[36:39], 0\n"
"s_mov_b32 s52, 1048576\n"
"buffer_load_dwordx4 v245, s[36:39], s52 offen lds\n"
"v_mfma_f32_16x16x32_bf16 v[88:91], a[16:19], a[40:43], 0\n"
"ds_read_b64_tr_b16 v[178:179], v224 offset:17984\n"
"v_mfma_f32_16x16x32_bf16 v[92:95], a[16:19], a[44:47], 0\n"
"ds_read_b64_tr_b16 v[180:181], v224 offset:16968\n"
"v_mfma_f32_16x16x32_bf16 v[96:99], a[16:19], a[48:51], 0\n"
"s_add_u32 m0, s54, 107776\n"
"s_mov_b32 s52, 1081344\n"
"v_mfma_f32_16x16x32_bf16 v[100:103], a[20:23], a[32:35], 0\n"
"ds_read_b64_tr_b16 v[182:183], v224 offset:17992\n"
"v_mfma_f32_16x16x32_bf16 v[104:107], a[20:23], a[36:39], 0\n"
"ds_read_b64_tr_b16 v[184:185], v224 offset:17216\n"
"v_mfma_f32_16x16x32_bf16 v[108:111], a[20:23], a[40:43], 0\n"
"buffer_load_dwordx4 v245, s[36:39], s52 offen lds\n"
"v_mfma_f32_16x16x32_bf16 v[112:115], a[20:23], a[44:47], 0\n"
"ds_read_b64_tr_b16 v[186:187], v224 offset:18240\n"
"s_add_u32 m0, s54, 114976\n"
"v_mfma_f32_16x16x32_bf16 v[116:119], a[20:23], a[48:51], 0\n"
"ds_read_b64_tr_b16 v[188:189], v224 offset:17224\n"
"s_mov_b32 s52, 1048832\n"
"v_mfma_f32_16x16x32_bf16 v[120:123], a[24:27], a[32:35], 0\n"
"buffer_load_dwordx4 v245, s[36:39], s52 offen lds\n"
"v_mfma_f32_16x16x32_bf16 v[124:127], a[24:27], a[36:39], 0\n"
"ds_read_b64_tr_b16 v[190:191], v224 offset:18248\n"
"v_mfma_f32_16x16x32_bf16 v[128:131], a[24:27], a[40:43], 0\n"
"v_mfma_f32_16x16x32_bf16 v[132:135], a[24:27], a[44:47], 0\n"
"v_mfma_f32_16x16x32_bf16 v[136:139], a[24:27], a[48:51], 0\n"
"s_add_u32 s52, s90, 640\n"
"v_mfma_f32_16x16x32_bf16 v[140:143], a[28:31], a[32:35], 0\n"
"s_mov_b32 exec_lo, 1023\n"
"s_mov_b32 exec_hi, 0\n"
"buffer_load_dword v254, v251, s[84:87], s52 offen\n"
"s_mov_b64 exec, -1\n"
"v_mfma_f32_16x16x32_bf16 v[144:147], a[28:31], a[36:39], 0\n"
"s_add_u32 s52, s91, 2621440\n"
"v_mfma_f32_16x16x32_bf16 v[148:151], a[28:31], a[40:43], 0\n"
"s_mov_b32 exec_lo, 255\n"
"s_mov_b32 exec_hi, 0\n"
"buffer_load_dword v254, v252, s[36:39], s52 offen\n"
"s_mov_b64 exec, -1\n"
"v_mfma_f32_16x16x32_bf16 v[152:155], a[28:31], a[44:47], 0\n"
"v_mfma_f32_16x16x32_bf16 v[156:159], a[28:31], a[48:51], 0\n"
"L_qpa_%=:\n"
"s_waitcnt vmcnt(10) lgkmcnt(0)\n"
"s_barrier\n"
"v_mfma_f32_16x16x32_bf16 v[0:3], v[160:163], v[192:195], v[0:3]\n"
"ds_read_b128 a[32:35], v231 offset:0\n"
"s_add_u32 m0, s54, 116000\n"
"v_mfma_f32_16x16x32_bf16 v[4:7], v[160:163], v[196:199], v[4:7]\n"
"ds_read_b128 a[36:39], v231 offset:2048\n"
"s_mov_b32 s52, 1081600\n"
"v_mfma_f32_16x16x32_bf16 v[8:11], v[160:163], v[200:203], v[8:11]\n"
"buffer_load_dwordx4 v245, s[36:39], s52 offen lds\n"
"v_mfma_f32_16x16x32_bf16 v[12:15], v[160:163], v[204:207], v[12:15]\n"
"ds_read_b128 a[40:43], v231 offset:4096\n"
"v_mfma_f32_16x16x32_bf16 v[16:19], v[160:163], v[208:211], v[16:19]\n"
"ds_read_b128 a[44:47], v231 offset:6144\n"
"s_add_u32 m0, s54, 123200\n"
"v_mfma_f32_16x16x32_bf16 v[20:23], v[164:167], v[192:195], v[20:23]\n"
"s_mov_b32 s52, 1310720\n"
"buffer_load_dwordx4 v245, s[36:39], s52 offen lds\n"
"v_mfma_f32_16x16x32_bf16 v[24:27], v[164:167], v[196:199], v[24:27]\n"
"ds_read_b128 a[48:51], v231 offset:8192\n"
"v_mfma_f32_16x16x32_bf16 v[28:31], v[164:167], v[200:203], v[28:31]\n"
"ds_read_b64_tr_b16 a[0:1], v225 offset:0\n"
"v_mfma_f32_16x16x32_bf16 v[32:35], v[164:167], v[204:207], v[32:35]\n"
"s_add_u32 m0, s54, 124224\n"
"s_mov_b32 s52, 1343488\n"
"v_mfma_f32_16x16x32_bf16 v[36:39], v[164:167], v[208:211], v[36:39]\n"
"ds_read_b64_tr_b16 a[2:3], v225 offset:1024\n"
"v_mfma_f32_16x16x32_bf16 v[40:43], v[168:171], v[192:195], v[40:43]\n"
"buffer_load_dwordx4 v245, s[36:39], s52 offen lds\n"
"v_mfma_f32_16x16x32_bf16 v[44:47], v[168:171], v[196:199], v[44:47]\n"
"ds_read_b64_tr_b16 a[4:5], v225 offset:8\n"
"v_mfma_f32_16x16x32_bf16 v[48:51], v[168:171], v[200:203], v[48:51]\n"
"ds_read_b64_tr_b16 a[6:7], v225 offset:1032\n"
"s_add_u32 m0, s54, 131424\n"
"v_mfma_f32_16x16x32_bf16 v[52:55], v[168:171], v[204:207], v[52:55]\n"
"s_mov_b32 s52, 1310976\n"
"buffer_load_dwordx4 v245, s[36:39], s52 offen lds\n"
"v_mfma_f32_16x16x32_bf16 v[56:59], v[168:171], v[208:211], v[56:59]\n"
"ds_read_b64_tr_b16 a[8:9], v225 offset:256\n"
"v_mfma_f32_16x16x32_bf16 v[60:63], v[172:175], v[192:195], v[60:63]\n"
"ds_read_b64_tr_b16 a[10:11], v225 offset:1280\n"
"v_mfma_f32_16x16x32_bf16 v[64:67], v[172:175], v[196:199], v[64:67]\n"
"s_add_u32 m0, s54, 132448\n"
"s_mov_b32 s52, 1343744\n"
"v_mfma_f32_16x16x32_bf16 v[68:71], v[172:175], v[200:203], v[68:71]\n"
"ds_read_b64_tr_b16 a[12:13], v225 offset:264\n"
"v_mfma_f32_16x16x32_bf16 v[72:75], v[172:175], v[204:207], v[72:75]\n"
"ds_read_b64_tr_b16 a[14:15], v225 offset:1288\n"
"v_mfma_f32_16x16x32_bf16 v[76:79], v[172:175], v[208:211], v[76:79]\n"
"buffer_load_dwordx4 v245, s[36:39], s52 offen lds\n"
"v_mfma_f32_16x16x32_bf16 v[80:83], v[176:179], v[192:195], v[80:83]\n"
"ds_read_b64_tr_b16 a[16:17], v225 offset:512\n"
"s_add_u32 m0, s49, 32896\n"
"v_mfma_f32_16x16x32_bf16 v[84:87], v[176:179], v[196:199], v[84:87]\n"
"s_mov_b32 s52, 384\n"
"buffer_load_dwordx4 v246, s[84:87], s52 offen lds\n"
"v_mfma_f32_16x16x32_bf16 v[88:91], v[176:179], v[200:203], v[88:91]\n"
"ds_read_b64_tr_b16 a[18:19], v225 offset:1536\n"
"v_mfma_f32_16x16x32_bf16 v[92:95], v[176:179], v[204:207], v[92:95]\n"
"ds_read_b64_tr_b16 a[20:21], v225 offset:520\n"
"v_mfma_f32_16x16x32_bf16 v[96:99], v[176:179], v[208:211], v[96:99]\n"
"s_add_u32 m0, s49, 36992\n"
"s_mov_b32 s52, 41344\n"
"v_mfma_f32_16x16x32_bf16 v[100:103], v[180:183], v[192:195], v[100:103]\n"
"ds_read_b64_tr_b16 a[22:23], v225 offset:1544\n"
"v_mfma_f32_16x16x32_bf16 v[104:107], v[180:183], v[196:199], v[104:107]\n"
"ds_read_b64_tr_b16 a[24:25], v225 offset:768\n"
"v_mfma_f32_16x16x32_bf16 v[108:111], v[180:183], v[200:203], v[108:111]\n"
"buffer_load_dwordx4 v246, s[84:87], s52 offen lds\n"
"v_mfma_f32_16x16x32_bf16 v[112:115], v[180:183], v[204:207], v[112:115]\n"
"ds_read_b64_tr_b16 a[26:27], v225 offset:1792\n"
"s_add_u32 m0, s49, 41088\n"
"v_mfma_f32_16x16x32_bf16 v[116:119], v[180:183], v[208:211], v[116:119]\n"
"ds_read_b64_tr_b16 a[28:29], v225 offset:776\n"
"s_mov_b32 s52, 82304\n"
"v_mfma_f32_16x16x32_bf16 v[120:123], v[184:187], v[192:195], v[120:123]\n"
"buffer_load_dwordx4 v246, s[84:87], s52 offen lds\n"
"v_mfma_f32_16x16x32_bf16 v[124:127], v[184:187], v[196:199], v[124:127]\n"
"ds_read_b64_tr_b16 a[30:31], v225 offset:1800\n"
"v_mfma_f32_16x16x32_bf16 v[128:131], v[184:187], v[200:203], v[128:131]\n"
"s_add_u32 m0, s49, 45184\n"
"s_mov_b32 s52, 123264\n"
"v_mfma_f32_16x16x32_bf16 v[132:135], v[184:187], v[204:207], v[132:135]\n"
"buffer_load_dwordx4 v246, s[84:87], s52 offen lds\n"
"v_mfma_f32_16x16x32_bf16 v[136:139], v[184:187], v[208:211], v[136:139]\n"
"v_mfma_f32_16x16x32_bf16 v[140:143], v[188:191], v[192:195], v[140:143]\n"
"v_mfma_f32_16x16x32_bf16 v[144:147], v[188:191], v[196:199], v[144:147]\n"
"s_add_u32 m0, s49, 49280\n"
"s_mov_b32 s52, 164224\n"
"v_mfma_f32_16x16x32_bf16 v[148:151], v[188:191], v[200:203], v[148:151]\n"
"buffer_load_dwordx4 v246, s[84:87], s52 offen lds\n"
"v_mfma_f32_16x16x32_bf16 v[152:155], v[188:191], v[204:207], v[152:155]\n"
"v_mfma_f32_16x16x32_bf16 v[156:159], v[188:191], v[208:211], v[156:159]\n"
"s_waitcnt lgkmcnt(0)\n"
"v_mfma_f32_16x16x32_bf16 v[0:3], a[0:3], a[32:35], v[0:3]\n"
"ds_read_b128 v[192:195], v231 offset:512\n"
"s_add_u32 m0, s54, 0\n"
"v_mfma_f32_16x16x32_bf16 v[4:7], a[0:3], a[36:39], v[4:7]\n"
"ds_read_b128 v[196:199], v231 offset:2560\n"
"s_mov_b32 s52, 1572864\n"
"v_mfma_f32_16x16x32_bf16 v[8:11], a[0:3], a[40:43], v[8:11]\n"
"buffer_load_dwordx4 v245, s[36:39], s52 offen lds\n"
"v_mfma_f32_16x16x32_bf16 v[12:15], a[0:3], a[44:47], v[12:15]\n"
"ds_read_b128 v[200:203], v231 offset:4608\n"
"v_mfma_f32_16x16x32_bf16 v[16:19], a[0:3], a[48:51], v[16:19]\n"
"ds_read_b128 v[204:207], v231 offset:6656\n"
"s_add_u32 m0, s54, 1024\n"
"v_mfma_f32_16x16x32_bf16 v[20:23], a[4:7], a[32:35], v[20:23]\n"
"s_mov_b32 s52, 1605632\n"
"buffer_load_dwordx4 v245, s[36:39], s52 offen lds\n"
"v_mfma_f32_16x16x32_bf16 v[24:27], a[4:7], a[36:39], v[24:27]\n"
"ds_read_b128 v[208:211], v231 offset:8704\n"
"v_mfma_f32_16x16x32_bf16 v[28:31], a[4:7], a[40:43], v[28:31]\n"
"ds_read_b64_tr_b16 v[160:161], v225 offset:16448\n"
"v_mfma_f32_16x16x32_bf16 v[32:35], a[4:7], a[44:47], v[32:35]\n"
"s_add_u32 m0, s54, 8224\n"
"s_mov_b32 s52, 1573120\n"
"v_mfma_f32_16x16x32_bf16 v[36:39], a[4:7], a[48:51], v[36:39]\n"
"ds_read_b64_tr_b16 v[162:163], v225 offset:17472\n"
"v_mfma_f32_16x16x32_bf16 v[40:43], a[8:11], a[32:35], v[40:43]\n"
"buffer_load_dwordx4 v245, s[36:39], s52 offen lds\n"
"v_mfma_f32_16x16x32_bf16 v[44:47], a[8:11], a[36:39], v[44:47]\n"
"ds_read_b64_tr_b16 v[164:165], v225 offset:16456\n"
"v_mfma_f32_16x16x32_bf16 v[48:51], a[8:11], a[40:43], v[48:51]\n"
"ds_read_b64_tr_b16 v[166:167], v225 offset:17480\n"
"s_add_u32 m0, s54, 9248\n"
"v_mfma_f32_16x16x32_bf16 v[52:55], a[8:11], a[44:47], v[52:55]\n"
"s_mov_b32 s52, 1605888\n"
"buffer_load_dwordx4 v245, s[36:39], s52 offen lds\n"
"v_mfma_f32_16x16x32_bf16 v[56:59], a[8:11], a[48:51], v[56:59]\n"
"ds_read_b64_tr_b16 v[168:169], v225 offset:16704\n"
"v_mfma_f32_16x16x32_bf16 v[60:63], a[12:15], a[32:35], v[60:63]\n"
"ds_read_b64_tr_b16 v[170:171], v225 offset:17728\n"
"v_mfma_f32_16x16x32_bf16 v[64:67], a[12:15], a[36:39], v[64:67]\n"
"s_add_u32 m0, s54, 16448\n"
"s_mov_b32 s52, 1835008\n"
"v_mfma_f32_16x16x32_bf16 v[68:71], a[12:15], a[40:43], v[68:71]\n"
"ds_read_b64_tr_b16 v[172:173], v225 offset:16712\n"
"v_mfma_f32_16x16x32_bf16 v[72:75], a[12:15], a[44:47], v[72:75]\n"
"ds_read_b64_tr_b16 v[174:175], v225 offset:17736\n"
"v_mfma_f32_16x16x32_bf16 v[76:79], a[12:15], a[48:51], v[76:79]\n"
"buffer_load_dwordx4 v245, s[36:39], s52 offen lds\n"
"v_mfma_f32_16x16x32_bf16 v[80:83], a[16:19], a[32:35], v[80:83]\n"
"ds_read_b64_tr_b16 v[176:177], v225 offset:16960\n"
"s_add_u32 m0, s54, 17472\n"
"v_mfma_f32_16x16x32_bf16 v[84:87], a[16:19], a[36:39], v[84:87]\n"
"s_mov_b32 s52, 1867776\n"
"buffer_load_dwordx4 v245, s[36:39], s52 offen lds\n"
"v_mfma_f32_16x16x32_bf16 v[88:91], a[16:19], a[40:43], v[88:91]\n"
"ds_read_b64_tr_b16 v[178:179], v225 offset:17984\n"
"v_mfma_f32_16x16x32_bf16 v[92:95], a[16:19], a[44:47], v[92:95]\n"
"ds_read_b64_tr_b16 v[180:181], v225 offset:16968\n"
"v_mfma_f32_16x16x32_bf16 v[96:99], a[16:19], a[48:51], v[96:99]\n"
"s_add_u32 m0, s54, 24672\n"
"s_mov_b32 s52, 1835264\n"
"v_mfma_f32_16x16x32_bf16 v[100:103], a[20:23], a[32:35], v[100:103]\n"
"ds_read_b64_tr_b16 v[182:183], v225 offset:17992\n"
"v_mfma_f32_16x16x32_bf16 v[104:107], a[20:23], a[36:39], v[104:107]\n"
"ds_read_b64_tr_b16 v[184:185], v225 offset:17216\n"
"v_mfma_f32_16x16x32_bf16 v[108:111], a[20:23], a[40:43], v[108:111]\n"
"buffer_load_dwordx4 v245, s[36:39], s52 offen lds\n"
"v_mfma_f32_16x16x32_bf16 v[112:115], a[20:23], a[44:47], v[112:115]\n"
"ds_read_b64_tr_b16 v[186:187], v225 offset:18240\n"
"s_add_u32 m0, s54, 25696\n"
"v_mfma_f32_16x16x32_bf16 v[116:119], a[20:23], a[48:51], v[116:119]\n"
"ds_read_b64_tr_b16 v[188:189], v225 offset:17224\n"
"s_mov_b32 s52, 1868032\n"
"v_mfma_f32_16x16x32_bf16 v[120:123], a[24:27], a[32:35], v[120:123]\n"
"buffer_load_dwordx4 v245, s[36:39], s52 offen lds\n"
"v_mfma_f32_16x16x32_bf16 v[124:127], a[24:27], a[36:39], v[124:127]\n"
"ds_read_b64_tr_b16 v[190:191], v225 offset:18248\n"
"v_mfma_f32_16x16x32_bf16 v[128:131], a[24:27], a[40:43], v[128:131]\n"
"s_add_u32 s52, s90, 640\n"
"v_mfma_f32_16x16x32_bf16 v[132:135], a[24:27], a[44:47], v[132:135]\n"
"s_mov_b32 exec_lo, 1047552\n"
"s_mov_b32 exec_hi, 0\n"
"buffer_load_dword v254, v251, s[84:87], s52 offen\n"
"s_mov_b64 exec, -1\n"
"v_mfma_f32_16x16x32_bf16 v[136:139], a[24:27], a[48:51], v[136:139]\n"
"s_add_u32 s52, s91, 2621440\n"
"v_mfma_f32_16x16x32_bf16 v[140:143], a[28:31], a[32:35], v[140:143]\n"
"s_mov_b32 exec_lo, 65280\n"
"s_mov_b32 exec_hi, 0\n"
"buffer_load_dword v254, v252, s[36:39], s52 offen\n"
"s_mov_b64 exec, -1\n"
"v_mfma_f32_16x16x32_bf16 v[144:147], a[28:31], a[36:39], v[144:147]\n"
"v_mfma_f32_16x16x32_bf16 v[148:151], a[28:31], a[40:43], v[148:151]\n"
"v_mfma_f32_16x16x32_bf16 v[152:155], a[28:31], a[44:47], v[152:155]\n"
"v_mfma_f32_16x16x32_bf16 v[156:159], a[28:31], a[48:51], v[156:159]\n"
"s_waitcnt vmcnt(15) lgkmcnt(0)\n"
"s_barrier\n"
"v_mfma_f32_16x16x32_bf16 v[0:3], v[160:163], v[192:195], v[0:3]\n"
"ds_read_b128 a[32:35], v232 offset:0\n"
"s_add_u32 m0, s49, 86272\n"
"v_mfma_f32_16x16x32_bf16 v[4:7], v[160:163], v[196:199], v[4:7]\n"
"ds_read_b128 a[36:39], v232 offset:2048\n"
"s_mov_b32 s52, 512\n"
"v_mfma_f32_16x16x32_bf16 v[8:11], v[160:163], v[200:203], v[8:11]\n"
"buffer_load_dwordx4 v246, s[84:87], s52 offen lds\n"
"v_mfma_f32_16x16x32_bf16 v[12:15], v[160:163], v[204:207], v[12:15]\n"
"ds_read_b128 a[40:43], v232 offset:4096\n"
"v_mfma_f32_16x16x32_bf16 v[16:19], v[160:163], v[208:211], v[16:19]\n"
"ds_read_b128 a[44:47], v232 offset:6144\n"
"v_mfma_f32_16x16x32_bf16 v[20:23], v[164:167], v[192:195], v[20:23]\n"
"s_add_u32 m0, s49, 90368\n"
"s_mov_b32 s52, 41472\n"
"v_mfma_f32_16x16x32_bf16 v[24:27], v[164:167], v[196:199], v[24:27]\n"
"ds_read_b128 a[48:51], v232 offset:8192\n"
"v_mfma_f32_16x16x32_bf16 v[28:31], v[164:167], v[200:203], v[28:31]\n"
"ds_read_b64_tr_b16 a[0:1], v226 offset:0\n"
"v_mfma_f32_16x16x32_bf16 v[32:35], v[164:167], v[204:207], v[32:35]\n"
"buffer_load_dwordx4 v246, s[84:87], s52 offen lds\n"
"v_mfma_f32_16x16x32_bf16 v[36:39], v[164:167], v[208:211], v[36:39]\n"
"ds_read_b64_tr_b16 a[2:3], v226 offset:1024\n"
"v_mfma_f32_16x16x32_bf16 v[40:43], v[168:171], v[192:195], v[40:43]\n"
"s_add_u32 m0, s49, 94464\n"
"s_mov_b32 s52, 82432\n"
"v_mfma_f32_16x16x32_bf16 v[44:47], v[168:171], v[196:199], v[44:47]\n"
"ds_read_b64_tr_b16 a[4:5], v226 offset:8\n"
"v_mfma_f32_16x16x32_bf16 v[48:51], v[168:171], v[200:203], v[48:51]\n"
"ds_read_b64_tr_b16 a[6:7], v226 offset:1032\n"
"v_mfma_f32_16x16x32_bf16 v[52:55], v[168:171], v[204:207], v[52:55]\n"
"buffer_load_dwordx4 v246, s[84:87], s52 offen lds\n"
"v_mfma_f32_16x16x32_bf16 v[56:59], v[168:171], v[208:211], v[56:59]\n"
"ds_read_b64_tr_b16 a[8:9], v226 offset:256\n"
"v_mfma_f32_16x16x32_bf16 v[60:63], v[172:175], v[192:195], v[60:63]\n"
"ds_read_b64_tr_b16 a[10:11], v226 offset:1280\n"
"s_add_u32 m0, s49, 98560\n"
"v_mfma_f32_16x16x32_bf16 v[64:67], v[172:175], v[196:199], v[64:67]\n"
"s_mov_b32 s52, 123392\n"
"buffer_load_dwordx4 v246, s[84:87], s52 offen lds\n"
"v_mfma_f32_16x16x32_bf16 v[68:71], v[172:175], v[200:203], v[68:71]\n"
"ds_read_b64_tr_b16 a[12:13], v226 offset:264\n"
"v_mfma_f32_16x16x32_bf16 v[72:75], v[172:175], v[204:207], v[72:75]\n"
"ds_read_b64_tr_b16 a[14:15], v226 offset:1288\n"
"v_mfma_f32_16x16x32_bf16 v[76:79], v[172:175], v[208:211], v[76:79]\n"
"v_mfma_f32_16x16x32_bf16 v[80:83], v[176:179], v[192:195], v[80:83]\n"
"ds_read_b64_tr_b16 a[16:17], v226 offset:512\n"
"s_add_u32 m0, s49, 102656\n"
"v_mfma_f32_16x16x32_bf16 v[84:87], v[176:179], v[196:199], v[84:87]\n"
"s_mov_b32 s52, 164352\n"
"buffer_load_dwordx4 v246, s[84:87], s52 offen lds\n"
"v_mfma_f32_16x16x32_bf16 v[88:91], v[176:179], v[200:203], v[88:91]\n"
"ds_read_b64_tr_b16 a[18:19], v226 offset:1536\n"
"v_mfma_f32_16x16x32_bf16 v[92:95], v[176:179], v[204:207], v[92:95]\n"
"ds_read_b64_tr_b16 a[20:21], v226 offset:520\n"
"v_mfma_f32_16x16x32_bf16 v[96:99], v[176:179], v[208:211], v[96:99]\n"
"v_mfma_f32_16x16x32_bf16 v[100:103], v[180:183], v[192:195], v[100:103]\n"
"ds_read_b64_tr_b16 a[22:23], v226 offset:1544\n"
"s_add_u32 m0, s54, 53376\n"
"v_mfma_f32_16x16x32_bf16 v[104:107], v[180:183], v[196:199], v[104:107]\n"
"ds_read_b64_tr_b16 a[24:25], v226 offset:768\n"
"s_mov_b32 s52, 2097152\n"
"v_mfma_f32_16x16x32_bf16 v[108:111], v[180:183], v[200:203], v[108:111]\n"
"buffer_load_dwordx4 v245, s[36:39], s52 offen lds\n"
"v_mfma_f32_16x16x32_bf16 v[112:115], v[180:183], v[204:207], v[112:115]\n"
"ds_read_b64_tr_b16 a[26:27], v226 offset:1792\n"
"v_mfma_f32_16x16x32_bf16 v[116:119], v[180:183], v[208:211], v[116:119]\n"
"ds_read_b64_tr_b16 a[28:29], v226 offset:776\n"
"v_mfma_f32_16x16x32_bf16 v[120:123], v[184:187], v[192:195], v[120:123]\n"
"s_add_u32 m0, s54, 54400\n"
"s_mov_b32 s52, 2129920\n"
"v_mfma_f32_16x16x32_bf16 v[124:127], v[184:187], v[196:199], v[124:127]\n"
"ds_read_b64_tr_b16 a[30:31], v226 offset:1800\n"
"v_mfma_f32_16x16x32_bf16 v[128:131], v[184:187], v[200:203], v[128:131]\n"
"buffer_load_dwordx4 v245, s[36:39], s52 offen lds\n"
"v_mfma_f32_16x16x32_bf16 v[132:135], v[184:187], v[204:207], v[132:135]\n"
"v_mfma_f32_16x16x32_bf16 v[136:139], v[184:187], v[208:211], v[136:139]\n"
"v_mfma_f32_16x16x32_bf16 v[140:143], v[188:191], v[192:195], v[140:143]\n"
"s_add_u32 m0, s54, 61600\n"
"s_mov_b32 s52, 2097408\n"
"v_mfma_f32_16x16x32_bf16 v[144:147], v[188:191], v[196:199], v[144:147]\n"
"buffer_load_dwordx4 v245, s[36:39], s52 offen lds\n"
"v_mfma_f32_16x16x32_bf16 v[148:151], v[188:191], v[200:203], v[148:151]\n"
"v_mfma_f32_16x16x32_bf16 v[152:155], v[188:191], v[204:207], v[152:155]\n"
"v_mfma_f32_16x16x32_bf16 v[156:159], v[188:191], v[208:211], v[156:159]\n"
"s_waitcnt lgkmcnt(0)\n"
"v_mfma_f32_16x16x32_bf16 v[0:3], a[0:3], a[32:35], v[0:3]\n"
"ds_read_b128 v[192:195], v232 offset:512\n"
"s_add_u32 m0, s54, 62624\n"
"v_mfma_f32_16x16x32_bf16 v[4:7], a[0:3], a[36:39], v[4:7]\n"
"ds_read_b128 v[196:199], v232 offset:2560\n"
"s_mov_b32 s52, 2130176\n"
"v_mfma_f32_16x16x32_bf16 v[8:11], a[0:3], a[40:43], v[8:11]\n"
"buffer_load_dwordx4 v245, s[36:39], s52 offen lds\n"
"v_mfma_f32_16x16x32_bf16 v[12:15], a[0:3], a[44:47], v[12:15]\n"
"ds_read_b128 v[200:203], v232 offset:4608\n"
"v_mfma_f32_16x16x32_bf16 v[16:19], a[0:3], a[48:51], v[16:19]\n"
"ds_read_b128 v[204:207], v232 offset:6656\n"
"v_mfma_f32_16x16x32_bf16 v[20:23], a[4:7], a[32:35], v[20:23]\n"
"s_add_u32 m0, s54, 69824\n"
"s_mov_b32 s52, 2359296\n"
"v_mfma_f32_16x16x32_bf16 v[24:27], a[4:7], a[36:39], v[24:27]\n"
"ds_read_b128 v[208:211], v232 offset:8704\n"
"v_mfma_f32_16x16x32_bf16 v[28:31], a[4:7], a[40:43], v[28:31]\n"
"ds_read_b64_tr_b16 v[160:161], v226 offset:16448\n"
"v_mfma_f32_16x16x32_bf16 v[32:35], a[4:7], a[44:47], v[32:35]\n"
"buffer_load_dwordx4 v245, s[36:39], s52 offen lds\n"
"v_mfma_f32_16x16x32_bf16 v[36:39], a[4:7], a[48:51], v[36:39]\n"
"ds_read_b64_tr_b16 v[162:163], v226 offset:17472\n"
"v_mfma_f32_16x16x32_bf16 v[40:43], a[8:11], a[32:35], v[40:43]\n"
"s_add_u32 m0, s54, 70848\n"
"s_mov_b32 s52, 2392064\n"
"v_mfma_f32_16x16x32_bf16 v[44:47], a[8:11], a[36:39], v[44:47]\n"
"ds_read_b64_tr_b16 v[164:165], v226 offset:16456\n"
"v_mfma_f32_16x16x32_bf16 v[48:51], a[8:11], a[40:43], v[48:51]\n"
"ds_read_b64_tr_b16 v[166:167], v226 offset:17480\n"
"v_mfma_f32_16x16x32_bf16 v[52:55], a[8:11], a[44:47], v[52:55]\n"
"buffer_load_dwordx4 v245, s[36:39], s52 offen lds\n"
"v_mfma_f32_16x16x32_bf16 v[56:59], a[8:11], a[48:51], v[56:59]\n"
"ds_read_b64_tr_b16 v[168:169], v226 offset:16704\n"
"v_mfma_f32_16x16x32_bf16 v[60:63], a[12:15], a[32:35], v[60:63]\n"
"ds_read_b64_tr_b16 v[170:171], v226 offset:17728\n"
"s_add_u32 m0, s54, 78048\n"
"v_mfma_f32_16x16x32_bf16 v[64:67], a[12:15], a[36:39], v[64:67]\n"
"s_mov_b32 s52, 2359552\n"
"buffer_load_dwordx4 v245, s[36:39], s52 offen lds\n"
"v_mfma_f32_16x16x32_bf16 v[68:71], a[12:15], a[40:43], v[68:71]\n"
"ds_read_b64_tr_b16 v[172:173], v226 offset:16712\n"
"v_mfma_f32_16x16x32_bf16 v[72:75], a[12:15], a[44:47], v[72:75]\n"
"ds_read_b64_tr_b16 v[174:175], v226 offset:17736\n"
"v_mfma_f32_16x16x32_bf16 v[76:79], a[12:15], a[48:51], v[76:79]\n"
"v_mfma_f32_16x16x32_bf16 v[80:83], a[16:19], a[32:35], v[80:83]\n"
"ds_read_b64_tr_b16 v[176:177], v226 offset:16960\n"
"s_add_u32 m0, s54, 79072\n"
"v_mfma_f32_16x16x32_bf16 v[84:87], a[16:19], a[36:39], v[84:87]\n"
"s_mov_b32 s52, 2392320\n"
"buffer_load_dwordx4 v245, s[36:39], s52 offen lds\n"
"v_mfma_f32_16x16x32_bf16 v[88:91], a[16:19], a[40:43], v[88:91]\n"
"ds_read_b64_tr_b16 v[178:179], v226 offset:17984\n"
"v_mfma_f32_16x16x32_bf16 v[92:95], a[16:19], a[44:47], v[92:95]\n"
"ds_read_b64_tr_b16 v[180:181], v226 offset:16968\n"
"v_mfma_f32_16x16x32_bf16 v[96:99], a[16:19], a[48:51], v[96:99]\n"
"v_mfma_f32_16x16x32_bf16 v[100:103], a[20:23], a[32:35], v[100:103]\n"
"ds_read_b64_tr_b16 v[182:183], v226 offset:17992\n"
"v_mfma_f32_16x16x32_bf16 v[104:107], a[20:23], a[36:39], v[104:107]\n"
"ds_read_b64_tr_b16 v[184:185], v226 offset:17216\n"
"s_add_u32 s52, s90, 640\n"
"v_mfma_f32_16x16x32_bf16 v[108:111], a[20:23], a[40:43], v[108:111]\n"
"s_mov_b32 exec_lo, 1072693248\n"
"s_mov_b32 exec_hi, 0\n"
"buffer_load_dword v254, v251, s[84:87], s52 offen\n"
"s_mov_b64 exec, -1\n"
"v_mfma_f32_16x16x32_bf16 v[112:115], a[20:23], a[44:47], v[112:115]\n"
"ds_read_b64_tr_b16 v[186:187], v226 offset:18240\n"
"s_add_u32 s52, s91, 2621440\n"
"v_mfma_f32_16x16x32_bf16 v[116:119], a[20:23], a[48:51], v[116:119]\n"
"ds_read_b64_tr_b16 v[188:189], v226 offset:17224\n"
"v_mfma_f32_16x16x32_bf16 v[120:123], a[24:27], a[32:35], v[120:123]\n"
"s_mov_b32 exec_lo, 16711680\n"
"s_mov_b32 exec_hi, 0\n"
"buffer_load_dword v254, v252, s[36:39], s52 offen\n"
"s_mov_b64 exec, -1\n"
"v_mfma_f32_16x16x32_bf16 v[124:127], a[24:27], a[36:39], v[124:127]\n"
"ds_read_b64_tr_b16 v[190:191], v226 offset:18248\n"
"v_mfma_f32_16x16x32_bf16 v[128:131], a[24:27], a[40:43], v[128:131]\n"
"v_mfma_f32_16x16x32_bf16 v[132:135], a[24:27], a[44:47], v[132:135]\n"
"v_mfma_f32_16x16x32_bf16 v[136:139], a[24:27], a[48:51], v[136:139]\n"
"v_mfma_f32_16x16x32_bf16 v[140:143], a[28:31], a[32:35], v[140:143]\n"
"v_mfma_f32_16x16x32_bf16 v[144:147], a[28:31], a[36:39], v[144:147]\n"
"v_mfma_f32_16x16x32_bf16 v[148:151], a[28:31], a[40:43], v[148:151]\n"
"v_mfma_f32_16x16x32_bf16 v[152:155], a[28:31], a[44:47], v[152:155]\n"
"v_mfma_f32_16x16x32_bf16 v[156:159], a[28:31], a[48:51], v[156:159]\n"
"s_waitcnt vmcnt(17) lgkmcnt(0)\n"
"s_barrier\n"
"v_mfma_f32_16x16x32_bf16 v[0:3], v[160:163], v[192:195], v[0:3]\n"
"ds_read_b128 a[32:35], v230 offset:0\n"
"s_add_u32 m0, s49, 139648\n"
"v_mfma_f32_16x16x32_bf16 v[4:7], v[160:163], v[196:199], v[4:7]\n"
"ds_read_b128 a[36:39], v230 offset:2048\n"
"s_mov_b32 s52, 640\n"
"v_mfma_f32_16x16x32_bf16 v[8:11], v[160:163], v[200:203], v[8:11]\n"
"buffer_load_dwordx4 v246, s[84:87], s52 offen lds\n"
"v_mfma_f32_16x16x32_bf16 v[12:15], v[160:163], v[204:207], v[12:15]\n"
"ds_read_b128 a[40:43], v230 offset:4096\n"
"v_mfma_f32_16x16x32_bf16 v[16:19], v[160:163], v[208:211], v[16:19]\n"
"ds_read_b128 a[44:47], v230 offset:6144\n"
"v_mfma_f32_16x16x32_bf16 v[20:23], v[164:167], v[192:195], v[20:23]\n"
"s_add_u32 m0, s49, 143744\n"
"s_mov_b32 s52, 41600\n"
"v_mfma_f32_16x16x32_bf16 v[24:27], v[164:167], v[196:199], v[24:27]\n"
"ds_read_b128 a[48:51], v230 offset:8192\n"
"v_mfma_f32_16x16x32_bf16 v[28:31], v[164:167], v[200:203], v[28:31]\n"
"ds_read_b64_tr_b16 a[0:1], v224 offset:0\n"
"v_mfma_f32_16x16x32_bf16 v[32:35], v[164:167], v[204:207], v[32:35]\n"
"buffer_load_dwordx4 v246, s[84:87], s52 offen lds\n"
"v_mfma_f32_16x16x32_bf16 v[36:39], v[164:167], v[208:211], v[36:39]\n"
"ds_read_b64_tr_b16 a[2:3], v224 offset:1024\n"
"v_mfma_f32_16x16x32_bf16 v[40:43], v[168:171], v[192:195], v[40:43]\n"
"s_add_u32 m0, s49, 147840\n"
"s_mov_b32 s52, 82560\n"
"v_mfma_f32_16x16x32_bf16 v[44:47], v[168:171], v[196:199], v[44:47]\n"
"ds_read_b64_tr_b16 a[4:5], v224 offset:8\n"
"v_mfma_f32_16x16x32_bf16 v[48:51], v[168:171], v[200:203], v[48:51]\n"
"ds_read_b64_tr_b16 a[6:7], v224 offset:1032\n"
"v_mfma_f32_16x16x32_bf16 v[52:55], v[168:171], v[204:207], v[52:55]\n"
"buffer_load_dwordx4 v246, s[84:87], s52 offen lds\n"
"v_mfma_f32_16x16x32_bf16 v[56:59], v[168:171], v[208:211], v[56:59]\n"
"ds_read_b64_tr_b16 a[8:9], v224 offset:256\n"
"v_mfma_f32_16x16x32_bf16 v[60:63], v[172:175], v[192:195], v[60:63]\n"
"ds_read_b64_tr_b16 a[10:11], v224 offset:1280\n"
"s_add_u32 m0, s49, 151936\n"
"v_mfma_f32_16x16x32_bf16 v[64:67], v[172:175], v[196:199], v[64:67]\n"
"s_mov_b32 s52, 123520\n"
"buffer_load_dwordx4 v246, s[84:87], s52 offen lds\n"
"v_mfma_f32_16x16x32_bf16 v[68:71], v[172:175], v[200:203], v[68:71]\n"
"ds_read_b64_tr_b16 a[12:13], v224 offset:264\n"
"v_mfma_f32_16x16x32_bf16 v[72:75], v[172:175], v[204:207], v[72:75]\n"
"ds_read_b64_tr_b16 a[14:15], v224 offset:1288\n"
"v_mfma_f32_16x16x32_bf16 v[76:79], v[172:175], v[208:211], v[76:79]\n"
"v_mfma_f32_16x16x32_bf16 v[80:83], v[176:179], v[192:195], v[80:83]\n"
"ds_read_b64_tr_b16 a[16:17], v224 offset:512\n"
"s_add_u32 m0, s49, 156032\n"
"v_mfma_f32_16x16x32_bf16 v[84:87], v[176:179], v[196:199], v[84:87]\n"
"s_mov_b32 s52, 164480\n"
"buffer_load_dwordx4 v246, s[84:87], s52 offen lds\n"
"v_mfma_f32_16x16x32_bf16 v[88:91], v[176:179], v[200:203], v[88:91]\n"
"ds_read_b64_tr_b16 a[18:19], v224 offset:1536\n"
"v_mfma_f32_16x16x32_bf16 v[92:95], v[176:179], v[204:207], v[92:95]\n"
"ds_read_b64_tr_b16 a[20:21], v224 offset:520\n"
"v_mfma_f32_16x16x32_bf16 v[96:99], v[176:179], v[208:211], v[96:99]\n"
"v_mfma_f32_16x16x32_bf16 v[100:103], v[180:183], v[192:195], v[100:103]\n"
"ds_read_b64_tr_b16 a[22:23], v224 offset:1544\n"
"s_add_u32 m0, s54, 106752\n"
"v_mfma_f32_16x16x32_bf16 v[104:107], v[180:183], v[196:199], v[104:107]\n"
"ds_read_b64_tr_b16 a[24:25], v224 offset:768\n"
"s_mov_b32 s52, 2621440\n"
"v_mfma_f32_16x16x32_bf16 v[108:111], v[180:183], v[200:203], v[108:111]\n"
"buffer_load_dwordx4 v245, s[36:39], s52 offen lds\n"
"v_mfma_f32_16x16x32_bf16 v[112:115], v[180:183], v[204:207], v[112:115]\n"
"ds_read_b64_tr_b16 a[26:27], v224 offset:1792\n"
"v_mfma_f32_16x16x32_bf16 v[116:119], v[180:183], v[208:211], v[116:119]\n"
"ds_read_b64_tr_b16 a[28:29], v224 offset:776\n"
"v_mfma_f32_16x16x32_bf16 v[120:123], v[184:187], v[192:195], v[120:123]\n"
"s_add_u32 m0, s54, 107776\n"
"s_mov_b32 s52, 2654208\n"
"v_mfma_f32_16x16x32_bf16 v[124:127], v[184:187], v[196:199], v[124:127]\n"
"ds_read_b64_tr_b16 a[30:31], v224 offset:1800\n"
"v_mfma_f32_16x16x32_bf16 v[128:131], v[184:187], v[200:203], v[128:131]\n"
"buffer_load_dwordx4 v245, s[36:39], s52 offen lds\n"
"v_mfma_f32_16x16x32_bf16 v[132:135], v[184:187], v[204:207], v[132:135]\n"
"v_mfma_f32_16x16x32_bf16 v[136:139], v[184:187], v[208:211], v[136:139]\n"
"v_mfma_f32_16x16x32_bf16 v[140:143], v[188:191], v[192:195], v[140:143]\n"
"s_add_u32 m0, s54, 114976\n"
"s_mov_b32 s52, 2621696\n"
"v_mfma_f32_16x16x32_bf16 v[144:147], v[188:191], v[196:199], v[144:147]\n"
"buffer_load_dwordx4 v245, s[36:39], s52 offen lds\n"
"v_mfma_f32_16x16x32_bf16 v[148:151], v[188:191], v[200:203], v[148:151]\n"
"v_mfma_f32_16x16x32_bf16 v[152:155], v[188:191], v[204:207], v[152:155]\n"
"v_mfma_f32_16x16x32_bf16 v[156:159], v[188:191], v[208:211], v[156:159]\n"
"s_waitcnt lgkmcnt(0)\n"
"v_mfma_f32_16x16x32_bf16 v[0:3], a[0:3], a[32:35], v[0:3]\n"
"ds_read_b128 v[192:195], v230 offset:512\n"
"s_add_u32 m0, s54, 116000\n"
"v_mfma_f32_16x16x32_bf16 v[4:7], a[0:3], a[36:39], v[4:7]\n"
"ds_read_b128 v[196:199], v230 offset:2560\n"
"s_mov_b32 s52, 2654464\n"
"v_mfma_f32_16x16x32_bf16 v[8:11], a[0:3], a[40:43], v[8:11]\n"
"buffer_load_dwordx4 v245, s[36:39], s52 offen lds\n"
"v_mfma_f32_16x16x32_bf16 v[12:15], a[0:3], a[44:47], v[12:15]\n"
"ds_read_b128 v[200:203], v230 offset:4608\n"
"v_mfma_f32_16x16x32_bf16 v[16:19], a[0:3], a[48:51], v[16:19]\n"
"ds_read_b128 v[204:207], v230 offset:6656\n"
"v_mfma_f32_16x16x32_bf16 v[20:23], a[4:7], a[32:35], v[20:23]\n"
"s_add_u32 m0, s54, 123200\n"
"s_mov_b32 s52, 2883584\n"
"v_mfma_f32_16x16x32_bf16 v[24:27], a[4:7], a[36:39], v[24:27]\n"
"ds_read_b128 v[208:211], v230 offset:8704\n"
"v_mfma_f32_16x16x32_bf16 v[28:31], a[4:7], a[40:43], v[28:31]\n"
"ds_read_b64_tr_b16 v[160:161], v224 offset:16448\n"
"v_mfma_f32_16x16x32_bf16 v[32:35], a[4:7], a[44:47], v[32:35]\n"
"buffer_load_dwordx4 v245, s[36:39], s52 offen lds\n"
"v_mfma_f32_16x16x32_bf16 v[36:39], a[4:7], a[48:51], v[36:39]\n"
"ds_read_b64_tr_b16 v[162:163], v224 offset:17472\n"
"v_mfma_f32_16x16x32_bf16 v[40:43], a[8:11], a[32:35], v[40:43]\n"
"s_add_u32 m0, s54, 124224\n"
"s_mov_b32 s52, 2916352\n"
"v_mfma_f32_16x16x32_bf16 v[44:47], a[8:11], a[36:39], v[44:47]\n"
"ds_read_b64_tr_b16 v[164:165], v224 offset:16456\n"
"v_mfma_f32_16x16x32_bf16 v[48:51], a[8:11], a[40:43], v[48:51]\n"
"ds_read_b64_tr_b16 v[166:167], v224 offset:17480\n"
"v_mfma_f32_16x16x32_bf16 v[52:55], a[8:11], a[44:47], v[52:55]\n"
"buffer_load_dwordx4 v245, s[36:39], s52 offen lds\n"
"v_mfma_f32_16x16x32_bf16 v[56:59], a[8:11], a[48:51], v[56:59]\n"
"ds_read_b64_tr_b16 v[168:169], v224 offset:16704\n"
"v_mfma_f32_16x16x32_bf16 v[60:63], a[12:15], a[32:35], v[60:63]\n"
"ds_read_b64_tr_b16 v[170:171], v224 offset:17728\n"
"s_add_u32 m0, s54, 131424\n"
"v_mfma_f32_16x16x32_bf16 v[64:67], a[12:15], a[36:39], v[64:67]\n"
"s_mov_b32 s52, 2883840\n"
"buffer_load_dwordx4 v245, s[36:39], s52 offen lds\n"
"v_mfma_f32_16x16x32_bf16 v[68:71], a[12:15], a[40:43], v[68:71]\n"
"ds_read_b64_tr_b16 v[172:173], v224 offset:16712\n"
"v_mfma_f32_16x16x32_bf16 v[72:75], a[12:15], a[44:47], v[72:75]\n"
"ds_read_b64_tr_b16 v[174:175], v224 offset:17736\n"
"v_mfma_f32_16x16x32_bf16 v[76:79], a[12:15], a[48:51], v[76:79]\n"
"v_mfma_f32_16x16x32_bf16 v[80:83], a[16:19], a[32:35], v[80:83]\n"
"ds_read_b64_tr_b16 v[176:177], v224 offset:16960\n"
"s_add_u32 m0, s54, 132448\n"
"v_mfma_f32_16x16x32_bf16 v[84:87], a[16:19], a[36:39], v[84:87]\n"
"s_mov_b32 s52, 2916608\n"
"buffer_load_dwordx4 v245, s[36:39], s52 offen lds\n"
"v_mfma_f32_16x16x32_bf16 v[88:91], a[16:19], a[40:43], v[88:91]\n"
"ds_read_b64_tr_b16 v[178:179], v224 offset:17984\n"
"v_mfma_f32_16x16x32_bf16 v[92:95], a[16:19], a[44:47], v[92:95]\n"
"ds_read_b64_tr_b16 v[180:181], v224 offset:16968\n"
"v_mfma_f32_16x16x32_bf16 v[96:99], a[16:19], a[48:51], v[96:99]\n"
"v_mfma_f32_16x16x32_bf16 v[100:103], a[20:23], a[32:35], v[100:103]\n"
"ds_read_b64_tr_b16 v[182:183], v224 offset:17992\n"
"v_mfma_f32_16x16x32_bf16 v[104:107], a[20:23], a[36:39], v[104:107]\n"
"ds_read_b64_tr_b16 v[184:185], v224 offset:17216\n"
"s_add_u32 s52, s90, 640\n"
"v_mfma_f32_16x16x32_bf16 v[108:111], a[20:23], a[40:43], v[108:111]\n"
"s_mov_b32 exec_lo, 3221225472\n"
"s_mov_b32 exec_hi, 255\n"
"buffer_load_dword v254, v251, s[84:87], s52 offen\n"
"s_mov_b64 exec, -1\n"
"v_mfma_f32_16x16x32_bf16 v[112:115], a[20:23], a[44:47], v[112:115]\n"
"ds_read_b64_tr_b16 v[186:187], v224 offset:18240\n"
"s_add_u32 s52, s91, 2621440\n"
"v_mfma_f32_16x16x32_bf16 v[116:119], a[20:23], a[48:51], v[116:119]\n"
"ds_read_b64_tr_b16 v[188:189], v224 offset:17224\n"
"v_mfma_f32_16x16x32_bf16 v[120:123], a[24:27], a[32:35], v[120:123]\n"
"s_mov_b32 exec_lo, 4278190080\n"
"s_mov_b32 exec_hi, 0\n"
"buffer_load_dword v254, v252, s[36:39], s52 offen\n"
"s_mov_b64 exec, -1\n"
"v_mfma_f32_16x16x32_bf16 v[124:127], a[24:27], a[36:39], v[124:127]\n"
"ds_read_b64_tr_b16 v[190:191], v224 offset:18248\n"
"s_add_u32 s52, s91, 0\n"
"v_mfma_f32_16x16x32_bf16 v[128:131], a[24:27], a[40:43], v[128:131]\n"
"s_mov_b32 exec_lo, 255\n"
"s_mov_b32 exec_hi, 0\n"
"buffer_load_dword v254, v252, s[96:99], s52 offen\n"
"s_mov_b64 exec, -1\n"
"v_mfma_f32_16x16x32_bf16 v[132:135], a[24:27], a[44:47], v[132:135]\n"
"s_add_u32 s52, s91, 0\n"
"v_mfma_f32_16x16x32_bf16 v[136:139], a[24:27], a[48:51], v[136:139]\n"
"s_mov_b32 exec_lo, 65280\n"
"s_mov_b32 exec_hi, 0\n"
"buffer_load_dword v254, v252, s[96:99], s52 offen\n"
"s_mov_b64 exec, -1\n"
"v_mfma_f32_16x16x32_bf16 v[140:143], a[28:31], a[32:35], v[140:143]\n"
"v_mfma_f32_16x16x32_bf16 v[144:147], a[28:31], a[36:39], v[144:147]\n"
"v_mfma_f32_16x16x32_bf16 v[148:151], a[28:31], a[40:43], v[148:151]\n"
"v_mfma_f32_16x16x32_bf16 v[152:155], a[28:31], a[44:47], v[152:155]\n"
"v_mfma_f32_16x16x32_bf16 v[156:159], a[28:31], a[48:51], v[156:159]\n"
"s_waitcnt vmcnt(19) lgkmcnt(0)\n"
"s_barrier\n"
"v_mfma_f32_16x16x32_bf16 v[0:3], v[160:163], v[192:195], v[0:3]\n"
"ds_read_b128 a[32:35], v231 offset:0\n"
"s_add_u32 m0, s49, 32896\n"
"v_mfma_f32_16x16x32_bf16 v[4:7], v[160:163], v[196:199], v[4:7]\n"
"ds_read_b128 a[36:39], v231 offset:2048\n"
"s_mov_b32 s52, 768\n"
"v_mfma_f32_16x16x32_bf16 v[8:11], v[160:163], v[200:203], v[8:11]\n"
"buffer_load_dwordx4 v246, s[84:87], s52 offen lds\n"
"v_mfma_f32_16x16x32_bf16 v[12:15], v[160:163], v[204:207], v[12:15]\n"
"ds_read_b128 a[40:43], v231 offset:4096\n"
"v_mfma_f32_16x16x32_bf16 v[16:19], v[160:163], v[208:211], v[16:19]\n"
"ds_read_b128 a[44:47], v231 offset:6144\n"
"v_mfma_f32_16x16x32_bf16 v[20:23], v[164:167], v[192:195], v[20:23]\n"
"s_add_u32 m0, s49, 36992\n"
"s_mov_b32 s52, 41728\n"
"v_mfma_f32_16x16x32_bf16 v[24:27], v[164:167], v[196:199], v[24:27]\n"
"ds_read_b128 a[48:51], v231 offset:8192\n"
"v_mfma_f32_16x16x32_bf16 v[28:31], v[164:167], v[200:203], v[28:31]\n"
"ds_read_b64_tr_b16 a[0:1], v225 offset:0\n"
"v_mfma_f32_16x16x32_bf16 v[32:35], v[164:167], v[204:207], v[32:35]\n"
"buffer_load_dwordx4 v246, s[84:87], s52 offen lds\n"
"v_mfma_f32_16x16x32_bf16 v[36:39], v[164:167], v[208:211], v[36:39]\n"
"ds_read_b64_tr_b16 a[2:3], v225 offset:1024\n"
"v_mfma_f32_16x16x32_bf16 v[40:43], v[168:171], v[192:195], v[40:43]\n"
"s_add_u32 m0, s49, 41088\n"
"s_mov_b32 s52, 82688\n"
"v_mfma_f32_16x16x32_bf16 v[44:47], v[168:171], v[196:199], v[44:47]\n"
"ds_read_b64_tr_b16 a[4:5], v225 offset:8\n"
"v_mfma_f32_16x16x32_bf16 v[48:51], v[168:171], v[200:203], v[48:51]\n"
"ds_read_b64_tr_b16 a[6:7], v225 offset:1032\n"
"v_mfma_f32_16x16x32_bf16 v[52:55], v[168:171], v[204:207], v[52:55]\n"
"buffer_load_dwordx4 v246, s[84:87], s52 offen lds\n"
"v_mfma_f32_16x16x32_bf16 v[56:59], v[168:171], v[208:211], v[56:59]\n"
"ds_read_b64_tr_b16 a[8:9], v225 offset:256\n"
"v_mfma_f32_16x16x32_bf16 v[60:63], v[172:175], v[192:195], v[60:63]\n"
"ds_read_b64_tr_b16 a[10:11], v225 offset:1280\n"
"s_add_u32 m0, s49, 45184\n"
"v_mfma_f32_16x16x32_bf16 v[64:67], v[172:175], v[196:199], v[64:67]\n"
"s_mov_b32 s52, 123648\n"
"buffer_load_dwordx4 v246, s[84:87], s52 offen lds\n"
"v_mfma_f32_16x16x32_bf16 v[68:71], v[172:175], v[200:203], v[68:71]\n"
"ds_read_b64_tr_b16 a[12:13], v225 offset:264\n"
"v_mfma_f32_16x16x32_bf16 v[72:75], v[172:175], v[204:207], v[72:75]\n"
"ds_read_b64_tr_b16 a[14:15], v225 offset:1288\n"
"v_mfma_f32_16x16x32_bf16 v[76:79], v[172:175], v[208:211], v[76:79]\n"
"v_mfma_f32_16x16x32_bf16 v[80:83], v[176:179], v[192:195], v[80:83]\n"
"ds_read_b64_tr_b16 a[16:17], v225 offset:512\n"
"s_add_u32 m0, s49, 49280\n"
"v_mfma_f32_16x16x32_bf16 v[84:87], v[176:179], v[196:199], v[84:87]\n"
"s_mov_b32 s52, 164608\n"
"buffer_load_dwordx4 v246, s[84:87], s52 offen lds\n"
"v_mfma_f32_16x16x32_bf16 v[88:91], v[176:179], v[200:203], v[88:91]\n"
"ds_read_b64_tr_b16 a[18:19], v225 offset:1536\n"
"v_mfma_f32_16x16x32_bf16 v[92:95], v[176:179], v[204:207], v[92:95]\n"
"ds_read_b64_tr_b16 a[20:21], v225 offset:520\n"
"v_mfma_f32_16x16x32_bf16 v[96:99], v[176:179], v[208:211], v[96:99]\n"
"v_mfma_f32_16x16x32_bf16 v[100:103], v[180:183], v[192:195], v[100:103]\n"
"ds_read_b64_tr_b16 a[22:23], v225 offset:1544\n"
"s_add_u32 m0, s54, 0\n"
"v_mfma_f32_16x16x32_bf16 v[104:107], v[180:183], v[196:199], v[104:107]\n"
"ds_read_b64_tr_b16 a[24:25], v225 offset:768\n"
"s_mov_b32 s52, 3145728\n"
"v_mfma_f32_16x16x32_bf16 v[108:111], v[180:183], v[200:203], v[108:111]\n"
"buffer_load_dwordx4 v245, s[36:39], s52 offen lds\n"
"v_mfma_f32_16x16x32_bf16 v[112:115], v[180:183], v[204:207], v[112:115]\n"
"ds_read_b64_tr_b16 a[26:27], v225 offset:1792\n"
"v_mfma_f32_16x16x32_bf16 v[116:119], v[180:183], v[208:211], v[116:119]\n"
"ds_read_b64_tr_b16 a[28:29], v225 offset:776\n"
"v_mfma_f32_16x16x32_bf16 v[120:123], v[184:187], v[192:195], v[120:123]\n"
"s_add_u32 m0, s54, 1024\n"
"s_mov_b32 s52, 3178496\n"
"v_mfma_f32_16x16x32_bf16 v[124:127], v[184:187], v[196:199], v[124:127]\n"
"ds_read_b64_tr_b16 a[30:31], v225 offset:1800\n"
"v_mfma_f32_16x16x32_bf16 v[128:131], v[184:187], v[200:203], v[128:131]\n"
"buffer_load_dwordx4 v245, s[36:39], s52 offen lds\n"
"v_mfma_f32_16x16x32_bf16 v[132:135], v[184:187], v[204:207], v[132:135]\n"
"v_mfma_f32_16x16x32_bf16 v[136:139], v[184:187], v[208:211], v[136:139]\n"
"v_mfma_f32_16x16x32_bf16 v[140:143], v[188:191], v[192:195], v[140:143]\n"
"s_add_u32 m0, s54, 8224\n"
"s_mov_b32 s52, 3145984\n"
"v_mfma_f32_16x16x32_bf16 v[144:147], v[188:191], v[196:199], v[144:147]\n"
"buffer_load_dwordx4 v245, s[36:39], s52 offen lds\n"
"v_mfma_f32_16x16x32_bf16 v[148:151], v[188:191], v[200:203], v[148:151]\n"
"v_mfma_f32_16x16x32_bf16 v[152:155], v[188:191], v[204:207], v[152:155]\n"
"v_mfma_f32_16x16x32_bf16 v[156:159], v[188:191], v[208:211], v[156:159]\n"
"s_waitcnt lgkmcnt(0)\n"
"v_mfma_f32_16x16x32_bf16 v[0:3], a[0:3], a[32:35], v[0:3]\n"
"ds_read_b128 v[192:195], v231 offset:512\n"
"s_add_u32 m0, s54, 9248\n"
"v_mfma_f32_16x16x32_bf16 v[4:7], a[0:3], a[36:39], v[4:7]\n"
"ds_read_b128 v[196:199], v231 offset:2560\n"
"s_mov_b32 s52, 3178752\n"
"v_mfma_f32_16x16x32_bf16 v[8:11], a[0:3], a[40:43], v[8:11]\n"
"buffer_load_dwordx4 v245, s[36:39], s52 offen lds\n"
"v_mfma_f32_16x16x32_bf16 v[12:15], a[0:3], a[44:47], v[12:15]\n"
"ds_read_b128 v[200:203], v231 offset:4608\n"
"v_mfma_f32_16x16x32_bf16 v[16:19], a[0:3], a[48:51], v[16:19]\n"
"ds_read_b128 v[204:207], v231 offset:6656\n"
"v_mfma_f32_16x16x32_bf16 v[20:23], a[4:7], a[32:35], v[20:23]\n"
"s_add_u32 m0, s54, 16448\n"
"s_mov_b32 s52, 3407872\n"
"v_mfma_f32_16x16x32_bf16 v[24:27], a[4:7], a[36:39], v[24:27]\n"
"ds_read_b128 v[208:211], v231 offset:8704\n"
"v_mfma_f32_16x16x32_bf16 v[28:31], a[4:7], a[40:43], v[28:31]\n"
"ds_read_b64_tr_b16 v[160:161], v225 offset:16448\n"
"v_mfma_f32_16x16x32_bf16 v[32:35], a[4:7], a[44:47], v[32:35]\n"
"buffer_load_dwordx4 v245, s[36:39], s52 offen lds\n"
"v_mfma_f32_16x16x32_bf16 v[36:39], a[4:7], a[48:51], v[36:39]\n"
"ds_read_b64_tr_b16 v[162:163], v225 offset:17472\n"
"v_mfma_f32_16x16x32_bf16 v[40:43], a[8:11], a[32:35], v[40:43]\n"
"s_add_u32 m0, s54, 17472\n"
"s_mov_b32 s52, 3440640\n"
"v_mfma_f32_16x16x32_bf16 v[44:47], a[8:11], a[36:39], v[44:47]\n"
"ds_read_b64_tr_b16 v[164:165], v225 offset:16456\n"
"v_mfma_f32_16x16x32_bf16 v[48:51], a[8:11], a[40:43], v[48:51]\n"
"ds_read_b64_tr_b16 v[166:167], v225 offset:17480\n"
"v_mfma_f32_16x16x32_bf16 v[52:55], a[8:11], a[44:47], v[52:55]\n"
"buffer_load_dwordx4 v245, s[36:39], s52 offen lds\n"
"v_mfma_f32_16x16x32_bf16 v[56:59], a[8:11], a[48:51], v[56:59]\n"
"ds_read_b64_tr_b16 v[168:169], v225 offset:16704\n"
"v_mfma_f32_16x16x32_bf16 v[60:63], a[12:15], a[32:35], v[60:63]\n"
"ds_read_b64_tr_b16 v[170:171], v225 offset:17728\n"
"s_add_u32 m0, s54, 24672\n"
"v_mfma_f32_16x16x32_bf16 v[64:67], a[12:15], a[36:39], v[64:67]\n"
"s_mov_b32 s52, 3408128\n"
"buffer_load_dwordx4 v245, s[36:39], s52 offen lds\n"
"v_mfma_f32_16x16x32_bf16 v[68:71], a[12:15], a[40:43], v[68:71]\n"
"ds_read_b64_tr_b16 v[172:173], v225 offset:16712\n"
"v_mfma_f32_16x16x32_bf16 v[72:75], a[12:15], a[44:47], v[72:75]\n"
"ds_read_b64_tr_b16 v[174:175], v225 offset:17736\n"
"v_mfma_f32_16x16x32_bf16 v[76:79], a[12:15], a[48:51], v[76:79]\n"
"v_mfma_f32_16x16x32_bf16 v[80:83], a[16:19], a[32:35], v[80:83]\n"
"ds_read_b64_tr_b16 v[176:177], v225 offset:16960\n"
"s_add_u32 m0, s54, 25696\n"
"v_mfma_f32_16x16x32_bf16 v[84:87], a[16:19], a[36:39], v[84:87]\n"
"s_mov_b32 s52, 3440896\n"
"buffer_load_dwordx4 v245, s[36:39], s52 offen lds\n"
"v_mfma_f32_16x16x32_bf16 v[88:91], a[16:19], a[40:43], v[88:91]\n"
"ds_read_b64_tr_b16 v[178:179], v225 offset:17984\n"
"v_mfma_f32_16x16x32_bf16 v[92:95], a[16:19], a[44:47], v[92:95]\n"
"ds_read_b64_tr_b16 v[180:181], v225 offset:16968\n"
"v_mfma_f32_16x16x32_bf16 v[96:99], a[16:19], a[48:51], v[96:99]\n"
"v_mfma_f32_16x16x32_bf16 v[100:103], a[20:23], a[32:35], v[100:103]\n"
"ds_read_b64_tr_b16 v[182:183], v225 offset:17992\n"
"v_mfma_f32_16x16x32_bf16 v[104:107], a[20:23], a[36:39], v[104:107]\n"
"ds_read_b64_tr_b16 v[184:185], v225 offset:17216\n"
"s_add_u32 s52, s90, 640\n"
"v_mfma_f32_16x16x32_bf16 v[108:111], a[20:23], a[40:43], v[108:111]\n"
"s_mov_b32 exec_lo, 0\n"
"s_mov_b32 exec_hi, 261888\n"
"buffer_load_dword v254, v251, s[84:87], s52 offen\n"
"s_mov_b64 exec, -1\n"
"v_mfma_f32_16x16x32_bf16 v[112:115], a[20:23], a[44:47], v[112:115]\n"
"ds_read_b64_tr_b16 v[186:187], v225 offset:18240\n"
"s_add_u32 s52, s91, 2621440\n"
"v_mfma_f32_16x16x32_bf16 v[116:119], a[20:23], a[48:51], v[116:119]\n"
"ds_read_b64_tr_b16 v[188:189], v225 offset:17224\n"
"v_mfma_f32_16x16x32_bf16 v[120:123], a[24:27], a[32:35], v[120:123]\n"
"s_mov_b32 exec_lo, 0\n"
"s_mov_b32 exec_hi, 255\n"
"buffer_load_dword v254, v252, s[36:39], s52 offen\n"
"s_mov_b64 exec, -1\n"
"v_mfma_f32_16x16x32_bf16 v[124:127], a[24:27], a[36:39], v[124:127]\n"
"ds_read_b64_tr_b16 v[190:191], v225 offset:18248\n"
"s_add_u32 s52, s91, 0\n"
"v_mfma_f32_16x16x32_bf16 v[128:131], a[24:27], a[40:43], v[128:131]\n"
"s_mov_b32 exec_lo, 16711680\n"
"s_mov_b32 exec_hi, 0\n"
"buffer_load_dword v254, v252, s[96:99], s52 offen\n"
"s_mov_b64 exec, -1\n"
"v_mfma_f32_16x16x32_bf16 v[132:135], a[24:27], a[44:47], v[132:135]\n"
"s_add_u32 s52, s91, 0\n"
"v_mfma_f32_16x16x32_bf16 v[136:139], a[24:27], a[48:51], v[136:139]\n"
"s_mov_b32 exec_lo, 4278190080\n"
"s_mov_b32 exec_hi, 0\n"
"buffer_load_dword v254, v252, s[96:99], s52 offen\n"
"s_mov_b64 exec, -1\n"
"v_mfma_f32_16x16x32_bf16 v[140:143], a[28:31], a[32:35], v[140:143]\n"
"v_mfma_f32_16x16x32_bf16 v[144:147], a[28:31], a[36:39], v[144:147]\n"
"v_mfma_f32_16x16x32_bf16 v[148:151], a[28:31], a[40:43], v[148:151]\n"
"v_mfma_f32_16x16x32_bf16 v[152:155], a[28:31], a[44:47], v[152:155]\n"
"v_mfma_f32_16x16x32_bf16 v[156:159], a[28:31], a[48:51], v[156:159]\n"
"s_waitcnt vmcnt(21) lgkmcnt(0)\n"
"s_barrier\n"
"v_mfma_f32_16x16x32_bf16 v[0:3], v[160:163], v[192:195], v[0:3]\n"
"ds_read_b128 a[32:35], v232 offset:0\n"
"s_add_u32 m0, s49, 86272\n"
"v_mfma_f32_16x16x32_bf16 v[4:7], v[160:163], v[196:199], v[4:7]\n"
"ds_read_b128 a[36:39], v232 offset:2048\n"
"s_mov_b32 s52, 896\n"
"v_mfma_f32_16x16x32_bf16 v[8:11], v[160:163], v[200:203], v[8:11]\n"
"buffer_load_dwordx4 v246, s[84:87], s52 offen lds\n"
"v_mfma_f32_16x16x32_bf16 v[12:15], v[160:163], v[204:207], v[12:15]\n"
"ds_read_b128 a[40:43], v232 offset:4096\n"
"v_mfma_f32_16x16x32_bf16 v[16:19], v[160:163], v[208:211], v[16:19]\n"
"ds_read_b128 a[44:47], v232 offset:6144\n"
"v_mfma_f32_16x16x32_bf16 v[20:23], v[164:167], v[192:195], v[20:23]\n"
"s_add_u32 m0, s49, 90368\n"
"s_mov_b32 s52, 41856\n"
"v_mfma_f32_16x16x32_bf16 v[24:27], v[164:167], v[196:199], v[24:27]\n"
"ds_read_b128 a[48:51], v232 offset:8192\n"
"v_mfma_f32_16x16x32_bf16 v[28:31], v[164:167], v[200:203], v[28:31]\n"
"ds_read_b64_tr_b16 a[0:1], v226 offset:0\n"
"v_mfma_f32_16x16x32_bf16 v[32:35], v[164:167], v[204:207], v[32:35]\n"
"buffer_load_dwordx4 v246, s[84:87], s52 offen lds\n"
"v_mfma_f32_16x16x32_bf16 v[36:39], v[164:167], v[208:211], v[36:39]\n"
"ds_read_b64_tr_b16 a[2:3], v226 offset:1024\n"
"v_mfma_f32_16x16x32_bf16 v[40:43], v[168:171], v[192:195], v[40:43]\n"
"s_add_u32 m0, s49, 94464\n"
"s_mov_b32 s52, 82816\n"
"v_mfma_f32_16x16x32_bf16 v[44:47], v[168:171], v[196:199], v[44:47]\n"
"ds_read_b64_tr_b16 a[4:5], v226 offset:8\n"
"v_mfma_f32_16x16x32_bf16 v[48:51], v[168:171], v[200:203], v[48:51]\n"
"ds_read_b64_tr_b16 a[6:7], v226 offset:1032\n"
"v_mfma_f32_16x16x32_bf16 v[52:55], v[168:171], v[204:207], v[52:55]\n"
"buffer_load_dwordx4 v246, s[84:87], s52 offen lds\n"
"v_mfma_f32_16x16x32_bf16 v[56:59], v[168:171], v[208:211], v[56:59]\n"
"ds_read_b64_tr_b16 a[8:9], v226 offset:256\n"
"v_mfma_f32_16x16x32_bf16 v[60:63], v[172:175], v[192:195], v[60:63]\n"
"ds_read_b64_tr_b16 a[10:11], v226 offset:1280\n"
"s_add_u32 m0, s49, 98560\n"
"v_mfma_f32_16x16x32_bf16 v[64:67], v[172:175], v[196:199], v[64:67]\n"
"s_mov_b32 s52, 123776\n"
"buffer_load_dwordx4 v246, s[84:87], s52 offen lds\n"
"v_mfma_f32_16x16x32_bf16 v[68:71], v[172:175], v[200:203], v[68:71]\n"
"ds_read_b64_tr_b16 a[12:13], v226 offset:264\n"
"v_mfma_f32_16x16x32_bf16 v[72:75], v[172:175], v[204:207], v[72:75]\n"
"ds_read_b64_tr_b16 a[14:15], v226 offset:1288\n"
"v_mfma_f32_16x16x32_bf16 v[76:79], v[172:175], v[208:211], v[76:79]\n"
"v_mfma_f32_16x16x32_bf16 v[80:83], v[176:179], v[192:195], v[80:83]\n"
"ds_read_b64_tr_b16 a[16:17], v226 offset:512\n"
"s_add_u32 m0, s49, 102656\n"
"v_mfma_f32_16x16x32_bf16 v[84:87], v[176:179], v[196:199], v[84:87]\n"
"s_mov_b32 s52, 164736\n"
"buffer_load_dwordx4 v246, s[84:87], s52 offen lds\n"
"v_mfma_f32_16x16x32_bf16 v[88:91], v[176:179], v[200:203], v[88:91]\n"
"ds_read_b64_tr_b16 a[18:19], v226 offset:1536\n"
"v_mfma_f32_16x16x32_bf16 v[92:95], v[176:179], v[204:207], v[92:95]\n"
"ds_read_b64_tr_b16 a[20:21], v226 offset:520\n"
"v_mfma_f32_16x16x32_bf16 v[96:99], v[176:179], v[208:211], v[96:99]\n"
"v_mfma_f32_16x16x32_bf16 v[100:103], v[180:183], v[192:195], v[100:103]\n"
"ds_read_b64_tr_b16 a[22:23], v226 offset:1544\n"
"s_add_u32 m0, s54, 53376\n"
"v_mfma_f32_16x16x32_bf16 v[104:107], v[180:183], v[196:199], v[104:107]\n"
"ds_read_b64_tr_b16 a[24:25], v226 offset:768\n"
"s_mov_b32 s52, 3670016\n"
"v_mfma_f32_16x16x32_bf16 v[108:111], v[180:183], v[200:203], v[108:111]\n"
"buffer_load_dwordx4 v245, s[36:39], s52 offen lds\n"
"v_mfma_f32_16x16x32_bf16 v[112:115], v[180:183], v[204:207], v[112:115]\n"
"ds_read_b64_tr_b16 a[26:27], v226 offset:1792\n"
"v_mfma_f32_16x16x32_bf16 v[116:119], v[180:183], v[208:211], v[116:119]\n"
"ds_read_b64_tr_b16 a[28:29], v226 offset:776\n"
"v_mfma_f32_16x16x32_bf16 v[120:123], v[184:187], v[192:195], v[120:123]\n"
"s_add_u32 m0, s54, 54400\n"
"s_mov_b32 s52, 3702784\n"
"v_mfma_f32_16x16x32_bf16 v[124:127], v[184:187], v[196:199], v[124:127]\n"
"ds_read_b64_tr_b16 a[30:31], v226 offset:1800\n"
"v_mfma_f32_16x16x32_bf16 v[128:131], v[184:187], v[200:203], v[128:131]\n"
"buffer_load_dwordx4 v245, s[36:39], s52 offen lds\n"
"v_mfma_f32_16x16x32_bf16 v[132:135], v[184:187], v[204:207], v[132:135]\n"
"v_mfma_f32_16x16x32_bf16 v[136:139], v[184:187], v[208:211], v[136:139]\n"
"v_mfma_f32_16x16x32_bf16 v[140:143], v[188:191], v[192:195], v[140:143]\n"
"s_add_u32 m0, s54, 61600\n"
"s_mov_b32 s52, 3670272\n"
"v_mfma_f32_16x16x32_bf16 v[144:147], v[188:191], v[196:199], v[144:147]\n"
"buffer_load_dwordx4 v245, s[36:39], s52 offen lds\n"
"v_mfma_f32_16x16x32_bf16 v[148:151], v[188:191], v[200:203], v[148:151]\n"
"v_mfma_f32_16x16x32_bf16 v[152:155], v[188:191], v[204:207], v[152:155]\n"
"v_mfma_f32_16x16x32_bf16 v[156:159], v[188:191], v[208:211], v[156:159]\n"
"s_waitcnt lgkmcnt(0)\n"
"v_mfma_f32_16x16x32_bf16 v[0:3], a[0:3], a[32:35], v[0:3]\n"
"ds_read_b128 v[192:195], v232 offset:512\n"
"s_add_u32 m0, s54, 62624\n"
"v_mfma_f32_16x16x32_bf16 v[4:7], a[0:3], a[36:39], v[4:7]\n"
"ds_read_b128 v[196:199], v232 offset:2560\n"
"s_mov_b32 s52, 3703040\n"
"v_mfma_f32_16x16x32_bf16 v[8:11], a[0:3], a[40:43], v[8:11]\n"
"buffer_load_dwordx4 v245, s[36:39], s52 offen lds\n"
"v_mfma_f32_16x16x32_bf16 v[12:15], a[0:3], a[44:47], v[12:15]\n"
"ds_read_b128 v[200:203], v232 offset:4608\n"
"v_mfma_f32_16x16x32_bf16 v[16:19], a[0:3], a[48:51], v[16:19]\n"
"ds_read_b128 v[204:207], v232 offset:6656\n"
"v_mfma_f32_16x16x32_bf16 v[20:23], a[4:7], a[32:35], v[20:23]\n"
"s_add_u32 m0, s54, 69824\n"
"s_mov_b32 s52, 3932160\n"
"v_mfma_f32_16x16x32_bf16 v[24:27], a[4:7], a[36:39], v[24:27]\n"
"ds_read_b128 v[208:211], v232 offset:8704\n"
"v_mfma_f32_16x16x32_bf16 v[28:31], a[4:7], a[40:43], v[28:31]\n"
"ds_read_b64_tr_b16 v[160:161], v226 offset:16448\n"
"v_mfma_f32_16x16x32_bf16 v[32:35], a[4:7], a[44:47], v[32:35]\n"
"buffer_load_dwordx4 v245, s[36:39], s52 offen lds\n"
"v_mfma_f32_16x16x32_bf16 v[36:39], a[4:7], a[48:51], v[36:39]\n"
"ds_read_b64_tr_b16 v[162:163], v226 offset:17472\n"
"v_mfma_f32_16x16x32_bf16 v[40:43], a[8:11], a[32:35], v[40:43]\n"
"s_add_u32 m0, s54, 70848\n"
"s_mov_b32 s52, 3964928\n"
"v_mfma_f32_16x16x32_bf16 v[44:47], a[8:11], a[36:39], v[44:47]\n"
"ds_read_b64_tr_b16 v[164:165], v226 offset:16456\n"
"v_mfma_f32_16x16x32_bf16 v[48:51], a[8:11], a[40:43], v[48:51]\n"
"ds_read_b64_tr_b16 v[166:167], v226 offset:17480\n"
"v_mfma_f32_16x16x32_bf16 v[52:55], a[8:11], a[44:47], v[52:55]\n"
"buffer_load_dwordx4 v245, s[36:39], s52 offen lds\n"
"v_mfma_f32_16x16x32_bf16 v[56:59], a[8:11], a[48:51], v[56:59]\n"
"ds_read_b64_tr_b16 v[168:169], v226 offset:16704\n"
"v_mfma_f32_16x16x32_bf16 v[60:63], a[12:15], a[32:35], v[60:63]\n"
"ds_read_b64_tr_b16 v[170:171], v226 offset:17728\n"
"s_add_u32 m0, s54, 78048\n"
"v_mfma_f32_16x16x32_bf16 v[64:67], a[12:15], a[36:39], v[64:67]\n"
"s_mov_b32 s52, 3932416\n"
"buffer_load_dwordx4 v245, s[36:39], s52 offen lds\n"
"v_mfma_f32_16x16x32_bf16 v[68:71], a[12:15], a[40:43], v[68:71]\n"
"ds_read_b64_tr_b16 v[172:173], v226 offset:16712\n"
"v_mfma_f32_16x16x32_bf16 v[72:75], a[12:15], a[44:47], v[72:75]\n"
"ds_read_b64_tr_b16 v[174:175], v226 offset:17736\n"
"v_mfma_f32_16x16x32_bf16 v[76:79], a[12:15], a[48:51], v[76:79]\n"
"v_mfma_f32_16x16x32_bf16 v[80:83], a[16:19], a[32:35], v[80:83]\n"
"ds_read_b64_tr_b16 v[176:177], v226 offset:16960\n"
"s_add_u32 m0, s54, 79072\n"
"v_mfma_f32_16x16x32_bf16 v[84:87], a[16:19], a[36:39], v[84:87]\n"
"s_mov_b32 s52, 3965184\n"
"buffer_load_dwordx4 v245, s[36:39], s52 offen lds\n"
"v_mfma_f32_16x16x32_bf16 v[88:91], a[16:19], a[40:43], v[88:91]\n"
"ds_read_b64_tr_b16 v[178:179], v226 offset:17984\n"
"v_mfma_f32_16x16x32_bf16 v[92:95], a[16:19], a[44:47], v[92:95]\n"
"ds_read_b64_tr_b16 v[180:181], v226 offset:16968\n"
"v_mfma_f32_16x16x32_bf16 v[96:99], a[16:19], a[48:51], v[96:99]\n"
"v_mfma_f32_16x16x32_bf16 v[100:103], a[20:23], a[32:35], v[100:103]\n"
"ds_read_b64_tr_b16 v[182:183], v226 offset:17992\n"
"v_mfma_f32_16x16x32_bf16 v[104:107], a[20:23], a[36:39], v[104:107]\n"
"ds_read_b64_tr_b16 v[184:185], v226 offset:17216\n"
"s_add_u32 s52, s91, 0\n"
"v_mfma_f32_16x16x32_bf16 v[108:111], a[20:23], a[40:43], v[108:111]\n"
"s_mov_b32 exec_lo, 0\n"
"s_mov_b32 exec_hi, 255\n"
"buffer_load_dword v254, v252, s[96:99], s52 offen\n"
"s_mov_b64 exec, -1\n"
"v_mfma_f32_16x16x32_bf16 v[112:115], a[20:23], a[44:47], v[112:115]\n"
"ds_read_b64_tr_b16 v[186:187], v226 offset:18240\n"
"s_add_u32 s52, s91, 2621440\n"
"v_mfma_f32_16x16x32_bf16 v[116:119], a[20:23], a[48:51], v[116:119]\n"
"ds_read_b64_tr_b16 v[188:189], v226 offset:17224\n"
"v_mfma_f32_16x16x32_bf16 v[120:123], a[24:27], a[32:35], v[120:123]\n"
"s_mov_b32 exec_lo, 255\n"
"s_mov_b32 exec_hi, 0\n"
"buffer_load_dword v254, v252, s[96:99], s52 offen\n"
"s_mov_b64 exec, -1\n"
"v_mfma_f32_16x16x32_bf16 v[124:127], a[24:27], a[36:39], v[124:127]\n"
"ds_read_b64_tr_b16 v[190:191], v226 offset:18248\n"
"s_add_u32 s52, s92, 0\n"
"v_mfma_f32_16x16x32_bf16 v[128:131], a[24:27], a[40:43], v[128:131]\n"
"s_mov_b32 exec_lo, 4294967295\n"
"s_mov_b32 exec_hi, 255\n"
"buffer_load_dword v254, v253, s[76:79], s52 offen\n"
"s_mov_b64 exec, -1\n"
"v_mfma_f32_16x16x32_bf16 v[132:135], a[24:27], a[44:47], v[132:135]\n"
"v_mfma_f32_16x16x32_bf16 v[136:139], a[24:27], a[48:51], v[136:139]\n"
"v_mfma_f32_16x16x32_bf16 v[140:143], a[28:31], a[32:35], v[140:143]\n"
"v_mfma_f32_16x16x32_bf16 v[144:147], a[28:31], a[36:39], v[144:147]\n"
"v_mfma_f32_16x16x32_bf16 v[148:151], a[28:31], a[40:43], v[148:151]\n"
"v_mfma_f32_16x16x32_bf16 v[152:155], a[28:31], a[44:47], v[152:155]\n"
"v_mfma_f32_16x16x32_bf16 v[156:159], a[28:31], a[48:51], v[156:159]\n"
"s_waitcnt vmcnt(20) lgkmcnt(0)\n"
"s_barrier\n"
"v_mfma_f32_16x16x32_bf16 v[0:3], v[160:163], v[192:195], v[0:3]\n"
"ds_read_b128 a[32:35], v230 offset:0\n"
"s_add_u32 m0, s49, 139648\n"
"v_mfma_f32_16x16x32_bf16 v[4:7], v[160:163], v[196:199], v[4:7]\n"
"ds_read_b128 a[36:39], v230 offset:2048\n"
"s_mov_b32 s52, 1024\n"
"v_mfma_f32_16x16x32_bf16 v[8:11], v[160:163], v[200:203], v[8:11]\n"
"buffer_load_dwordx4 v246, s[84:87], s52 offen lds\n"
"v_mfma_f32_16x16x32_bf16 v[12:15], v[160:163], v[204:207], v[12:15]\n"
"ds_read_b128 a[40:43], v230 offset:4096\n"
"v_mfma_f32_16x16x32_bf16 v[16:19], v[160:163], v[208:211], v[16:19]\n"
"ds_read_b128 a[44:47], v230 offset:6144\n"
"v_mfma_f32_16x16x32_bf16 v[20:23], v[164:167], v[192:195], v[20:23]\n"
"s_add_u32 m0, s49, 143744\n"
"s_mov_b32 s52, 41984\n"
"v_mfma_f32_16x16x32_bf16 v[24:27], v[164:167], v[196:199], v[24:27]\n"
"ds_read_b128 a[48:51], v230 offset:8192\n"
"v_mfma_f32_16x16x32_bf16 v[28:31], v[164:167], v[200:203], v[28:31]\n"
"ds_read_b64_tr_b16 a[0:1], v224 offset:0\n"
"v_mfma_f32_16x16x32_bf16 v[32:35], v[164:167], v[204:207], v[32:35]\n"
"buffer_load_dwordx4 v246, s[84:87], s52 offen lds\n"
"v_mfma_f32_16x16x32_bf16 v[36:39], v[164:167], v[208:211], v[36:39]\n"
"ds_read_b64_tr_b16 a[2:3], v224 offset:1024\n"
"v_mfma_f32_16x16x32_bf16 v[40:43], v[168:171], v[192:195], v[40:43]\n"
"s_add_u32 m0, s49, 147840\n"
"s_mov_b32 s52, 82944\n"
"v_mfma_f32_16x16x32_bf16 v[44:47], v[168:171], v[196:199], v[44:47]\n"
"ds_read_b64_tr_b16 a[4:5], v224 offset:8\n"
"v_mfma_f32_16x16x32_bf16 v[48:51], v[168:171], v[200:203], v[48:51]\n"
"ds_read_b64_tr_b16 a[6:7], v224 offset:1032\n"
"v_mfma_f32_16x16x32_bf16 v[52:55], v[168:171], v[204:207], v[52:55]\n"
"buffer_load_dwordx4 v246, s[84:87], s52 offen lds\n"
"v_mfma_f32_16x16x32_bf16 v[56:59], v[168:171], v[208:211], v[56:59]\n"
"ds_read_b64_tr_b16 a[8:9], v224 offset:256\n"
"v_mfma_f32_16x16x32_bf16 v[60:63], v[172:175], v[192:195], v[60:63]\n"
"ds_read_b64_tr_b16 a[10:11], v224 offset:1280\n"
"s_add_u32 m0, s49, 151936\n"
"v_mfma_f32_16x16x32_bf16 v[64:67], v[172:175], v[196:199], v[64:67]\n"
"s_mov_b32 s52, 123904\n"
"buffer_load_dwordx4 v246, s[84:87], s52 offen lds\n"
"v_mfma_f32_16x16x32_bf16 v[68:71], v[172:175], v[200:203], v[68:71]\n"
"ds_read_b64_tr_b16 a[12:13], v224 offset:264\n"
"v_mfma_f32_16x16x32_bf16 v[72:75], v[172:175], v[204:207], v[72:75]\n"
"ds_read_b64_tr_b16 a[14:15], v224 offset:1288\n"
"v_mfma_f32_16x16x32_bf16 v[76:79], v[172:175], v[208:211], v[76:79]\n"
"v_mfma_f32_16x16x32_bf16 v[80:83], v[176:179], v[192:195], v[80:83]\n"
"ds_read_b64_tr_b16 a[16:17], v224 offset:512\n"
"s_add_u32 m0, s49, 156032\n"
"v_mfma_f32_16x16x32_bf16 v[84:87], v[176:179], v[196:199], v[84:87]\n"
"s_mov_b32 s52, 164864\n"
"buffer_load_dwordx4 v246, s[84:87], s52 offen lds\n"
"v_mfma_f32_16x16x32_bf16 v[88:91], v[176:179], v[200:203], v[88:91]\n"
"ds_read_b64_tr_b16 a[18:19], v224 offset:1536\n"
"v_mfma_f32_16x16x32_bf16 v[92:95], v[176:179], v[204:207], v[92:95]\n"
"ds_read_b64_tr_b16 a[20:21], v224 offset:520\n"
"v_mfma_f32_16x16x32_bf16 v[96:99], v[176:179], v[208:211], v[96:99]\n"
"v_mfma_f32_16x16x32_bf16 v[100:103], v[180:183], v[192:195], v[100:103]\n"
"ds_read_b64_tr_b16 a[22:23], v224 offset:1544\n"
"s_add_u32 m0, s54, 106752\n"
"v_mfma_f32_16x16x32_bf16 v[104:107], v[180:183], v[196:199], v[104:107]\n"
"ds_read_b64_tr_b16 a[24:25], v224 offset:768\n"
"s_mov_b32 s52, 4194304\n"
"v_mfma_f32_16x16x32_bf16 v[108:111], v[180:183], v[200:203], v[108:111]\n"
"buffer_load_dwordx4 v245, s[36:39], s52 offen lds\n"
"v_mfma_f32_16x16x32_bf16 v[112:115], v[180:183], v[204:207], v[112:115]\n"
"ds_read_b64_tr_b16 a[26:27], v224 offset:1792\n"
"v_mfma_f32_16x16x32_bf16 v[116:119], v[180:183], v[208:211], v[116:119]\n"
"ds_read_b64_tr_b16 a[28:29], v224 offset:776\n"
"v_mfma_f32_16x16x32_bf16 v[120:123], v[184:187], v[192:195], v[120:123]\n"
"s_add_u32 m0, s54, 107776\n"
"s_mov_b32 s52, 4227072\n"
"v_mfma_f32_16x16x32_bf16 v[124:127], v[184:187], v[196:199], v[124:127]\n"
"ds_read_b64_tr_b16 a[30:31], v224 offset:1800\n"
"v_mfma_f32_16x16x32_bf16 v[128:131], v[184:187], v[200:203], v[128:131]\n"
"buffer_load_dwordx4 v245, s[36:39], s52 offen lds\n"
"v_mfma_f32_16x16x32_bf16 v[132:135], v[184:187], v[204:207], v[132:135]\n"
"v_mfma_f32_16x16x32_bf16 v[136:139], v[184:187], v[208:211], v[136:139]\n"
"v_mfma_f32_16x16x32_bf16 v[140:143], v[188:191], v[192:195], v[140:143]\n"
"s_add_u32 m0, s54, 114976\n"
"s_mov_b32 s52, 4194560\n"
"v_mfma_f32_16x16x32_bf16 v[144:147], v[188:191], v[196:199], v[144:147]\n"
"buffer_load_dwordx4 v245, s[36:39], s52 offen lds\n"
"v_mfma_f32_16x16x32_bf16 v[148:151], v[188:191], v[200:203], v[148:151]\n"
"v_mfma_f32_16x16x32_bf16 v[152:155], v[188:191], v[204:207], v[152:155]\n"
"v_mfma_f32_16x16x32_bf16 v[156:159], v[188:191], v[208:211], v[156:159]\n"
"s_waitcnt lgkmcnt(0)\n"
"v_mfma_f32_16x16x32_bf16 v[0:3], a[0:3], a[32:35], v[0:3]\n"
"ds_read_b128 v[192:195], v230 offset:512\n"
"s_add_u32 m0, s54, 116000\n"
"v_mfma_f32_16x16x32_bf16 v[4:7], a[0:3], a[36:39], v[4:7]\n"
"ds_read_b128 v[196:199], v230 offset:2560\n"
"s_mov_b32 s52, 4227328\n"
"v_mfma_f32_16x16x32_bf16 v[8:11], a[0:3], a[40:43], v[8:11]\n"
"buffer_load_dwordx4 v245, s[36:39], s52 offen lds\n"
"v_mfma_f32_16x16x32_bf16 v[12:15], a[0:3], a[44:47], v[12:15]\n"
"ds_read_b128 v[200:203], v230 offset:4608\n"
"v_mfma_f32_16x16x32_bf16 v[16:19], a[0:3], a[48:51], v[16:19]\n"
"ds_read_b128 v[204:207], v230 offset:6656\n"
"v_mfma_f32_16x16x32_bf16 v[20:23], a[4:7], a[32:35], v[20:23]\n"
"s_add_u32 m0, s54, 123200\n"
"s_mov_b32 s52, 4456448\n"
"v_mfma_f32_16x16x32_bf16 v[24:27], a[4:7], a[36:39], v[24:27]\n"
"ds_read_b128 v[208:211], v230 offset:8704\n"
"v_mfma_f32_16x16x32_bf16 v[28:31], a[4:7], a[40:43], v[28:31]\n"
"ds_read_b64_tr_b16 v[160:161], v224 offset:16448\n"
"v_mfma_f32_16x16x32_bf16 v[32:35], a[4:7], a[44:47], v[32:35]\n"
"buffer_load_dwordx4 v245, s[36:39], s52 offen lds\n"
"v_mfma_f32_16x16x32_bf16 v[36:39], a[4:7], a[48:51], v[36:39]\n"
"ds_read_b64_tr_b16 v[162:163], v224 offset:17472\n"
"v_mfma_f32_16x16x32_bf16 v[40:43], a[8:11], a[32:35], v[40:43]\n"
"s_add_u32 m0, s54, 124224\n"
"s_mov_b32 s52, 4489216\n"
"v_mfma_f32_16x16x32_bf16 v[44:47], a[8:11], a[36:39], v[44:47]\n"
"ds_read_b64_tr_b16 v[164:165], v224 offset:16456\n"
"v_mfma_f32_16x16x32_bf16 v[48:51], a[8:11], a[40:43], v[48:51]\n"
"ds_read_b64_tr_b16 v[166:167], v224 offset:17480\n"
"v_mfma_f32_16x16x32_bf16 v[52:55], a[8:11], a[44:47], v[52:55]\n"
"buffer_load_dwordx4 v245, s[36:39], s52 offen lds\n"
"v_mfma_f32_16x16x32_bf16 v[56:59], a[8:11], a[48:51], v[56:59]\n"
"ds_read_b64_tr_b16 v[168:169], v224 offset:16704\n"
"v_mfma_f32_16x16x32_bf16 v[60:63], a[12:15], a[32:35], v[60:63]\n"
"ds_read_b64_tr_b16 v[170:171], v224 offset:17728\n"
"s_add_u32 m0, s54, 131424\n"
"v_mfma_f32_16x16x32_bf16 v[64:67], a[12:15], a[36:39], v[64:67]\n"
"s_mov_b32 s52, 4456704\n"
"buffer_load_dwordx4 v245, s[36:39], s52 offen lds\n"
"v_mfma_f32_16x16x32_bf16 v[68:71], a[12:15], a[40:43], v[68:71]\n"
"ds_read_b64_tr_b16 v[172:173], v224 offset:16712\n"
"v_mfma_f32_16x16x32_bf16 v[72:75], a[12:15], a[44:47], v[72:75]\n"
"ds_read_b64_tr_b16 v[174:175], v224 offset:17736\n"
"v_mfma_f32_16x16x32_bf16 v[76:79], a[12:15], a[48:51], v[76:79]\n"
"v_mfma_f32_16x16x32_bf16 v[80:83], a[16:19], a[32:35], v[80:83]\n"
"ds_read_b64_tr_b16 v[176:177], v224 offset:16960\n"
"s_add_u32 m0, s54, 132448\n"
"v_mfma_f32_16x16x32_bf16 v[84:87], a[16:19], a[36:39], v[84:87]\n"
"s_mov_b32 s52, 4489472\n"
"buffer_load_dwordx4 v245, s[36:39], s52 offen lds\n"
"v_mfma_f32_16x16x32_bf16 v[88:91], a[16:19], a[40:43], v[88:91]\n"
"ds_read_b64_tr_b16 v[178:179], v224 offset:17984\n"
"v_mfma_f32_16x16x32_bf16 v[92:95], a[16:19], a[44:47], v[92:95]\n"
"ds_read_b64_tr_b16 v[180:181], v224 offset:16968\n"
"v_mfma_f32_16x16x32_bf16 v[96:99], a[16:19], a[48:51], v[96:99]\n"
"v_mfma_f32_16x16x32_bf16 v[100:103], a[20:23], a[32:35], v[100:103]\n"
"ds_read_b64_tr_b16 v[182:183], v224 offset:17992\n"
"v_mfma_f32_16x16x32_bf16 v[104:107], a[20:23], a[36:39], v[104:107]\n"
"ds_read_b64_tr_b16 v[184:185], v224 offset:17216\n"
"s_add_u32 s52, s91, 2621440\n"
"v_mfma_f32_16x16x32_bf16 v[108:111], a[20:23], a[40:43], v[108:111]\n"
"s_mov_b32 exec_lo, 65280\n"
"s_mov_b32 exec_hi, 0\n"
"buffer_load_dword v254, v252, s[96:99], s52 offen\n"
"s_mov_b64 exec, -1\n"
"v_mfma_f32_16x16x32_bf16 v[112:115], a[20:23], a[44:47], v[112:115]\n"
"ds_read_b64_tr_b16 v[186:187], v224 offset:18240\n"
"s_add_u32 s52, s91, 2621440\n"
"v_mfma_f32_16x16x32_bf16 v[116:119], a[20:23], a[48:51], v[116:119]\n"
"ds_read_b64_tr_b16 v[188:189], v224 offset:17224\n"
"v_mfma_f32_16x16x32_bf16 v[120:123], a[24:27], a[32:35], v[120:123]\n"
"s_mov_b32 exec_lo, 16711680\n"
"s_mov_b32 exec_hi, 0\n"
"buffer_load_dword v254, v252, s[96:99], s52 offen\n"
"s_mov_b64 exec, -1\n"
"v_mfma_f32_16x16x32_bf16 v[124:127], a[24:27], a[36:39], v[124:127]\n"
"ds_read_b64_tr_b16 v[190:191], v224 offset:18248\n"
"s_add_u32 s52, s92, 2621440\n"
"v_mfma_f32_16x16x32_bf16 v[128:131], a[24:27], a[40:43], v[128:131]\n"
"s_mov_b32 exec_lo, 4294967295\n"
"s_mov_b32 exec_hi, 255\n"
"buffer_load_dword v254, v253, s[76:79], s52 offen\n"
"s_mov_b64 exec, -1\n"
"v_mfma_f32_16x16x32_bf16 v[132:135], a[24:27], a[44:47], v[132:135]\n"
"s_mov_b32 s78, 0\n"
"v_mfma_f32_16x16x32_bf16 v[136:139], a[24:27], a[48:51], v[136:139]\n"
"v_mfma_f32_16x16x32_bf16 v[140:143], a[28:31], a[32:35], v[140:143]\n"
"v_mfma_f32_16x16x32_bf16 v[144:147], a[28:31], a[36:39], v[144:147]\n"
"v_mfma_f32_16x16x32_bf16 v[148:151], a[28:31], a[40:43], v[148:151]\n"
"v_mfma_f32_16x16x32_bf16 v[152:155], a[28:31], a[44:47], v[152:155]\n"
"v_mfma_f32_16x16x32_bf16 v[156:159], a[28:31], a[48:51], v[156:159]\n"
"s_waitcnt vmcnt(19) lgkmcnt(0)\n"
"s_barrier\n"
"v_mfma_f32_16x16x32_bf16 v[0:3], v[160:163], v[192:195], v[0:3]\n"
"ds_read_b128 a[32:35], v231 offset:0\n"
"s_add_u32 m0, s49, 32896\n"
"v_mfma_f32_16x16x32_bf16 v[4:7], v[160:163], v[196:199], v[4:7]\n"
"ds_read_b128 a[36:39], v231 offset:2048\n"
"s_mov_b32 s52, 1152\n"
"v_mfma_f32_16x16x32_bf16 v[8:11], v[160:163], v[200:203], v[8:11]\n"
"buffer_load_dwordx4 v246, s[84:87], s52 offen lds\n"
"v_mfma_f32_16x16x32_bf16 v[12:15], v[160:163], v[204:207], v[12:15]\n"
"ds_read_b128 a[40:43], v231 offset:4096\n"
"v_mfma_f32_16x16x32_bf16 v[16:19], v[160:163], v[208:211], v[16:19]\n"
"ds_read_b128 a[44:47], v231 offset:6144\n"
"v_mfma_f32_16x16x32_bf16 v[20:23], v[164:167], v[192:195], v[20:23]\n"
"s_add_u32 m0, s49, 36992\n"
"s_mov_b32 s52, 42112\n"
"v_mfma_f32_16x16x32_bf16 v[24:27], v[164:167], v[196:199], v[24:27]\n"
"ds_read_b128 a[48:51], v231 offset:8192\n"
"v_mfma_f32_16x16x32_bf16 v[28:31], v[164:167], v[200:203], v[28:31]\n"
"ds_read_b64_tr_b16 a[0:1], v225 offset:0\n"
"v_mfma_f32_16x16x32_bf16 v[32:35], v[164:167], v[204:207], v[32:35]\n"
"buffer_load_dwordx4 v246, s[84:87], s52 offen lds\n"
"v_mfma_f32_16x16x32_bf16 v[36:39], v[164:167], v[208:211], v[36:39]\n"
"ds_read_b64_tr_b16 a[2:3], v225 offset:1024\n"
"v_mfma_f32_16x16x32_bf16 v[40:43], v[168:171], v[192:195], v[40:43]\n"
"s_add_u32 m0, s49, 41088\n"
"s_mov_b32 s52, 83072\n"
"v_mfma_f32_16x16x32_bf16 v[44:47], v[168:171], v[196:199], v[44:47]\n"
"ds_read_b64_tr_b16 a[4:5], v225 offset:8\n"
"v_mfma_f32_16x16x32_bf16 v[48:51], v[168:171], v[200:203], v[48:51]\n"
"ds_read_b64_tr_b16 a[6:7], v225 offset:1032\n"
"v_mfma_f32_16x16x32_bf16 v[52:55], v[168:171], v[204:207], v[52:55]\n"
"buffer_load_dwordx4 v246, s[84:87], s52 offen lds\n"
"v_mfma_f32_16x16x32_bf16 v[56:59], v[168:171], v[208:211], v[56:59]\n"
"ds_read_b64_tr_b16 a[8:9], v225 offset:256\n"
"v_mfma_f32_16x16x32_bf16 v[60:63], v[172:175], v[192:195], v[60:63]\n"
"ds_read_b64_tr_b16 a[10:11], v225 offset:1280\n"
"s_add_u32 m0, s49, 45184\n"
"v_mfma_f32_16x16x32_bf16 v[64:67], v[172:175], v[196:199], v[64:67]\n"
"s_mov_b32 s52, 124032\n"
"buffer_load_dwordx4 v246, s[84:87], s52 offen lds\n"
"v_mfma_f32_16x16x32_bf16 v[68:71], v[172:175], v[200:203], v[68:71]\n"
"ds_read_b64_tr_b16 a[12:13], v225 offset:264\n"
"v_mfma_f32_16x16x32_bf16 v[72:75], v[172:175], v[204:207], v[72:75]\n"
"ds_read_b64_tr_b16 a[14:15], v225 offset:1288\n"
"v_mfma_f32_16x16x32_bf16 v[76:79], v[172:175], v[208:211], v[76:79]\n"
"v_mfma_f32_16x16x32_bf16 v[80:83], v[176:179], v[192:195], v[80:83]\n"
"ds_read_b64_tr_b16 a[16:17], v225 offset:512\n"
"s_add_u32 m0, s49, 49280\n"
"v_mfma_f32_16x16x32_bf16 v[84:87], v[176:179], v[196:199], v[84:87]\n"
"s_mov_b32 s52, 164992\n"
"buffer_load_dwordx4 v246, s[84:87], s52 offen lds\n"
"v_mfma_f32_16x16x32_bf16 v[88:91], v[176:179], v[200:203], v[88:91]\n"
"ds_read_b64_tr_b16 a[18:19], v225 offset:1536\n"
"v_mfma_f32_16x16x32_bf16 v[92:95], v[176:179], v[204:207], v[92:95]\n"
"ds_read_b64_tr_b16 a[20:21], v225 offset:520\n"
"v_mfma_f32_16x16x32_bf16 v[96:99], v[176:179], v[208:211], v[96:99]\n"
"v_mfma_f32_16x16x32_bf16 v[100:103], v[180:183], v[192:195], v[100:103]\n"
"ds_read_b64_tr_b16 a[22:23], v225 offset:1544\n"
"s_add_u32 m0, s54, 0\n"
"v_mfma_f32_16x16x32_bf16 v[104:107], v[180:183], v[196:199], v[104:107]\n"
"ds_read_b64_tr_b16 a[24:25], v225 offset:768\n"
"s_mov_b32 s52, 4718592\n"
"v_mfma_f32_16x16x32_bf16 v[108:111], v[180:183], v[200:203], v[108:111]\n"
"buffer_load_dwordx4 v245, s[36:39], s52 offen lds\n"
"v_mfma_f32_16x16x32_bf16 v[112:115], v[180:183], v[204:207], v[112:115]\n"
"ds_read_b64_tr_b16 a[26:27], v225 offset:1792\n"
"v_mfma_f32_16x16x32_bf16 v[116:119], v[180:183], v[208:211], v[116:119]\n"
"ds_read_b64_tr_b16 a[28:29], v225 offset:776\n"
"v_mfma_f32_16x16x32_bf16 v[120:123], v[184:187], v[192:195], v[120:123]\n"
"s_add_u32 m0, s54, 1024\n"
"s_mov_b32 s52, 4751360\n"
"v_mfma_f32_16x16x32_bf16 v[124:127], v[184:187], v[196:199], v[124:127]\n"
"ds_read_b64_tr_b16 a[30:31], v225 offset:1800\n"
"v_mfma_f32_16x16x32_bf16 v[128:131], v[184:187], v[200:203], v[128:131]\n"
"buffer_load_dwordx4 v245, s[36:39], s52 offen lds\n"
"v_mfma_f32_16x16x32_bf16 v[132:135], v[184:187], v[204:207], v[132:135]\n"
"v_mfma_f32_16x16x32_bf16 v[136:139], v[184:187], v[208:211], v[136:139]\n"
"v_mfma_f32_16x16x32_bf16 v[140:143], v[188:191], v[192:195], v[140:143]\n"
"s_add_u32 m0, s54, 8224\n"
"s_mov_b32 s52, 4718848\n"
"v_mfma_f32_16x16x32_bf16 v[144:147], v[188:191], v[196:199], v[144:147]\n"
"buffer_load_dwordx4 v245, s[36:39], s52 offen lds\n"
"v_mfma_f32_16x16x32_bf16 v[148:151], v[188:191], v[200:203], v[148:151]\n"
"v_mfma_f32_16x16x32_bf16 v[152:155], v[188:191], v[204:207], v[152:155]\n"
"v_mfma_f32_16x16x32_bf16 v[156:159], v[188:191], v[208:211], v[156:159]\n"
"s_waitcnt lgkmcnt(0)\n"
"v_mfma_f32_16x16x32_bf16 v[0:3], a[0:3], a[32:35], v[0:3]\n"
"ds_read_b128 v[192:195], v231 offset:512\n"
"s_add_u32 m0, s54, 9248\n"
"v_mfma_f32_16x16x32_bf16 v[4:7], a[0:3], a[36:39], v[4:7]\n"
"ds_read_b128 v[196:199], v231 offset:2560\n"
"s_mov_b32 s52, 4751616\n"
"v_mfma_f32_16x16x32_bf16 v[8:11], a[0:3], a[40:43], v[8:11]\n"
"buffer_load_dwordx4 v245, s[36:39], s52 offen lds\n"
"v_mfma_f32_16x16x32_bf16 v[12:15], a[0:3], a[44:47], v[12:15]\n"
"ds_read_b128 v[200:203], v231 offset:4608\n"
"v_mfma_f32_16x16x32_bf16 v[16:19], a[0:3], a[48:51], v[16:19]\n"
"ds_read_b128 v[204:207], v231 offset:6656\n"
"v_mfma_f32_16x16x32_bf16 v[20:23], a[4:7], a[32:35], v[20:23]\n"
"s_add_u32 m0, s54, 16448\n"
"s_mov_b32 s52, 4980736\n"
"v_mfma_f32_16x16x32_bf16 v[24:27], a[4:7], a[36:39], v[24:27]\n"
"ds_read_b128 v[208:211], v231 offset:8704\n"
"v_mfma_f32_16x16x32_bf16 v[28:31], a[4:7], a[40:43], v[28:31]\n"
"ds_read_b64_tr_b16 v[160:161], v225 offset:16448\n"
"v_mfma_f32_16x16x32_bf16 v[32:35], a[4:7], a[44:47], v[32:35]\n"
"buffer_load_dwordx4 v245, s[36:39], s52 offen lds\n"
"v_mfma_f32_16x16x32_bf16 v[36:39], a[4:7], a[48:51], v[36:39]\n"
"ds_read_b64_tr_b16 v[162:163], v225 offset:17472\n"
"v_mfma_f32_16x16x32_bf16 v[40:43], a[8:11], a[32:35], v[40:43]\n"
"s_add_u32 m0, s54, 17472\n"
"s_mov_b32 s52, 5013504\n"
"v_mfma_f32_16x16x32_bf16 v[44:47], a[8:11], a[36:39], v[44:47]\n"
"ds_read_b64_tr_b16 v[164:165], v225 offset:16456\n"
"v_mfma_f32_16x16x32_bf16 v[48:51], a[8:11], a[40:43], v[48:51]\n"
"ds_read_b64_tr_b16 v[166:167], v225 offset:17480\n"
"v_mfma_f32_16x16x32_bf16 v[52:55], a[8:11], a[44:47], v[52:55]\n"
"buffer_load_dwordx4 v245, s[36:39], s52 offen lds\n"
"v_mfma_f32_16x16x32_bf16 v[56:59], a[8:11], a[48:51], v[56:59]\n"
"ds_read_b64_tr_b16 v[168:169], v225 offset:16704\n"
"v_mfma_f32_16x16x32_bf16 v[60:63], a[12:15], a[32:35], v[60:63]\n"
"ds_read_b64_tr_b16 v[170:171], v225 offset:17728\n"
"s_add_u32 m0, s54, 24672\n"
"v_mfma_f32_16x16x32_bf16 v[64:67], a[12:15], a[36:39], v[64:67]\n"
"s_mov_b32 s52, 4980992\n"
"buffer_load_dwordx4 v245, s[36:39], s52 offen lds\n"
"v_mfma_f32_16x16x32_bf16 v[68:71], a[12:15], a[40:43], v[68:71]\n"
"ds_read_b64_tr_b16 v[172:173], v225 offset:16712\n"
"v_mfma_f32_16x16x32_bf16 v[72:75], a[12:15], a[44:47], v[72:75]\n"
"ds_read_b64_tr_b16 v[174:175], v225 offset:17736\n"
"v_mfma_f32_16x16x32_bf16 v[76:79], a[12:15], a[48:51], v[76:79]\n"
"v_mfma_f32_16x16x32_bf16 v[80:83], a[16:19], a[32:35], v[80:83]\n"
"ds_read_b64_tr_b16 v[176:177], v225 offset:16960\n"
"s_add_u32 m0, s54, 25696\n"
"v_mfma_f32_16x16x32_bf16 v[84:87], a[16:19], a[36:39], v[84:87]\n"
"s_mov_b32 s52, 5013760\n"
"buffer_load_dwordx4 v245, s[36:39], s52 offen lds\n"
"v_mfma_f32_16x16x32_bf16 v[88:91], a[16:19], a[40:43], v[88:91]\n"
"ds_read_b64_tr_b16 v[178:179], v225 offset:17984\n"
"v_mfma_f32_16x16x32_bf16 v[92:95], a[16:19], a[44:47], v[92:95]\n"
"ds_read_b64_tr_b16 v[180:181], v225 offset:16968\n"
"v_mfma_f32_16x16x32_bf16 v[96:99], a[16:19], a[48:51], v[96:99]\n"
"v_mfma_f32_16x16x32_bf16 v[100:103], a[20:23], a[32:35], v[100:103]\n"
"ds_read_b64_tr_b16 v[182:183], v225 offset:17992\n"
"v_mfma_f32_16x16x32_bf16 v[104:107], a[20:23], a[36:39], v[104:107]\n"
"ds_read_b64_tr_b16 v[184:185], v225 offset:17216\n"
"s_add_u32 s52, s91, 2621440\n"
"v_mfma_f32_16x16x32_bf16 v[108:111], a[20:23], a[40:43], v[108:111]\n"
"s_mov_b32 exec_lo, 4278190080\n"
"s_mov_b32 exec_hi, 0\n"
"buffer_load_dword v254, v252, s[96:99], s52 offen\n"
"s_mov_b64 exec, -1\n"
"v_mfma_f32_16x16x32_bf16 v[112:115], a[20:23], a[44:47], v[112:115]\n"
"ds_read_b64_tr_b16 v[186:187], v225 offset:18240\n"
"s_add_u32 s52, s91, 2621440\n"
"v_mfma_f32_16x16x32_bf16 v[116:119], a[20:23], a[48:51], v[116:119]\n"
"ds_read_b64_tr_b16 v[188:189], v225 offset:17224\n"
"v_mfma_f32_16x16x32_bf16 v[120:123], a[24:27], a[32:35], v[120:123]\n"
"s_mov_b32 exec_lo, 0\n"
"s_mov_b32 exec_hi, 255\n"
"buffer_load_dword v254, v252, s[96:99], s52 offen\n"
"s_mov_b64 exec, -1\n"
"v_mfma_f32_16x16x32_bf16 v[124:127], a[24:27], a[36:39], v[124:127]\n"
"ds_read_b64_tr_b16 v[190:191], v225 offset:18248\n"
"v_mfma_f32_16x16x32_bf16 v[128:131], a[24:27], a[40:43], v[128:131]\n"
"v_mfma_f32_16x16x32_bf16 v[132:135], a[24:27], a[44:47], v[132:135]\n"
"v_mfma_f32_16x16x32_bf16 v[136:139], a[24:27], a[48:51], v[136:139]\n"
"v_mfma_f32_16x16x32_bf16 v[140:143], a[28:31], a[32:35], v[140:143]\n"
"v_mfma_f32_16x16x32_bf16 v[144:147], a[28:31], a[36:39], v[144:147]\n"
"v_mfma_f32_16x16x32_bf16 v[148:151], a[28:31], a[40:43], v[148:151]\n"
"v_mfma_f32_16x16x32_bf16 v[152:155], a[28:31], a[44:47], v[152:155]\n"
"v_mfma_f32_16x16x32_bf16 v[156:159], a[28:31], a[48:51], v[156:159]\n"
"s_cmp_eq_u32 s51, 2\n"
"s_cbranch_scc1 L_qpbq_%=\n"
"s_waitcnt vmcnt(18) lgkmcnt(0)\n"
"s_barrier\n"
"v_mfma_f32_16x16x32_bf16 v[0:3], v[160:163], v[192:195], v[0:3]\n"
"ds_read_b128 a[32:35], v232 offset:0\n"
"s_add_u32 m0, s48, 53376\n"
"v_mfma_f32_16x16x32_bf16 v[4:7], v[160:163], v[196:199], v[4:7]\n"
"ds_read_b128 a[36:39], v232 offset:2048\n"
"s_add_u32 s52, s81, 131072\n"
"v_mfma_f32_16x16x32_bf16 v[8:11], v[160:163], v[200:203], v[8:11]\n"
"buffer_load_dwordx4 v243, s[40:43], s81 offen nt lds\n"
"v_mfma_f32_16x16x32_bf16 v[12:15], v[160:163], v[204:207], v[12:15]\n"
"ds_read_b128 a[40:43], v232 offset:4096\n"
"v_mfma_f32_16x16x32_bf16 v[16:19], v[160:163], v[208:211], v[16:19]\n"
"ds_read_b128 a[44:47], v232 offset:6144\n"
"v_mfma_f32_16x16x32_bf16 v[20:23], v[164:167], v[192:195], v[20:23]\n"
"buffer_load_dwordx4 v243, s[40:43], s81 offen offset:1024 nt lds\n"
"v_mfma_f32_16x16x32_bf16 v[24:27], v[164:167], v[196:199], v[24:27]\n"
"ds_read_b128 a[48:51], v232 offset:8192\n"
"v_mfma_f32_16x16x32_bf16 v[28:31], v[164:167], v[200:203], v[28:31]\n"
"ds_read_b64_tr_b16 a[0:1], v226 offset:0\n"
"v_mfma_f32_16x16x32_bf16 v[32:35], v[164:167], v[204:207], v[32:35]\n"
"s_add_u32 m0, s48, 61568\n"
"s_add_u32 s81, s81, 8192\n"
"v_mfma_f32_16x16x32_bf16 v[36:39], v[164:167], v[208:211], v[36:39]\n"
"ds_read_b64_tr_b16 a[2:3], v226 offset:1024\n"
"v_mfma_f32_16x16x32_bf16 v[40:43], v[168:171], v[192:195], v[40:43]\n"
"buffer_load_dwordx4 v243, s[40:43], s52 offen nt lds\n"
"v_mfma_f32_16x16x32_bf16 v[44:47], v[168:171], v[196:199], v[44:47]\n"
"ds_read_b64_tr_b16 a[4:5], v226 offset:8\n"
"v_mfma_f32_16x16x32_bf16 v[48:51], v[168:171], v[200:203], v[48:51]\n"
"ds_read_b64_tr_b16 a[6:7], v226 offset:1032\n"
"v_mfma_f32_16x16x32_bf16 v[52:55], v[168:171], v[204:207], v[52:55]\n"
"buffer_load_dwordx4 v243, s[40:43], s52 offen offset:1024 nt lds\n"
"s_and_b32 s81, s81, 131071\n"
"v_mfma_f32_16x16x32_bf16 v[56:59], v[168:171], v[208:211], v[56:59]\n"
"ds_read_b64_tr_b16 a[8:9], v226 offset:256\n"
"v_mfma_f32_16x16x32_bf16 v[60:63], v[172:175], v[192:195], v[60:63]\n"
"ds_read_b64_tr_b16 a[10:11], v226 offset:1280\n"
"v_mfma_f32_16x16x32_bf16 v[64:67], v[172:175], v[196:199], v[64:67]\n"
"s_add_u32 m0, s48, 69760\n"
"s_add_u32 s52, s81, 131072\n"
"v_mfma_f32_16x16x32_bf16 v[68:71], v[172:175], v[200:203], v[68:71]\n"
"ds_read_b64_tr_b16 a[12:13], v226 offset:264\n"
"v_mfma_f32_16x16x32_bf16 v[72:75], v[172:175], v[204:207], v[72:75]\n"
"ds_read_b64_tr_b16 a[14:15], v226 offset:1288\n"
"v_mfma_f32_16x16x32_bf16 v[76:79], v[172:175], v[208:211], v[76:79]\n"
"buffer_load_dwordx4 v243, s[40:43], s81 offen nt lds\n"
"v_mfma_f32_16x16x32_bf16 v[80:83], v[176:179], v[192:195], v[80:83]\n"
"ds_read_b64_tr_b16 a[16:17], v226 offset:512\n"
"v_mfma_f32_16x16x32_bf16 v[84:87], v[176:179], v[196:199], v[84:87]\n"
"buffer_load_dwordx4 v243, s[40:43], s81 offen offset:1024 nt lds\n"
"v_mfma_f32_16x16x32_bf16 v[88:91], v[176:179], v[200:203], v[88:91]\n"
"ds_read_b64_tr_b16 a[18:19], v226 offset:1536\n"
"v_mfma_f32_16x16x32_bf16 v[92:95], v[176:179], v[204:207], v[92:95]\n"
"ds_read_b64_tr_b16 a[20:21], v226 offset:520\n"
"v_mfma_f32_16x16x32_bf16 v[96:99], v[176:179], v[208:211], v[96:99]\n"
"s_add_u32 m0, s48, 77952\n"
"s_add_u32 s81, s81, 8192\n"
"v_mfma_f32_16x16x32_bf16 v[100:103], v[180:183], v[192:195], v[100:103]\n"
"ds_read_b64_tr_b16 a[22:23], v226 offset:1544\n"
"v_mfma_f32_16x16x32_bf16 v[104:107], v[180:183], v[196:199], v[104:107]\n"
"ds_read_b64_tr_b16 a[24:25], v226 offset:768\n"
"v_mfma_f32_16x16x32_bf16 v[108:111], v[180:183], v[200:203], v[108:111]\n"
"buffer_load_dwordx4 v243, s[40:43], s52 offen nt lds\n"
"v_mfma_f32_16x16x32_bf16 v[112:115], v[180:183], v[204:207], v[112:115]\n"
"ds_read_b64_tr_b16 a[26:27], v226 offset:1792\n"
"v_mfma_f32_16x16x32_bf16 v[116:119], v[180:183], v[208:211], v[116:119]\n"
"ds_read_b64_tr_b16 a[28:29], v226 offset:776\n"
"v_mfma_f32_16x16x32_bf16 v[120:123], v[184:187], v[192:195], v[120:123]\n"
"buffer_load_dwordx4 v243, s[40:43], s52 offen offset:1024 nt lds\n"
"s_and_b32 s81, s81, 131071\n"
"v_mfma_f32_16x16x32_bf16 v[124:127], v[184:187], v[196:199], v[124:127]\n"
"ds_read_b64_tr_b16 a[30:31], v226 offset:1800\n"
"v_mfma_f32_16x16x32_bf16 v[128:131], v[184:187], v[200:203], v[128:131]\n"
"s_add_u32 m0, s48, 86144\n"
"s_add_u32 s52, s95, 131072\n"
"v_mfma_f32_16x16x32_bf16 v[132:135], v[184:187], v[204:207], v[132:135]\n"
"buffer_load_dwordx4 v244, s[60:63], s95 offen lds\n"
"v_mfma_f32_16x16x32_bf16 v[136:139], v[184:187], v[208:211], v[136:139]\n"
"buffer_load_dwordx4 v244, s[60:63], s95 offen offset:1024 lds\n"
"v_mfma_f32_16x16x32_bf16 v[140:143], v[188:191], v[192:195], v[140:143]\n"
"v_mfma_f32_16x16x32_bf16 v[144:147], v[188:191], v[196:199], v[144:147]\n"
"v_mfma_f32_16x16x32_bf16 v[148:151], v[188:191], v[200:203], v[148:151]\n"
"v_mfma_f32_16x16x32_bf16 v[152:155], v[188:191], v[204:207], v[152:155]\n"
"v_mfma_f32_16x16x32_bf16 v[156:159], v[188:191], v[208:211], v[156:159]\n"
"s_waitcnt lgkmcnt(0)\n"
"v_mfma_f32_16x16x32_bf16 v[0:3], a[0:3], a[32:35], v[0:3]\n"
"ds_read_b128 v[192:195], v232 offset:512\n"
"s_add_u32 m0, s48, 94336\n"
"v_mfma_f32_16x16x32_bf16 v[4:7], a[0:3], a[36:39], v[4:7]\n"
"ds_read_b128 v[196:199], v232 offset:2560\n"
"s_add_u32 s95, s95, 8192\n"
"v_mfma_f32_16x16x32_bf16 v[8:11], a[0:3], a[40:43], v[8:11]\n"
"buffer_load_dwordx4 v244, s[60:63], s52 offen lds\n"
"v_mfma_f32_16x16x32_bf16 v[12:15], a[0:3], a[44:47], v[12:15]\n"
"ds_read_b128 v[200:203], v232 offset:4608\n"
"v_mfma_f32_16x16x32_bf16 v[16:19], a[0:3], a[48:51], v[16:19]\n"
"ds_read_b128 v[204:207], v232 offset:6656\n"
"v_mfma_f32_16x16x32_bf16 v[20:23], a[4:7], a[32:35], v[20:23]\n"
"buffer_load_dwordx4 v244, s[60:63], s52 offen offset:1024 lds\n"
"s_and_b32 s95, s95, 131071\n"
"v_mfma_f32_16x16x32_bf16 v[24:27], a[4:7], a[36:39], v[24:27]\n"
"ds_read_b128 v[208:211], v232 offset:8704\n"
"v_mfma_f32_16x16x32_bf16 v[28:31], a[4:7], a[40:43], v[28:31]\n"
"ds_read_b64_tr_b16 v[160:161], v226 offset:16448\n"
"v_mfma_f32_16x16x32_bf16 v[32:35], a[4:7], a[44:47], v[32:35]\n"
"v_mfma_f32_16x16x32_bf16 v[36:39], a[4:7], a[48:51], v[36:39]\n"
"ds_read_b64_tr_b16 v[162:163], v226 offset:17472\n"
"v_mfma_f32_16x16x32_bf16 v[40:43], a[8:11], a[32:35], v[40:43]\n"
"v_mfma_f32_16x16x32_bf16 v[44:47], a[8:11], a[36:39], v[44:47]\n"
"ds_read_b64_tr_b16 v[164:165], v226 offset:16456\n"
"v_mfma_f32_16x16x32_bf16 v[48:51], a[8:11], a[40:43], v[48:51]\n"
"ds_read_b64_tr_b16 v[166:167], v226 offset:17480\n"
"v_mfma_f32_16x16x32_bf16 v[52:55], a[8:11], a[44:47], v[52:55]\n"
"v_mfma_f32_16x16x32_bf16 v[56:59], a[8:11], a[48:51], v[56:59]\n"
"ds_read_b64_tr_b16 v[168:169], v226 offset:16704\n"
"v_mfma_f32_16x16x32_bf16 v[60:63], a[12:15], a[32:35], v[60:63]\n"
"ds_read_b64_tr_b16 v[170:171], v226 offset:17728\n"
"v_mfma_f32_16x16x32_bf16 v[64:67], a[12:15], a[36:39], v[64:67]\n"
"v_mfma_f32_16x16x32_bf16 v[68:71], a[12:15], a[40:43], v[68:71]\n"
"ds_read_b64_tr_b16 v[172:173], v226 offset:16712\n"
"v_mfma_f32_16x16x32_bf16 v[72:75], a[12:15], a[44:47], v[72:75]\n"
"ds_read_b64_tr_b16 v[174:175], v226 offset:17736\n"
"v_mfma_f32_16x16x32_bf16 v[76:79], a[12:15], a[48:51], v[76:79]\n"
"v_mfma_f32_16x16x32_bf16 v[80:83], a[16:19], a[32:35], v[80:83]\n"
"ds_read_b64_tr_b16 v[176:177], v226 offset:16960\n"
"v_mfma_f32_16x16x32_bf16 v[84:87], a[16:19], a[36:39], v[84:87]\n"
"v_mfma_f32_16x16x32_bf16 v[88:91], a[16:19], a[40:43], v[88:91]\n"
"ds_read_b64_tr_b16 v[178:179], v226 offset:17984\n"
"v_mfma_f32_16x16x32_bf16 v[92:95], a[16:19], a[44:47], v[92:95]\n"
"ds_read_b64_tr_b16 v[180:181], v226 offset:16968\n"
"v_mfma_f32_16x16x32_bf16 v[96:99], a[16:19], a[48:51], v[96:99]\n"
"v_mfma_f32_16x16x32_bf16 v[100:103], a[20:23], a[32:35], v[100:103]\n"
"ds_read_b64_tr_b16 v[182:183], v226 offset:17992\n"
"v_mfma_f32_16x16x32_bf16 v[104:107], a[20:23], a[36:39], v[104:107]\n"
"ds_read_b64_tr_b16 v[184:185], v226 offset:17216\n"
"v_mfma_f32_16x16x32_bf16 v[108:111], a[20:23], a[40:43], v[108:111]\n"
"v_mfma_f32_16x16x32_bf16 v[112:115], a[20:23], a[44:47], v[112:115]\n"
"ds_read_b64_tr_b16 v[186:187], v226 offset:18240\n"
"v_mfma_f32_16x16x32_bf16 v[116:119], a[20:23], a[48:51], v[116:119]\n"
"ds_read_b64_tr_b16 v[188:189], v226 offset:17224\n"
"v_mfma_f32_16x16x32_bf16 v[120:123], a[24:27], a[32:35], v[120:123]\n"
"v_mfma_f32_16x16x32_bf16 v[124:127], a[24:27], a[36:39], v[124:127]\n"
"ds_read_b64_tr_b16 v[190:191], v226 offset:18248\n"
"v_mfma_f32_16x16x32_bf16 v[128:131], a[24:27], a[40:43], v[128:131]\n"
"v_mfma_f32_16x16x32_bf16 v[132:135], a[24:27], a[44:47], v[132:135]\n"
"v_mfma_f32_16x16x32_bf16 v[136:139], a[24:27], a[48:51], v[136:139]\n"
"v_mfma_f32_16x16x32_bf16 v[140:143], a[28:31], a[32:35], v[140:143]\n"
"v_mfma_f32_16x16x32_bf16 v[144:147], a[28:31], a[36:39], v[144:147]\n"
"v_mfma_f32_16x16x32_bf16 v[148:151], a[28:31], a[40:43], v[148:151]\n"
"v_mfma_f32_16x16x32_bf16 v[152:155], a[28:31], a[44:47], v[152:155]\n"
"v_mfma_f32_16x16x32_bf16 v[156:159], a[28:31], a[48:51], v[156:159]\n"
"s_waitcnt vmcnt(14) lgkmcnt(0)\n"
"s_barrier\n"
"v_mfma_f32_16x16x32_bf16 v[0:3], v[160:163], v[192:195], v[0:3]\n"
"ds_read_b128 a[32:35], v230 offset:0\n"
"s_add_u32 m0, s48, 106752\n"
"v_mfma_f32_16x16x32_bf16 v[4:7], v[160:163], v[196:199], v[4:7]\n"
"ds_read_b128 a[36:39], v230 offset:2048\n"
"s_add_u32 s52, s81, 131072\n"
"v_mfma_f32_16x16x32_bf16 v[8:11], v[160:163], v[200:203], v[8:11]\n"
"buffer_load_dwordx4 v243, s[40:43], s81 offen nt lds\n"
"v_mfma_f32_16x16x32_bf16 v[12:15], v[160:163], v[204:207], v[12:15]\n"
"ds_read_b128 a[40:43], v230 offset:4096\n"
"v_mfma_f32_16x16x32_bf16 v[16:19], v[160:163], v[208:211], v[16:19]\n"
"ds_read_b128 a[44:47], v230 offset:6144\n"
"v_mfma_f32_16x16x32_bf16 v[20:23], v[164:167], v[192:195], v[20:23]\n"
"buffer_load_dwordx4 v243, s[40:43], s81 offen offset:1024 nt lds\n"
"v_mfma_f32_16x16x32_bf16 v[24:27], v[164:167], v[196:199], v[24:27]\n"
"ds_read_b128 a[48:51], v230 offset:8192\n"
"v_mfma_f32_16x16x32_bf16 v[28:31], v[164:167], v[200:203], v[28:31]\n"
"ds_read_b64_tr_b16 a[0:1], v224 offset:0\n"
"v_mfma_f32_16x16x32_bf16 v[32:35], v[164:167], v[204:207], v[32:35]\n"
"v_mfma_f32_16x16x32_bf16 v[36:39], v[164:167], v[208:211], v[36:39]\n"
"ds_read_b64_tr_b16 a[2:3], v224 offset:1024\n"
"v_mfma_f32_16x16x32_bf16 v[40:43], v[168:171], v[192:195], v[40:43]\n"
"v_mfma_f32_16x16x32_bf16 v[44:47], v[168:171], v[196:199], v[44:47]\n"
"ds_read_b64_tr_b16 a[4:5], v224 offset:8\n"
"v_mfma_f32_16x16x32_bf16 v[48:51], v[168:171], v[200:203], v[48:51]\n"
"ds_read_b64_tr_b16 a[6:7], v224 offset:1032\n"
"v_mfma_f32_16x16x32_bf16 v[52:55], v[168:171], v[204:207], v[52:55]\n"
"v_mfma_f32_16x16x32_bf16 v[56:59], v[168:171], v[208:211], v[56:59]\n"
"ds_read_b64_tr_b16 a[8:9], v224 offset:256\n"
"v_mfma_f32_16x16x32_bf16 v[60:63], v[172:175], v[192:195], v[60:63]\n"
"ds_read_b64_tr_b16 a[10:11], v224 offset:1280\n"
"v_mfma_f32_16x16x32_bf16 v[64:67], v[172:175], v[196:199], v[64:67]\n"
"v_mfma_f32_16x16x32_bf16 v[68:71], v[172:175], v[200:203], v[68:71]\n"
"ds_read_b64_tr_b16 a[12:13], v224 offset:264\n"
"v_mfma_f32_16x16x32_bf16 v[72:75], v[172:175], v[204:207], v[72:75]\n"
"ds_read_b64_tr_b16 a[14:15], v224 offset:1288\n"
"v_mfma_f32_16x16x32_bf16 v[76:79], v[172:175], v[208:211], v[76:79]\n"
"v_mfma_f32_16x16x32_bf16 v[80:83], v[176:179], v[192:195], v[80:83]\n"
"ds_read_b64_tr_b16 a[16:17], v224 offset:512\n"
"s_add_u32 m0, s48, 114944\n"
"v_mfma_f32_16x16x32_bf16 v[84:87], v[176:179], v[196:199], v[84:87]\n"
"s_add_u32 s81, s81, 8192\n"
"buffer_load_dwordx4 v243, s[40:43], s52 offen nt lds\n"
"v_mfma_f32_16x16x32_bf16 v[88:91], v[176:179], v[200:203], v[88:91]\n"
"ds_read_b64_tr_b16 a[18:19], v224 offset:1536\n"
"v_mfma_f32_16x16x32_bf16 v[92:95], v[176:179], v[204:207], v[92:95]\n"
"ds_read_b64_tr_b16 a[20:21], v224 offset:520\n"
"v_mfma_f32_16x16x32_bf16 v[96:99], v[176:179], v[208:211], v[96:99]\n"
"buffer_load_dwordx4 v243, s[40:43], s52 offen offset:1024 nt lds\n"
"s_and_b32 s81, s81, 131071\n"
"v_mfma_f32_16x16x32_bf16 v[100:103], v[180:183], v[192:195], v[100:103]\n"
"ds_read_b64_tr_b16 a[22:23], v224 offset:1544\n"
"v_mfma_f32_16x16x32_bf16 v[104:107], v[180:183], v[196:199], v[104:107]\n"
"ds_read_b64_tr_b16 a[24:25], v224 offset:768\n"
"v_mfma_f32_16x16x32_bf16 v[108:111], v[180:183], v[200:203], v[108:111]\n"
"v_mfma_f32_16x16x32_bf16 v[112:115], v[180:183], v[204:207], v[112:115]\n"
"ds_read_b64_tr_b16 a[26:27], v224 offset:1792\n"
"v_mfma_f32_16x16x32_bf16 v[116:119], v[180:183], v[208:211], v[116:119]\n"
"ds_read_b64_tr_b16 a[28:29], v224 offset:776\n"
"v_mfma_f32_16x16x32_bf16 v[120:123], v[184:187], v[192:195], v[120:123]\n"
"v_mfma_f32_16x16x32_bf16 v[124:127], v[184:187], v[196:199], v[124:127]\n"
"ds_read_b64_tr_b16 a[30:31], v224 offset:1800\n"
"v_mfma_f32_16x16x32_bf16 v[128:131], v[184:187], v[200:203], v[128:131]\n"
"v_mfma_f32_16x16x32_bf16 v[132:135], v[184:187], v[204:207], v[132:135]\n"
"v_mfma_f32_16x16x32_bf16 v[136:139], v[184:187], v[208:211], v[136:139]\n"
"v_mfma_f32_16x16x32_bf16 v[140:143], v[188:191], v[192:195], v[140:143]\n"
"v_mfma_f32_16x16x32_bf16 v[144:147], v[188:191], v[196:199], v[144:147]\n"
"v_mfma_f32_16x16x32_bf16 v[148:151], v[188:191], v[200:203], v[148:151]\n"
"v_mfma_f32_16x16x32_bf16 v[152:155], v[188:191], v[204:207], v[152:155]\n"
"v_mfma_f32_16x16x32_bf16 v[156:159], v[188:191], v[208:211], v[156:159]\n"
"s_waitcnt lgkmcnt(0)\n"
"v_mfma_f32_16x16x32_bf16 v[0:3], a[0:3], a[32:35], v[0:3]\n"
"ds_read_b128 v[192:195], v230 offset:512\n"
"s_add_u32 m0, s48, 123136\n"
"v_mfma_f32_16x16x32_bf16 v[4:7], a[0:3], a[36:39], v[4:7]\n"
"ds_read_b128 v[196:199], v230 offset:2560\n"
"s_add_u32 s52, s95, 131072\n"
"v_mfma_f32_16x16x32_bf16 v[8:11], a[0:3], a[40:43], v[8:11]\n"
"buffer_load_dwordx4 v244, s[60:63], s95 offen lds\n"
"v_mfma_f32_16x16x32_bf16 v[12:15], a[0:3], a[44:47], v[12:15]\n"
"ds_read_b128 v[200:203], v230 offset:4608\n"
"v_mfma_f32_16x16x32_bf16 v[16:19], a[0:3], a[48:51], v[16:19]\n"
"ds_read_b128 v[204:207], v230 offset:6656\n"
"v_mfma_f32_16x16x32_bf16 v[20:23], a[4:7], a[32:35], v[20:23]\n"
"buffer_load_dwordx4 v244, s[60:63], s95 offen offset:1024 lds\n"
"v_mfma_f32_16x16x32_bf16 v[24:27], a[4:7], a[36:39], v[24:27]\n"
"ds_read_b128 v[208:211], v230 offset:8704\n"
"v_mfma_f32_16x16x32_bf16 v[28:31], a[4:7], a[40:43], v[28:31]\n"
"ds_read_b64_tr_b16 v[160:161], v224 offset:16448\n"
"v_mfma_f32_16x16x32_bf16 v[32:35], a[4:7], a[44:47], v[32:35]\n"
"v_mfma_f32_16x16x32_bf16 v[36:39], a[4:7], a[48:51], v[36:39]\n"
"ds_read_b64_tr_b16 v[162:163], v224 offset:17472\n"
"v_mfma_f32_16x16x32_bf16 v[40:43], a[8:11], a[32:35], v[40:43]\n"
"v_mfma_f32_16x16x32_bf16 v[44:47], a[8:11], a[36:39], v[44:47]\n"
"ds_read_b64_tr_b16 v[164:165], v224 offset:16456\n"
"v_mfma_f32_16x16x32_bf16 v[48:51], a[8:11], a[40:43], v[48:51]\n"
"ds_read_b64_tr_b16 v[166:167], v224 offset:17480\n"
"v_mfma_f32_16x16x32_bf16 v[52:55], a[8:11], a[44:47], v[52:55]\n"
"v_mfma_f32_16x16x32_bf16 v[56:59], a[8:11], a[48:51], v[56:59]\n"
"ds_read_b64_tr_b16 v[168:169], v224 offset:16704\n"
"v_mfma_f32_16x16x32_bf16 v[60:63], a[12:15], a[32:35], v[60:63]\n"
"ds_read_b64_tr_b16 v[170:171], v224 offset:17728\n"
"v_mfma_f32_16x16x32_bf16 v[64:67], a[12:15], a[36:39], v[64:67]\n"
"v_mfma_f32_16x16x32_bf16 v[68:71], a[12:15], a[40:43], v[68:71]\n"
"ds_read_b64_tr_b16 v[172:173], v224 offset:16712\n"
"v_mfma_f32_16x16x32_bf16 v[72:75], a[12:15], a[44:47], v[72:75]\n"
"ds_read_b64_tr_b16 v[174:175], v224 offset:17736\n"
"v_mfma_f32_16x16x32_bf16 v[76:79], a[12:15], a[48:51], v[76:79]\n"
"v_mfma_f32_16x16x32_bf16 v[80:83], a[16:19], a[32:35], v[80:83]\n"
"ds_read_b64_tr_b16 v[176:177], v224 offset:16960\n"
"s_add_u32 m0, s48, 131328\n"
"v_mfma_f32_16x16x32_bf16 v[84:87], a[16:19], a[36:39], v[84:87]\n"
"s_add_u32 s95, s95, 8192\n"
"buffer_load_dwordx4 v244, s[60:63], s52 offen lds\n"
"v_mfma_f32_16x16x32_bf16 v[88:91], a[16:19], a[40:43], v[88:91]\n"
"ds_read_b64_tr_b16 v[178:179], v224 offset:17984\n"
"v_mfma_f32_16x16x32_bf16 v[92:95], a[16:19], a[44:47], v[92:95]\n"
"ds_read_b64_tr_b16 v[180:181], v224 offset:16968\n"
"v_mfma_f32_16x16x32_bf16 v[96:99], a[16:19], a[48:51], v[96:99]\n"
"buffer_load_dwordx4 v244, s[60:63], s52 offen offset:1024 lds\n"
"s_and_b32 s95, s95, 131071\n"
"v_mfma_f32_16x16x32_bf16 v[100:103], a[20:23], a[32:35], v[100:103]\n"
"ds_read_b64_tr_b16 v[182:183], v224 offset:17992\n"
"v_mfma_f32_16x16x32_bf16 v[104:107], a[20:23], a[36:39], v[104:107]\n"
"ds_read_b64_tr_b16 v[184:185], v224 offset:17216\n"
"v_mfma_f32_16x16x32_bf16 v[108:111], a[20:23], a[40:43], v[108:111]\n"
"v_mfma_f32_16x16x32_bf16 v[112:115], a[20:23], a[44:47], v[112:115]\n"
"ds_read_b64_tr_b16 v[186:187], v224 offset:18240\n"
"v_mfma_f32_16x16x32_bf16 v[116:119], a[20:23], a[48:51], v[116:119]\n"
"ds_read_b64_tr_b16 v[188:189], v224 offset:17224\n"
"v_mfma_f32_16x16x32_bf16 v[120:123], a[24:27], a[32:35], v[120:123]\n"
"v_mfma_f32_16x16x32_bf16 v[124:127], a[24:27], a[36:39], v[124:127]\n"
"ds_read_b64_tr_b16 v[190:191], v224 offset:18248\n"
"v_mfma_f32_16x16x32_bf16 v[128:131], a[24:27], a[40:43], v[128:131]\n"
"v_add_u32 v240, 49152, v240\n"
"v_add_u32 v241, 49152, v241\n"
"v_mfma_f32_16x16x32_bf16 v[132:135], a[24:27], a[44:47], v[132:135]\n"
"v_add_u32 v242, 49152, v242\n"
"v_mfma_f32_16x16x32_bf16 v[136:139], a[24:27], a[48:51], v[136:139]\n"
"v_mfma_f32_16x16x32_bf16 v[140:143], a[28:31], a[32:35], v[140:143]\n"
"v_mfma_f32_16x16x32_bf16 v[144:147], a[28:31], a[36:39], v[144:147]\n"
"v_mfma_f32_16x16x32_bf16 v[148:151], a[28:31], a[40:43], v[148:151]\n"
"v_mfma_f32_16x16x32_bf16 v[152:155], a[28:31], a[44:47], v[152:155]\n"
"v_mfma_f32_16x16x32_bf16 v[156:159], a[28:31], a[48:51], v[156:159]\n"
"s_waitcnt vmcnt(16) lgkmcnt(0)\n"
"s_barrier\n"
"v_mfma_f32_16x16x32_bf16 v[0:3], v[160:163], v[192:195], v[0:3]\n"
"ds_read_b128 a[80:83], v240 offset:4224\n"
"ds_read_b128 a[84:87], v240 offset:4480\n"
"v_mfma_f32_16x16x32_bf16 v[20:23], v[164:167], v[192:195], v[20:23]\n"
"ds_read_b128 a[88:91], v240 offset:4736\n"
"ds_read_b128 a[92:95], v240 offset:4992\n"
"v_mfma_f32_16x16x32_bf16 v[40:43], v[168:171], v[192:195], v[40:43]\n"
"ds_read_b128 a[96:99], v240 offset:5248\n"
"ds_read_b128 a[100:103], v240 offset:5504\n"
"v_mfma_f32_16x16x32_bf16 v[60:63], v[172:175], v[192:195], v[60:63]\n"
"ds_read_b128 a[104:107], v240 offset:5760\n"
"ds_read_b128 a[108:111], v240 offset:6016\n"
"v_mfma_f32_16x16x32_bf16 v[80:83], v[176:179], v[192:195], v[80:83]\n"
"s_add_u32 m0, s48, 139520\n"
"s_add_u32 s52, s81, 131072\n"
"buffer_load_dwordx4 v243, s[40:43], s81 offen nt lds\n"
"v_mfma_f32_16x16x32_bf16 v[100:103], v[180:183], v[192:195], v[100:103]\n"
"buffer_load_dwordx4 v243, s[40:43], s81 offen offset:1024 nt lds\n"
"v_mfma_f32_16x16x32_bf16 v[120:123], v[184:187], v[192:195], v[120:123]\n"
"v_mfma_f32_16x16x32_bf16 v[140:143], v[188:191], v[192:195], v[140:143]\n"
"v_mfma_f32_16x16x32_bf16 v[4:7], v[160:163], v[196:199], v[4:7]\n"
"v_mfma_f32_16x16x32_bf16 v[24:27], v[164:167], v[196:199], v[24:27]\n"
"v_mfma_f32_16x16x32_bf16 v[44:47], v[168:171], v[196:199], v[44:47]\n"
"v_mul_f32 v0, s50, v0\n"
"v_mul_f32 v1, s50, v1\n"
"v_mul_f32 v2, s50, v2\n"
"v_mfma_f32_16x16x32_bf16 v[64:67], v[172:175], v[196:199], v[64:67]\n"
"v_mul_f32 v3, s50, v3\n"
"v_mul_f32 v20, s50, v20\n"
"v_mul_f32 v21, s50, v21\n"
"v_mfma_f32_16x16x32_bf16 v[84:87], v[176:179], v[196:199], v[84:87]\n"
"v_mul_f32 v22, s50, v22\n"
"v_mul_f32 v23, s50, v23\n"
"v_cvt_pk_bf16_f32 v212, v0, v1\n"
"v_mfma_f32_16x16x32_bf16 v[104:107], v[180:183], v[196:199], v[104:107]\n"
"v_cvt_pk_bf16_f32 v213, v2, v3\n"
"v_cvt_pk_bf16_f32 v214, v20, v21\n"
"v_cvt_pk_bf16_f32 v215, v22, v23\n"
"v_mfma_f32_16x16x32_bf16 v[124:127], v[184:187], v[196:199], v[124:127]\n"
"v_accvgpr_write_b32 a0, v212\n"
"v_accvgpr_write_b32 a1, v213\n"
"v_accvgpr_write_b32 a2, v214\n"
"v_mfma_f32_16x16x32_bf16 v[144:147], v[188:191], v[196:199], v[144:147]\n"
"v_accvgpr_write_b32 a3, v215\n"
"v_mul_f32 v40, s50, v40\n"
"v_mul_f32 v41, s50, v41\n"
"v_mfma_f32_16x16x32_bf16 v[8:11], v[160:163], v[200:203], v[8:11]\n"
"v_mul_f32 v42, s50, v42\n"
"v_mul_f32 v43, s50, v43\n"
"v_mul_f32 v60, s50, v60\n"
"v_mfma_f32_16x16x32_bf16 v[28:31], v[164:167], v[200:203], v[28:31]\n"
"v_mul_f32 v61, s50, v61\n"
"v_mul_f32 v62, s50, v62\n"
"v_mul_f32 v63, s50, v63\n"
"v_mfma_f32_16x16x32_bf16 v[48:51], v[168:171], v[200:203], v[48:51]\n"
"v_cvt_pk_bf16_f32 v212, v40, v41\n"
"v_cvt_pk_bf16_f32 v213, v42, v43\n"
"v_cvt_pk_bf16_f32 v214, v60, v61\n"
"v_mfma_f32_16x16x32_bf16 v[68:71], v[172:175], v[200:203], v[68:71]\n"
"v_cvt_pk_bf16_f32 v215, v62, v63\n"
"v_accvgpr_write_b32 a20, v212\n"
"v_accvgpr_write_b32 a21, v213\n"
"v_mfma_f32_16x16x32_bf16 v[88:91], v[176:179], v[200:203], v[88:91]\n"
"v_accvgpr_write_b32 a22, v214\n"
"v_accvgpr_write_b32 a23, v215\n"
"v_mul_f32 v80, s50, v80\n"
"v_mfma_f32_16x16x32_bf16 v[108:111], v[180:183], v[200:203], v[108:111]\n"
"v_mul_f32 v81, s50, v81\n"
"v_mul_f32 v82, s50, v82\n"
"v_mul_f32 v83, s50, v83\n"
"v_mfma_f32_16x16x32_bf16 v[128:131], v[184:187], v[200:203], v[128:131]\n"
"s_add_u32 m0, s48, 147712\n"
"s_add_u32 s81, s81, 8192\n"
"v_mul_f32 v100, s50, v100\n"
"v_mul_f32 v101, s50, v101\n"
"v_mul_f32 v102, s50, v102\n"
"v_mfma_f32_16x16x32_bf16 v[148:151], v[188:191], v[200:203], v[148:151]\n"
"buffer_load_dwordx4 v243, s[40:43], s52 offen nt lds\n"
"v_mul_f32 v103, s50, v103\n"
"v_cvt_pk_bf16_f32 v212, v80, v81\n"
"v_cvt_pk_bf16_f32 v213, v82, v83\n"
"v_mfma_f32_16x16x32_bf16 v[12:15], v[160:163], v[204:207], v[12:15]\n"
"buffer_load_dwordx4 v243, s[40:43], s52 offen offset:1024 nt lds\n"
"v_cvt_pk_bf16_f32 v214, v100, v101\n"
"v_cvt_pk_bf16_f32 v215, v102, v103\n"
"v_accvgpr_write_b32 a40, v212\n"
"v_mfma_f32_16x16x32_bf16 v[32:35], v[164:167], v[204:207], v[32:35]\n"
"s_and_b32 s81, s81, 131071\n"
"v_accvgpr_write_b32 a41, v213\n"
"v_accvgpr_write_b32 a42, v214\n"
"v_accvgpr_write_b32 a43, v215\n"
"v_mfma_f32_16x16x32_bf16 v[52:55], v[168:171], v[204:207], v[52:55]\n"
"v_mul_f32 v120, s50, v120\n"
"v_mul_f32 v121, s50, v121\n"
"v_mul_f32 v122, s50, v122\n"
"v_mfma_f32_16x16x32_bf16 v[72:75], v[172:175], v[204:207], v[72:75]\n"
"v_mul_f32 v123, s50, v123\n"
"v_mul_f32 v140, s50, v140\n"
"v_mul_f32 v141, s50, v141\n"
"v_mfma_f32_16x16x32_bf16 v[92:95], v[176:179], v[204:207], v[92:95]\n"
"v_mul_f32 v142, s50, v142\n"
"v_mul_f32 v143, s50, v143\n"
"v_cvt_pk_bf16_f32 v212, v120, v121\n"
"v_mfma_f32_16x16x32_bf16 v[112:115], v[180:183], v[204:207], v[112:115]\n"
"v_cvt_pk_bf16_f32 v213, v122, v123\n"
"v_cvt_pk_bf16_f32 v214, v140, v141\n"
"v_cvt_pk_bf16_f32 v215, v142, v143\n"
"v_mfma_f32_16x16x32_bf16 v[132:135], v[184:187], v[204:207], v[132:135]\n"
"v_accvgpr_write_b32 a60, v212\n"
"v_accvgpr_write_b32 a61, v213\n"
"v_accvgpr_write_b32 a62, v214\n"
"v_mfma_f32_16x16x32_bf16 v[152:155], v[188:191], v[204:207], v[152:155]\n"
"v_accvgpr_write_b32 a63, v215\n"
"v_mul_f32 v4, s50, v4\n"
"v_mul_f32 v5, s50, v5\n"
"v_mfma_f32_16x16x32_bf16 v[16:19], v[160:163], v[208:211], v[16:19]\n"
"v_mul_f32 v6, s50, v6\n"
"v_mul_f32 v7, s50, v7\n"
"v_mul_f32 v24, s50, v24\n"
"v_mfma_f32_16x16x32_bf16 v[36:39], v[164:167], v[208:211], v[36:39]\n"
"v_mul_f32 v25, s50, v25\n"
"v_mul_f32 v26, s50, v26\n"
"v_mul_f32 v27, s50, v27\n"
"v_mfma_f32_16x16x32_bf16 v[56:59], v[168:171], v[208:211], v[56:59]\n"
"v_cvt_pk_bf16_f32 v212, v4, v5\n"
"v_cvt_pk_bf16_f32 v213, v6, v7\n"
"v_cvt_pk_bf16_f32 v214, v24, v25\n"
"v_mfma_f32_16x16x32_bf16 v[76:79], v[172:175], v[208:211], v[76:79]\n"
"v_cvt_pk_bf16_f32 v215, v26, v27\n"
"v_accvgpr_write_b32 a4, v212\n"
"v_accvgpr_write_b32 a5, v213\n"
"v_mfma_f32_16x16x32_bf16 v[96:99], v[176:179], v[208:211], v[96:99]\n"
"v_accvgpr_write_b32 a6, v214\n"
"v_accvgpr_write_b32 a7, v215\n"
"v_mul_f32 v44, s50, v44\n"
"v_mfma_f32_16x16x32_bf16 v[116:119], v[180:183], v[208:211], v[116:119]\n"
"v_mul_f32 v45, s50, v45\n"
"v_mul_f32 v46, s50, v46\n"
"v_mul_f32 v47, s50, v47\n"
"v_mfma_f32_16x16x32_bf16 v[136:139], v[184:187], v[208:211], v[136:139]\n"
"v_mul_f32 v64, s50, v64\n"
"v_mul_f32 v65, s50, v65\n"
"v_mul_f32 v66, s50, v66\n"
"v_mfma_f32_16x16x32_bf16 v[156:159], v[188:191], v[208:211], v[156:159]\n"
"s_waitcnt lgkmcnt(0)\n"
"v_mul_f32 v67, s50, v67\n"
"v_cvt_pk_bf16_f32 v212, v44, v45\n"
"v_cvt_pk_bf16_f32 v213, v46, v47\n"
"v_mfma_f32_16x16x32_bf16 v[160:163], a[80:83], a[0:3], 0\n"
"s_add_u32 m0, s48, 0\n"
"s_add_u32 s52, s95, 131072\n"
"v_cvt_pk_bf16_f32 v214, v64, v65\n"
"v_cvt_pk_bf16_f32 v215, v66, v67\n"
"v_accvgpr_write_b32 a24, v212\n"
"v_mfma_f32_16x16x32_bf16 v[164:167], a[96:99], a[0:3], 0\n"
"buffer_load_dwordx4 v244, s[60:63], s95 offen lds\n"
"v_accvgpr_write_b32 a25, v213\n"
"v_accvgpr_write_b32 a26, v214\n"
"v_accvgpr_write_b32 a27, v215\n"
"v_mfma_f32_16x16x32_bf16 v[160:163], a[84:87], a[20:23], v[160:163]\n"
"buffer_load_dwordx4 v244, s[60:63], s95 offen offset:1024 lds\n"
"v_mul_f32 v84, s50, v84\n"
"v_mul_f32 v85, s50, v85\n"
"v_mul_f32 v86, s50, v86\n"
"v_mfma_f32_16x16x32_bf16 v[164:167], a[100:103], a[20:23], v[164:167]\n"
"v_mul_f32 v87, s50, v87\n"
"v_mul_f32 v104, s50, v104\n"
"v_mul_f32 v105, s50, v105\n"
"v_mfma_f32_16x16x32_bf16 v[160:163], a[88:91], a[40:43], v[160:163]\n"
"v_mul_f32 v106, s50, v106\n"
"v_mul_f32 v107, s50, v107\n"
"v_cvt_pk_bf16_f32 v212, v84, v85\n"
"v_mfma_f32_16x16x32_bf16 v[164:167], a[104:107], a[40:43], v[164:167]\n"
"v_cvt_pk_bf16_f32 v213, v86, v87\n"
"v_cvt_pk_bf16_f32 v214, v104, v105\n"
"v_cvt_pk_bf16_f32 v215, v106, v107\n"
"v_mfma_f32_16x16x32_bf16 v[160:163], a[92:95], a[60:63], v[160:163]\n"
"v_accvgpr_write_b32 a44, v212\n"
"v_accvgpr_write_b32 a45, v213\n"
"v_accvgpr_write_b32 a46, v214\n"
"v_accvgpr_write_b32 a47, v215\n"
"v_pk_mul_f32 v[124:125], v[124:125], s[50:51] op_sel_hi:[1,0]\n"
"v_pk_mul_f32 v[126:127], v[126:127], s[50:51] op_sel_hi:[1,0]\n"
"v_pk_mul_f32 v[144:145], v[144:145], s[50:51] op_sel_hi:[1,0]\n"
"v_pk_mul_f32 v[146:147], v[146:147], s[50:51] op_sel_hi:[1,0]\n"
"v_cvt_pk_bf16_f32 v212, v124, v125\n"
"v_cvt_pk_bf16_f32 v213, v126, v127\n"
"v_cvt_pk_bf16_f32 v214, v144, v145\n"
"v_cvt_pk_bf16_f32 v215, v146, v147\n"
"v_accvgpr_write_b32 a64, v212\n"
"v_accvgpr_write_b32 a65, v213\n"
"v_accvgpr_write_b32 a66, v214\n"
"v_accvgpr_write_b32 a67, v215\n"
"v_mfma_f32_16x16x32_bf16 v[164:167], a[108:111], a[60:63], v[164:167]\n"
"v_mul_f32 v8, s50, v8\n"
"v_mul_f32 v9, s50, v9\n"
"v_mul_f32 v10, s50, v10\n"
"v_mfma_f32_16x16x32_bf16 v[168:171], a[80:83], a[4:7], 0\n"
"v_mul_f32 v11, s50, v11\n"
"v_mul_f32 v28, s50, v28\n"
"v_mul_f32 v29, s50, v29\n"
"v_mfma_f32_16x16x32_bf16 v[172:175], a[96:99], a[4:7], 0\n"
"v_mul_f32 v30, s50, v30\n"
"v_mul_f32 v31, s50, v31\n"
"v_cvt_pk_bf16_f32 v212, v8, v9\n"
"v_mfma_f32_16x16x32_bf16 v[168:171], a[84:87], a[24:27], v[168:171]\n"
"s_waitcnt vmcnt(14)\n"
"s_barrier\n"
"v_cvt_pk_bf16_f32 v213, v10, v11\n"
"v_cvt_pk_bf16_f32 v214, v28, v29\n"
"v_cvt_pk_bf16_f32 v215, v30, v31\n"
"v_mfma_f32_16x16x32_bf16 v[172:175], a[100:103], a[24:27], v[172:175]\n"
"ds_read_b64_tr_b16 a[112:113], v241 offset:36992\n"
"v_accvgpr_write_b32 a8, v212\n"
"v_accvgpr_write_b32 a9, v213\n"
"v_accvgpr_write_b32 a10, v214\n"
"v_mfma_f32_16x16x32_bf16 v[168:171], a[88:91], a[44:47], v[168:171]\n"
"ds_read_b64_tr_b16 a[114:115], v241 offset:38016\n"
"v_accvgpr_write_b32 a11, v215\n"
"v_mul_f32 v48, s50, v48\n"
"v_mul_f32 v49, s50, v49\n"
"v_mfma_f32_16x16x32_bf16 v[172:175], a[104:107], a[44:47], v[172:175]\n"
"ds_read_b64_tr_b16 a[116:117], v242 offset:36992\n"
"v_mul_f32 v50, s50, v50\n"
"v_mul_f32 v51, s50, v51\n"
"v_mul_f32 v68, s50, v68\n"
"v_mfma_f32_16x16x32_bf16 v[168:171], a[92:95], a[64:67], v[168:171]\n"
"v_mul_f32 v69, s50, v69\n"
"v_mul_f32 v70, s50, v70\n"
"v_mul_f32 v71, s50, v71\n"
"v_cvt_pk_bf16_f32 v212, v48, v49\n"
"v_cvt_pk_bf16_f32 v213, v50, v51\n"
"v_cvt_pk_bf16_f32 v214, v68, v69\n"
"v_cvt_pk_bf16_f32 v215, v70, v71\n"
"v_accvgpr_write_b32 a28, v212\n"
"v_accvgpr_write_b32 a29, v213\n"
"v_accvgpr_write_b32 a30, v214\n"
"v_accvgpr_write_b32 a31, v215\n"
"v_pk_mul_f32 v[88:89], v[88:89], s[50:51] op_sel_hi:[1,0]\n"
"v_pk_mul_f32 v[90:91], v[90:91], s[50:51] op_sel_hi:[1,0]\n"
"v_pk_mul_f32 v[108:109], v[108:109], s[50:51] op_sel_hi:[1,0]\n"
"v_pk_mul_f32 v[110:111], v[110:111], s[50:51] op_sel_hi:[1,0]\n"
"v_cvt_pk_bf16_f32 v212, v88, v89\n"
"v_cvt_pk_bf16_f32 v213, v90, v91\n"
"v_cvt_pk_bf16_f32 v214, v108, v109\n"
"v_cvt_pk_bf16_f32 v215, v110, v111\n"
"v_accvgpr_write_b32 a48, v212\n"
"v_accvgpr_write_b32 a49, v213\n"
"v_accvgpr_write_b32 a50, v214\n"
"v_accvgpr_write_b32 a51, v215\n"
"v_pk_mul_f32 v[128:129], v[128:129], s[50:51] op_sel_hi:[1,0]\n"
"v_pk_mul_f32 v[130:131], v[130:131], s[50:51] op_sel_hi:[1,0]\n"
"v_pk_mul_f32 v[148:149], v[148:149], s[50:51] op_sel_hi:[1,0]\n"
"v_pk_mul_f32 v[150:151], v[150:151], s[50:51] op_sel_hi:[1,0]\n"
"v_cvt_pk_bf16_f32 v212, v128, v129\n"
"v_cvt_pk_bf16_f32 v213, v130, v131\n"
"v_cvt_pk_bf16_f32 v214, v148, v149\n"
"v_cvt_pk_bf16_f32 v215, v150, v151\n"
"v_accvgpr_write_b32 a68, v212\n"
"v_accvgpr_write_b32 a69, v213\n"
"v_accvgpr_write_b32 a70, v214\n"
"v_accvgpr_write_b32 a71, v215\n"
"v_mfma_f32_16x16x32_bf16 v[172:175], a[108:111], a[64:67], v[172:175]\n"
"ds_read_b64_tr_b16 a[118:119], v242 offset:38016\n"
"v_mul_f32 v12, s50, v12\n"
"v_mul_f32 v13, s50, v13\n"
"v_mul_f32 v14, s50, v14\n"
"v_mfma_f32_16x16x32_bf16 v[176:179], a[80:83], a[8:11], 0\n"
"ds_read_b64_tr_b16 a[120:121], v241 offset:37248\n"
"v_mul_f32 v15, s50, v15\n"
"v_mul_f32 v32, s50, v32\n"
"v_mul_f32 v33, s50, v33\n"
"v_mfma_f32_16x16x32_bf16 v[180:183], a[96:99], a[8:11], 0\n"
"ds_read_b64_tr_b16 a[122:123], v241 offset:38272\n"
"v_mul_f32 v34, s50, v34\n"
"v_mul_f32 v35, s50, v35\n"
"v_cvt_pk_bf16_f32 v212, v12, v13\n"
"v_mfma_f32_16x16x32_bf16 v[176:179], a[84:87], a[28:31], v[176:179]\n"
"ds_read_b64_tr_b16 a[124:125], v242 offset:37248\n"
"v_cvt_pk_bf16_f32 v213, v14, v15\n"
"v_cvt_pk_bf16_f32 v214, v32, v33\n"
"v_cvt_pk_bf16_f32 v215, v34, v35\n"
"v_mfma_f32_16x16x32_bf16 v[180:183], a[100:103], a[28:31], v[180:183]\n"
"ds_read_b64_tr_b16 a[126:127], v242 offset:38272\n"
"v_accvgpr_write_b32 a12, v212\n"
"v_accvgpr_write_b32 a13, v213\n"
"v_accvgpr_write_b32 a14, v214\n"
"v_mfma_f32_16x16x32_bf16 v[176:179], a[88:91], a[48:51], v[176:179]\n"
"ds_read_b64_tr_b16 a[128:129], v241 offset:37504\n"
"v_accvgpr_write_b32 a15, v215\n"
"v_mul_f32 v52, s50, v52\n"
"v_mul_f32 v53, s50, v53\n"
"v_mfma_f32_16x16x32_bf16 v[180:183], a[104:107], a[48:51], v[180:183]\n"
"ds_read_b64_tr_b16 a[130:131], v241 offset:38528\n"
"v_mul_f32 v54, s50, v54\n"
"v_mul_f32 v55, s50, v55\n"
"v_mul_f32 v72, s50, v72\n"
"v_mfma_f32_16x16x32_bf16 v[176:179], a[92:95], a[68:71], v[176:179]\n"
"v_mul_f32 v73, s50, v73\n"
"v_mul_f32 v74, s50, v74\n"
"v_mul_f32 v75, s50, v75\n"
"v_cvt_pk_bf16_f32 v212, v52, v53\n"
"v_cvt_pk_bf16_f32 v213, v54, v55\n"
"v_cvt_pk_bf16_f32 v214, v72, v73\n"
"v_cvt_pk_bf16_f32 v215, v74, v75\n"
"v_accvgpr_write_b32 a32, v212\n"
"v_accvgpr_write_b32 a33, v213\n"
"v_accvgpr_write_b32 a34, v214\n"
"v_accvgpr_write_b32 a35, v215\n"
"v_pk_mul_f32 v[92:93], v[92:93], s[50:51] op_sel_hi:[1,0]\n"
"v_pk_mul_f32 v[94:95], v[94:95], s[50:51] op_sel_hi:[1,0]\n"
"v_pk_mul_f32 v[112:113], v[112:113], s[50:51] op_sel_hi:[1,0]\n"
"v_pk_mul_f32 v[114:115], v[114:115], s[50:51] op_sel_hi:[1,0]\n"
"v_cvt_pk_bf16_f32 v212, v92, v93\n"
"v_cvt_pk_bf16_f32 v213, v94, v95\n"
"v_cvt_pk_bf16_f32 v214, v112, v113\n"
"v_cvt_pk_bf16_f32 v215, v114, v115\n"
"v_accvgpr_write_b32 a52, v212\n"
"v_accvgpr_write_b32 a53, v213\n"
"v_accvgpr_write_b32 a54, v214\n"
"v_accvgpr_write_b32 a55, v215\n"
"v_pk_mul_f32 v[132:133], v[132:133], s[50:51] op_sel_hi:[1,0]\n"
"v_pk_mul_f32 v[134:135], v[134:135], s[50:51] op_sel_hi:[1,0]\n"
"v_pk_mul_f32 v[152:153], v[152:153], s[50:51] op_sel_hi:[1,0]\n"
"v_pk_mul_f32 v[154:155], v[154:155], s[50:51] op_sel_hi:[1,0]\n"
"v_cvt_pk_bf16_f32 v212, v132, v133\n"
"v_cvt_pk_bf16_f32 v213, v134, v135\n"
"v_cvt_pk_bf16_f32 v214, v152, v153\n"
"v_cvt_pk_bf16_f32 v215, v154, v155\n"
"v_accvgpr_write_b32 a72, v212\n"
"v_accvgpr_write_b32 a73, v213\n"
"v_accvgpr_write_b32 a74, v214\n"
"v_accvgpr_write_b32 a75, v215\n"
"v_mfma_f32_16x16x32_bf16 v[180:183], a[108:111], a[68:71], v[180:183]\n"
"ds_read_b64_tr_b16 a[132:133], v242 offset:37504\n"
"v_mul_f32 v16, s50, v16\n"
"v_mul_f32 v17, s50, v17\n"
"v_mul_f32 v18, s50, v18\n"
"v_mfma_f32_16x16x32_bf16 v[184:187], a[80:83], a[12:15], 0\n"
"ds_read_b64_tr_b16 a[134:135], v242 offset:38528\n"
"v_mul_f32 v19, s50, v19\n"
"v_mul_f32 v36, s50, v36\n"
"v_mul_f32 v37, s50, v37\n"
"v_mfma_f32_16x16x32_bf16 v[188:191], a[96:99], a[12:15], 0\n"
"ds_read_b64_tr_b16 a[136:137], v241 offset:37760\n"
"v_mul_f32 v38, s50, v38\n"
"v_mul_f32 v39, s50, v39\n"
"v_cvt_pk_bf16_f32 v212, v16, v17\n"
"v_mfma_f32_16x16x32_bf16 v[184:187], a[84:87], a[32:35], v[184:187]\n"
"ds_read_b64_tr_b16 a[138:139], v241 offset:38784\n"
"v_cvt_pk_bf16_f32 v213, v18, v19\n"
"v_cvt_pk_bf16_f32 v214, v36, v37\n"
"v_cvt_pk_bf16_f32 v215, v38, v39\n"
"v_mfma_f32_16x16x32_bf16 v[188:191], a[100:103], a[32:35], v[188:191]\n"
"ds_read_b64_tr_b16 a[140:141], v242 offset:37760\n"
"v_accvgpr_write_b32 a16, v212\n"
"v_accvgpr_write_b32 a17, v213\n"
"v_accvgpr_write_b32 a18, v214\n"
"v_mfma_f32_16x16x32_bf16 v[184:187], a[88:91], a[52:55], v[184:187]\n"
"ds_read_b64_tr_b16 a[142:143], v242 offset:38784\n"
"v_accvgpr_write_b32 a19, v215\n"
"v_mul_f32 v56, s50, v56\n"
"v_mul_f32 v57, s50, v57\n"
"v_mfma_f32_16x16x32_bf16 v[188:191], a[104:107], a[52:55], v[188:191]\n"
"s_add_u32 m0, s48, 8192\n"
"s_add_u32 s95, s95, 8192\n"
"v_mul_f32 v58, s50, v58\n"
"v_mul_f32 v59, s50, v59\n"
"v_mul_f32 v76, s50, v76\n"
"v_mfma_f32_16x16x32_bf16 v[184:187], a[92:95], a[72:75], v[184:187]\n"
"v_mul_f32 v77, s50, v77\n"
"v_mul_f32 v78, s50, v78\n"
"v_mul_f32 v79, s50, v79\n"
"v_cvt_pk_bf16_f32 v212, v56, v57\n"
"v_cvt_pk_bf16_f32 v213, v58, v59\n"
"v_cvt_pk_bf16_f32 v214, v76, v77\n"
"v_cvt_pk_bf16_f32 v215, v78, v79\n"
"v_accvgpr_write_b32 a36, v212\n"
"v_accvgpr_write_b32 a37, v213\n"
"v_accvgpr_write_b32 a38, v214\n"
"v_accvgpr_write_b32 a39, v215\n"
"v_pk_mul_f32 v[96:97], v[96:97], s[50:51] op_sel_hi:[1,0]\n"
"v_pk_mul_f32 v[98:99], v[98:99], s[50:51] op_sel_hi:[1,0]\n"
"v_pk_mul_f32 v[116:117], v[116:117], s[50:51] op_sel_hi:[1,0]\n"
"v_pk_mul_f32 v[118:119], v[118:119], s[50:51] op_sel_hi:[1,0]\n"
"v_cvt_pk_bf16_f32 v212, v96, v97\n"
"v_cvt_pk_bf16_f32 v213, v98, v99\n"
"v_cvt_pk_bf16_f32 v214, v116, v117\n"
"v_cvt_pk_bf16_f32 v215, v118, v119\n"
"v_accvgpr_write_b32 a56, v212\n"
"v_accvgpr_write_b32 a57, v213\n"
"v_accvgpr_write_b32 a58, v214\n"
"v_accvgpr_write_b32 a59, v215\n"
"v_pk_mul_f32 v[136:137], v[136:137], s[50:51] op_sel_hi:[1,0]\n"
"v_pk_mul_f32 v[138:139], v[138:139], s[50:51] op_sel_hi:[1,0]\n"
"v_pk_mul_f32 v[156:157], v[156:157], s[50:51] op_sel_hi:[1,0]\n"
"v_pk_mul_f32 v[158:159], v[158:159], s[50:51] op_sel_hi:[1,0]\n"
"v_cvt_pk_bf16_f32 v212, v136, v137\n"
"v_cvt_pk_bf16_f32 v213, v138, v139\n"
"v_cvt_pk_bf16_f32 v214, v156, v157\n"
"v_cvt_pk_bf16_f32 v215, v158, v159\n"
"v_accvgpr_write_b32 a76, v212\n"
"v_accvgpr_write_b32 a77, v213\n"
"v_accvgpr_write_b32 a78, v214\n"
"v_accvgpr_write_b32 a79, v215\n"
"v_mfma_f32_16x16x32_bf16 v[188:191], a[108:111], a[72:75], v[188:191]\n"
"buffer_load_dwordx4 v244, s[60:63], s52 offen lds\n"
"v_max3_f32 v220, v160, v161, v162\n"
"v_max3_f32 v221, v163, v164, v165\n"
"v_max3_f32 v220, v220, v166, v167\n"
"v_mfma_f32_16x16x32_bf16 v[192:195], a[80:83], a[16:19], 0\n"
"buffer_load_dwordx4 v244, s[60:63], s52 offen offset:1024 lds\n"
"v_max_f32 v220, v220, v221\n"
"v_mov_b32 v221, v220\n"
"s_nop 0\n"
"s_nop 0\n"
"v_permlane32_swap_b32 v220, v221\n"
"v_mfma_f32_16x16x32_bf16 v[196:199], a[96:99], a[16:19], 0\n"
"s_and_b32 s95, s95, 131071\n"
"v_max_f32 v220, v220, v221\n"
"v_mov_b32 v221, v220\n"
"s_nop 0\n"
"s_nop 0\n"
"v_permlane16_swap_b32 v220, v221\n"
"v_mfma_f32_16x16x32_bf16 v[192:195], a[84:87], a[36:39], v[192:195]\n"
"ds_read_b128 a[80:83], v240 offset:20608\n"
"v_max_f32 v221, v220, v221\n"
"v_sub_f32 v220, 0, v221\n"
"v_add_f32 v160, v160, v220\n"
"v_mfma_f32_16x16x32_bf16 v[196:199], a[100:103], a[36:39], v[196:199]\n"
"ds_read_b128 a[96:99], v240 offset:21632\n"
"v_add_f32 v161, v161, v220\n"
"v_add_f32 v162, v162, v220\n"
"v_add_f32 v163, v163, v220\n"
"v_mfma_f32_16x16x32_bf16 v[192:195], a[88:91], a[56:59], v[192:195]\n"
"ds_read_b128 a[84:87], v240 offset:20864\n"
"v_add_f32 v164, v164, v220\n"
"v_add_f32 v165, v165, v220\n"
"v_add_f32 v166, v166, v220\n"
"v_mfma_f32_16x16x32_bf16 v[196:199], a[104:107], a[56:59], v[196:199]\n"
"v_add_f32 v167, v167, v220\n"
"v_mov_b32 v221, v220\n"
"v_mov_b32 v222, v220\n"
"v_mov_b32 v223, v220\n"
"v_exp_f32 v160, v160\n"
"v_exp_f32 v161, v161\n"
"v_exp_f32 v162, v162\n"
"v_exp_f32 v163, v163\n"
"v_cvt_pk_bf16_f32 v200, v160, v161\n"
"v_exp_f32 v164, v164\n"
"v_exp_f32 v165, v165\n"
"v_cvt_pk_bf16_f32 v201, v162, v163\n"
"v_exp_f32 v166, v166\n"
"v_exp_f32 v167, v167\n"
"v_cvt_pk_bf16_f32 v202, v164, v165\n"
"v_cvt_pk_bf16_f32 v203, v166, v167\n"
"v_mfma_f32_16x16x32_bf16 v[192:195], a[92:95], a[76:79], v[192:195]\n"
"ds_read_b128 a[100:103], v240 offset:21888\n"
"ds_read_b128 a[88:91], v240 offset:21120\n"
"v_max3_f32 v224, v168, v169, v170\n"
"v_mfma_f32_16x16x32_bf16 v[196:199], a[108:111], a[76:79], v[196:199]\n"
"s_waitcnt lgkmcnt(5)\n"
"ds_read_b128 a[104:107], v240 offset:22144\n"
"ds_read_b128 a[92:95], v240 offset:21376\n"
"v_mfma_f32_16x16x32_bf16 v[0:3], a[112:115], v[200:203], 0\n"
"ds_read_b128 a[108:111], v240 offset:22400\n"
"v_max3_f32 v225, v171, v172, v173\n"
"v_max3_f32 v224, v224, v174, v175\n"
"v_max_f32 v224, v224, v225\n"
"v_mfma_f32_16x16x32_bf16 v[20:23], a[116:119], v[200:203], 0\n"
"v_mov_b32 v225, v224\n"
"s_nop 0\n"
"s_nop 0\n"
"v_permlane32_swap_b32 v224, v225\n"
"v_max_f32 v224, v224, v225\n"
"v_mfma_f32_16x16x32_bf16 v[40:43], a[120:123], v[200:203], 0\n"
"v_mov_b32 v225, v224\n"
"s_nop 0\n"
"s_nop 0\n"
"v_permlane16_swap_b32 v224, v225\n"
"v_max_f32 v225, v224, v225\n"
"v_mfma_f32_16x16x32_bf16 v[60:63], a[124:127], v[200:203], 0\n"
"v_sub_f32 v224, 0, v225\n"
"v_add_f32 v168, v168, v224\n"
"v_add_f32 v169, v169, v224\n"
"v_mfma_f32_16x16x32_bf16 v[80:83], a[128:131], v[200:203], 0\n"
"v_add_f32 v170, v170, v224\n"
"v_add_f32 v171, v171, v224\n"
"v_add_f32 v172, v172, v224\n"
"v_mfma_f32_16x16x32_bf16 v[100:103], a[132:135], v[200:203], 0\n"
"v_add_f32 v173, v173, v224\n"
"v_add_f32 v174, v174, v224\n"
"v_add_f32 v175, v175, v224\n"
"v_mfma_f32_16x16x32_bf16 v[120:123], a[136:139], v[200:203], 0\n"
"v_mov_b32 v225, v224\n"
"v_mov_b32 v226, v224\n"
"v_mov_b32 v227, v224\n"
"v_exp_f32 v168, v168\n"
"v_exp_f32 v169, v169\n"
"v_exp_f32 v170, v170\n"
"v_exp_f32 v171, v171\n"
"v_cvt_pk_bf16_f32 v204, v168, v169\n"
"v_exp_f32 v172, v172\n"
"v_exp_f32 v173, v173\n"
"v_cvt_pk_bf16_f32 v205, v170, v171\n"
"v_exp_f32 v174, v174\n"
"v_exp_f32 v175, v175\n"
"v_cvt_pk_bf16_f32 v206, v172, v173\n"
"v_cvt_pk_bf16_f32 v207, v174, v175\n"
"v_mfma_f32_16x16x32_bf16 v[140:143], a[140:143], v[200:203], 0\n"
"v_max3_f32 v228, v176, v177, v178\n"
"v_max3_f32 v229, v179, v180, v181\n"
"v_max3_f32 v228, v228, v182, v183\n"
"v_mfma_f32_16x16x32_bf16 a[144:147], a[164:167], v[200:203], 0\n"
"s_waitcnt lgkmcnt(4)\n"
"v_max_f32 v228, v228, v229\n"
"v_mov_b32 v229, v228\n"
"s_nop 0\n"
"s_nop 0\n"
"v_permlane32_swap_b32 v228, v229\n"
"v_mfma_f32_16x16x32_bf16 v[160:163], a[80:83], a[0:3], v[220:223]\n"
"v_max_f32 v228, v228, v229\n"
"v_mov_b32 v229, v228\n"
"s_nop 0\n"
"s_nop 0\n"
"v_permlane16_swap_b32 v228, v229\n"
"v_mfma_f32_16x16x32_bf16 v[4:7], a[112:115], v[204:207], 0\n"
"s_add_u32 m0, s48, 16384\n"
"s_add_u32 s52, s81, 131072\n"
"v_max_f32 v229, v228, v229\n"
"v_sub_f32 v228, 0, v229\n"
"v_add_f32 v176, v176, v228\n"
"v_mfma_f32_16x16x32_bf16 v[164:167], a[96:99], a[0:3], v[220:223]\n"
"buffer_load_dwordx4 v243, s[40:43], s81 offen nt lds\n"
"v_add_f32 v177, v177, v228\n"
"v_add_f32 v178, v178, v228\n"
"v_add_f32 v179, v179, v228\n"
"v_mfma_f32_16x16x32_bf16 v[24:27], a[116:119], v[204:207], 0\n"
"buffer_load_dwordx4 v243, s[40:43], s81 offen offset:1024 nt lds\n"
"v_add_f32 v180, v180, v228\n"
"v_add_f32 v181, v181, v228\n"
"v_add_f32 v182, v182, v228\n"
"v_mfma_f32_16x16x32_bf16 v[160:163], a[84:87], a[20:23], v[160:163]\n"
"v_add_f32 v183, v183, v228\n"
"v_mov_b32 v229, v228\n"
"v_mov_b32 v230, v228\n"
"v_mfma_f32_16x16x32_bf16 v[44:47], a[120:123], v[204:207], 0\n"
"v_mov_b32 v231, v228\n"
"v_exp_f32 v176, v176\n"
"v_mfma_f32_16x16x32_bf16 v[164:167], a[100:103], a[20:23], v[164:167]\n"
"v_exp_f32 v177, v177\n"
"v_exp_f32 v178, v178\n"
"v_mfma_f32_16x16x32_bf16 v[64:67], a[124:127], v[204:207], 0\n"
"s_waitcnt lgkmcnt(0)\n"
"v_exp_f32 v179, v179\n"
"v_cvt_pk_bf16_f32 v208, v176, v177\n"
"v_mfma_f32_16x16x32_bf16 v[160:163], a[88:91], a[40:43], v[160:163]\n"
"v_exp_f32 v180, v180\n"
"v_exp_f32 v181, v181\n"
"v_mfma_f32_16x16x32_bf16 v[84:87], a[128:131], v[204:207], 0\n"
"v_cvt_pk_bf16_f32 v209, v178, v179\n"
"v_exp_f32 v182, v182\n"
"v_mfma_f32_16x16x32_bf16 v[164:167], a[104:107], a[40:43], v[164:167]\n"
"v_exp_f32 v183, v183\n"
"v_cvt_pk_bf16_f32 v210, v180, v181\n"
"v_cvt_pk_bf16_f32 v211, v182, v183\n"
"v_mfma_f32_16x16x32_bf16 v[104:107], a[132:135], v[204:207], 0\n"
"v_max3_f32 v232, v184, v185, v186\n"
"v_max3_f32 v233, v187, v188, v189\n"
"v_max3_f32 v232, v232, v190, v191\n"
"v_mfma_f32_16x16x32_bf16 v[160:163], a[92:95], a[60:63], v[160:163]\n"
"v_max_f32 v232, v232, v233\n"
"v_mov_b32 v233, v232\n"
"s_nop 0\n"
"s_nop 0\n"
"v_permlane32_swap_b32 v232, v233\n"
"v_mfma_f32_16x16x32_bf16 v[124:127], a[136:139], v[204:207], 0\n"
"v_max_f32 v232, v232, v233\n"
"v_mov_b32 v233, v232\n"
"s_nop 0\n"
"s_nop 0\n"
"v_permlane16_swap_b32 v232, v233\n"
"v_mfma_f32_16x16x32_bf16 v[164:167], a[108:111], a[60:63], v[164:167]\n"
"v_max_f32 v233, v232, v233\n"
"v_sub_f32 v232, 0, v233\n"
"v_add_f32 v184, v184, v232\n"
"v_mfma_f32_16x16x32_bf16 v[144:147], a[140:143], v[204:207], 0\n"
"s_add_u32 m0, s48, 24576\n"
"s_add_u32 s81, s81, 8192\n"
"v_add_f32 v185, v185, v232\n"
"v_add_f32 v186, v186, v232\n"
"v_add_f32 v187, v187, v232\n"
"v_mfma_f32_16x16x32_bf16 a[148:151], a[164:167], v[204:207], 0\n"
"buffer_load_dwordx4 v243, s[40:43], s52 offen nt lds\n"
"v_add_f32 v188, v188, v232\n"
"v_add_f32 v189, v189, v232\n"
"v_add_f32 v190, v190, v232\n"
"v_mfma_f32_16x16x32_bf16 v[168:171], a[80:83], a[4:7], v[224:227]\n"
"buffer_load_dwordx4 v243, s[40:43], s52 offen offset:1024 nt lds\n"
"v_add_f32 v191, v191, v232\n"
"v_mov_b32 v233, v232\n"
"v_mov_b32 v234, v232\n"
"v_mfma_f32_16x16x32_bf16 v[8:11], a[112:115], v[208:211], 0\n"
"s_and_b32 s81, s81, 131071\n"
"v_mov_b32 v235, v232\n"
"v_exp_f32 v184, v184\n"
"v_mfma_f32_16x16x32_bf16 v[172:175], a[96:99], a[4:7], v[224:227]\n"
"v_exp_f32 v185, v185\n"
"v_exp_f32 v186, v186\n"
"v_mfma_f32_16x16x32_bf16 v[28:31], a[116:119], v[208:211], 0\n"
"v_exp_f32 v187, v187\n"
"v_cvt_pk_bf16_f32 v212, v184, v185\n"
"v_mfma_f32_16x16x32_bf16 v[168:171], a[84:87], a[24:27], v[168:171]\n"
"v_exp_f32 v188, v188\n"
"v_exp_f32 v189, v189\n"
"v_mfma_f32_16x16x32_bf16 v[48:51], a[120:123], v[208:211], 0\n"
"v_cvt_pk_bf16_f32 v213, v186, v187\n"
"v_exp_f32 v190, v190\n"
"v_mfma_f32_16x16x32_bf16 v[172:175], a[100:103], a[24:27], v[172:175]\n"
"v_exp_f32 v191, v191\n"
"v_cvt_pk_bf16_f32 v214, v188, v189\n"
"v_cvt_pk_bf16_f32 v215, v190, v191\n"
"v_mfma_f32_16x16x32_bf16 v[68:71], a[124:127], v[208:211], 0\n"
"v_max3_f32 v236, v192, v193, v194\n"
"v_max3_f32 v237, v195, v196, v197\n"
"v_max3_f32 v236, v236, v198, v199\n"
"v_mfma_f32_16x16x32_bf16 v[168:171], a[88:91], a[44:47], v[168:171]\n"
"v_max_f32 v236, v236, v237\n"
"v_mov_b32 v237, v236\n"
"s_nop 0\n"
"s_nop 0\n"
"v_permlane32_swap_b32 v236, v237\n"
"v_mfma_f32_16x16x32_bf16 v[88:91], a[128:131], v[208:211], 0\n"
"v_max_f32 v236, v236, v237\n"
"v_mov_b32 v237, v236\n"
"s_nop 0\n"
"s_nop 0\n"
"v_permlane16_swap_b32 v236, v237\n"
"v_mfma_f32_16x16x32_bf16 v[172:175], a[104:107], a[44:47], v[172:175]\n"
"v_max_f32 v237, v236, v237\n"
"v_sub_f32 v236, 0, v237\n"
"v_add_f32 v192, v192, v236\n"
"v_mfma_f32_16x16x32_bf16 v[108:111], a[132:135], v[208:211], 0\n"
"v_add_f32 v193, v193, v236\n"
"v_add_f32 v194, v194, v236\n"
"v_add_f32 v195, v195, v236\n"
"v_mfma_f32_16x16x32_bf16 v[168:171], a[92:95], a[64:67], v[168:171]\n"
"s_add_u32 m0, s48, 32768\n"
"s_add_u32 s52, s95, 131072\n"
"v_add_f32 v196, v196, v236\n"
"v_add_f32 v197, v197, v236\n"
"v_add_f32 v198, v198, v236\n"
"v_mfma_f32_16x16x32_bf16 v[128:131], a[136:139], v[208:211], 0\n"
"buffer_load_dwordx4 v244, s[60:63], s95 offen lds\n"
"v_add_f32 v199, v199, v236\n"
"v_mov_b32 v237, v236\n"
"v_mov_b32 v238, v236\n"
"v_mfma_f32_16x16x32_bf16 v[172:175], a[108:111], a[64:67], v[172:175]\n"
"buffer_load_dwordx4 v244, s[60:63], s95 offen offset:1024 lds\n"
"v_mov_b32 v239, v236\n"
"v_exp_f32 v192, v192\n"
"v_mfma_f32_16x16x32_bf16 v[148:151], a[140:143], v[208:211], 0\n"
"v_exp_f32 v193, v193\n"
"v_exp_f32 v194, v194\n"
"v_mfma_f32_16x16x32_bf16 a[152:155], a[164:167], v[208:211], 0\n"
"v_exp_f32 v195, v195\n"
"v_cvt_pk_bf16_f32 v216, v192, v193\n"
"v_mfma_f32_16x16x32_bf16 v[176:179], a[80:83], a[8:11], v[228:231]\n"
"v_exp_f32 v196, v196\n"
"v_exp_f32 v197, v197\n"
"v_mfma_f32_16x16x32_bf16 v[12:15], a[112:115], v[212:215], 0\n"
"v_cvt_pk_bf16_f32 v217, v194, v195\n"
"v_exp_f32 v198, v198\n"
"v_mfma_f32_16x16x32_bf16 v[180:183], a[96:99], a[8:11], v[228:231]\n"
"v_exp_f32 v199, v199\n"
"v_cvt_pk_bf16_f32 v218, v196, v197\n"
"v_cvt_pk_bf16_f32 v219, v198, v199\n"
"v_mfma_f32_16x16x32_bf16 v[32:35], a[116:119], v[212:215], 0\n"
"v_exp_f32 v160, v160\n"
"v_exp_f32 v161, v161\n"
"v_mfma_f32_16x16x32_bf16 v[176:179], a[84:87], a[28:31], v[176:179]\n"
"v_exp_f32 v162, v162\n"
"v_exp_f32 v163, v163\n"
"v_mfma_f32_16x16x32_bf16 v[52:55], a[120:123], v[212:215], 0\n"
"v_cvt_pk_bf16_f32 v200, v160, v161\n"
"v_exp_f32 v164, v164\n"
"v_mfma_f32_16x16x32_bf16 v[180:183], a[100:103], a[28:31], v[180:183]\n"
"v_exp_f32 v165, v165\n"
"v_cvt_pk_bf16_f32 v201, v162, v163\n"
"v_mfma_f32_16x16x32_bf16 v[72:75], a[124:127], v[212:215], 0\n"
"v_exp_f32 v166, v166\n"
"v_exp_f32 v167, v167\n"
"v_mfma_f32_16x16x32_bf16 v[176:179], a[88:91], a[48:51], v[176:179]\n"
"v_cvt_pk_bf16_f32 v202, v164, v165\n"
"v_cvt_pk_bf16_f32 v203, v166, v167\n"
"v_mfma_f32_16x16x32_bf16 v[92:95], a[128:131], v[212:215], 0\n"
"s_add_u32 m0, s48, 40960\n"
"s_add_u32 s95, s95, 8192\n"
"buffer_load_dwordx4 v244, s[60:63], s52 offen lds\n"
"v_mfma_f32_16x16x32_bf16 v[180:183], a[104:107], a[48:51], v[180:183]\n"
"buffer_load_dwordx4 v244, s[60:63], s52 offen offset:1024 lds\n"
"s_and_b32 s95, s95, 131071\n"
"v_mfma_f32_16x16x32_bf16 v[112:115], a[132:135], v[212:215], 0\n"
"v_add_u32 v241, 49152, v241\n"
"v_add_u32 v242, 49152, v242\n"
"v_mfma_f32_16x16x32_bf16 v[176:179], a[92:95], a[68:71], v[176:179]\n"
"v_mfma_f32_16x16x32_bf16 v[132:135], a[136:139], v[212:215], 0\n"
"v_mfma_f32_16x16x32_bf16 v[180:183], a[108:111], a[68:71], v[180:183]\n"
"v_mfma_f32_16x16x32_bf16 v[152:155], a[140:143], v[212:215], 0\n"
"v_mfma_f32_16x16x32_bf16 a[156:159], a[164:167], v[212:215], 0\n"
"s_waitcnt vmcnt(16) lgkmcnt(0)\n"
"s_barrier\n"
"v_mfma_f32_16x16x32_bf16 v[184:187], a[80:83], a[12:15], v[232:235]\n"
"v_mfma_f32_16x16x32_bf16 v[16:19], a[112:115], v[216:219], 0\n"
"ds_read_b64_tr_b16 a[112:113], v241 offset:24832\n"
"v_mfma_f32_16x16x32_bf16 v[188:191], a[96:99], a[12:15], v[232:235]\n"
"ds_read_b64_tr_b16 a[114:115], v241 offset:25856\n"
"v_mfma_f32_16x16x32_bf16 v[36:39], a[116:119], v[216:219], 0\n"
"ds_read_b64_tr_b16 a[116:117], v242 offset:24832\n"
"v_mfma_f32_16x16x32_bf16 v[184:187], a[84:87], a[32:35], v[184:187]\n"
"ds_read_b64_tr_b16 a[118:119], v242 offset:25856\n"
"v_mfma_f32_16x16x32_bf16 v[56:59], a[120:123], v[216:219], 0\n"
"ds_read_b64_tr_b16 a[120:121], v241 offset:25088\n"
"v_mfma_f32_16x16x32_bf16 v[188:191], a[100:103], a[32:35], v[188:191]\n"
"ds_read_b64_tr_b16 a[122:123], v241 offset:26112\n"
"v_mfma_f32_16x16x32_bf16 v[76:79], a[124:127], v[216:219], 0\n"
"ds_read_b64_tr_b16 a[124:125], v242 offset:25088\n"
"v_mfma_f32_16x16x32_bf16 v[184:187], a[88:91], a[52:55], v[184:187]\n"
"ds_read_b64_tr_b16 a[126:127], v242 offset:26112\n"
"v_mfma_f32_16x16x32_bf16 v[96:99], a[128:131], v[216:219], 0\n"
"ds_read_b64_tr_b16 a[128:129], v241 offset:25344\n"
"v_mfma_f32_16x16x32_bf16 v[188:191], a[104:107], a[52:55], v[188:191]\n"
"ds_read_b64_tr_b16 a[130:131], v241 offset:26368\n"
"v_mfma_f32_16x16x32_bf16 v[116:119], a[132:135], v[216:219], 0\n"
"ds_read_b64_tr_b16 a[132:133], v242 offset:25344\n"
"v_mfma_f32_16x16x32_bf16 v[184:187], a[92:95], a[72:75], v[184:187]\n"
"ds_read_b64_tr_b16 a[134:135], v242 offset:26368\n"
"v_mfma_f32_16x16x32_bf16 v[136:139], a[136:139], v[216:219], 0\n"
"ds_read_b64_tr_b16 a[136:137], v241 offset:25600\n"
"v_mfma_f32_16x16x32_bf16 v[188:191], a[108:111], a[72:75], v[188:191]\n"
"ds_read_b64_tr_b16 a[138:139], v241 offset:26624\n"
"v_mfma_f32_16x16x32_bf16 v[156:159], a[140:143], v[216:219], 0\n"
"ds_read_b64_tr_b16 a[140:141], v242 offset:25600\n"
"v_mfma_f32_16x16x32_bf16 a[160:163], a[164:167], v[216:219], 0\n"
"ds_read_b64_tr_b16 a[142:143], v242 offset:26624\n"
"L_ac_%=:\n"
"v_add_u32 v240, 49152, v240\n"
"v_mfma_f32_16x16x32_bf16 v[192:195], a[80:83], a[16:19], v[236:239]\n"
"s_waitcnt lgkmcnt(12)\n"
"ds_read_b128 a[80:83], v240 offset:8448\n"
"v_mfma_f32_16x16x32_bf16 v[0:3], a[112:115], v[200:203], v[0:3]\n"
"v_exp_f32 v168, v168\n"
"v_mfma_f32_16x16x32_bf16 v[196:199], a[96:99], a[16:19], v[236:239]\n"
"ds_read_b128 a[96:99], v240 offset:9472\n"
"v_mfma_f32_16x16x32_bf16 v[20:23], a[116:119], v[200:203], v[20:23]\n"
"v_exp_f32 v169, v169\n"
"v_mfma_f32_16x16x32_bf16 v[192:195], a[84:87], a[36:39], v[192:195]\n"
"s_waitcnt lgkmcnt(10)\n"
"ds_read_b128 a[84:87], v240 offset:8704\n"
"v_mfma_f32_16x16x32_bf16 v[40:43], a[120:123], v[200:203], v[40:43]\n"
"v_exp_f32 v170, v170\n"
"v_mfma_f32_16x16x32_bf16 v[196:199], a[100:103], a[36:39], v[196:199]\n"
"ds_read_b128 a[100:103], v240 offset:9728\n"
"v_mfma_f32_16x16x32_bf16 v[60:63], a[124:127], v[200:203], v[60:63]\n"
"v_exp_f32 v171, v171\n"
"v_mfma_f32_16x16x32_bf16 v[192:195], a[88:91], a[56:59], v[192:195]\n"
"s_waitcnt lgkmcnt(8)\n"
"ds_read_b128 a[88:91], v240 offset:8960\n"
"v_mfma_f32_16x16x32_bf16 v[80:83], a[128:131], v[200:203], v[80:83]\n"
"v_cvt_pk_bf16_f32 v204, v168, v169\n"
"v_mfma_f32_16x16x32_bf16 v[196:199], a[104:107], a[56:59], v[196:199]\n"
"ds_read_b128 a[104:107], v240 offset:9984\n"
"s_add_u32 m0, s48, 53376\n"
"v_mfma_f32_16x16x32_bf16 v[100:103], a[132:135], v[200:203], v[100:103]\n"
"s_add_u32 s52, s81, 131072\n"
"v_exp_f32 v172, v172\n"
"v_mfma_f32_16x16x32_bf16 v[192:195], a[92:95], a[76:79], v[192:195]\n"
"s_waitcnt lgkmcnt(6)\n"
"v_exp_f32 v173, v173\n"
"v_mfma_f32_16x16x32_bf16 v[120:123], a[136:139], v[200:203], v[120:123]\n"
"ds_read_b128 a[92:95], v240 offset:9216\n"
"v_cvt_pk_bf16_f32 v205, v170, v171\n"
"v_mfma_f32_16x16x32_bf16 v[196:199], a[108:111], a[76:79], v[196:199]\n"
"v_exp_f32 v174, v174\n"
"v_exp_f32 v175, v175\n"
"v_cvt_pk_bf16_f32 v206, v172, v173\n"
"v_cvt_pk_bf16_f32 v207, v174, v175\n"
"v_mfma_f32_16x16x32_bf16 v[140:143], a[140:143], v[200:203], v[140:143]\n"
"ds_read_b128 a[108:111], v240 offset:10240\n"
"v_mfma_f32_16x16x32_bf16 a[144:147], a[164:167], v[200:203], a[144:147]\n"
"s_waitcnt lgkmcnt(4)\n"
"v_exp_f32 v176, v176\n"
"v_mfma_f32_16x16x32_bf16 v[160:163], a[80:83], a[0:3], v[220:223]\n"
"v_exp_f32 v177, v177\n"
"v_mfma_f32_16x16x32_bf16 v[4:7], a[112:115], v[204:207], v[4:7]\n"
"v_exp_f32 v178, v178\n"
"v_mfma_f32_16x16x32_bf16 v[164:167], a[96:99], a[0:3], v[220:223]\n"
"v_exp_f32 v179, v179\n"
"v_mfma_f32_16x16x32_bf16 v[24:27], a[116:119], v[204:207], v[24:27]\n"
"buffer_load_dwordx4 v243, s[40:43], s81 offen nt lds\n"
"v_cvt_pk_bf16_f32 v208, v176, v177\n"
"v_mfma_f32_16x16x32_bf16 v[160:163], a[84:87], a[20:23], v[160:163]\n"
"v_exp_f32 v180, v180\n"
"v_mfma_f32_16x16x32_bf16 v[44:47], a[120:123], v[204:207], v[44:47]\n"
"v_exp_f32 v181, v181\n"
"v_mfma_f32_16x16x32_bf16 v[164:167], a[100:103], a[20:23], v[164:167]\n"
"buffer_load_dwordx4 v243, s[40:43], s81 offen offset:1024 nt lds\n"
"v_cvt_pk_bf16_f32 v209, v178, v179\n"
"v_mfma_f32_16x16x32_bf16 v[64:67], a[124:127], v[204:207], v[64:67]\n"
"s_waitcnt lgkmcnt(0)\n"
"v_exp_f32 v182, v182\n"
"v_mfma_f32_16x16x32_bf16 v[160:163], a[88:91], a[40:43], v[160:163]\n"
"s_add_u32 m0, s48, 61568\n"
"v_exp_f32 v183, v183\n"
"v_mfma_f32_16x16x32_bf16 v[84:87], a[128:131], v[204:207], v[84:87]\n"
"s_add_u32 s81, s81, 8192\n"
"v_cvt_pk_bf16_f32 v210, v180, v181\n"
"v_cvt_pk_bf16_f32 v211, v182, v183\n"
"v_mfma_f32_16x16x32_bf16 v[164:167], a[104:107], a[40:43], v[164:167]\n"
"v_exp_f32 v184, v184\n"
"v_mfma_f32_16x16x32_bf16 v[104:107], a[132:135], v[204:207], v[104:107]\n"
"v_exp_f32 v185, v185\n"
"v_mfma_f32_16x16x32_bf16 v[160:163], a[92:95], a[60:63], v[160:163]\n"
"v_exp_f32 v186, v186\n"
"v_mfma_f32_16x16x32_bf16 v[124:127], a[136:139], v[204:207], v[124:127]\n"
"v_exp_f32 v187, v187\n"
"v_mfma_f32_16x16x32_bf16 v[164:167], a[108:111], a[60:63], v[164:167]\n"
"buffer_load_dwordx4 v243, s[40:43], s52 offen nt lds\n"
"v_cvt_pk_bf16_f32 v212, v184, v185\n"
"v_mfma_f32_16x16x32_bf16 v[144:147], a[140:143], v[204:207], v[144:147]\n"
"v_exp_f32 v188, v188\n"
"v_mfma_f32_16x16x32_bf16 a[148:151], a[164:167], v[204:207], a[148:151]\n"
"v_exp_f32 v189, v189\n"
"v_mfma_f32_16x16x32_bf16 v[168:171], a[80:83], a[4:7], v[224:227]\n"
"buffer_load_dwordx4 v243, s[40:43], s52 offen offset:1024 nt lds\n"
"v_cvt_pk_bf16_f32 v213, v186, v187\n"
"v_mfma_f32_16x16x32_bf16 v[8:11], a[112:115], v[208:211], v[8:11]\n"
"s_and_b32 s81, s81, 131071\n"
"v_exp_f32 v190, v190\n"
"v_mfma_f32_16x16x32_bf16 v[172:175], a[96:99], a[4:7], v[224:227]\n"
"v_exp_f32 v191, v191\n"
"v_mfma_f32_16x16x32_bf16 v[28:31], a[116:119], v[208:211], v[28:31]\n"
"v_cvt_pk_bf16_f32 v214, v188, v189\n"
"v_cvt_pk_bf16_f32 v215, v190, v191\n"
"v_mfma_f32_16x16x32_bf16 v[168:171], a[84:87], a[24:27], v[168:171]\n"
"s_add_u32 m0, s48, 69760\n"
"v_exp_f32 v192, v192\n"
"v_mfma_f32_16x16x32_bf16 v[48:51], a[120:123], v[208:211], v[48:51]\n"
"s_add_u32 s52, s95, 131072\n"
"v_exp_f32 v193, v193\n"
"v_mfma_f32_16x16x32_bf16 v[172:175], a[100:103], a[24:27], v[172:175]\n"
"v_exp_f32 v194, v194\n"
"v_mfma_f32_16x16x32_bf16 v[68:71], a[124:127], v[208:211], v[68:71]\n"
"v_exp_f32 v195, v195\n"
"v_mfma_f32_16x16x32_bf16 v[168:171], a[88:91], a[44:47], v[168:171]\n"
"buffer_load_dwordx4 v244, s[60:63], s95 offen lds\n"
"v_cvt_pk_bf16_f32 v216, v192, v193\n"
"v_mfma_f32_16x16x32_bf16 v[88:91], a[128:131], v[208:211], v[88:91]\n"
"v_exp_f32 v196, v196\n"
"v_mfma_f32_16x16x32_bf16 v[172:175], a[104:107], a[44:47], v[172:175]\n"
"v_exp_f32 v197, v197\n"
"v_mfma_f32_16x16x32_bf16 v[108:111], a[132:135], v[208:211], v[108:111]\n"
"buffer_load_dwordx4 v244, s[60:63], s95 offen offset:1024 lds\n"
"v_cvt_pk_bf16_f32 v217, v194, v195\n"
"v_mfma_f32_16x16x32_bf16 v[168:171], a[92:95], a[64:67], v[168:171]\n"
"v_exp_f32 v198, v198\n"
"v_mfma_f32_16x16x32_bf16 v[128:131], a[136:139], v[208:211], v[128:131]\n"
"v_exp_f32 v199, v199\n"
"v_mfma_f32_16x16x32_bf16 v[172:175], a[108:111], a[64:67], v[172:175]\n"
"v_cvt_pk_bf16_f32 v218, v196, v197\n"
"v_cvt_pk_bf16_f32 v219, v198, v199\n"
"v_mfma_f32_16x16x32_bf16 v[148:151], a[140:143], v[208:211], v[148:151]\n"
"v_exp_f32 v160, v160\n"
"v_mfma_f32_16x16x32_bf16 a[152:155], a[164:167], v[208:211], a[152:155]\n"
"v_exp_f32 v161, v161\n"
"v_mfma_f32_16x16x32_bf16 v[176:179], a[80:83], a[8:11], v[228:231]\n"
"v_exp_f32 v162, v162\n"
"v_mfma_f32_16x16x32_bf16 v[12:15], a[112:115], v[212:215], v[12:15]\n"
"s_add_u32 m0, s48, 77952\n"
"v_exp_f32 v163, v163\n"
"v_mfma_f32_16x16x32_bf16 v[180:183], a[96:99], a[8:11], v[228:231]\n"
"s_add_u32 s95, s95, 8192\n"
"v_cvt_pk_bf16_f32 v200, v160, v161\n"
"v_mfma_f32_16x16x32_bf16 v[32:35], a[116:119], v[212:215], v[32:35]\n"
"v_exp_f32 v164, v164\n"
"v_mfma_f32_16x16x32_bf16 v[176:179], a[84:87], a[28:31], v[176:179]\n"
"v_exp_f32 v165, v165\n"
"v_mfma_f32_16x16x32_bf16 v[52:55], a[120:123], v[212:215], v[52:55]\n"
"buffer_load_dwordx4 v244, s[60:63], s52 offen lds\n"
"v_cvt_pk_bf16_f32 v201, v162, v163\n"
"v_mfma_f32_16x16x32_bf16 v[180:183], a[100:103], a[28:31], v[180:183]\n"
"v_exp_f32 v166, v166\n"
"v_mfma_f32_16x16x32_bf16 v[72:75], a[124:127], v[212:215], v[72:75]\n"
"v_exp_f32 v167, v167\n"
"v_mfma_f32_16x16x32_bf16 v[176:179], a[88:91], a[48:51], v[176:179]\n"
"v_cvt_pk_bf16_f32 v202, v164, v165\n"
"v_cvt_pk_bf16_f32 v203, v166, v167\n"
"v_mfma_f32_16x16x32_bf16 v[92:95], a[128:131], v[212:215], v[92:95]\n"
"buffer_load_dwordx4 v244, s[60:63], s52 offen offset:1024 lds\n"
"s_and_b32 s95, s95, 131071\n"
"v_mfma_f32_16x16x32_bf16 v[180:183], a[104:107], a[48:51], v[180:183]\n"
"v_mfma_f32_16x16x32_bf16 v[112:115], a[132:135], v[212:215], v[112:115]\n"
"v_add_u32 v241, 4294868992, v241\n"
"v_add_u32 v242, 4294868992, v242\n"
"v_mfma_f32_16x16x32_bf16 v[176:179], a[92:95], a[68:71], v[176:179]\n"
"v_mfma_f32_16x16x32_bf16 v[132:135], a[136:139], v[212:215], v[132:135]\n"
"v_mfma_f32_16x16x32_bf16 v[180:183], a[108:111], a[68:71], v[180:183]\n"
"v_mfma_f32_16x16x32_bf16 v[152:155], a[140:143], v[212:215], v[152:155]\n"
"v_mfma_f32_16x16x32_bf16 a[156:159], a[164:167], v[212:215], a[156:159]\n"
"s_waitcnt vmcnt(16) lgkmcnt(0)\n"
"s_barrier\n"
"v_mfma_f32_16x16x32_bf16 v[184:187], a[80:83], a[12:15], v[232:235]\n"
"v_mfma_f32_16x16x32_bf16 v[16:19], a[112:115], v[216:219], v[16:19]\n"
"ds_read_b64_tr_b16 a[112:113], v241 offset:0\n"
"v_mfma_f32_16x16x32_bf16 v[188:191], a[96:99], a[12:15], v[232:235]\n"
"ds_read_b64_tr_b16 a[114:115], v241 offset:1024\n"
"v_mfma_f32_16x16x32_bf16 v[36:39], a[116:119], v[216:219], v[36:39]\n"
"ds_read_b64_tr_b16 a[116:117], v242 offset:0\n"
"v_mfma_f32_16x16x32_bf16 v[184:187], a[84:87], a[32:35], v[184:187]\n"
"ds_read_b64_tr_b16 a[118:119], v242 offset:1024\n"
"v_mfma_f32_16x16x32_bf16 v[56:59], a[120:123], v[216:219], v[56:59]\n"
"ds_read_b64_tr_b16 a[120:121], v241 offset:256\n"
"v_mfma_f32_16x16x32_bf16 v[188:191], a[100:103], a[32:35], v[188:191]\n"
"ds_read_b64_tr_b16 a[122:123], v241 offset:1280\n"
"v_mfma_f32_16x16x32_bf16 v[76:79], a[124:127], v[216:219], v[76:79]\n"
"ds_read_b64_tr_b16 a[124:125], v242 offset:256\n"
"v_mfma_f32_16x16x32_bf16 v[184:187], a[88:91], a[52:55], v[184:187]\n"
"ds_read_b64_tr_b16 a[126:127], v242 offset:1280\n"
"v_mfma_f32_16x16x32_bf16 v[96:99], a[128:131], v[216:219], v[96:99]\n"
"ds_read_b64_tr_b16 a[128:129], v241 offset:512\n"
"v_mfma_f32_16x16x32_bf16 v[188:191], a[104:107], a[52:55], v[188:191]\n"
"ds_read_b64_tr_b16 a[130:131], v241 offset:1536\n"
"v_mfma_f32_16x16x32_bf16 v[116:119], a[132:135], v[216:219], v[116:119]\n"
"ds_read_b64_tr_b16 a[132:133], v242 offset:512\n"
"v_mfma_f32_16x16x32_bf16 v[184:187], a[92:95], a[72:75], v[184:187]\n"
"ds_read_b64_tr_b16 a[134:135], v242 offset:1536\n"
"v_mfma_f32_16x16x32_bf16 v[136:139], a[136:139], v[216:219], v[136:139]\n"
"ds_read_b64_tr_b16 a[136:137], v241 offset:768\n"
"v_mfma_f32_16x16x32_bf16 v[188:191], a[108:111], a[72:75], v[188:191]\n"
"ds_read_b64_tr_b16 a[138:139], v241 offset:1792\n"
"v_mfma_f32_16x16x32_bf16 v[156:159], a[140:143], v[216:219], v[156:159]\n"
"ds_read_b64_tr_b16 a[140:141], v242 offset:768\n"
"v_mfma_f32_16x16x32_bf16 a[160:163], a[164:167], v[216:219], a[160:163]\n"
"ds_read_b64_tr_b16 a[142:143], v242 offset:1792\n"
"v_mfma_f32_16x16x32_bf16 v[192:195], a[80:83], a[16:19], v[236:239]\n"
"s_waitcnt lgkmcnt(12)\n"
"ds_read_b128 a[80:83], v240 offset:41216\n"
"v_mfma_f32_16x16x32_bf16 v[0:3], a[112:115], v[200:203], v[0:3]\n"
"v_exp_f32 v168, v168\n"
"v_mfma_f32_16x16x32_bf16 v[196:199], a[96:99], a[16:19], v[236:239]\n"
"ds_read_b128 a[96:99], v240 offset:42240\n"
"v_mfma_f32_16x16x32_bf16 v[20:23], a[116:119], v[200:203], v[20:23]\n"
"v_exp_f32 v169, v169\n"
"v_mfma_f32_16x16x32_bf16 v[192:195], a[84:87], a[36:39], v[192:195]\n"
"s_waitcnt lgkmcnt(10)\n"
"ds_read_b128 a[84:87], v240 offset:41472\n"
"v_mfma_f32_16x16x32_bf16 v[40:43], a[120:123], v[200:203], v[40:43]\n"
"v_exp_f32 v170, v170\n"
"v_mfma_f32_16x16x32_bf16 v[196:199], a[100:103], a[36:39], v[196:199]\n"
"ds_read_b128 a[100:103], v240 offset:42496\n"
"v_mfma_f32_16x16x32_bf16 v[60:63], a[124:127], v[200:203], v[60:63]\n"
"v_exp_f32 v171, v171\n"
"v_mfma_f32_16x16x32_bf16 v[192:195], a[88:91], a[56:59], v[192:195]\n"
"s_waitcnt lgkmcnt(8)\n"
"ds_read_b128 a[88:91], v240 offset:41728\n"
"v_mfma_f32_16x16x32_bf16 v[80:83], a[128:131], v[200:203], v[80:83]\n"
"v_cvt_pk_bf16_f32 v204, v168, v169\n"
"v_mfma_f32_16x16x32_bf16 v[196:199], a[104:107], a[56:59], v[196:199]\n"
"ds_read_b128 a[104:107], v240 offset:42752\n"
"s_add_u32 m0, s48, 86144\n"
"v_mfma_f32_16x16x32_bf16 v[100:103], a[132:135], v[200:203], v[100:103]\n"
"s_add_u32 s52, s81, 131072\n"
"v_exp_f32 v172, v172\n"
"v_mfma_f32_16x16x32_bf16 v[192:195], a[92:95], a[76:79], v[192:195]\n"
"s_waitcnt lgkmcnt(6)\n"
"v_exp_f32 v173, v173\n"
"v_mfma_f32_16x16x32_bf16 v[120:123], a[136:139], v[200:203], v[120:123]\n"
"ds_read_b128 a[92:95], v240 offset:41984\n"
"v_cvt_pk_bf16_f32 v205, v170, v171\n"
"v_mfma_f32_16x16x32_bf16 v[196:199], a[108:111], a[76:79], v[196:199]\n"
"v_exp_f32 v174, v174\n"
"v_exp_f32 v175, v175\n"
"v_cvt_pk_bf16_f32 v206, v172, v173\n"
"v_cvt_pk_bf16_f32 v207, v174, v175\n"
"v_mfma_f32_16x16x32_bf16 v[140:143], a[140:143], v[200:203], v[140:143]\n"
"ds_read_b128 a[108:111], v240 offset:43008\n"
"v_mfma_f32_16x16x32_bf16 a[144:147], a[164:167], v[200:203], a[144:147]\n"
"s_waitcnt lgkmcnt(4)\n"
"v_exp_f32 v176, v176\n"
"v_mfma_f32_16x16x32_bf16 v[160:163], a[80:83], a[0:3], v[220:223]\n"
"v_exp_f32 v177, v177\n"
"v_mfma_f32_16x16x32_bf16 v[4:7], a[112:115], v[204:207], v[4:7]\n"
"v_exp_f32 v178, v178\n"
"v_mfma_f32_16x16x32_bf16 v[164:167], a[96:99], a[0:3], v[220:223]\n"
"v_exp_f32 v179, v179\n"
"v_mfma_f32_16x16x32_bf16 v[24:27], a[116:119], v[204:207], v[24:27]\n"
"buffer_load_dwordx4 v243, s[40:43], s81 offen nt lds\n"
"v_cvt_pk_bf16_f32 v208, v176, v177\n"
"v_mfma_f32_16x16x32_bf16 v[160:163], a[84:87], a[20:23], v[160:163]\n"
"v_exp_f32 v180, v180\n"
"v_mfma_f32_16x16x32_bf16 v[44:47], a[120:123], v[204:207], v[44:47]\n"
"v_exp_f32 v181, v181\n"
"v_mfma_f32_16x16x32_bf16 v[164:167], a[100:103], a[20:23], v[164:167]\n"
"buffer_load_dwordx4 v243, s[40:43], s81 offen offset:1024 nt lds\n"
"v_cvt_pk_bf16_f32 v209, v178, v179\n"
"v_mfma_f32_16x16x32_bf16 v[64:67], a[124:127], v[204:207], v[64:67]\n"
"s_waitcnt lgkmcnt(0)\n"
"v_exp_f32 v182, v182\n"
"v_mfma_f32_16x16x32_bf16 v[160:163], a[88:91], a[40:43], v[160:163]\n"
"s_add_u32 m0, s48, 94336\n"
"v_exp_f32 v183, v183\n"
"v_mfma_f32_16x16x32_bf16 v[84:87], a[128:131], v[204:207], v[84:87]\n"
"s_add_u32 s81, s81, 8192\n"
"v_cvt_pk_bf16_f32 v210, v180, v181\n"
"v_cvt_pk_bf16_f32 v211, v182, v183\n"
"v_mfma_f32_16x16x32_bf16 v[164:167], a[104:107], a[40:43], v[164:167]\n"
"v_exp_f32 v184, v184\n"
"v_mfma_f32_16x16x32_bf16 v[104:107], a[132:135], v[204:207], v[104:107]\n"
"v_exp_f32 v185, v185\n"
"v_mfma_f32_16x16x32_bf16 v[160:163], a[92:95], a[60:63], v[160:163]\n"
"v_exp_f32 v186, v186\n"
"v_mfma_f32_16x16x32_bf16 v[124:127], a[136:139], v[204:207], v[124:127]\n"
"v_exp_f32 v187, v187\n"
"v_mfma_f32_16x16x32_bf16 v[164:167], a[108:111], a[60:63], v[164:167]\n"
"buffer_load_dwordx4 v243, s[40:43], s52 offen nt lds\n"
"v_cvt_pk_bf16_f32 v212, v184, v185\n"
"v_mfma_f32_16x16x32_bf16 v[144:147], a[140:143], v[204:207], v[144:147]\n"
"v_exp_f32 v188, v188\n"
"v_mfma_f32_16x16x32_bf16 a[148:151], a[164:167], v[204:207], a[148:151]\n"
"v_exp_f32 v189, v189\n"
"v_mfma_f32_16x16x32_bf16 v[168:171], a[80:83], a[4:7], v[224:227]\n"
"buffer_load_dwordx4 v243, s[40:43], s52 offen offset:1024 nt lds\n"
"v_cvt_pk_bf16_f32 v213, v186, v187\n"
"v_mfma_f32_16x16x32_bf16 v[8:11], a[112:115], v[208:211], v[8:11]\n"
"s_and_b32 s81, s81, 131071\n"
"v_exp_f32 v190, v190\n"
"v_mfma_f32_16x16x32_bf16 v[172:175], a[96:99], a[4:7], v[224:227]\n"
"v_exp_f32 v191, v191\n"
"v_mfma_f32_16x16x32_bf16 v[28:31], a[116:119], v[208:211], v[28:31]\n"
"v_cvt_pk_bf16_f32 v214, v188, v189\n"
"v_cvt_pk_bf16_f32 v215, v190, v191\n"
"v_mfma_f32_16x16x32_bf16 v[168:171], a[84:87], a[24:27], v[168:171]\n"
"s_add_u32 m0, s48, 106752\n"
"v_exp_f32 v192, v192\n"
"v_mfma_f32_16x16x32_bf16 v[48:51], a[120:123], v[208:211], v[48:51]\n"
"s_add_u32 s52, s95, 131072\n"
"v_exp_f32 v193, v193\n"
"v_mfma_f32_16x16x32_bf16 v[172:175], a[100:103], a[24:27], v[172:175]\n"
"v_exp_f32 v194, v194\n"
"v_mfma_f32_16x16x32_bf16 v[68:71], a[124:127], v[208:211], v[68:71]\n"
"v_exp_f32 v195, v195\n"
"v_mfma_f32_16x16x32_bf16 v[168:171], a[88:91], a[44:47], v[168:171]\n"
"buffer_load_dwordx4 v244, s[60:63], s95 offen lds\n"
"v_cvt_pk_bf16_f32 v216, v192, v193\n"
"v_mfma_f32_16x16x32_bf16 v[88:91], a[128:131], v[208:211], v[88:91]\n"
"v_exp_f32 v196, v196\n"
"v_mfma_f32_16x16x32_bf16 v[172:175], a[104:107], a[44:47], v[172:175]\n"
"v_exp_f32 v197, v197\n"
"v_mfma_f32_16x16x32_bf16 v[108:111], a[132:135], v[208:211], v[108:111]\n"
"buffer_load_dwordx4 v244, s[60:63], s95 offen offset:1024 lds\n"
"v_cvt_pk_bf16_f32 v217, v194, v195\n"
"v_mfma_f32_16x16x32_bf16 v[168:171], a[92:95], a[64:67], v[168:171]\n"
"v_exp_f32 v198, v198\n"
"v_mfma_f32_16x16x32_bf16 v[128:131], a[136:139], v[208:211], v[128:131]\n"
"v_exp_f32 v199, v199\n"
"v_mfma_f32_16x16x32_bf16 v[172:175], a[108:111], a[64:67], v[172:175]\n"
"v_cvt_pk_bf16_f32 v218, v196, v197\n"
"v_cvt_pk_bf16_f32 v219, v198, v199\n"
"v_mfma_f32_16x16x32_bf16 v[148:151], a[140:143], v[208:211], v[148:151]\n"
"v_exp_f32 v160, v160\n"
"v_mfma_f32_16x16x32_bf16 a[152:155], a[164:167], v[208:211], a[152:155]\n"
"v_exp_f32 v161, v161\n"
"v_mfma_f32_16x16x32_bf16 v[176:179], a[80:83], a[8:11], v[228:231]\n"
"v_exp_f32 v162, v162\n"
"v_mfma_f32_16x16x32_bf16 v[12:15], a[112:115], v[212:215], v[12:15]\n"
"s_add_u32 m0, s48, 114944\n"
"v_exp_f32 v163, v163\n"
"v_mfma_f32_16x16x32_bf16 v[180:183], a[96:99], a[8:11], v[228:231]\n"
"s_add_u32 s95, s95, 8192\n"
"v_cvt_pk_bf16_f32 v200, v160, v161\n"
"v_mfma_f32_16x16x32_bf16 v[32:35], a[116:119], v[212:215], v[32:35]\n"
"v_exp_f32 v164, v164\n"
"v_mfma_f32_16x16x32_bf16 v[176:179], a[84:87], a[28:31], v[176:179]\n"
"v_exp_f32 v165, v165\n"
"v_mfma_f32_16x16x32_bf16 v[52:55], a[120:123], v[212:215], v[52:55]\n"
"buffer_load_dwordx4 v244, s[60:63], s52 offen lds\n"
"v_cvt_pk_bf16_f32 v201, v162, v163\n"
"v_mfma_f32_16x16x32_bf16 v[180:183], a[100:103], a[28:31], v[180:183]\n"
"v_exp_f32 v166, v166\n"
"v_mfma_f32_16x16x32_bf16 v[72:75], a[124:127], v[212:215], v[72:75]\n"
"v_exp_f32 v167, v167\n"
"v_mfma_f32_16x16x32_bf16 v[176:179], a[88:91], a[48:51], v[176:179]\n"
"v_cvt_pk_bf16_f32 v202, v164, v165\n"
"v_cvt_pk_bf16_f32 v203, v166, v167\n"
"v_mfma_f32_16x16x32_bf16 v[92:95], a[128:131], v[212:215], v[92:95]\n"
"buffer_load_dwordx4 v244, s[60:63], s52 offen offset:1024 lds\n"
"s_and_b32 s95, s95, 131071\n"
"v_mfma_f32_16x16x32_bf16 v[180:183], a[104:107], a[48:51], v[180:183]\n"
"v_mfma_f32_16x16x32_bf16 v[112:115], a[132:135], v[212:215], v[112:115]\n"
"v_mfma_f32_16x16x32_bf16 v[176:179], a[92:95], a[68:71], v[176:179]\n"
"v_mfma_f32_16x16x32_bf16 v[132:135], a[136:139], v[212:215], v[132:135]\n"
"v_mfma_f32_16x16x32_bf16 v[180:183], a[108:111], a[68:71], v[180:183]\n"
"v_mfma_f32_16x16x32_bf16 v[152:155], a[140:143], v[212:215], v[152:155]\n"
"v_mfma_f32_16x16x32_bf16 a[156:159], a[164:167], v[212:215], a[156:159]\n"
"s_waitcnt vmcnt(16) lgkmcnt(0)\n"
"s_barrier\n"
"v_mfma_f32_16x16x32_bf16 v[184:187], a[80:83], a[12:15], v[232:235]\n"
"v_mfma_f32_16x16x32_bf16 v[16:19], a[112:115], v[216:219], v[16:19]\n"
"ds_read_b64_tr_b16 a[112:113], v241 offset:32768\n"
"v_mfma_f32_16x16x32_bf16 v[188:191], a[96:99], a[12:15], v[232:235]\n"
"ds_read_b64_tr_b16 a[114:115], v241 offset:33792\n"
"v_mfma_f32_16x16x32_bf16 v[36:39], a[116:119], v[216:219], v[36:39]\n"
"ds_read_b64_tr_b16 a[116:117], v242 offset:32768\n"
"v_mfma_f32_16x16x32_bf16 v[184:187], a[84:87], a[32:35], v[184:187]\n"
"ds_read_b64_tr_b16 a[118:119], v242 offset:33792\n"
"v_mfma_f32_16x16x32_bf16 v[56:59], a[120:123], v[216:219], v[56:59]\n"
"ds_read_b64_tr_b16 a[120:121], v241 offset:33024\n"
"v_mfma_f32_16x16x32_bf16 v[188:191], a[100:103], a[32:35], v[188:191]\n"
"ds_read_b64_tr_b16 a[122:123], v241 offset:34048\n"
"v_mfma_f32_16x16x32_bf16 v[76:79], a[124:127], v[216:219], v[76:79]\n"
"ds_read_b64_tr_b16 a[124:125], v242 offset:33024\n"
"v_mfma_f32_16x16x32_bf16 v[184:187], a[88:91], a[52:55], v[184:187]\n"
"ds_read_b64_tr_b16 a[126:127], v242 offset:34048\n"
"v_mfma_f32_16x16x32_bf16 v[96:99], a[128:131], v[216:219], v[96:99]\n"
"ds_read_b64_tr_b16 a[128:129], v241 offset:33280\n"
"v_mfma_f32_16x16x32_bf16 v[188:191], a[104:107], a[52:55], v[188:191]\n"
"ds_read_b64_tr_b16 a[130:131], v241 offset:34304\n"
"v_mfma_f32_16x16x32_bf16 v[116:119], a[132:135], v[216:219], v[116:119]\n"
"ds_read_b64_tr_b16 a[132:133], v242 offset:33280\n"
"v_mfma_f32_16x16x32_bf16 v[184:187], a[92:95], a[72:75], v[184:187]\n"
"ds_read_b64_tr_b16 a[134:135], v242 offset:34304\n"
"v_mfma_f32_16x16x32_bf16 v[136:139], a[136:139], v[216:219], v[136:139]\n"
"ds_read_b64_tr_b16 a[136:137], v241 offset:33536\n"
"v_mfma_f32_16x16x32_bf16 v[188:191], a[108:111], a[72:75], v[188:191]\n"
"ds_read_b64_tr_b16 a[138:139], v241 offset:34560\n"
"v_mfma_f32_16x16x32_bf16 v[156:159], a[140:143], v[216:219], v[156:159]\n"
"ds_read_b64_tr_b16 a[140:141], v242 offset:33536\n"
"v_mfma_f32_16x16x32_bf16 a[160:163], a[164:167], v[216:219], a[160:163]\n"
"ds_read_b64_tr_b16 a[142:143], v242 offset:34560\n"
"v_add_u32 v240, 4294868992, v240\n"
"v_mfma_f32_16x16x32_bf16 v[192:195], a[80:83], a[16:19], v[236:239]\n"
"s_waitcnt lgkmcnt(12)\n"
"ds_read_b128 a[80:83], v240 offset:16384\n"
"v_mfma_f32_16x16x32_bf16 v[0:3], a[112:115], v[200:203], v[0:3]\n"
"v_exp_f32 v168, v168\n"
"v_mfma_f32_16x16x32_bf16 v[196:199], a[96:99], a[16:19], v[236:239]\n"
"ds_read_b128 a[96:99], v240 offset:17408\n"
"v_mfma_f32_16x16x32_bf16 v[20:23], a[116:119], v[200:203], v[20:23]\n"
"v_exp_f32 v169, v169\n"
"v_mfma_f32_16x16x32_bf16 v[192:195], a[84:87], a[36:39], v[192:195]\n"
"s_waitcnt lgkmcnt(10)\n"
"ds_read_b128 a[84:87], v240 offset:16640\n"
"v_mfma_f32_16x16x32_bf16 v[40:43], a[120:123], v[200:203], v[40:43]\n"
"v_exp_f32 v170, v170\n"
"v_mfma_f32_16x16x32_bf16 v[196:199], a[100:103], a[36:39], v[196:199]\n"
"ds_read_b128 a[100:103], v240 offset:17664\n"
"v_mfma_f32_16x16x32_bf16 v[60:63], a[124:127], v[200:203], v[60:63]\n"
"v_exp_f32 v171, v171\n"
"v_mfma_f32_16x16x32_bf16 v[192:195], a[88:91], a[56:59], v[192:195]\n"
"s_waitcnt lgkmcnt(8)\n"
"ds_read_b128 a[88:91], v240 offset:16896\n"
"v_mfma_f32_16x16x32_bf16 v[80:83], a[128:131], v[200:203], v[80:83]\n"
"v_cvt_pk_bf16_f32 v204, v168, v169\n"
"v_mfma_f32_16x16x32_bf16 v[196:199], a[104:107], a[56:59], v[196:199]\n"
"ds_read_b128 a[104:107], v240 offset:17920\n"
"s_add_u32 m0, s48, 123136\n"
"v_mfma_f32_16x16x32_bf16 v[100:103], a[132:135], v[200:203], v[100:103]\n"
"s_add_u32 s52, s81, 131072\n"
"v_exp_f32 v172, v172\n"
"v_mfma_f32_16x16x32_bf16 v[192:195], a[92:95], a[76:79], v[192:195]\n"
"s_waitcnt lgkmcnt(6)\n"
"v_exp_f32 v173, v173\n"
"v_mfma_f32_16x16x32_bf16 v[120:123], a[136:139], v[200:203], v[120:123]\n"
"ds_read_b128 a[92:95], v240 offset:17152\n"
"v_cvt_pk_bf16_f32 v205, v170, v171\n"
"v_mfma_f32_16x16x32_bf16 v[196:199], a[108:111], a[76:79], v[196:199]\n"
"v_exp_f32 v174, v174\n"
"v_exp_f32 v175, v175\n"
"v_cvt_pk_bf16_f32 v206, v172, v173\n"
"v_cvt_pk_bf16_f32 v207, v174, v175\n"
"v_mfma_f32_16x16x32_bf16 v[140:143], a[140:143], v[200:203], v[140:143]\n"
"ds_read_b128 a[108:111], v240 offset:18176\n"
"v_mfma_f32_16x16x32_bf16 a[144:147], a[164:167], v[200:203], a[144:147]\n"
"s_waitcnt lgkmcnt(4)\n"
"v_exp_f32 v176, v176\n"
"v_mfma_f32_16x16x32_bf16 v[160:163], a[80:83], a[0:3], v[220:223]\n"
"v_exp_f32 v177, v177\n"
"v_mfma_f32_16x16x32_bf16 v[4:7], a[112:115], v[204:207], v[4:7]\n"
"v_exp_f32 v178, v178\n"
"v_mfma_f32_16x16x32_bf16 v[164:167], a[96:99], a[0:3], v[220:223]\n"
"v_exp_f32 v179, v179\n"
"v_mfma_f32_16x16x32_bf16 v[24:27], a[116:119], v[204:207], v[24:27]\n"
"buffer_load_dwordx4 v243, s[40:43], s81 offen nt lds\n"
"v_cvt_pk_bf16_f32 v208, v176, v177\n"
"v_mfma_f32_16x16x32_bf16 v[160:163], a[84:87], a[20:23], v[160:163]\n"
"v_exp_f32 v180, v180\n"
"v_mfma_f32_16x16x32_bf16 v[44:47], a[120:123], v[204:207], v[44:47]\n"
"v_exp_f32 v181, v181\n"
"v_mfma_f32_16x16x32_bf16 v[164:167], a[100:103], a[20:23], v[164:167]\n"
"buffer_load_dwordx4 v243, s[40:43], s81 offen offset:1024 nt lds\n"
"v_cvt_pk_bf16_f32 v209, v178, v179\n"
"v_mfma_f32_16x16x32_bf16 v[64:67], a[124:127], v[204:207], v[64:67]\n"
"s_waitcnt lgkmcnt(0)\n"
"v_exp_f32 v182, v182\n"
"v_mfma_f32_16x16x32_bf16 v[160:163], a[88:91], a[40:43], v[160:163]\n"
"s_add_u32 m0, s48, 131328\n"
"v_exp_f32 v183, v183\n"
"v_mfma_f32_16x16x32_bf16 v[84:87], a[128:131], v[204:207], v[84:87]\n"
"s_add_u32 s81, s81, 8192\n"
"v_cvt_pk_bf16_f32 v210, v180, v181\n"
"v_cvt_pk_bf16_f32 v211, v182, v183\n"
"v_mfma_f32_16x16x32_bf16 v[164:167], a[104:107], a[40:43], v[164:167]\n"
"v_exp_f32 v184, v184\n"
"v_mfma_f32_16x16x32_bf16 v[104:107], a[132:135], v[204:207], v[104:107]\n"
"v_exp_f32 v185, v185\n"
"v_mfma_f32_16x16x32_bf16 v[160:163], a[92:95], a[60:63], v[160:163]\n"
"v_exp_f32 v186, v186\n"
"v_mfma_f32_16x16x32_bf16 v[124:127], a[136:139], v[204:207], v[124:127]\n"
"v_exp_f32 v187, v187\n"
"v_mfma_f32_16x16x32_bf16 v[164:167], a[108:111], a[60:63], v[164:167]\n"
"buffer_load_dwordx4 v243, s[40:43], s52 offen nt lds\n"
"v_cvt_pk_bf16_f32 v212, v184, v185\n"
"v_mfma_f32_16x16x32_bf16 v[144:147], a[140:143], v[204:207], v[144:147]\n"
"v_exp_f32 v188, v188\n"
"v_mfma_f32_16x16x32_bf16 a[148:151], a[164:167], v[204:207], a[148:151]\n"
"v_exp_f32 v189, v189\n"
"v_mfma_f32_16x16x32_bf16 v[168:171], a[80:83], a[4:7], v[224:227]\n"
"buffer_load_dwordx4 v243, s[40:43], s52 offen offset:1024 nt lds\n"
"v_cvt_pk_bf16_f32 v213, v186, v187\n"
"v_mfma_f32_16x16x32_bf16 v[8:11], a[112:115], v[208:211], v[8:11]\n"
"s_and_b32 s81, s81, 131071\n"
"v_exp_f32 v190, v190\n"
"v_mfma_f32_16x16x32_bf16 v[172:175], a[96:99], a[4:7], v[224:227]\n"
"v_exp_f32 v191, v191\n"
"v_mfma_f32_16x16x32_bf16 v[28:31], a[116:119], v[208:211], v[28:31]\n"
"v_cvt_pk_bf16_f32 v214, v188, v189\n"
"v_cvt_pk_bf16_f32 v215, v190, v191\n"
"v_mfma_f32_16x16x32_bf16 v[168:171], a[84:87], a[24:27], v[168:171]\n"
"s_add_u32 m0, s48, 139520\n"
"v_exp_f32 v192, v192\n"
"v_mfma_f32_16x16x32_bf16 v[48:51], a[120:123], v[208:211], v[48:51]\n"
"s_add_u32 s52, s95, 131072\n"
"v_exp_f32 v193, v193\n"
"v_mfma_f32_16x16x32_bf16 v[172:175], a[100:103], a[24:27], v[172:175]\n"
"v_exp_f32 v194, v194\n"
"v_mfma_f32_16x16x32_bf16 v[68:71], a[124:127], v[208:211], v[68:71]\n"
"v_exp_f32 v195, v195\n"
"v_mfma_f32_16x16x32_bf16 v[168:171], a[88:91], a[44:47], v[168:171]\n"
"buffer_load_dwordx4 v244, s[60:63], s95 offen lds\n"
"v_cvt_pk_bf16_f32 v216, v192, v193\n"
"v_mfma_f32_16x16x32_bf16 v[88:91], a[128:131], v[208:211], v[88:91]\n"
"v_exp_f32 v196, v196\n"
"v_mfma_f32_16x16x32_bf16 v[172:175], a[104:107], a[44:47], v[172:175]\n"
"v_exp_f32 v197, v197\n"
"v_mfma_f32_16x16x32_bf16 v[108:111], a[132:135], v[208:211], v[108:111]\n"
"buffer_load_dwordx4 v244, s[60:63], s95 offen offset:1024 lds\n"
"v_cvt_pk_bf16_f32 v217, v194, v195\n"
"v_mfma_f32_16x16x32_bf16 v[168:171], a[92:95], a[64:67], v[168:171]\n"
"v_exp_f32 v198, v198\n"
"v_mfma_f32_16x16x32_bf16 v[128:131], a[136:139], v[208:211], v[128:131]\n"
"v_exp_f32 v199, v199\n"
"v_mfma_f32_16x16x32_bf16 v[172:175], a[108:111], a[64:67], v[172:175]\n"
"v_cvt_pk_bf16_f32 v218, v196, v197\n"
"v_cvt_pk_bf16_f32 v219, v198, v199\n"
"v_mfma_f32_16x16x32_bf16 v[148:151], a[140:143], v[208:211], v[148:151]\n"
"v_exp_f32 v160, v160\n"
"v_mfma_f32_16x16x32_bf16 a[152:155], a[164:167], v[208:211], a[152:155]\n"
"v_exp_f32 v161, v161\n"
"v_mfma_f32_16x16x32_bf16 v[176:179], a[80:83], a[8:11], v[228:231]\n"
"v_exp_f32 v162, v162\n"
"v_mfma_f32_16x16x32_bf16 v[12:15], a[112:115], v[212:215], v[12:15]\n"
"s_add_u32 m0, s48, 147712\n"
"v_exp_f32 v163, v163\n"
"v_mfma_f32_16x16x32_bf16 v[180:183], a[96:99], a[8:11], v[228:231]\n"
"s_add_u32 s95, s95, 8192\n"
"v_cvt_pk_bf16_f32 v200, v160, v161\n"
"v_mfma_f32_16x16x32_bf16 v[32:35], a[116:119], v[212:215], v[32:35]\n"
"v_exp_f32 v164, v164\n"
"v_mfma_f32_16x16x32_bf16 v[176:179], a[84:87], a[28:31], v[176:179]\n"
"v_exp_f32 v165, v165\n"
"v_mfma_f32_16x16x32_bf16 v[52:55], a[120:123], v[212:215], v[52:55]\n"
"buffer_load_dwordx4 v244, s[60:63], s52 offen lds\n"
"v_cvt_pk_bf16_f32 v201, v162, v163\n"
"v_mfma_f32_16x16x32_bf16 v[180:183], a[100:103], a[28:31], v[180:183]\n"
"v_exp_f32 v166, v166\n"
"v_mfma_f32_16x16x32_bf16 v[72:75], a[124:127], v[212:215], v[72:75]\n"
"v_exp_f32 v167, v167\n"
"v_mfma_f32_16x16x32_bf16 v[176:179], a[88:91], a[48:51], v[176:179]\n"
"v_cvt_pk_bf16_f32 v202, v164, v165\n"
"v_cvt_pk_bf16_f32 v203, v166, v167\n"
"v_mfma_f32_16x16x32_bf16 v[92:95], a[128:131], v[212:215], v[92:95]\n"
"buffer_load_dwordx4 v244, s[60:63], s52 offen offset:1024 lds\n"
"s_and_b32 s95, s95, 131071\n"
"v_mfma_f32_16x16x32_bf16 v[180:183], a[104:107], a[48:51], v[180:183]\n"
"v_mfma_f32_16x16x32_bf16 v[112:115], a[132:135], v[212:215], v[112:115]\n"
"v_add_u32 v241, 49152, v241\n"
"v_add_u32 v242, 49152, v242\n"
"v_mfma_f32_16x16x32_bf16 v[176:179], a[92:95], a[68:71], v[176:179]\n"
"v_mfma_f32_16x16x32_bf16 v[132:135], a[136:139], v[212:215], v[132:135]\n"
"v_mfma_f32_16x16x32_bf16 v[180:183], a[108:111], a[68:71], v[180:183]\n"
"v_mfma_f32_16x16x32_bf16 v[152:155], a[140:143], v[212:215], v[152:155]\n"
"v_mfma_f32_16x16x32_bf16 a[156:159], a[164:167], v[212:215], a[156:159]\n"
"s_waitcnt vmcnt(16) lgkmcnt(0)\n"
"s_barrier\n"
"v_mfma_f32_16x16x32_bf16 v[184:187], a[80:83], a[12:15], v[232:235]\n"
"v_mfma_f32_16x16x32_bf16 v[16:19], a[112:115], v[216:219], v[16:19]\n"
"ds_read_b64_tr_b16 a[112:113], v241 offset:20608\n"
"v_mfma_f32_16x16x32_bf16 v[188:191], a[96:99], a[12:15], v[232:235]\n"
"ds_read_b64_tr_b16 a[114:115], v241 offset:21632\n"
"v_mfma_f32_16x16x32_bf16 v[36:39], a[116:119], v[216:219], v[36:39]\n"
"ds_read_b64_tr_b16 a[116:117], v242 offset:20608\n"
"v_mfma_f32_16x16x32_bf16 v[184:187], a[84:87], a[32:35], v[184:187]\n"
"ds_read_b64_tr_b16 a[118:119], v242 offset:21632\n"
"v_mfma_f32_16x16x32_bf16 v[56:59], a[120:123], v[216:219], v[56:59]\n"
"ds_read_b64_tr_b16 a[120:121], v241 offset:20864\n"
"v_mfma_f32_16x16x32_bf16 v[188:191], a[100:103], a[32:35], v[188:191]\n"
"ds_read_b64_tr_b16 a[122:123], v241 offset:21888\n"
"v_mfma_f32_16x16x32_bf16 v[76:79], a[124:127], v[216:219], v[76:79]\n"
"ds_read_b64_tr_b16 a[124:125], v242 offset:20864\n"
"v_mfma_f32_16x16x32_bf16 v[184:187], a[88:91], a[52:55], v[184:187]\n"
"ds_read_b64_tr_b16 a[126:127], v242 offset:21888\n"
"v_mfma_f32_16x16x32_bf16 v[96:99], a[128:131], v[216:219], v[96:99]\n"
"ds_read_b64_tr_b16 a[128:129], v241 offset:21120\n"
"v_mfma_f32_16x16x32_bf16 v[188:191], a[104:107], a[52:55], v[188:191]\n"
"ds_read_b64_tr_b16 a[130:131], v241 offset:22144\n"
"v_mfma_f32_16x16x32_bf16 v[116:119], a[132:135], v[216:219], v[116:119]\n"
"ds_read_b64_tr_b16 a[132:133], v242 offset:21120\n"
"v_mfma_f32_16x16x32_bf16 v[184:187], a[92:95], a[72:75], v[184:187]\n"
"ds_read_b64_tr_b16 a[134:135], v242 offset:22144\n"
"v_mfma_f32_16x16x32_bf16 v[136:139], a[136:139], v[216:219], v[136:139]\n"
"ds_read_b64_tr_b16 a[136:137], v241 offset:21376\n"
"v_mfma_f32_16x16x32_bf16 v[188:191], a[108:111], a[72:75], v[188:191]\n"
"ds_read_b64_tr_b16 a[138:139], v241 offset:22400\n"
"v_mfma_f32_16x16x32_bf16 v[156:159], a[140:143], v[216:219], v[156:159]\n"
"ds_read_b64_tr_b16 a[140:141], v242 offset:21376\n"
"v_mfma_f32_16x16x32_bf16 a[160:163], a[164:167], v[216:219], a[160:163]\n"
"ds_read_b64_tr_b16 a[142:143], v242 offset:22400\n"
"v_add_u32 v240, 49152, v240\n"
"v_mfma_f32_16x16x32_bf16 v[192:195], a[80:83], a[16:19], v[236:239]\n"
"s_waitcnt lgkmcnt(12)\n"
"ds_read_b128 a[80:83], v240 offset:4224\n"
"v_mfma_f32_16x16x32_bf16 v[0:3], a[112:115], v[200:203], v[0:3]\n"
"v_exp_f32 v168, v168\n"
"v_mfma_f32_16x16x32_bf16 v[196:199], a[96:99], a[16:19], v[236:239]\n"
"ds_read_b128 a[96:99], v240 offset:5248\n"
"v_mfma_f32_16x16x32_bf16 v[20:23], a[116:119], v[200:203], v[20:23]\n"
"v_exp_f32 v169, v169\n"
"v_mfma_f32_16x16x32_bf16 v[192:195], a[84:87], a[36:39], v[192:195]\n"
"s_waitcnt lgkmcnt(10)\n"
"ds_read_b128 a[84:87], v240 offset:4480\n"
"v_mfma_f32_16x16x32_bf16 v[40:43], a[120:123], v[200:203], v[40:43]\n"
"v_exp_f32 v170, v170\n"
"v_mfma_f32_16x16x32_bf16 v[196:199], a[100:103], a[36:39], v[196:199]\n"
"ds_read_b128 a[100:103], v240 offset:5504\n"
"v_mfma_f32_16x16x32_bf16 v[60:63], a[124:127], v[200:203], v[60:63]\n"
"v_exp_f32 v171, v171\n"
"v_mfma_f32_16x16x32_bf16 v[192:195], a[88:91], a[56:59], v[192:195]\n"
"s_waitcnt lgkmcnt(8)\n"
"ds_read_b128 a[88:91], v240 offset:4736\n"
"v_mfma_f32_16x16x32_bf16 v[80:83], a[128:131], v[200:203], v[80:83]\n"
"v_cvt_pk_bf16_f32 v204, v168, v169\n"
"v_mfma_f32_16x16x32_bf16 v[196:199], a[104:107], a[56:59], v[196:199]\n"
"ds_read_b128 a[104:107], v240 offset:5760\n"
"s_add_u32 m0, s48, 0\n"
"v_mfma_f32_16x16x32_bf16 v[100:103], a[132:135], v[200:203], v[100:103]\n"
"s_add_u32 s52, s81, 131072\n"
"v_exp_f32 v172, v172\n"
"v_mfma_f32_16x16x32_bf16 v[192:195], a[92:95], a[76:79], v[192:195]\n"
"s_waitcnt lgkmcnt(6)\n"
"v_exp_f32 v173, v173\n"
"v_mfma_f32_16x16x32_bf16 v[120:123], a[136:139], v[200:203], v[120:123]\n"
"ds_read_b128 a[92:95], v240 offset:4992\n"
"v_cvt_pk_bf16_f32 v205, v170, v171\n"
"v_mfma_f32_16x16x32_bf16 v[196:199], a[108:111], a[76:79], v[196:199]\n"
"v_exp_f32 v174, v174\n"
"v_exp_f32 v175, v175\n"
"v_cvt_pk_bf16_f32 v206, v172, v173\n"
"v_cvt_pk_bf16_f32 v207, v174, v175\n"
"v_mfma_f32_16x16x32_bf16 v[140:143], a[140:143], v[200:203], v[140:143]\n"
"ds_read_b128 a[108:111], v240 offset:6016\n"
"v_mfma_f32_16x16x32_bf16 a[144:147], a[164:167], v[200:203], a[144:147]\n"
"s_waitcnt lgkmcnt(4)\n"
"v_exp_f32 v176, v176\n"
"v_mfma_f32_16x16x32_bf16 v[160:163], a[80:83], a[0:3], v[220:223]\n"
"v_exp_f32 v177, v177\n"
"v_mfma_f32_16x16x32_bf16 v[4:7], a[112:115], v[204:207], v[4:7]\n"
"v_exp_f32 v178, v178\n"
"v_mfma_f32_16x16x32_bf16 v[164:167], a[96:99], a[0:3], v[220:223]\n"
"v_exp_f32 v179, v179\n"
"v_mfma_f32_16x16x32_bf16 v[24:27], a[116:119], v[204:207], v[24:27]\n"
"buffer_load_dwordx4 v243, s[40:43], s81 offen nt lds\n"
"v_cvt_pk_bf16_f32 v208, v176, v177\n"
"v_mfma_f32_16x16x32_bf16 v[160:163], a[84:87], a[20:23], v[160:163]\n"
"v_exp_f32 v180, v180\n"
"v_mfma_f32_16x16x32_bf16 v[44:47], a[120:123], v[204:207], v[44:47]\n"
"v_exp_f32 v181, v181\n"
"v_mfma_f32_16x16x32_bf16 v[164:167], a[100:103], a[20:23], v[164:167]\n"
"buffer_load_dwordx4 v243, s[40:43], s81 offen offset:1024 nt lds\n"
"v_cvt_pk_bf16_f32 v209, v178, v179\n"
"v_mfma_f32_16x16x32_bf16 v[64:67], a[124:127], v[204:207], v[64:67]\n"
"s_waitcnt lgkmcnt(0)\n"
"v_exp_f32 v182, v182\n"
"v_mfma_f32_16x16x32_bf16 v[160:163], a[88:91], a[40:43], v[160:163]\n"
"s_add_u32 m0, s48, 8192\n"
"v_exp_f32 v183, v183\n"
"v_mfma_f32_16x16x32_bf16 v[84:87], a[128:131], v[204:207], v[84:87]\n"
"s_add_u32 s81, s81, 8192\n"
"v_cvt_pk_bf16_f32 v210, v180, v181\n"
"v_cvt_pk_bf16_f32 v211, v182, v183\n"
"v_mfma_f32_16x16x32_bf16 v[164:167], a[104:107], a[40:43], v[164:167]\n"
"v_exp_f32 v184, v184\n"
"v_mfma_f32_16x16x32_bf16 v[104:107], a[132:135], v[204:207], v[104:107]\n"
"v_exp_f32 v185, v185\n"
"v_mfma_f32_16x16x32_bf16 v[160:163], a[92:95], a[60:63], v[160:163]\n"
"v_exp_f32 v186, v186\n"
"v_mfma_f32_16x16x32_bf16 v[124:127], a[136:139], v[204:207], v[124:127]\n"
"v_exp_f32 v187, v187\n"
"v_mfma_f32_16x16x32_bf16 v[164:167], a[108:111], a[60:63], v[164:167]\n"
"buffer_load_dwordx4 v243, s[40:43], s52 offen nt lds\n"
"v_cvt_pk_bf16_f32 v212, v184, v185\n"
"v_mfma_f32_16x16x32_bf16 v[144:147], a[140:143], v[204:207], v[144:147]\n"
"v_exp_f32 v188, v188\n"
"v_mfma_f32_16x16x32_bf16 a[148:151], a[164:167], v[204:207], a[148:151]\n"
"v_exp_f32 v189, v189\n"
"v_mfma_f32_16x16x32_bf16 v[168:171], a[80:83], a[4:7], v[224:227]\n"
"buffer_load_dwordx4 v243, s[40:43], s52 offen offset:1024 nt lds\n"
"v_cvt_pk_bf16_f32 v213, v186, v187\n"
"v_mfma_f32_16x16x32_bf16 v[8:11], a[112:115], v[208:211], v[8:11]\n"
"s_and_b32 s81, s81, 131071\n"
"v_exp_f32 v190, v190\n"
"v_mfma_f32_16x16x32_bf16 v[172:175], a[96:99], a[4:7], v[224:227]\n"
"v_exp_f32 v191, v191\n"
"v_mfma_f32_16x16x32_bf16 v[28:31], a[116:119], v[208:211], v[28:31]\n"
"v_cvt_pk_bf16_f32 v214, v188, v189\n"
"v_cvt_pk_bf16_f32 v215, v190, v191\n"
"v_mfma_f32_16x16x32_bf16 v[168:171], a[84:87], a[24:27], v[168:171]\n"
"s_add_u32 m0, s48, 16384\n"
"v_exp_f32 v192, v192\n"
"v_mfma_f32_16x16x32_bf16 v[48:51], a[120:123], v[208:211], v[48:51]\n"
"s_add_u32 s52, s95, 131072\n"
"v_exp_f32 v193, v193\n"
"v_mfma_f32_16x16x32_bf16 v[172:175], a[100:103], a[24:27], v[172:175]\n"
"v_exp_f32 v194, v194\n"
"v_mfma_f32_16x16x32_bf16 v[68:71], a[124:127], v[208:211], v[68:71]\n"
"v_exp_f32 v195, v195\n"
"v_mfma_f32_16x16x32_bf16 v[168:171], a[88:91], a[44:47], v[168:171]\n"
"buffer_load_dwordx4 v244, s[60:63], s95 offen lds\n"
"v_cvt_pk_bf16_f32 v216, v192, v193\n"
"v_mfma_f32_16x16x32_bf16 v[88:91], a[128:131], v[208:211], v[88:91]\n"
"v_exp_f32 v196, v196\n"
"v_mfma_f32_16x16x32_bf16 v[172:175], a[104:107], a[44:47], v[172:175]\n"
"v_exp_f32 v197, v197\n"
"v_mfma_f32_16x16x32_bf16 v[108:111], a[132:135], v[208:211], v[108:111]\n"
"buffer_load_dwordx4 v244, s[60:63], s95 offen offset:1024 lds\n"
"v_cvt_pk_bf16_f32 v217, v194, v195\n"
"v_mfma_f32_16x16x32_bf16 v[168:171], a[92:95], a[64:67], v[168:171]\n"
"v_exp_f32 v198, v198\n"
"v_mfma_f32_16x16x32_bf16 v[128:131], a[136:139], v[208:211], v[128:131]\n"
"v_exp_f32 v199, v199\n"
"v_mfma_f32_16x16x32_bf16 v[172:175], a[108:111], a[64:67], v[172:175]\n"
"v_cvt_pk_bf16_f32 v218, v196, v197\n"
"v_cvt_pk_bf16_f32 v219, v198, v199\n"
"v_mfma_f32_16x16x32_bf16 v[148:151], a[140:143], v[208:211], v[148:151]\n"
"v_exp_f32 v160, v160\n"
"v_mfma_f32_16x16x32_bf16 a[152:155], a[164:167], v[208:211], a[152:155]\n"
"v_exp_f32 v161, v161\n"
"v_mfma_f32_16x16x32_bf16 v[176:179], a[80:83], a[8:11], v[228:231]\n"
"v_exp_f32 v162, v162\n"
"v_mfma_f32_16x16x32_bf16 v[12:15], a[112:115], v[212:215], v[12:15]\n"
"s_add_u32 m0, s48, 24576\n"
"v_exp_f32 v163, v163\n"
"v_mfma_f32_16x16x32_bf16 v[180:183], a[96:99], a[8:11], v[228:231]\n"
"s_add_u32 s95, s95, 8192\n"
"v_cvt_pk_bf16_f32 v200, v160, v161\n"
"v_mfma_f32_16x16x32_bf16 v[32:35], a[116:119], v[212:215], v[32:35]\n"
"v_exp_f32 v164, v164\n"
"v_mfma_f32_16x16x32_bf16 v[176:179], a[84:87], a[28:31], v[176:179]\n"
"v_exp_f32 v165, v165\n"
"v_mfma_f32_16x16x32_bf16 v[52:55], a[120:123], v[212:215], v[52:55]\n"
"buffer_load_dwordx4 v244, s[60:63], s52 offen lds\n"
"v_cvt_pk_bf16_f32 v201, v162, v163\n"
"v_mfma_f32_16x16x32_bf16 v[180:183], a[100:103], a[28:31], v[180:183]\n"
"v_exp_f32 v166, v166\n"
"v_mfma_f32_16x16x32_bf16 v[72:75], a[124:127], v[212:215], v[72:75]\n"
"v_exp_f32 v167, v167\n"
"v_mfma_f32_16x16x32_bf16 v[176:179], a[88:91], a[48:51], v[176:179]\n"
"v_cvt_pk_bf16_f32 v202, v164, v165\n"
"v_cvt_pk_bf16_f32 v203, v166, v167\n"
"v_mfma_f32_16x16x32_bf16 v[92:95], a[128:131], v[212:215], v[92:95]\n"
"buffer_load_dwordx4 v244, s[60:63], s52 offen offset:1024 lds\n"
"s_and_b32 s95, s95, 131071\n"
"v_mfma_f32_16x16x32_bf16 v[180:183], a[104:107], a[48:51], v[180:183]\n"
"v_mfma_f32_16x16x32_bf16 v[112:115], a[132:135], v[212:215], v[112:115]\n"
"v_add_u32 v241, 49152, v241\n"
"v_add_u32 v242, 49152, v242\n"
"v_mfma_f32_16x16x32_bf16 v[176:179], a[92:95], a[68:71], v[176:179]\n"
"v_mfma_f32_16x16x32_bf16 v[132:135], a[136:139], v[212:215], v[132:135]\n"
"v_mfma_f32_16x16x32_bf16 v[180:183], a[108:111], a[68:71], v[180:183]\n"
"v_mfma_f32_16x16x32_bf16 v[152:155], a[140:143], v[212:215], v[152:155]\n"
"v_mfma_f32_16x16x32_bf16 a[156:159], a[164:167], v[212:215], a[156:159]\n"
"s_waitcnt vmcnt(16) lgkmcnt(0)\n"
"s_barrier\n"
"v_mfma_f32_16x16x32_bf16 v[184:187], a[80:83], a[12:15], v[232:235]\n"
"v_mfma_f32_16x16x32_bf16 v[16:19], a[112:115], v[216:219], v[16:19]\n"
"ds_read_b64_tr_b16 a[112:113], v241 offset:8448\n"
"v_mfma_f32_16x16x32_bf16 v[188:191], a[96:99], a[12:15], v[232:235]\n"
"ds_read_b64_tr_b16 a[114:115], v241 offset:9472\n"
"v_mfma_f32_16x16x32_bf16 v[36:39], a[116:119], v[216:219], v[36:39]\n"
"ds_read_b64_tr_b16 a[116:117], v242 offset:8448\n"
"v_mfma_f32_16x16x32_bf16 v[184:187], a[84:87], a[32:35], v[184:187]\n"
"ds_read_b64_tr_b16 a[118:119], v242 offset:9472\n"
"v_mfma_f32_16x16x32_bf16 v[56:59], a[120:123], v[216:219], v[56:59]\n"
"ds_read_b64_tr_b16 a[120:121], v241 offset:8704\n"
"v_mfma_f32_16x16x32_bf16 v[188:191], a[100:103], a[32:35], v[188:191]\n"
"ds_read_b64_tr_b16 a[122:123], v241 offset:9728\n"
"v_mfma_f32_16x16x32_bf16 v[76:79], a[124:127], v[216:219], v[76:79]\n"
"ds_read_b64_tr_b16 a[124:125], v242 offset:8704\n"
"v_mfma_f32_16x16x32_bf16 v[184:187], a[88:91], a[52:55], v[184:187]\n"
"ds_read_b64_tr_b16 a[126:127], v242 offset:9728\n"
"v_mfma_f32_16x16x32_bf16 v[96:99], a[128:131], v[216:219], v[96:99]\n"
"ds_read_b64_tr_b16 a[128:129], v241 offset:8960\n"
"v_mfma_f32_16x16x32_bf16 v[188:191], a[104:107], a[52:55], v[188:191]\n"
"ds_read_b64_tr_b16 a[130:131], v241 offset:9984\n"
"v_mfma_f32_16x16x32_bf16 v[116:119], a[132:135], v[216:219], v[116:119]\n"
"ds_read_b64_tr_b16 a[132:133], v242 offset:8960\n"
"v_mfma_f32_16x16x32_bf16 v[184:187], a[92:95], a[72:75], v[184:187]\n"
"ds_read_b64_tr_b16 a[134:135], v242 offset:9984\n"
"v_mfma_f32_16x16x32_bf16 v[136:139], a[136:139], v[216:219], v[136:139]\n"
"ds_read_b64_tr_b16 a[136:137], v241 offset:9216\n"
"v_mfma_f32_16x16x32_bf16 v[188:191], a[108:111], a[72:75], v[188:191]\n"
"ds_read_b64_tr_b16 a[138:139], v241 offset:10240\n"
"v_mfma_f32_16x16x32_bf16 v[156:159], a[140:143], v[216:219], v[156:159]\n"
"ds_read_b64_tr_b16 a[140:141], v242 offset:9216\n"
"v_mfma_f32_16x16x32_bf16 a[160:163], a[164:167], v[216:219], a[160:163]\n"
"ds_read_b64_tr_b16 a[142:143], v242 offset:10240\n"
"v_mfma_f32_16x16x32_bf16 v[192:195], a[80:83], a[16:19], v[236:239]\n"
"s_waitcnt lgkmcnt(12)\n"
"ds_read_b128 a[80:83], v240 offset:36992\n"
"v_mfma_f32_16x16x32_bf16 v[0:3], a[112:115], v[200:203], v[0:3]\n"
"v_exp_f32 v168, v168\n"
"v_mfma_f32_16x16x32_bf16 v[196:199], a[96:99], a[16:19], v[236:239]\n"
"ds_read_b128 a[96:99], v240 offset:38016\n"
"v_mfma_f32_16x16x32_bf16 v[20:23], a[116:119], v[200:203], v[20:23]\n"
"v_exp_f32 v169, v169\n"
"v_mfma_f32_16x16x32_bf16 v[192:195], a[84:87], a[36:39], v[192:195]\n"
"s_waitcnt lgkmcnt(10)\n"
"ds_read_b128 a[84:87], v240 offset:37248\n"
"v_mfma_f32_16x16x32_bf16 v[40:43], a[120:123], v[200:203], v[40:43]\n"
"v_exp_f32 v170, v170\n"
"v_mfma_f32_16x16x32_bf16 v[196:199], a[100:103], a[36:39], v[196:199]\n"
"ds_read_b128 a[100:103], v240 offset:38272\n"
"v_mfma_f32_16x16x32_bf16 v[60:63], a[124:127], v[200:203], v[60:63]\n"
"v_exp_f32 v171, v171\n"
"v_mfma_f32_16x16x32_bf16 v[192:195], a[88:91], a[56:59], v[192:195]\n"
"s_waitcnt lgkmcnt(8)\n"
"ds_read_b128 a[88:91], v240 offset:37504\n"
"v_mfma_f32_16x16x32_bf16 v[80:83], a[128:131], v[200:203], v[80:83]\n"
"v_cvt_pk_bf16_f32 v204, v168, v169\n"
"v_mfma_f32_16x16x32_bf16 v[196:199], a[104:107], a[56:59], v[196:199]\n"
"ds_read_b128 a[104:107], v240 offset:38528\n"
"s_add_u32 m0, s48, 32768\n"
"v_mfma_f32_16x16x32_bf16 v[100:103], a[132:135], v[200:203], v[100:103]\n"
"s_add_u32 s52, s81, 131072\n"
"v_exp_f32 v172, v172\n"
"v_mfma_f32_16x16x32_bf16 v[192:195], a[92:95], a[76:79], v[192:195]\n"
"s_waitcnt lgkmcnt(6)\n"
"v_exp_f32 v173, v173\n"
"v_mfma_f32_16x16x32_bf16 v[120:123], a[136:139], v[200:203], v[120:123]\n"
"ds_read_b128 a[92:95], v240 offset:37760\n"
"v_cvt_pk_bf16_f32 v205, v170, v171\n"
"v_mfma_f32_16x16x32_bf16 v[196:199], a[108:111], a[76:79], v[196:199]\n"
"v_exp_f32 v174, v174\n"
"v_exp_f32 v175, v175\n"
"v_cvt_pk_bf16_f32 v206, v172, v173\n"
"v_cvt_pk_bf16_f32 v207, v174, v175\n"
"v_mfma_f32_16x16x32_bf16 v[140:143], a[140:143], v[200:203], v[140:143]\n"
"ds_read_b128 a[108:111], v240 offset:38784\n"
"v_mfma_f32_16x16x32_bf16 a[144:147], a[164:167], v[200:203], a[144:147]\n"
"s_waitcnt lgkmcnt(4)\n"
"v_exp_f32 v176, v176\n"
"v_mfma_f32_16x16x32_bf16 v[160:163], a[80:83], a[0:3], v[220:223]\n"
"v_exp_f32 v177, v177\n"
"v_mfma_f32_16x16x32_bf16 v[4:7], a[112:115], v[204:207], v[4:7]\n"
"v_exp_f32 v178, v178\n"
"v_mfma_f32_16x16x32_bf16 v[164:167], a[96:99], a[0:3], v[220:223]\n"
"v_exp_f32 v179, v179\n"
"v_mfma_f32_16x16x32_bf16 v[24:27], a[116:119], v[204:207], v[24:27]\n"
"buffer_load_dwordx4 v243, s[40:43], s81 offen nt lds\n"
"v_cvt_pk_bf16_f32 v208, v176, v177\n"
"v_mfma_f32_16x16x32_bf16 v[160:163], a[84:87], a[20:23], v[160:163]\n"
"v_exp_f32 v180, v180\n"
"v_mfma_f32_16x16x32_bf16 v[44:47], a[120:123], v[204:207], v[44:47]\n"
"v_exp_f32 v181, v181\n"
"v_mfma_f32_16x16x32_bf16 v[164:167], a[100:103], a[20:23], v[164:167]\n"
"buffer_load_dwordx4 v243, s[40:43], s81 offen offset:1024 nt lds\n"
"v_cvt_pk_bf16_f32 v209, v178, v179\n"
"v_mfma_f32_16x16x32_bf16 v[64:67], a[124:127], v[204:207], v[64:67]\n"
"s_waitcnt lgkmcnt(0)\n"
"v_exp_f32 v182, v182\n"
"v_mfma_f32_16x16x32_bf16 v[160:163], a[88:91], a[40:43], v[160:163]\n"
"s_add_u32 m0, s48, 40960\n"
"v_exp_f32 v183, v183\n"
"v_mfma_f32_16x16x32_bf16 v[84:87], a[128:131], v[204:207], v[84:87]\n"
"s_add_u32 s81, s81, 8192\n"
"v_cvt_pk_bf16_f32 v210, v180, v181\n"
"v_cvt_pk_bf16_f32 v211, v182, v183\n"
"v_mfma_f32_16x16x32_bf16 v[164:167], a[104:107], a[40:43], v[164:167]\n"
"v_exp_f32 v184, v184\n"
"v_mfma_f32_16x16x32_bf16 v[104:107], a[132:135], v[204:207], v[104:107]\n"
"v_exp_f32 v185, v185\n"
"v_mfma_f32_16x16x32_bf16 v[160:163], a[92:95], a[60:63], v[160:163]\n"
"v_exp_f32 v186, v186\n"
"v_mfma_f32_16x16x32_bf16 v[124:127], a[136:139], v[204:207], v[124:127]\n"
"v_exp_f32 v187, v187\n"
"v_mfma_f32_16x16x32_bf16 v[164:167], a[108:111], a[60:63], v[164:167]\n"
"buffer_load_dwordx4 v243, s[40:43], s52 offen nt lds\n"
"v_cvt_pk_bf16_f32 v212, v184, v185\n"
"v_mfma_f32_16x16x32_bf16 v[144:147], a[140:143], v[204:207], v[144:147]\n"
"v_exp_f32 v188, v188\n"
"v_mfma_f32_16x16x32_bf16 a[148:151], a[164:167], v[204:207], a[148:151]\n"
"v_exp_f32 v189, v189\n"
"v_mfma_f32_16x16x32_bf16 v[168:171], a[80:83], a[4:7], v[224:227]\n"
"buffer_load_dwordx4 v243, s[40:43], s52 offen offset:1024 nt lds\n"
"v_cvt_pk_bf16_f32 v213, v186, v187\n"
"v_mfma_f32_16x16x32_bf16 v[8:11], a[112:115], v[208:211], v[8:11]\n"
"s_and_b32 s81, s81, 131071\n"
"v_exp_f32 v190, v190\n"
"v_mfma_f32_16x16x32_bf16 v[172:175], a[96:99], a[4:7], v[224:227]\n"
"v_exp_f32 v191, v191\n"
"v_mfma_f32_16x16x32_bf16 v[28:31], a[116:119], v[208:211], v[28:31]\n"
"v_cvt_pk_bf16_f32 v214, v188, v189\n"
"v_cvt_pk_bf16_f32 v215, v190, v191\n"
"v_mfma_f32_16x16x32_bf16 v[168:171], a[84:87], a[24:27], v[168:171]\n"
"s_add_u32 m0, s48, 53376\n"
"v_exp_f32 v192, v192\n"
"v_mfma_f32_16x16x32_bf16 v[48:51], a[120:123], v[208:211], v[48:51]\n"
"s_add_u32 s52, s95, 131072\n"
"v_exp_f32 v193, v193\n"
"v_mfma_f32_16x16x32_bf16 v[172:175], a[100:103], a[24:27], v[172:175]\n"
"v_exp_f32 v194, v194\n"
"v_mfma_f32_16x16x32_bf16 v[68:71], a[124:127], v[208:211], v[68:71]\n"
"v_exp_f32 v195, v195\n"
"v_mfma_f32_16x16x32_bf16 v[168:171], a[88:91], a[44:47], v[168:171]\n"
"buffer_load_dwordx4 v244, s[60:63], s95 offen lds\n"
"v_cvt_pk_bf16_f32 v216, v192, v193\n"
"v_mfma_f32_16x16x32_bf16 v[88:91], a[128:131], v[208:211], v[88:91]\n"
"v_exp_f32 v196, v196\n"
"v_mfma_f32_16x16x32_bf16 v[172:175], a[104:107], a[44:47], v[172:175]\n"
"v_exp_f32 v197, v197\n"
"v_mfma_f32_16x16x32_bf16 v[108:111], a[132:135], v[208:211], v[108:111]\n"
"buffer_load_dwordx4 v244, s[60:63], s95 offen offset:1024 lds\n"
"v_cvt_pk_bf16_f32 v217, v194, v195\n"
"v_mfma_f32_16x16x32_bf16 v[168:171], a[92:95], a[64:67], v[168:171]\n"
"v_exp_f32 v198, v198\n"
"v_mfma_f32_16x16x32_bf16 v[128:131], a[136:139], v[208:211], v[128:131]\n"
"v_exp_f32 v199, v199\n"
"v_mfma_f32_16x16x32_bf16 v[172:175], a[108:111], a[64:67], v[172:175]\n"
"v_cvt_pk_bf16_f32 v218, v196, v197\n"
"v_cvt_pk_bf16_f32 v219, v198, v199\n"
"v_mfma_f32_16x16x32_bf16 v[148:151], a[140:143], v[208:211], v[148:151]\n"
"v_exp_f32 v160, v160\n"
"v_mfma_f32_16x16x32_bf16 a[152:155], a[164:167], v[208:211], a[152:155]\n"
"v_exp_f32 v161, v161\n"
"v_mfma_f32_16x16x32_bf16 v[176:179], a[80:83], a[8:11], v[228:231]\n"
"v_exp_f32 v162, v162\n"
"v_mfma_f32_16x16x32_bf16 v[12:15], a[112:115], v[212:215], v[12:15]\n"
"s_add_u32 m0, s48, 61568\n"
"v_exp_f32 v163, v163\n"
"v_mfma_f32_16x16x32_bf16 v[180:183], a[96:99], a[8:11], v[228:231]\n"
"s_add_u32 s95, s95, 8192\n"
"v_cvt_pk_bf16_f32 v200, v160, v161\n"
"v_mfma_f32_16x16x32_bf16 v[32:35], a[116:119], v[212:215], v[32:35]\n"
"v_exp_f32 v164, v164\n"
"v_mfma_f32_16x16x32_bf16 v[176:179], a[84:87], a[28:31], v[176:179]\n"
"v_exp_f32 v165, v165\n"
"v_mfma_f32_16x16x32_bf16 v[52:55], a[120:123], v[212:215], v[52:55]\n"
"buffer_load_dwordx4 v244, s[60:63], s52 offen lds\n"
"v_cvt_pk_bf16_f32 v201, v162, v163\n"
"v_mfma_f32_16x16x32_bf16 v[180:183], a[100:103], a[28:31], v[180:183]\n"
"v_exp_f32 v166, v166\n"
"v_mfma_f32_16x16x32_bf16 v[72:75], a[124:127], v[212:215], v[72:75]\n"
"v_exp_f32 v167, v167\n"
"v_mfma_f32_16x16x32_bf16 v[176:179], a[88:91], a[48:51], v[176:179]\n"
"v_cvt_pk_bf16_f32 v202, v164, v165\n"
"v_cvt_pk_bf16_f32 v203, v166, v167\n"
"v_mfma_f32_16x16x32_bf16 v[92:95], a[128:131], v[212:215], v[92:95]\n"
"buffer_load_dwordx4 v244, s[60:63], s52 offen offset:1024 lds\n"
"s_and_b32 s95, s95, 131071\n"
"v_mfma_f32_16x16x32_bf16 v[180:183], a[104:107], a[48:51], v[180:183]\n"
"v_mfma_f32_16x16x32_bf16 v[112:115], a[132:135], v[212:215], v[112:115]\n"
"v_mfma_f32_16x16x32_bf16 v[176:179], a[92:95], a[68:71], v[176:179]\n"
"v_mfma_f32_16x16x32_bf16 v[132:135], a[136:139], v[212:215], v[132:135]\n"
"v_mfma_f32_16x16x32_bf16 v[180:183], a[108:111], a[68:71], v[180:183]\n"
"v_mfma_f32_16x16x32_bf16 v[152:155], a[140:143], v[212:215], v[152:155]\n"
"v_mfma_f32_16x16x32_bf16 a[156:159], a[164:167], v[212:215], a[156:159]\n"
"s_waitcnt vmcnt(16) lgkmcnt(0)\n"
"s_barrier\n"
"v_mfma_f32_16x16x32_bf16 v[184:187], a[80:83], a[12:15], v[232:235]\n"
"v_mfma_f32_16x16x32_bf16 v[16:19], a[112:115], v[216:219], v[16:19]\n"
"ds_read_b64_tr_b16 a[112:113], v241 offset:41216\n"
"v_mfma_f32_16x16x32_bf16 v[188:191], a[96:99], a[12:15], v[232:235]\n"
"ds_read_b64_tr_b16 a[114:115], v241 offset:42240\n"
"v_mfma_f32_16x16x32_bf16 v[36:39], a[116:119], v[216:219], v[36:39]\n"
"ds_read_b64_tr_b16 a[116:117], v242 offset:41216\n"
"v_mfma_f32_16x16x32_bf16 v[184:187], a[84:87], a[32:35], v[184:187]\n"
"ds_read_b64_tr_b16 a[118:119], v242 offset:42240\n"
"v_mfma_f32_16x16x32_bf16 v[56:59], a[120:123], v[216:219], v[56:59]\n"
"ds_read_b64_tr_b16 a[120:121], v241 offset:41472\n"
"v_mfma_f32_16x16x32_bf16 v[188:191], a[100:103], a[32:35], v[188:191]\n"
"ds_read_b64_tr_b16 a[122:123], v241 offset:42496\n"
"v_mfma_f32_16x16x32_bf16 v[76:79], a[124:127], v[216:219], v[76:79]\n"
"ds_read_b64_tr_b16 a[124:125], v242 offset:41472\n"
"v_mfma_f32_16x16x32_bf16 v[184:187], a[88:91], a[52:55], v[184:187]\n"
"ds_read_b64_tr_b16 a[126:127], v242 offset:42496\n"
"v_mfma_f32_16x16x32_bf16 v[96:99], a[128:131], v[216:219], v[96:99]\n"
"ds_read_b64_tr_b16 a[128:129], v241 offset:41728\n"
"v_mfma_f32_16x16x32_bf16 v[188:191], a[104:107], a[52:55], v[188:191]\n"
"ds_read_b64_tr_b16 a[130:131], v241 offset:42752\n"
"v_mfma_f32_16x16x32_bf16 v[116:119], a[132:135], v[216:219], v[116:119]\n"
"ds_read_b64_tr_b16 a[132:133], v242 offset:41728\n"
"v_mfma_f32_16x16x32_bf16 v[184:187], a[92:95], a[72:75], v[184:187]\n"
"ds_read_b64_tr_b16 a[134:135], v242 offset:42752\n"
"v_mfma_f32_16x16x32_bf16 v[136:139], a[136:139], v[216:219], v[136:139]\n"
"ds_read_b64_tr_b16 a[136:137], v241 offset:41984\n"
"v_mfma_f32_16x16x32_bf16 v[188:191], a[108:111], a[72:75], v[188:191]\n"
"ds_read_b64_tr_b16 a[138:139], v241 offset:43008\n"
"v_mfma_f32_16x16x32_bf16 v[156:159], a[140:143], v[216:219], v[156:159]\n"
"ds_read_b64_tr_b16 a[140:141], v242 offset:41984\n"
"v_mfma_f32_16x16x32_bf16 a[160:163], a[164:167], v[216:219], a[160:163]\n"
"ds_read_b64_tr_b16 a[142:143], v242 offset:43008\n"
"v_add_u32 v240, 49152, v240\n"
"v_mfma_f32_16x16x32_bf16 v[192:195], a[80:83], a[16:19], v[236:239]\n"
"s_waitcnt lgkmcnt(12)\n"
"ds_read_b128 a[80:83], v240 offset:24832\n"
"v_mfma_f32_16x16x32_bf16 v[0:3], a[112:115], v[200:203], v[0:3]\n"
"v_exp_f32 v168, v168\n"
"v_mfma_f32_16x16x32_bf16 v[196:199], a[96:99], a[16:19], v[236:239]\n"
"ds_read_b128 a[96:99], v240 offset:25856\n"
"v_mfma_f32_16x16x32_bf16 v[20:23], a[116:119], v[200:203], v[20:23]\n"
"v_exp_f32 v169, v169\n"
"v_mfma_f32_16x16x32_bf16 v[192:195], a[84:87], a[36:39], v[192:195]\n"
"s_waitcnt lgkmcnt(10)\n"
"ds_read_b128 a[84:87], v240 offset:25088\n"
"v_mfma_f32_16x16x32_bf16 v[40:43], a[120:123], v[200:203], v[40:43]\n"
"v_exp_f32 v170, v170\n"
"v_mfma_f32_16x16x32_bf16 v[196:199], a[100:103], a[36:39], v[196:199]\n"
"ds_read_b128 a[100:103], v240 offset:26112\n"
"v_mfma_f32_16x16x32_bf16 v[60:63], a[124:127], v[200:203], v[60:63]\n"
"v_exp_f32 v171, v171\n"
"v_mfma_f32_16x16x32_bf16 v[192:195], a[88:91], a[56:59], v[192:195]\n"
"s_waitcnt lgkmcnt(8)\n"
"ds_read_b128 a[88:91], v240 offset:25344\n"
"v_mfma_f32_16x16x32_bf16 v[80:83], a[128:131], v[200:203], v[80:83]\n"
"v_cvt_pk_bf16_f32 v204, v168, v169\n"
"v_mfma_f32_16x16x32_bf16 v[196:199], a[104:107], a[56:59], v[196:199]\n"
"ds_read_b128 a[104:107], v240 offset:26368\n"
"s_add_u32 m0, s48, 69760\n"
"v_mfma_f32_16x16x32_bf16 v[100:103], a[132:135], v[200:203], v[100:103]\n"
"s_add_u32 s52, s81, 131072\n"
"v_exp_f32 v172, v172\n"
"v_mfma_f32_16x16x32_bf16 v[192:195], a[92:95], a[76:79], v[192:195]\n"
"s_waitcnt lgkmcnt(6)\n"
"v_exp_f32 v173, v173\n"
"v_mfma_f32_16x16x32_bf16 v[120:123], a[136:139], v[200:203], v[120:123]\n"
"ds_read_b128 a[92:95], v240 offset:25600\n"
"v_cvt_pk_bf16_f32 v205, v170, v171\n"
"v_mfma_f32_16x16x32_bf16 v[196:199], a[108:111], a[76:79], v[196:199]\n"
"v_exp_f32 v174, v174\n"
"v_exp_f32 v175, v175\n"
"v_cvt_pk_bf16_f32 v206, v172, v173\n"
"v_cvt_pk_bf16_f32 v207, v174, v175\n"
"v_mfma_f32_16x16x32_bf16 v[140:143], a[140:143], v[200:203], v[140:143]\n"
"ds_read_b128 a[108:111], v240 offset:26624\n"
"v_mfma_f32_16x16x32_bf16 a[144:147], a[164:167], v[200:203], a[144:147]\n"
"s_waitcnt lgkmcnt(4)\n"
"v_exp_f32 v176, v176\n"
"v_mfma_f32_16x16x32_bf16 v[160:163], a[80:83], a[0:3], v[220:223]\n"
"v_exp_f32 v177, v177\n"
"v_mfma_f32_16x16x32_bf16 v[4:7], a[112:115], v[204:207], v[4:7]\n"
"v_exp_f32 v178, v178\n"
"v_mfma_f32_16x16x32_bf16 v[164:167], a[96:99], a[0:3], v[220:223]\n"
"v_exp_f32 v179, v179\n"
"v_mfma_f32_16x16x32_bf16 v[24:27], a[116:119], v[204:207], v[24:27]\n"
"buffer_load_dwordx4 v243, s[40:43], s81 offen nt lds\n"
"v_cvt_pk_bf16_f32 v208, v176, v177\n"
"v_mfma_f32_16x16x32_bf16 v[160:163], a[84:87], a[20:23], v[160:163]\n"
"v_exp_f32 v180, v180\n"
"v_mfma_f32_16x16x32_bf16 v[44:47], a[120:123], v[204:207], v[44:47]\n"
"v_exp_f32 v181, v181\n"
"v_mfma_f32_16x16x32_bf16 v[164:167], a[100:103], a[20:23], v[164:167]\n"
"buffer_load_dwordx4 v243, s[40:43], s81 offen offset:1024 nt lds\n"
"v_cvt_pk_bf16_f32 v209, v178, v179\n"
"v_mfma_f32_16x16x32_bf16 v[64:67], a[124:127], v[204:207], v[64:67]\n"
"s_waitcnt lgkmcnt(0)\n"
"v_exp_f32 v182, v182\n"
"v_mfma_f32_16x16x32_bf16 v[160:163], a[88:91], a[40:43], v[160:163]\n"
"s_add_u32 m0, s48, 77952\n"
"v_exp_f32 v183, v183\n"
"v_mfma_f32_16x16x32_bf16 v[84:87], a[128:131], v[204:207], v[84:87]\n"
"s_add_u32 s81, s81, 8192\n"
"v_cvt_pk_bf16_f32 v210, v180, v181\n"
"v_cvt_pk_bf16_f32 v211, v182, v183\n"
"v_mfma_f32_16x16x32_bf16 v[164:167], a[104:107], a[40:43], v[164:167]\n"
"v_exp_f32 v184, v184\n"
"v_mfma_f32_16x16x32_bf16 v[104:107], a[132:135], v[204:207], v[104:107]\n"
"v_exp_f32 v185, v185\n"
"v_mfma_f32_16x16x32_bf16 v[160:163], a[92:95], a[60:63], v[160:163]\n"
"v_exp_f32 v186, v186\n"
"v_mfma_f32_16x16x32_bf16 v[124:127], a[136:139], v[204:207], v[124:127]\n"
"v_exp_f32 v187, v187\n"
"v_mfma_f32_16x16x32_bf16 v[164:167], a[108:111], a[60:63], v[164:167]\n"
"buffer_load_dwordx4 v243, s[40:43], s52 offen nt lds\n"
"v_cvt_pk_bf16_f32 v212, v184, v185\n"
"v_mfma_f32_16x16x32_bf16 v[144:147], a[140:143], v[204:207], v[144:147]\n"
"v_exp_f32 v188, v188\n"
"v_mfma_f32_16x16x32_bf16 a[148:151], a[164:167], v[204:207], a[148:151]\n"
"v_exp_f32 v189, v189\n"
"v_mfma_f32_16x16x32_bf16 v[168:171], a[80:83], a[4:7], v[224:227]\n"
"buffer_load_dwordx4 v243, s[40:43], s52 offen offset:1024 nt lds\n"
"v_cvt_pk_bf16_f32 v213, v186, v187\n"
"v_mfma_f32_16x16x32_bf16 v[8:11], a[112:115], v[208:211], v[8:11]\n"
"s_and_b32 s81, s81, 131071\n"
"v_exp_f32 v190, v190\n"
"v_mfma_f32_16x16x32_bf16 v[172:175], a[96:99], a[4:7], v[224:227]\n"
"v_exp_f32 v191, v191\n"
"v_mfma_f32_16x16x32_bf16 v[28:31], a[116:119], v[208:211], v[28:31]\n"
"v_cvt_pk_bf16_f32 v214, v188, v189\n"
"v_cvt_pk_bf16_f32 v215, v190, v191\n"
"v_mfma_f32_16x16x32_bf16 v[168:171], a[84:87], a[24:27], v[168:171]\n"
"s_add_u32 m0, s48, 86144\n"
"v_exp_f32 v192, v192\n"
"v_mfma_f32_16x16x32_bf16 v[48:51], a[120:123], v[208:211], v[48:51]\n"
"s_add_u32 s52, s95, 131072\n"
"v_exp_f32 v193, v193\n"
"v_mfma_f32_16x16x32_bf16 v[172:175], a[100:103], a[24:27], v[172:175]\n"
"v_exp_f32 v194, v194\n"
"v_mfma_f32_16x16x32_bf16 v[68:71], a[124:127], v[208:211], v[68:71]\n"
"v_exp_f32 v195, v195\n"
"v_mfma_f32_16x16x32_bf16 v[168:171], a[88:91], a[44:47], v[168:171]\n"
"buffer_load_dwordx4 v244, s[60:63], s95 offen lds\n"
"v_cvt_pk_bf16_f32 v216, v192, v193\n"
"v_mfma_f32_16x16x32_bf16 v[88:91], a[128:131], v[208:211], v[88:91]\n"
"v_exp_f32 v196, v196\n"
"v_mfma_f32_16x16x32_bf16 v[172:175], a[104:107], a[44:47], v[172:175]\n"
"v_exp_f32 v197, v197\n"
"v_mfma_f32_16x16x32_bf16 v[108:111], a[132:135], v[208:211], v[108:111]\n"
"buffer_load_dwordx4 v244, s[60:63], s95 offen offset:1024 lds\n"
"v_cvt_pk_bf16_f32 v217, v194, v195\n"
"v_mfma_f32_16x16x32_bf16 v[168:171], a[92:95], a[64:67], v[168:171]\n"
"v_exp_f32 v198, v198\n"
"v_mfma_f32_16x16x32_bf16 v[128:131], a[136:139], v[208:211], v[128:131]\n"
"v_exp_f32 v199, v199\n"
"v_mfma_f32_16x16x32_bf16 v[172:175], a[108:111], a[64:67], v[172:175]\n"
"v_cvt_pk_bf16_f32 v218, v196, v197\n"
"v_cvt_pk_bf16_f32 v219, v198, v199\n"
"v_mfma_f32_16x16x32_bf16 v[148:151], a[140:143], v[208:211], v[148:151]\n"
"v_exp_f32 v160, v160\n"
"v_mfma_f32_16x16x32_bf16 a[152:155], a[164:167], v[208:211], a[152:155]\n"
"v_exp_f32 v161, v161\n"
"v_mfma_f32_16x16x32_bf16 v[176:179], a[80:83], a[8:11], v[228:231]\n"
"v_exp_f32 v162, v162\n"
"v_mfma_f32_16x16x32_bf16 v[12:15], a[112:115], v[212:215], v[12:15]\n"
"s_add_u32 m0, s48, 94336\n"
"v_exp_f32 v163, v163\n"
"v_mfma_f32_16x16x32_bf16 v[180:183], a[96:99], a[8:11], v[228:231]\n"
"s_add_u32 s95, s95, 8192\n"
"v_cvt_pk_bf16_f32 v200, v160, v161\n"
"v_mfma_f32_16x16x32_bf16 v[32:35], a[116:119], v[212:215], v[32:35]\n"
"v_exp_f32 v164, v164\n"
"v_mfma_f32_16x16x32_bf16 v[176:179], a[84:87], a[28:31], v[176:179]\n"
"v_exp_f32 v165, v165\n"
"v_mfma_f32_16x16x32_bf16 v[52:55], a[120:123], v[212:215], v[52:55]\n"
"buffer_load_dwordx4 v244, s[60:63], s52 offen lds\n"
"v_cvt_pk_bf16_f32 v201, v162, v163\n"
"v_mfma_f32_16x16x32_bf16 v[180:183], a[100:103], a[28:31], v[180:183]\n"
"v_exp_f32 v166, v166\n"
"v_mfma_f32_16x16x32_bf16 v[72:75], a[124:127], v[212:215], v[72:75]\n"
"v_exp_f32 v167, v167\n"
"v_mfma_f32_16x16x32_bf16 v[176:179], a[88:91], a[48:51], v[176:179]\n"
"v_cvt_pk_bf16_f32 v202, v164, v165\n"
"v_cvt_pk_bf16_f32 v203, v166, v167\n"
"v_mfma_f32_16x16x32_bf16 v[92:95], a[128:131], v[212:215], v[92:95]\n"
"buffer_load_dwordx4 v244, s[60:63], s52 offen offset:1024 lds\n"
"s_and_b32 s95, s95, 131071\n"
"v_mfma_f32_16x16x32_bf16 v[180:183], a[104:107], a[48:51], v[180:183]\n"
"v_mfma_f32_16x16x32_bf16 v[112:115], a[132:135], v[212:215], v[112:115]\n"
"v_add_u32 v241, 4294868992, v241\n"
"v_add_u32 v242, 4294868992, v242\n"
"v_mfma_f32_16x16x32_bf16 v[176:179], a[92:95], a[68:71], v[176:179]\n"
"v_mfma_f32_16x16x32_bf16 v[132:135], a[136:139], v[212:215], v[132:135]\n"
"v_mfma_f32_16x16x32_bf16 v[180:183], a[108:111], a[68:71], v[180:183]\n"
"v_mfma_f32_16x16x32_bf16 v[152:155], a[140:143], v[212:215], v[152:155]\n"
"v_mfma_f32_16x16x32_bf16 a[156:159], a[164:167], v[212:215], a[156:159]\n"
"s_waitcnt vmcnt(16) lgkmcnt(0)\n"
"s_barrier\n"
"v_mfma_f32_16x16x32_bf16 v[184:187], a[80:83], a[12:15], v[232:235]\n"
"v_mfma_f32_16x16x32_bf16 v[16:19], a[112:115], v[216:219], v[16:19]\n"
"ds_read_b64_tr_b16 a[112:113], v241 offset:16384\n"
"v_mfma_f32_16x16x32_bf16 v[188:191], a[96:99], a[12:15], v[232:235]\n"
"ds_read_b64_tr_b16 a[114:115], v241 offset:17408\n"
"v_mfma_f32_16x16x32_bf16 v[36:39], a[116:119], v[216:219], v[36:39]\n"
"ds_read_b64_tr_b16 a[116:117], v242 offset:16384\n"
"v_mfma_f32_16x16x32_bf16 v[184:187], a[84:87], a[32:35], v[184:187]\n"
"ds_read_b64_tr_b16 a[118:119], v242 offset:17408\n"
"v_mfma_f32_16x16x32_bf16 v[56:59], a[120:123], v[216:219], v[56:59]\n"
"ds_read_b64_tr_b16 a[120:121], v241 offset:16640\n"
"v_mfma_f32_16x16x32_bf16 v[188:191], a[100:103], a[32:35], v[188:191]\n"
"ds_read_b64_tr_b16 a[122:123], v241 offset:17664\n"
"v_mfma_f32_16x16x32_bf16 v[76:79], a[124:127], v[216:219], v[76:79]\n"
"ds_read_b64_tr_b16 a[124:125], v242 offset:16640\n"
"v_mfma_f32_16x16x32_bf16 v[184:187], a[88:91], a[52:55], v[184:187]\n"
"ds_read_b64_tr_b16 a[126:127], v242 offset:17664\n"
"v_mfma_f32_16x16x32_bf16 v[96:99], a[128:131], v[216:219], v[96:99]\n"
"ds_read_b64_tr_b16 a[128:129], v241 offset:16896\n"
"v_mfma_f32_16x16x32_bf16 v[188:191], a[104:107], a[52:55], v[188:191]\n"
"ds_read_b64_tr_b16 a[130:131], v241 offset:17920\n"
"v_mfma_f32_16x16x32_bf16 v[116:119], a[132:135], v[216:219], v[116:119]\n"
"ds_read_b64_tr_b16 a[132:133], v242 offset:16896\n"
"v_mfma_f32_16x16x32_bf16 v[184:187], a[92:95], a[72:75], v[184:187]\n"
"ds_read_b64_tr_b16 a[134:135], v242 offset:17920\n"
"v_mfma_f32_16x16x32_bf16 v[136:139], a[136:139], v[216:219], v[136:139]\n"
"ds_read_b64_tr_b16 a[136:137], v241 offset:17152\n"
"v_mfma_f32_16x16x32_bf16 v[188:191], a[108:111], a[72:75], v[188:191]\n"
"ds_read_b64_tr_b16 a[138:139], v241 offset:18176\n"
"v_mfma_f32_16x16x32_bf16 v[156:159], a[140:143], v[216:219], v[156:159]\n"
"ds_read_b64_tr_b16 a[140:141], v242 offset:17152\n"
"v_mfma_f32_16x16x32_bf16 a[160:163], a[164:167], v[216:219], a[160:163]\n"
"ds_read_b64_tr_b16 a[142:143], v242 offset:18176\n"
"v_add_u32 v240, 4294868992, v240\n"
"v_mfma_f32_16x16x32_bf16 v[192:195], a[80:83], a[16:19], v[236:239]\n"
"s_waitcnt lgkmcnt(12)\n"
"ds_read_b128 a[80:83], v240 offset:0\n"
"v_mfma_f32_16x16x32_bf16 v[0:3], a[112:115], v[200:203], v[0:3]\n"
"v_exp_f32 v168, v168\n"
"v_mfma_f32_16x16x32_bf16 v[196:199], a[96:99], a[16:19], v[236:239]\n"
"ds_read_b128 a[96:99], v240 offset:1024\n"
"v_mfma_f32_16x16x32_bf16 v[20:23], a[116:119], v[200:203], v[20:23]\n"
"v_exp_f32 v169, v169\n"
"v_mfma_f32_16x16x32_bf16 v[192:195], a[84:87], a[36:39], v[192:195]\n"
"s_waitcnt lgkmcnt(10)\n"
"ds_read_b128 a[84:87], v240 offset:256\n"
"v_mfma_f32_16x16x32_bf16 v[40:43], a[120:123], v[200:203], v[40:43]\n"
"v_exp_f32 v170, v170\n"
"v_mfma_f32_16x16x32_bf16 v[196:199], a[100:103], a[36:39], v[196:199]\n"
"ds_read_b128 a[100:103], v240 offset:1280\n"
"v_mfma_f32_16x16x32_bf16 v[60:63], a[124:127], v[200:203], v[60:63]\n"
"v_exp_f32 v171, v171\n"
"v_mfma_f32_16x16x32_bf16 v[192:195], a[88:91], a[56:59], v[192:195]\n"
"s_waitcnt lgkmcnt(8)\n"
"ds_read_b128 a[88:91], v240 offset:512\n"
"v_mfma_f32_16x16x32_bf16 v[80:83], a[128:131], v[200:203], v[80:83]\n"
"v_cvt_pk_bf16_f32 v204, v168, v169\n"
"v_mfma_f32_16x16x32_bf16 v[196:199], a[104:107], a[56:59], v[196:199]\n"
"ds_read_b128 a[104:107], v240 offset:1536\n"
"s_add_u32 m0, s48, 106752\n"
"v_mfma_f32_16x16x32_bf16 v[100:103], a[132:135], v[200:203], v[100:103]\n"
"s_add_u32 s52, s81, 131072\n"
"v_exp_f32 v172, v172\n"
"v_mfma_f32_16x16x32_bf16 v[192:195], a[92:95], a[76:79], v[192:195]\n"
"s_waitcnt lgkmcnt(6)\n"
"v_exp_f32 v173, v173\n"
"v_mfma_f32_16x16x32_bf16 v[120:123], a[136:139], v[200:203], v[120:123]\n"
"ds_read_b128 a[92:95], v240 offset:768\n"
"v_cvt_pk_bf16_f32 v205, v170, v171\n"
"v_mfma_f32_16x16x32_bf16 v[196:199], a[108:111], a[76:79], v[196:199]\n"
"v_exp_f32 v174, v174\n"
"v_exp_f32 v175, v175\n"
"v_cvt_pk_bf16_f32 v206, v172, v173\n"
"v_cvt_pk_bf16_f32 v207, v174, v175\n"
"v_mfma_f32_16x16x32_bf16 v[140:143], a[140:143], v[200:203], v[140:143]\n"
"ds_read_b128 a[108:111], v240 offset:1792\n"
"v_mfma_f32_16x16x32_bf16 a[144:147], a[164:167], v[200:203], a[144:147]\n"
"s_waitcnt lgkmcnt(4)\n"
"v_exp_f32 v176, v176\n"
"v_mfma_f32_16x16x32_bf16 v[160:163], a[80:83], a[0:3], v[220:223]\n"
"v_exp_f32 v177, v177\n"
"v_mfma_f32_16x16x32_bf16 v[4:7], a[112:115], v[204:207], v[4:7]\n"
"v_exp_f32 v178, v178\n"
"v_mfma_f32_16x16x32_bf16 v[164:167], a[96:99], a[0:3], v[220:223]\n"
"v_exp_f32 v179, v179\n"
"v_mfma_f32_16x16x32_bf16 v[24:27], a[116:119], v[204:207], v[24:27]\n"
"buffer_load_dwordx4 v243, s[40:43], s81 offen nt lds\n"
"v_cvt_pk_bf16_f32 v208, v176, v177\n"
"v_mfma_f32_16x16x32_bf16 v[160:163], a[84:87], a[20:23], v[160:163]\n"
"v_exp_f32 v180, v180\n"
"v_mfma_f32_16x16x32_bf16 v[44:47], a[120:123], v[204:207], v[44:47]\n"
"v_exp_f32 v181, v181\n"
"v_mfma_f32_16x16x32_bf16 v[164:167], a[100:103], a[20:23], v[164:167]\n"
"buffer_load_dwordx4 v243, s[40:43], s81 offen offset:1024 nt lds\n"
"v_cvt_pk_bf16_f32 v209, v178, v179\n"
"v_mfma_f32_16x16x32_bf16 v[64:67], a[124:127], v[204:207], v[64:67]\n"
"s_waitcnt lgkmcnt(0)\n"
"v_exp_f32 v182, v182\n"
"v_mfma_f32_16x16x32_bf16 v[160:163], a[88:91], a[40:43], v[160:163]\n"
"s_add_u32 m0, s48, 114944\n"
"v_exp_f32 v183, v183\n"
"v_mfma_f32_16x16x32_bf16 v[84:87], a[128:131], v[204:207], v[84:87]\n"
"s_add_u32 s81, s81, 8192\n"
"v_cvt_pk_bf16_f32 v210, v180, v181\n"
"v_cvt_pk_bf16_f32 v211, v182, v183\n"
"v_mfma_f32_16x16x32_bf16 v[164:167], a[104:107], a[40:43], v[164:167]\n"
"v_exp_f32 v184, v184\n"
"v_mfma_f32_16x16x32_bf16 v[104:107], a[132:135], v[204:207], v[104:107]\n"
"v_exp_f32 v185, v185\n"
"v_mfma_f32_16x16x32_bf16 v[160:163], a[92:95], a[60:63], v[160:163]\n"
"v_exp_f32 v186, v186\n"
"v_mfma_f32_16x16x32_bf16 v[124:127], a[136:139], v[204:207], v[124:127]\n"
"v_exp_f32 v187, v187\n"
"v_mfma_f32_16x16x32_bf16 v[164:167], a[108:111], a[60:63], v[164:167]\n"
"buffer_load_dwordx4 v243, s[40:43], s52 offen nt lds\n"
"v_cvt_pk_bf16_f32 v212, v184, v185\n"
"v_mfma_f32_16x16x32_bf16 v[144:147], a[140:143], v[204:207], v[144:147]\n"
"v_exp_f32 v188, v188\n"
"v_mfma_f32_16x16x32_bf16 a[148:151], a[164:167], v[204:207], a[148:151]\n"
"v_exp_f32 v189, v189\n"
"v_mfma_f32_16x16x32_bf16 v[168:171], a[80:83], a[4:7], v[224:227]\n"
"buffer_load_dwordx4 v243, s[40:43], s52 offen offset:1024 nt lds\n"
"v_cvt_pk_bf16_f32 v213, v186, v187\n"
"v_mfma_f32_16x16x32_bf16 v[8:11], a[112:115], v[208:211], v[8:11]\n"
"s_and_b32 s81, s81, 131071\n"
"v_exp_f32 v190, v190\n"
"v_mfma_f32_16x16x32_bf16 v[172:175], a[96:99], a[4:7], v[224:227]\n"
"v_exp_f32 v191, v191\n"
"v_mfma_f32_16x16x32_bf16 v[28:31], a[116:119], v[208:211], v[28:31]\n"
"v_cvt_pk_bf16_f32 v214, v188, v189\n"
"v_cvt_pk_bf16_f32 v215, v190, v191\n"
"v_mfma_f32_16x16x32_bf16 v[168:171], a[84:87], a[24:27], v[168:171]\n"
"s_add_u32 m0, s48, 123136\n"
"v_exp_f32 v192, v192\n"
"v_mfma_f32_16x16x32_bf16 v[48:51], a[120:123], v[208:211], v[48:51]\n"
"s_add_u32 s52, s95, 131072\n"
"v_exp_f32 v193, v193\n"
"v_mfma_f32_16x16x32_bf16 v[172:175], a[100:103], a[24:27], v[172:175]\n"
"v_exp_f32 v194, v194\n"
"v_mfma_f32_16x16x32_bf16 v[68:71], a[124:127], v[208:211], v[68:71]\n"
"v_exp_f32 v195, v195\n"
"v_mfma_f32_16x16x32_bf16 v[168:171], a[88:91], a[44:47], v[168:171]\n"
"buffer_load_dwordx4 v244, s[60:63], s95 offen lds\n"
"v_cvt_pk_bf16_f32 v216, v192, v193\n"
"v_mfma_f32_16x16x32_bf16 v[88:91], a[128:131], v[208:211], v[88:91]\n"
"v_exp_f32 v196, v196\n"
"v_mfma_f32_16x16x32_bf16 v[172:175], a[104:107], a[44:47], v[172:175]\n"
"v_exp_f32 v197, v197\n"
"v_mfma_f32_16x16x32_bf16 v[108:111], a[132:135], v[208:211], v[108:111]\n"
"buffer_load_dwordx4 v244, s[60:63], s95 offen offset:1024 lds\n"
"v_cvt_pk_bf16_f32 v217, v194, v195\n"
"v_mfma_f32_16x16x32_bf16 v[168:171], a[92:95], a[64:67], v[168:171]\n"
"v_exp_f32 v198, v198\n"
"v_mfma_f32_16x16x32_bf16 v[128:131], a[136:139], v[208:211], v[128:131]\n"
"v_exp_f32 v199, v199\n"
"v_mfma_f32_16x16x32_bf16 v[172:175], a[108:111], a[64:67], v[172:175]\n"
"v_cvt_pk_bf16_f32 v218, v196, v197\n"
"v_cvt_pk_bf16_f32 v219, v198, v199\n"
"v_mfma_f32_16x16x32_bf16 v[148:151], a[140:143], v[208:211], v[148:151]\n"
"v_exp_f32 v160, v160\n"
"v_mfma_f32_16x16x32_bf16 a[152:155], a[164:167], v[208:211], a[152:155]\n"
"v_exp_f32 v161, v161\n"
"v_mfma_f32_16x16x32_bf16 v[176:179], a[80:83], a[8:11], v[228:231]\n"
"v_exp_f32 v162, v162\n"
"v_mfma_f32_16x16x32_bf16 v[12:15], a[112:115], v[212:215], v[12:15]\n"
"s_add_u32 m0, s48, 131328\n"
"v_exp_f32 v163, v163\n"
"v_mfma_f32_16x16x32_bf16 v[180:183], a[96:99], a[8:11], v[228:231]\n"
"s_add_u32 s95, s95, 8192\n"
"v_cvt_pk_bf16_f32 v200, v160, v161\n"
"v_mfma_f32_16x16x32_bf16 v[32:35], a[116:119], v[212:215], v[32:35]\n"
"v_exp_f32 v164, v164\n"
"v_mfma_f32_16x16x32_bf16 v[176:179], a[84:87], a[28:31], v[176:179]\n"
"v_exp_f32 v165, v165\n"
"v_mfma_f32_16x16x32_bf16 v[52:55], a[120:123], v[212:215], v[52:55]\n"
"buffer_load_dwordx4 v244, s[60:63], s52 offen lds\n"
"v_cvt_pk_bf16_f32 v201, v162, v163\n"
"v_mfma_f32_16x16x32_bf16 v[180:183], a[100:103], a[28:31], v[180:183]\n"
"v_exp_f32 v166, v166\n"
"v_mfma_f32_16x16x32_bf16 v[72:75], a[124:127], v[212:215], v[72:75]\n"
"v_exp_f32 v167, v167\n"
"v_mfma_f32_16x16x32_bf16 v[176:179], a[88:91], a[48:51], v[176:179]\n"
"v_cvt_pk_bf16_f32 v202, v164, v165\n"
"v_cvt_pk_bf16_f32 v203, v166, v167\n"
"v_mfma_f32_16x16x32_bf16 v[92:95], a[128:131], v[212:215], v[92:95]\n"
"buffer_load_dwordx4 v244, s[60:63], s52 offen offset:1024 lds\n"
"s_and_b32 s95, s95, 131071\n"
"v_mfma_f32_16x16x32_bf16 v[180:183], a[104:107], a[48:51], v[180:183]\n"
"v_mfma_f32_16x16x32_bf16 v[112:115], a[132:135], v[212:215], v[112:115]\n"
"v_add_u32 v241, 49152, v241\n"
"v_add_u32 v242, 49152, v242\n"
"v_mfma_f32_16x16x32_bf16 v[176:179], a[92:95], a[68:71], v[176:179]\n"
"v_mfma_f32_16x16x32_bf16 v[132:135], a[136:139], v[212:215], v[132:135]\n"
"v_mfma_f32_16x16x32_bf16 v[180:183], a[108:111], a[68:71], v[180:183]\n"
"v_mfma_f32_16x16x32_bf16 v[152:155], a[140:143], v[212:215], v[152:155]\n"
"v_mfma_f32_16x16x32_bf16 a[156:159], a[164:167], v[212:215], a[156:159]\n"
"s_waitcnt vmcnt(16) lgkmcnt(0)\n"
"s_barrier\n"
"v_mfma_f32_16x16x32_bf16 v[184:187], a[80:83], a[12:15], v[232:235]\n"
"v_mfma_f32_16x16x32_bf16 v[16:19], a[112:115], v[216:219], v[16:19]\n"
"ds_read_b64_tr_b16 a[112:113], v241 offset:4224\n"
"v_mfma_f32_16x16x32_bf16 v[188:191], a[96:99], a[12:15], v[232:235]\n"
"ds_read_b64_tr_b16 a[114:115], v241 offset:5248\n"
"v_mfma_f32_16x16x32_bf16 v[36:39], a[116:119], v[216:219], v[36:39]\n"
"ds_read_b64_tr_b16 a[116:117], v242 offset:4224\n"
"v_mfma_f32_16x16x32_bf16 v[184:187], a[84:87], a[32:35], v[184:187]\n"
"ds_read_b64_tr_b16 a[118:119], v242 offset:5248\n"
"v_mfma_f32_16x16x32_bf16 v[56:59], a[120:123], v[216:219], v[56:59]\n"
"ds_read_b64_tr_b16 a[120:121], v241 offset:4480\n"
"v_mfma_f32_16x16x32_bf16 v[188:191], a[100:103], a[32:35], v[188:191]\n"
"ds_read_b64_tr_b16 a[122:123], v241 offset:5504\n"
"v_mfma_f32_16x16x32_bf16 v[76:79], a[124:127], v[216:219], v[76:79]\n"
"ds_read_b64_tr_b16 a[124:125], v242 offset:4480\n"
"v_mfma_f32_16x16x32_bf16 v[184:187], a[88:91], a[52:55], v[184:187]\n"
"ds_read_b64_tr_b16 a[126:127], v242 offset:5504\n"
"v_mfma_f32_16x16x32_bf16 v[96:99], a[128:131], v[216:219], v[96:99]\n"
"ds_read_b64_tr_b16 a[128:129], v241 offset:4736\n"
"v_mfma_f32_16x16x32_bf16 v[188:191], a[104:107], a[52:55], v[188:191]\n"
"ds_read_b64_tr_b16 a[130:131], v241 offset:5760\n"
"v_mfma_f32_16x16x32_bf16 v[116:119], a[132:135], v[216:219], v[116:119]\n"
"ds_read_b64_tr_b16 a[132:133], v242 offset:4736\n"
"v_mfma_f32_16x16x32_bf16 v[184:187], a[92:95], a[72:75], v[184:187]\n"
"ds_read_b64_tr_b16 a[134:135], v242 offset:5760\n"
"v_mfma_f32_16x16x32_bf16 v[136:139], a[136:139], v[216:219], v[136:139]\n"
"ds_read_b64_tr_b16 a[136:137], v241 offset:4992\n"
"v_mfma_f32_16x16x32_bf16 v[188:191], a[108:111], a[72:75], v[188:191]\n"
"ds_read_b64_tr_b16 a[138:139], v241 offset:6016\n"
"v_mfma_f32_16x16x32_bf16 v[156:159], a[140:143], v[216:219], v[156:159]\n"
"ds_read_b64_tr_b16 a[140:141], v242 offset:4992\n"
"v_mfma_f32_16x16x32_bf16 a[160:163], a[164:167], v[216:219], a[160:163]\n"
"ds_read_b64_tr_b16 a[142:143], v242 offset:6016\n"
"v_mfma_f32_16x16x32_bf16 v[192:195], a[80:83], a[16:19], v[236:239]\n"
"s_waitcnt lgkmcnt(12)\n"
"ds_read_b128 a[80:83], v240 offset:32768\n"
"v_mfma_f32_16x16x32_bf16 v[0:3], a[112:115], v[200:203], v[0:3]\n"
"v_exp_f32 v168, v168\n"
"v_mfma_f32_16x16x32_bf16 v[196:199], a[96:99], a[16:19], v[236:239]\n"
"ds_read_b128 a[96:99], v240 offset:33792\n"
"v_mfma_f32_16x16x32_bf16 v[20:23], a[116:119], v[200:203], v[20:23]\n"
"v_exp_f32 v169, v169\n"
"v_mfma_f32_16x16x32_bf16 v[192:195], a[84:87], a[36:39], v[192:195]\n"
"s_waitcnt lgkmcnt(10)\n"
"ds_read_b128 a[84:87], v240 offset:33024\n"
"v_mfma_f32_16x16x32_bf16 v[40:43], a[120:123], v[200:203], v[40:43]\n"
"v_exp_f32 v170, v170\n"
"v_mfma_f32_16x16x32_bf16 v[196:199], a[100:103], a[36:39], v[196:199]\n"
"ds_read_b128 a[100:103], v240 offset:34048\n"
"v_mfma_f32_16x16x32_bf16 v[60:63], a[124:127], v[200:203], v[60:63]\n"
"v_exp_f32 v171, v171\n"
"v_mfma_f32_16x16x32_bf16 v[192:195], a[88:91], a[56:59], v[192:195]\n"
"s_waitcnt lgkmcnt(8)\n"
"ds_read_b128 a[88:91], v240 offset:33280\n"
"v_mfma_f32_16x16x32_bf16 v[80:83], a[128:131], v[200:203], v[80:83]\n"
"v_cvt_pk_bf16_f32 v204, v168, v169\n"
"v_mfma_f32_16x16x32_bf16 v[196:199], a[104:107], a[56:59], v[196:199]\n"
"ds_read_b128 a[104:107], v240 offset:34304\n"
"s_add_u32 m0, s48, 139520\n"
"v_mfma_f32_16x16x32_bf16 v[100:103], a[132:135], v[200:203], v[100:103]\n"
"s_add_u32 s52, s81, 131072\n"
"v_exp_f32 v172, v172\n"
"v_mfma_f32_16x16x32_bf16 v[192:195], a[92:95], a[76:79], v[192:195]\n"
"s_waitcnt lgkmcnt(6)\n"
"v_exp_f32 v173, v173\n"
"v_mfma_f32_16x16x32_bf16 v[120:123], a[136:139], v[200:203], v[120:123]\n"
"ds_read_b128 a[92:95], v240 offset:33536\n"
"v_cvt_pk_bf16_f32 v205, v170, v171\n"
"v_mfma_f32_16x16x32_bf16 v[196:199], a[108:111], a[76:79], v[196:199]\n"
"v_exp_f32 v174, v174\n"
"v_exp_f32 v175, v175\n"
"v_cvt_pk_bf16_f32 v206, v172, v173\n"
"v_cvt_pk_bf16_f32 v207, v174, v175\n"
"v_mfma_f32_16x16x32_bf16 v[140:143], a[140:143], v[200:203], v[140:143]\n"
"ds_read_b128 a[108:111], v240 offset:34560\n"
"v_mfma_f32_16x16x32_bf16 a[144:147], a[164:167], v[200:203], a[144:147]\n"
"s_waitcnt lgkmcnt(4)\n"
"v_exp_f32 v176, v176\n"
"v_mfma_f32_16x16x32_bf16 v[160:163], a[80:83], a[0:3], v[220:223]\n"
"v_exp_f32 v177, v177\n"
"v_mfma_f32_16x16x32_bf16 v[4:7], a[112:115], v[204:207], v[4:7]\n"
"v_exp_f32 v178, v178\n"
"v_mfma_f32_16x16x32_bf16 v[164:167], a[96:99], a[0:3], v[220:223]\n"
"v_exp_f32 v179, v179\n"
"v_mfma_f32_16x16x32_bf16 v[24:27], a[116:119], v[204:207], v[24:27]\n"
"buffer_load_dwordx4 v243, s[40:43], s81 offen nt lds\n"
"v_cvt_pk_bf16_f32 v208, v176, v177\n"
"v_mfma_f32_16x16x32_bf16 v[160:163], a[84:87], a[20:23], v[160:163]\n"
"v_exp_f32 v180, v180\n"
"v_mfma_f32_16x16x32_bf16 v[44:47], a[120:123], v[204:207], v[44:47]\n"
"v_exp_f32 v181, v181\n"
"v_mfma_f32_16x16x32_bf16 v[164:167], a[100:103], a[20:23], v[164:167]\n"
"buffer_load_dwordx4 v243, s[40:43], s81 offen offset:1024 nt lds\n"
"v_cvt_pk_bf16_f32 v209, v178, v179\n"
"v_mfma_f32_16x16x32_bf16 v[64:67], a[124:127], v[204:207], v[64:67]\n"
"s_waitcnt lgkmcnt(0)\n"
"v_exp_f32 v182, v182\n"
"v_mfma_f32_16x16x32_bf16 v[160:163], a[88:91], a[40:43], v[160:163]\n"
"s_add_u32 m0, s48, 147712\n"
"v_exp_f32 v183, v183\n"
"v_mfma_f32_16x16x32_bf16 v[84:87], a[128:131], v[204:207], v[84:87]\n"
"s_add_u32 s81, s81, 8192\n"
"v_cvt_pk_bf16_f32 v210, v180, v181\n"
"v_cvt_pk_bf16_f32 v211, v182, v183\n"
"v_mfma_f32_16x16x32_bf16 v[164:167], a[104:107], a[40:43], v[164:167]\n"
"v_exp_f32 v184, v184\n"
"v_mfma_f32_16x16x32_bf16 v[104:107], a[132:135], v[204:207], v[104:107]\n"
"v_exp_f32 v185, v185\n"
"v_mfma_f32_16x16x32_bf16 v[160:163], a[92:95], a[60:63], v[160:163]\n"
"v_exp_f32 v186, v186\n"
"v_mfma_f32_16x16x32_bf16 v[124:127], a[136:139], v[204:207], v[124:127]\n"
"v_exp_f32 v187, v187\n"
"v_mfma_f32_16x16x32_bf16 v[164:167], a[108:111], a[60:63], v[164:167]\n"
"buffer_load_dwordx4 v243, s[40:43], s52 offen nt lds\n"
"v_cvt_pk_bf16_f32 v212, v184, v185\n"
"v_mfma_f32_16x16x32_bf16 v[144:147], a[140:143], v[204:207], v[144:147]\n"
"v_exp_f32 v188, v188\n"
"v_mfma_f32_16x16x32_bf16 a[148:151], a[164:167], v[204:207], a[148:151]\n"
"v_exp_f32 v189, v189\n"
"v_mfma_f32_16x16x32_bf16 v[168:171], a[80:83], a[4:7], v[224:227]\n"
"buffer_load_dwordx4 v243, s[40:43], s52 offen offset:1024 nt lds\n"
"v_cvt_pk_bf16_f32 v213, v186, v187\n"
"v_mfma_f32_16x16x32_bf16 v[8:11], a[112:115], v[208:211], v[8:11]\n"
"s_and_b32 s81, s81, 131071\n"
"v_exp_f32 v190, v190\n"
"v_mfma_f32_16x16x32_bf16 v[172:175], a[96:99], a[4:7], v[224:227]\n"
"v_exp_f32 v191, v191\n"
"v_mfma_f32_16x16x32_bf16 v[28:31], a[116:119], v[208:211], v[28:31]\n"
"v_cvt_pk_bf16_f32 v214, v188, v189\n"
"v_cvt_pk_bf16_f32 v215, v190, v191\n"
"v_mfma_f32_16x16x32_bf16 v[168:171], a[84:87], a[24:27], v[168:171]\n"
"s_add_u32 m0, s48, 0\n"
"v_exp_f32 v192, v192\n"
"v_mfma_f32_16x16x32_bf16 v[48:51], a[120:123], v[208:211], v[48:51]\n"
"s_add_u32 s52, s95, 131072\n"
"v_exp_f32 v193, v193\n"
"v_mfma_f32_16x16x32_bf16 v[172:175], a[100:103], a[24:27], v[172:175]\n"
"v_exp_f32 v194, v194\n"
"v_mfma_f32_16x16x32_bf16 v[68:71], a[124:127], v[208:211], v[68:71]\n"
"v_exp_f32 v195, v195\n"
"v_mfma_f32_16x16x32_bf16 v[168:171], a[88:91], a[44:47], v[168:171]\n"
"buffer_load_dwordx4 v244, s[60:63], s95 offen lds\n"
"v_cvt_pk_bf16_f32 v216, v192, v193\n"
"v_mfma_f32_16x16x32_bf16 v[88:91], a[128:131], v[208:211], v[88:91]\n"
"v_exp_f32 v196, v196\n"
"v_mfma_f32_16x16x32_bf16 v[172:175], a[104:107], a[44:47], v[172:175]\n"
"v_exp_f32 v197, v197\n"
"v_mfma_f32_16x16x32_bf16 v[108:111], a[132:135], v[208:211], v[108:111]\n"
"buffer_load_dwordx4 v244, s[60:63], s95 offen offset:1024 lds\n"
"v_cvt_pk_bf16_f32 v217, v194, v195\n"
"v_mfma_f32_16x16x32_bf16 v[168:171], a[92:95], a[64:67], v[168:171]\n"
"v_exp_f32 v198, v198\n"
"v_mfma_f32_16x16x32_bf16 v[128:131], a[136:139], v[208:211], v[128:131]\n"
"v_exp_f32 v199, v199\n"
"v_mfma_f32_16x16x32_bf16 v[172:175], a[108:111], a[64:67], v[172:175]\n"
"v_cvt_pk_bf16_f32 v218, v196, v197\n"
"v_cvt_pk_bf16_f32 v219, v198, v199\n"
"v_mfma_f32_16x16x32_bf16 v[148:151], a[140:143], v[208:211], v[148:151]\n"
"v_exp_f32 v160, v160\n"
"v_mfma_f32_16x16x32_bf16 a[152:155], a[164:167], v[208:211], a[152:155]\n"
"v_exp_f32 v161, v161\n"
"v_mfma_f32_16x16x32_bf16 v[176:179], a[80:83], a[8:11], v[228:231]\n"
"v_exp_f32 v162, v162\n"
"v_mfma_f32_16x16x32_bf16 v[12:15], a[112:115], v[212:215], v[12:15]\n"
"s_add_u32 m0, s48, 8192\n"
"v_exp_f32 v163, v163\n"
"v_mfma_f32_16x16x32_bf16 v[180:183], a[96:99], a[8:11], v[228:231]\n"
"s_add_u32 s95, s95, 8192\n"
"v_cvt_pk_bf16_f32 v200, v160, v161\n"
"v_mfma_f32_16x16x32_bf16 v[32:35], a[116:119], v[212:215], v[32:35]\n"
"v_exp_f32 v164, v164\n"
"v_mfma_f32_16x16x32_bf16 v[176:179], a[84:87], a[28:31], v[176:179]\n"
"v_exp_f32 v165, v165\n"
"v_mfma_f32_16x16x32_bf16 v[52:55], a[120:123], v[212:215], v[52:55]\n"
"buffer_load_dwordx4 v244, s[60:63], s52 offen lds\n"
"v_cvt_pk_bf16_f32 v201, v162, v163\n"
"v_mfma_f32_16x16x32_bf16 v[180:183], a[100:103], a[28:31], v[180:183]\n"
"v_exp_f32 v166, v166\n"
"v_mfma_f32_16x16x32_bf16 v[72:75], a[124:127], v[212:215], v[72:75]\n"
"v_exp_f32 v167, v167\n"
"v_mfma_f32_16x16x32_bf16 v[176:179], a[88:91], a[48:51], v[176:179]\n"
"v_cvt_pk_bf16_f32 v202, v164, v165\n"
"v_cvt_pk_bf16_f32 v203, v166, v167\n"
"v_mfma_f32_16x16x32_bf16 v[92:95], a[128:131], v[212:215], v[92:95]\n"
"buffer_load_dwordx4 v244, s[60:63], s52 offen offset:1024 lds\n"
"s_and_b32 s95, s95, 131071\n"
"v_mfma_f32_16x16x32_bf16 v[180:183], a[104:107], a[48:51], v[180:183]\n"
"v_mfma_f32_16x16x32_bf16 v[112:115], a[132:135], v[212:215], v[112:115]\n"
"v_mfma_f32_16x16x32_bf16 v[176:179], a[92:95], a[68:71], v[176:179]\n"
"v_mfma_f32_16x16x32_bf16 v[132:135], a[136:139], v[212:215], v[132:135]\n"
"v_mfma_f32_16x16x32_bf16 v[180:183], a[108:111], a[68:71], v[180:183]\n"
"v_mfma_f32_16x16x32_bf16 v[152:155], a[140:143], v[212:215], v[152:155]\n"
"v_mfma_f32_16x16x32_bf16 a[156:159], a[164:167], v[212:215], a[156:159]\n"
"s_waitcnt vmcnt(16) lgkmcnt(0)\n"
"s_barrier\n"
"v_mfma_f32_16x16x32_bf16 v[184:187], a[80:83], a[12:15], v[232:235]\n"
"v_mfma_f32_16x16x32_bf16 v[16:19], a[112:115], v[216:219], v[16:19]\n"
"ds_read_b64_tr_b16 a[112:113], v241 offset:36992\n"
"v_mfma_f32_16x16x32_bf16 v[188:191], a[96:99], a[12:15], v[232:235]\n"
"ds_read_b64_tr_b16 a[114:115], v241 offset:38016\n"
"v_mfma_f32_16x16x32_bf16 v[36:39], a[116:119], v[216:219], v[36:39]\n"
"ds_read_b64_tr_b16 a[116:117], v242 offset:36992\n"
"v_mfma_f32_16x16x32_bf16 v[184:187], a[84:87], a[32:35], v[184:187]\n"
"ds_read_b64_tr_b16 a[118:119], v242 offset:38016\n"
"v_mfma_f32_16x16x32_bf16 v[56:59], a[120:123], v[216:219], v[56:59]\n"
"ds_read_b64_tr_b16 a[120:121], v241 offset:37248\n"
"v_mfma_f32_16x16x32_bf16 v[188:191], a[100:103], a[32:35], v[188:191]\n"
"ds_read_b64_tr_b16 a[122:123], v241 offset:38272\n"
"v_mfma_f32_16x16x32_bf16 v[76:79], a[124:127], v[216:219], v[76:79]\n"
"ds_read_b64_tr_b16 a[124:125], v242 offset:37248\n"
"v_mfma_f32_16x16x32_bf16 v[184:187], a[88:91], a[52:55], v[184:187]\n"
"ds_read_b64_tr_b16 a[126:127], v242 offset:38272\n"
"v_mfma_f32_16x16x32_bf16 v[96:99], a[128:131], v[216:219], v[96:99]\n"
"ds_read_b64_tr_b16 a[128:129], v241 offset:37504\n"
"v_mfma_f32_16x16x32_bf16 v[188:191], a[104:107], a[52:55], v[188:191]\n"
"ds_read_b64_tr_b16 a[130:131], v241 offset:38528\n"
"v_mfma_f32_16x16x32_bf16 v[116:119], a[132:135], v[216:219], v[116:119]\n"
"ds_read_b64_tr_b16 a[132:133], v242 offset:37504\n"
"v_mfma_f32_16x16x32_bf16 v[184:187], a[92:95], a[72:75], v[184:187]\n"
"ds_read_b64_tr_b16 a[134:135], v242 offset:38528\n"
"v_mfma_f32_16x16x32_bf16 v[136:139], a[136:139], v[216:219], v[136:139]\n"
"ds_read_b64_tr_b16 a[136:137], v241 offset:37760\n"
"v_mfma_f32_16x16x32_bf16 v[188:191], a[108:111], a[72:75], v[188:191]\n"
"ds_read_b64_tr_b16 a[138:139], v241 offset:38784\n"
"v_mfma_f32_16x16x32_bf16 v[156:159], a[140:143], v[216:219], v[156:159]\n"
"ds_read_b64_tr_b16 a[140:141], v242 offset:37760\n"
"v_mfma_f32_16x16x32_bf16 a[160:163], a[164:167], v[216:219], a[160:163]\n"
"ds_read_b64_tr_b16 a[142:143], v242 offset:38784\n"
"v_add_u32 v240, 49152, v240\n"
"v_mfma_f32_16x16x32_bf16 v[192:195], a[80:83], a[16:19], v[236:239]\n"
"s_waitcnt lgkmcnt(12)\n"
"ds_read_b128 a[80:83], v240 offset:20608\n"
"v_mfma_f32_16x16x32_bf16 v[0:3], a[112:115], v[200:203], v[0:3]\n"
"v_exp_f32 v168, v168\n"
"v_mfma_f32_16x16x32_bf16 v[196:199], a[96:99], a[16:19], v[236:239]\n"
"ds_read_b128 a[96:99], v240 offset:21632\n"
"v_mfma_f32_16x16x32_bf16 v[20:23], a[116:119], v[200:203], v[20:23]\n"
"v_exp_f32 v169, v169\n"
"v_mfma_f32_16x16x32_bf16 v[192:195], a[84:87], a[36:39], v[192:195]\n"
"s_waitcnt lgkmcnt(10)\n"
"ds_read_b128 a[84:87], v240 offset:20864\n"
"v_mfma_f32_16x16x32_bf16 v[40:43], a[120:123], v[200:203], v[40:43]\n"
"v_exp_f32 v170, v170\n"
"v_mfma_f32_16x16x32_bf16 v[196:199], a[100:103], a[36:39], v[196:199]\n"
"ds_read_b128 a[100:103], v240 offset:21888\n"
"v_mfma_f32_16x16x32_bf16 v[60:63], a[124:127], v[200:203], v[60:63]\n"
"v_exp_f32 v171, v171\n"
"v_mfma_f32_16x16x32_bf16 v[192:195], a[88:91], a[56:59], v[192:195]\n"
"s_waitcnt lgkmcnt(8)\n"
"ds_read_b128 a[88:91], v240 offset:21120\n"
"v_mfma_f32_16x16x32_bf16 v[80:83], a[128:131], v[200:203], v[80:83]\n"
"v_cvt_pk_bf16_f32 v204, v168, v169\n"
"v_mfma_f32_16x16x32_bf16 v[196:199], a[104:107], a[56:59], v[196:199]\n"
"ds_read_b128 a[104:107], v240 offset:22144\n"
"s_add_u32 m0, s48, 16384\n"
"v_mfma_f32_16x16x32_bf16 v[100:103], a[132:135], v[200:203], v[100:103]\n"
"s_add_u32 s52, s81, 131072\n"
"v_exp_f32 v172, v172\n"
"v_mfma_f32_16x16x32_bf16 v[192:195], a[92:95], a[76:79], v[192:195]\n"
"s_waitcnt lgkmcnt(6)\n"
"v_exp_f32 v173, v173\n"
"v_mfma_f32_16x16x32_bf16 v[120:123], a[136:139], v[200:203], v[120:123]\n"
"ds_read_b128 a[92:95], v240 offset:21376\n"
"v_cvt_pk_bf16_f32 v205, v170, v171\n"
"v_mfma_f32_16x16x32_bf16 v[196:199], a[108:111], a[76:79], v[196:199]\n"
"v_exp_f32 v174, v174\n"
"v_exp_f32 v175, v175\n"
"v_cvt_pk_bf16_f32 v206, v172, v173\n"
"v_cvt_pk_bf16_f32 v207, v174, v175\n"
"v_mfma_f32_16x16x32_bf16 v[140:143], a[140:143], v[200:203], v[140:143]\n"
"ds_read_b128 a[108:111], v240 offset:22400\n"
"v_mfma_f32_16x16x32_bf16 a[144:147], a[164:167], v[200:203], a[144:147]\n"
"s_waitcnt lgkmcnt(4)\n"
"v_exp_f32 v176, v176\n"
"v_mfma_f32_16x16x32_bf16 v[160:163], a[80:83], a[0:3], v[220:223]\n"
"v_exp_f32 v177, v177\n"
"v_mfma_f32_16x16x32_bf16 v[4:7], a[112:115], v[204:207], v[4:7]\n"
"v_exp_f32 v178, v178\n"
"v_mfma_f32_16x16x32_bf16 v[164:167], a[96:99], a[0:3], v[220:223]\n"
"v_exp_f32 v179, v179\n"
"v_mfma_f32_16x16x32_bf16 v[24:27], a[116:119], v[204:207], v[24:27]\n"
"buffer_load_dwordx4 v243, s[40:43], s81 offen nt lds\n"
"v_cvt_pk_bf16_f32 v208, v176, v177\n"
"v_mfma_f32_16x16x32_bf16 v[160:163], a[84:87], a[20:23], v[160:163]\n"
"v_exp_f32 v180, v180\n"
"v_mfma_f32_16x16x32_bf16 v[44:47], a[120:123], v[204:207], v[44:47]\n"
"v_exp_f32 v181, v181\n"
"v_mfma_f32_16x16x32_bf16 v[164:167], a[100:103], a[20:23], v[164:167]\n"
"buffer_load_dwordx4 v243, s[40:43], s81 offen offset:1024 nt lds\n"
"v_cvt_pk_bf16_f32 v209, v178, v179\n"
"v_mfma_f32_16x16x32_bf16 v[64:67], a[124:127], v[204:207], v[64:67]\n"
"s_waitcnt lgkmcnt(0)\n"
"v_exp_f32 v182, v182\n"
"v_mfma_f32_16x16x32_bf16 v[160:163], a[88:91], a[40:43], v[160:163]\n"
"s_add_u32 m0, s48, 24576\n"
"v_exp_f32 v183, v183\n"
"v_mfma_f32_16x16x32_bf16 v[84:87], a[128:131], v[204:207], v[84:87]\n"
"s_add_u32 s81, s81, 8192\n"
"v_cvt_pk_bf16_f32 v210, v180, v181\n"
"v_cvt_pk_bf16_f32 v211, v182, v183\n"
"v_mfma_f32_16x16x32_bf16 v[164:167], a[104:107], a[40:43], v[164:167]\n"
"v_exp_f32 v184, v184\n"
"v_mfma_f32_16x16x32_bf16 v[104:107], a[132:135], v[204:207], v[104:107]\n"
"v_exp_f32 v185, v185\n"
"v_mfma_f32_16x16x32_bf16 v[160:163], a[92:95], a[60:63], v[160:163]\n"
"v_exp_f32 v186, v186\n"
"v_mfma_f32_16x16x32_bf16 v[124:127], a[136:139], v[204:207], v[124:127]\n"
"v_exp_f32 v187, v187\n"
"v_mfma_f32_16x16x32_bf16 v[164:167], a[108:111], a[60:63], v[164:167]\n"
"buffer_load_dwordx4 v243, s[40:43], s52 offen nt lds\n"
"v_cvt_pk_bf16_f32 v212, v184, v185\n"
"v_mfma_f32_16x16x32_bf16 v[144:147], a[140:143], v[204:207], v[144:147]\n"
"v_exp_f32 v188, v188\n"
"v_mfma_f32_16x16x32_bf16 a[148:151], a[164:167], v[204:207], a[148:151]\n"
"v_exp_f32 v189, v189\n"
"v_mfma_f32_16x16x32_bf16 v[168:171], a[80:83], a[4:7], v[224:227]\n"
"buffer_load_dwordx4 v243, s[40:43], s52 offen offset:1024 nt lds\n"
"v_cvt_pk_bf16_f32 v213, v186, v187\n"
"v_mfma_f32_16x16x32_bf16 v[8:11], a[112:115], v[208:211], v[8:11]\n"
"s_and_b32 s81, s81, 131071\n"
"v_exp_f32 v190, v190\n"
"v_mfma_f32_16x16x32_bf16 v[172:175], a[96:99], a[4:7], v[224:227]\n"
"v_exp_f32 v191, v191\n"
"v_mfma_f32_16x16x32_bf16 v[28:31], a[116:119], v[208:211], v[28:31]\n"
"v_cvt_pk_bf16_f32 v214, v188, v189\n"
"v_cvt_pk_bf16_f32 v215, v190, v191\n"
"v_mfma_f32_16x16x32_bf16 v[168:171], a[84:87], a[24:27], v[168:171]\n"
"s_add_u32 m0, s48, 32768\n"
"v_exp_f32 v192, v192\n"
"v_mfma_f32_16x16x32_bf16 v[48:51], a[120:123], v[208:211], v[48:51]\n"
"s_add_u32 s52, s95, 131072\n"
"v_exp_f32 v193, v193\n"
"v_mfma_f32_16x16x32_bf16 v[172:175], a[100:103], a[24:27], v[172:175]\n"
"v_exp_f32 v194, v194\n"
"v_mfma_f32_16x16x32_bf16 v[68:71], a[124:127], v[208:211], v[68:71]\n"
"v_exp_f32 v195, v195\n"
"v_mfma_f32_16x16x32_bf16 v[168:171], a[88:91], a[44:47], v[168:171]\n"
"buffer_load_dwordx4 v244, s[60:63], s95 offen lds\n"
"v_cvt_pk_bf16_f32 v216, v192, v193\n"
"v_mfma_f32_16x16x32_bf16 v[88:91], a[128:131], v[208:211], v[88:91]\n"
"v_exp_f32 v196, v196\n"
"v_mfma_f32_16x16x32_bf16 v[172:175], a[104:107], a[44:47], v[172:175]\n"
"v_exp_f32 v197, v197\n"
"v_mfma_f32_16x16x32_bf16 v[108:111], a[132:135], v[208:211], v[108:111]\n"
"buffer_load_dwordx4 v244, s[60:63], s95 offen offset:1024 lds\n"
"v_cvt_pk_bf16_f32 v217, v194, v195\n"
"v_mfma_f32_16x16x32_bf16 v[168:171], a[92:95], a[64:67], v[168:171]\n"
"v_exp_f32 v198, v198\n"
"v_mfma_f32_16x16x32_bf16 v[128:131], a[136:139], v[208:211], v[128:131]\n"
"v_exp_f32 v199, v199\n"
"v_mfma_f32_16x16x32_bf16 v[172:175], a[108:111], a[64:67], v[172:175]\n"
"v_cvt_pk_bf16_f32 v218, v196, v197\n"
"v_cvt_pk_bf16_f32 v219, v198, v199\n"
"v_mfma_f32_16x16x32_bf16 v[148:151], a[140:143], v[208:211], v[148:151]\n"
"v_exp_f32 v160, v160\n"
"v_mfma_f32_16x16x32_bf16 a[152:155], a[164:167], v[208:211], a[152:155]\n"
"v_exp_f32 v161, v161\n"
"v_mfma_f32_16x16x32_bf16 v[176:179], a[80:83], a[8:11], v[228:231]\n"
"v_exp_f32 v162, v162\n"
"v_mfma_f32_16x16x32_bf16 v[12:15], a[112:115], v[212:215], v[12:15]\n"
"s_add_u32 m0, s48, 40960\n"
"v_exp_f32 v163, v163\n"
"v_mfma_f32_16x16x32_bf16 v[180:183], a[96:99], a[8:11], v[228:231]\n"
"s_add_u32 s95, s95, 8192\n"
"v_cvt_pk_bf16_f32 v200, v160, v161\n"
"v_mfma_f32_16x16x32_bf16 v[32:35], a[116:119], v[212:215], v[32:35]\n"
"v_exp_f32 v164, v164\n"
"v_mfma_f32_16x16x32_bf16 v[176:179], a[84:87], a[28:31], v[176:179]\n"
"v_exp_f32 v165, v165\n"
"v_mfma_f32_16x16x32_bf16 v[52:55], a[120:123], v[212:215], v[52:55]\n"
"buffer_load_dwordx4 v244, s[60:63], s52 offen lds\n"
"v_cvt_pk_bf16_f32 v201, v162, v163\n"
"v_mfma_f32_16x16x32_bf16 v[180:183], a[100:103], a[28:31], v[180:183]\n"
"v_exp_f32 v166, v166\n"
"v_mfma_f32_16x16x32_bf16 v[72:75], a[124:127], v[212:215], v[72:75]\n"
"v_exp_f32 v167, v167\n"
"v_mfma_f32_16x16x32_bf16 v[176:179], a[88:91], a[48:51], v[176:179]\n"
"v_cvt_pk_bf16_f32 v202, v164, v165\n"
"v_cvt_pk_bf16_f32 v203, v166, v167\n"
"v_mfma_f32_16x16x32_bf16 v[92:95], a[128:131], v[212:215], v[92:95]\n"
"buffer_load_dwordx4 v244, s[60:63], s52 offen offset:1024 lds\n"
"s_and_b32 s95, s95, 131071\n"
"v_mfma_f32_16x16x32_bf16 v[180:183], a[104:107], a[48:51], v[180:183]\n"
"v_mfma_f32_16x16x32_bf16 v[112:115], a[132:135], v[212:215], v[112:115]\n"
"v_add_u32 v241, 49152, v241\n"
"v_add_u32 v242, 49152, v242\n"
"v_mfma_f32_16x16x32_bf16 v[176:179], a[92:95], a[68:71], v[176:179]\n"
"v_mfma_f32_16x16x32_bf16 v[132:135], a[136:139], v[212:215], v[132:135]\n"
"v_mfma_f32_16x16x32_bf16 v[180:183], a[108:111], a[68:71], v[180:183]\n"
"v_mfma_f32_16x16x32_bf16 v[152:155], a[140:143], v[212:215], v[152:155]\n"
"v_mfma_f32_16x16x32_bf16 a[156:159], a[164:167], v[212:215], a[156:159]\n"
"s_waitcnt vmcnt(16) lgkmcnt(0)\n"
"s_barrier\n"
"v_mfma_f32_16x16x32_bf16 v[184:187], a[80:83], a[12:15], v[232:235]\n"
"v_mfma_f32_16x16x32_bf16 v[16:19], a[112:115], v[216:219], v[16:19]\n"
"ds_read_b64_tr_b16 a[112:113], v241 offset:24832\n"
"v_mfma_f32_16x16x32_bf16 v[188:191], a[96:99], a[12:15], v[232:235]\n"
"ds_read_b64_tr_b16 a[114:115], v241 offset:25856\n"
"v_mfma_f32_16x16x32_bf16 v[36:39], a[116:119], v[216:219], v[36:39]\n"
"ds_read_b64_tr_b16 a[116:117], v242 offset:24832\n"
"v_mfma_f32_16x16x32_bf16 v[184:187], a[84:87], a[32:35], v[184:187]\n"
"ds_read_b64_tr_b16 a[118:119], v242 offset:25856\n"
"v_mfma_f32_16x16x32_bf16 v[56:59], a[120:123], v[216:219], v[56:59]\n"
"ds_read_b64_tr_b16 a[120:121], v241 offset:25088\n"
"v_mfma_f32_16x16x32_bf16 v[188:191], a[100:103], a[32:35], v[188:191]\n"
"ds_read_b64_tr_b16 a[122:123], v241 offset:26112\n"
"v_mfma_f32_16x16x32_bf16 v[76:79], a[124:127], v[216:219], v[76:79]\n"
"ds_read_b64_tr_b16 a[124:125], v242 offset:25088\n"
"v_mfma_f32_16x16x32_bf16 v[184:187], a[88:91], a[52:55], v[184:187]\n"
"ds_read_b64_tr_b16 a[126:127], v242 offset:26112\n"
"v_mfma_f32_16x16x32_bf16 v[96:99], a[128:131], v[216:219], v[96:99]\n"
"ds_read_b64_tr_b16 a[128:129], v241 offset:25344\n"
"v_mfma_f32_16x16x32_bf16 v[188:191], a[104:107], a[52:55], v[188:191]\n"
"ds_read_b64_tr_b16 a[130:131], v241 offset:26368\n"
"v_mfma_f32_16x16x32_bf16 v[116:119], a[132:135], v[216:219], v[116:119]\n"
"ds_read_b64_tr_b16 a[132:133], v242 offset:25344\n"
"v_mfma_f32_16x16x32_bf16 v[184:187], a[92:95], a[72:75], v[184:187]\n"
"ds_read_b64_tr_b16 a[134:135], v242 offset:26368\n"
"v_mfma_f32_16x16x32_bf16 v[136:139], a[136:139], v[216:219], v[136:139]\n"
"ds_read_b64_tr_b16 a[136:137], v241 offset:25600\n"
"v_mfma_f32_16x16x32_bf16 v[188:191], a[108:111], a[72:75], v[188:191]\n"
"ds_read_b64_tr_b16 a[138:139], v241 offset:26624\n"
"v_mfma_f32_16x16x32_bf16 v[156:159], a[140:143], v[216:219], v[156:159]\n"
"ds_read_b64_tr_b16 a[140:141], v242 offset:25600\n"
"v_mfma_f32_16x16x32_bf16 a[160:163], a[164:167], v[216:219], a[160:163]\n"
"ds_read_b64_tr_b16 a[142:143], v242 offset:26624\n"
"v_add_u32 v240, 49152, v240\n"
"v_mfma_f32_16x16x32_bf16 v[192:195], a[80:83], a[16:19], v[236:239]\n"
"s_waitcnt lgkmcnt(12)\n"
"ds_read_b128 a[80:83], v240 offset:8448\n"
"v_mfma_f32_16x16x32_bf16 v[0:3], a[112:115], v[200:203], v[0:3]\n"
"v_exp_f32 v168, v168\n"
"v_mfma_f32_16x16x32_bf16 v[196:199], a[96:99], a[16:19], v[236:239]\n"
"ds_read_b128 a[96:99], v240 offset:9472\n"
"v_mfma_f32_16x16x32_bf16 v[20:23], a[116:119], v[200:203], v[20:23]\n"
"v_exp_f32 v169, v169\n"
"v_mfma_f32_16x16x32_bf16 v[192:195], a[84:87], a[36:39], v[192:195]\n"
"s_waitcnt lgkmcnt(10)\n"
"ds_read_b128 a[84:87], v240 offset:8704\n"
"v_mfma_f32_16x16x32_bf16 v[40:43], a[120:123], v[200:203], v[40:43]\n"
"v_exp_f32 v170, v170\n"
"v_mfma_f32_16x16x32_bf16 v[196:199], a[100:103], a[36:39], v[196:199]\n"
"ds_read_b128 a[100:103], v240 offset:9728\n"
"v_mfma_f32_16x16x32_bf16 v[60:63], a[124:127], v[200:203], v[60:63]\n"
"v_exp_f32 v171, v171\n"
"v_mfma_f32_16x16x32_bf16 v[192:195], a[88:91], a[56:59], v[192:195]\n"
"s_waitcnt lgkmcnt(8)\n"
"ds_read_b128 a[88:91], v240 offset:8960\n"
"v_mfma_f32_16x16x32_bf16 v[80:83], a[128:131], v[200:203], v[80:83]\n"
"v_cvt_pk_bf16_f32 v204, v168, v169\n"
"v_mfma_f32_16x16x32_bf16 v[196:199], a[104:107], a[56:59], v[196:199]\n"
"ds_read_b128 a[104:107], v240 offset:9984\n"
"s_add_u32 m0, s48, 53376\n"
"v_mfma_f32_16x16x32_bf16 v[100:103], a[132:135], v[200:203], v[100:103]\n"
"s_add_u32 s52, s81, 131072\n"
"v_exp_f32 v172, v172\n"
"v_mfma_f32_16x16x32_bf16 v[192:195], a[92:95], a[76:79], v[192:195]\n"
"s_waitcnt lgkmcnt(6)\n"
"v_exp_f32 v173, v173\n"
"v_mfma_f32_16x16x32_bf16 v[120:123], a[136:139], v[200:203], v[120:123]\n"
"ds_read_b128 a[92:95], v240 offset:9216\n"
"v_cvt_pk_bf16_f32 v205, v170, v171\n"
"v_mfma_f32_16x16x32_bf16 v[196:199], a[108:111], a[76:79], v[196:199]\n"
"v_exp_f32 v174, v174\n"
"v_exp_f32 v175, v175\n"
"v_cvt_pk_bf16_f32 v206, v172, v173\n"
"v_cvt_pk_bf16_f32 v207, v174, v175\n"
"v_mfma_f32_16x16x32_bf16 v[140:143], a[140:143], v[200:203], v[140:143]\n"
"ds_read_b128 a[108:111], v240 offset:10240\n"
"v_mfma_f32_16x16x32_bf16 a[144:147], a[164:167], v[200:203], a[144:147]\n"
"s_waitcnt lgkmcnt(4)\n"
"v_exp_f32 v176, v176\n"
"v_mfma_f32_16x16x32_bf16 v[160:163], a[80:83], a[0:3], v[220:223]\n"
"v_exp_f32 v177, v177\n"
"v_mfma_f32_16x16x32_bf16 v[4:7], a[112:115], v[204:207], v[4:7]\n"
"v_exp_f32 v178, v178\n"
"v_mfma_f32_16x16x32_bf16 v[164:167], a[96:99], a[0:3], v[220:223]\n"
"v_exp_f32 v179, v179\n"
"v_mfma_f32_16x16x32_bf16 v[24:27], a[116:119], v[204:207], v[24:27]\n"
"buffer_load_dwordx4 v243, s[40:43], s81 offen nt lds\n"
"v_cvt_pk_bf16_f32 v208, v176, v177\n"
"v_mfma_f32_16x16x32_bf16 v[160:163], a[84:87], a[20:23], v[160:163]\n"
"v_exp_f32 v180, v180\n"
"v_mfma_f32_16x16x32_bf16 v[44:47], a[120:123], v[204:207], v[44:47]\n"
"v_exp_f32 v181, v181\n"
"v_mfma_f32_16x16x32_bf16 v[164:167], a[100:103], a[20:23], v[164:167]\n"
"buffer_load_dwordx4 v243, s[40:43], s81 offen offset:1024 nt lds\n"
"v_cvt_pk_bf16_f32 v209, v178, v179\n"
"v_mfma_f32_16x16x32_bf16 v[64:67], a[124:127], v[204:207], v[64:67]\n"
"s_waitcnt lgkmcnt(0)\n"
"v_exp_f32 v182, v182\n"
"v_mfma_f32_16x16x32_bf16 v[160:163], a[88:91], a[40:43], v[160:163]\n"
"s_add_u32 m0, s48, 61568\n"
"v_exp_f32 v183, v183\n"
"v_mfma_f32_16x16x32_bf16 v[84:87], a[128:131], v[204:207], v[84:87]\n"
"s_add_u32 s81, s81, 8192\n"
"v_cvt_pk_bf16_f32 v210, v180, v181\n"
"v_cvt_pk_bf16_f32 v211, v182, v183\n"
"v_mfma_f32_16x16x32_bf16 v[164:167], a[104:107], a[40:43], v[164:167]\n"
"v_exp_f32 v184, v184\n"
"v_mfma_f32_16x16x32_bf16 v[104:107], a[132:135], v[204:207], v[104:107]\n"
"v_exp_f32 v185, v185\n"
"v_mfma_f32_16x16x32_bf16 v[160:163], a[92:95], a[60:63], v[160:163]\n"
"v_exp_f32 v186, v186\n"
"v_mfma_f32_16x16x32_bf16 v[124:127], a[136:139], v[204:207], v[124:127]\n"
"v_exp_f32 v187, v187\n"
"v_mfma_f32_16x16x32_bf16 v[164:167], a[108:111], a[60:63], v[164:167]\n"
"buffer_load_dwordx4 v243, s[40:43], s52 offen nt lds\n"
"v_cvt_pk_bf16_f32 v212, v184, v185\n"
"v_mfma_f32_16x16x32_bf16 v[144:147], a[140:143], v[204:207], v[144:147]\n"
"v_exp_f32 v188, v188\n"
"v_mfma_f32_16x16x32_bf16 a[148:151], a[164:167], v[204:207], a[148:151]\n"
"v_exp_f32 v189, v189\n"
"v_mfma_f32_16x16x32_bf16 v[168:171], a[80:83], a[4:7], v[224:227]\n"
"buffer_load_dwordx4 v243, s[40:43], s52 offen offset:1024 nt lds\n"
"v_cvt_pk_bf16_f32 v213, v186, v187\n"
"v_mfma_f32_16x16x32_bf16 v[8:11], a[112:115], v[208:211], v[8:11]\n"
"s_and_b32 s81, s81, 131071\n"
"v_exp_f32 v190, v190\n"
"v_mfma_f32_16x16x32_bf16 v[172:175], a[96:99], a[4:7], v[224:227]\n"
"v_exp_f32 v191, v191\n"
"v_mfma_f32_16x16x32_bf16 v[28:31], a[116:119], v[208:211], v[28:31]\n"
"v_cvt_pk_bf16_f32 v214, v188, v189\n"
"v_cvt_pk_bf16_f32 v215, v190, v191\n"
"v_mfma_f32_16x16x32_bf16 v[168:171], a[84:87], a[24:27], v[168:171]\n"
"s_add_u32 m0, s48, 69760\n"
"v_exp_f32 v192, v192\n"
"v_mfma_f32_16x16x32_bf16 v[48:51], a[120:123], v[208:211], v[48:51]\n"
"s_add_u32 s52, s95, 131072\n"
"v_exp_f32 v193, v193\n"
"v_mfma_f32_16x16x32_bf16 v[172:175], a[100:103], a[24:27], v[172:175]\n"
"v_exp_f32 v194, v194\n"
"v_mfma_f32_16x16x32_bf16 v[68:71], a[124:127], v[208:211], v[68:71]\n"
"v_exp_f32 v195, v195\n"
"v_mfma_f32_16x16x32_bf16 v[168:171], a[88:91], a[44:47], v[168:171]\n"
"buffer_load_dwordx4 v244, s[60:63], s95 offen lds\n"
"v_cvt_pk_bf16_f32 v216, v192, v193\n"
"v_mfma_f32_16x16x32_bf16 v[88:91], a[128:131], v[208:211], v[88:91]\n"
"v_exp_f32 v196, v196\n"
"v_mfma_f32_16x16x32_bf16 v[172:175], a[104:107], a[44:47], v[172:175]\n"
"v_exp_f32 v197, v197\n"
"v_mfma_f32_16x16x32_bf16 v[108:111], a[132:135], v[208:211], v[108:111]\n"
"buffer_load_dwordx4 v244, s[60:63], s95 offen offset:1024 lds\n"
"v_cvt_pk_bf16_f32 v217, v194, v195\n"
"v_mfma_f32_16x16x32_bf16 v[168:171], a[92:95], a[64:67], v[168:171]\n"
"v_exp_f32 v198, v198\n"
"v_mfma_f32_16x16x32_bf16 v[128:131], a[136:139], v[208:211], v[128:131]\n"
"v_exp_f32 v199, v199\n"
"v_mfma_f32_16x16x32_bf16 v[172:175], a[108:111], a[64:67], v[172:175]\n"
"v_cvt_pk_bf16_f32 v218, v196, v197\n"
"v_cvt_pk_bf16_f32 v219, v198, v199\n"
"v_mfma_f32_16x16x32_bf16 v[148:151], a[140:143], v[208:211], v[148:151]\n"
"v_exp_f32 v160, v160\n"
"v_mfma_f32_16x16x32_bf16 a[152:155], a[164:167], v[208:211], a[152:155]\n"
"v_exp_f32 v161, v161\n"
"v_mfma_f32_16x16x32_bf16 v[176:179], a[80:83], a[8:11], v[228:231]\n"
"v_exp_f32 v162, v162\n"
"v_mfma_f32_16x16x32_bf16 v[12:15], a[112:115], v[212:215], v[12:15]\n"
"s_add_u32 m0, s48, 77952\n"
"v_exp_f32 v163, v163\n"
"v_mfma_f32_16x16x32_bf16 v[180:183], a[96:99], a[8:11], v[228:231]\n"
"s_add_u32 s95, s95, 8192\n"
"v_cvt_pk_bf16_f32 v200, v160, v161\n"
"v_mfma_f32_16x16x32_bf16 v[32:35], a[116:119], v[212:215], v[32:35]\n"
"v_exp_f32 v164, v164\n"
"v_mfma_f32_16x16x32_bf16 v[176:179], a[84:87], a[28:31], v[176:179]\n"
"v_exp_f32 v165, v165\n"
"v_mfma_f32_16x16x32_bf16 v[52:55], a[120:123], v[212:215], v[52:55]\n"
"buffer_load_dwordx4 v244, s[60:63], s52 offen lds\n"
"v_cvt_pk_bf16_f32 v201, v162, v163\n"
"v_mfma_f32_16x16x32_bf16 v[180:183], a[100:103], a[28:31], v[180:183]\n"
"v_exp_f32 v166, v166\n"
"v_mfma_f32_16x16x32_bf16 v[72:75], a[124:127], v[212:215], v[72:75]\n"
"v_exp_f32 v167, v167\n"
"v_mfma_f32_16x16x32_bf16 v[176:179], a[88:91], a[48:51], v[176:179]\n"
"v_cvt_pk_bf16_f32 v202, v164, v165\n"
"v_cvt_pk_bf16_f32 v203, v166, v167\n"
"v_mfma_f32_16x16x32_bf16 v[92:95], a[128:131], v[212:215], v[92:95]\n"
"buffer_load_dwordx4 v244, s[60:63], s52 offen offset:1024 lds\n"
"s_and_b32 s95, s95, 131071\n"
"v_mfma_f32_16x16x32_bf16 v[180:183], a[104:107], a[48:51], v[180:183]\n"
"v_mfma_f32_16x16x32_bf16 v[112:115], a[132:135], v[212:215], v[112:115]\n"
"v_add_u32 v241, 4294868992, v241\n"
"v_add_u32 v242, 4294868992, v242\n"
"v_mfma_f32_16x16x32_bf16 v[176:179], a[92:95], a[68:71], v[176:179]\n"
"v_mfma_f32_16x16x32_bf16 v[132:135], a[136:139], v[212:215], v[132:135]\n"
"v_mfma_f32_16x16x32_bf16 v[180:183], a[108:111], a[68:71], v[180:183]\n"
"v_mfma_f32_16x16x32_bf16 v[152:155], a[140:143], v[212:215], v[152:155]\n"
"v_mfma_f32_16x16x32_bf16 a[156:159], a[164:167], v[212:215], a[156:159]\n"
"s_waitcnt vmcnt(16) lgkmcnt(0)\n"
"s_barrier\n"
"v_mfma_f32_16x16x32_bf16 v[184:187], a[80:83], a[12:15], v[232:235]\n"
"v_mfma_f32_16x16x32_bf16 v[16:19], a[112:115], v[216:219], v[16:19]\n"
"ds_read_b64_tr_b16 a[112:113], v241 offset:0\n"
"v_mfma_f32_16x16x32_bf16 v[188:191], a[96:99], a[12:15], v[232:235]\n"
"ds_read_b64_tr_b16 a[114:115], v241 offset:1024\n"
"v_mfma_f32_16x16x32_bf16 v[36:39], a[116:119], v[216:219], v[36:39]\n"
"ds_read_b64_tr_b16 a[116:117], v242 offset:0\n"
"v_mfma_f32_16x16x32_bf16 v[184:187], a[84:87], a[32:35], v[184:187]\n"
"ds_read_b64_tr_b16 a[118:119], v242 offset:1024\n"
"v_mfma_f32_16x16x32_bf16 v[56:59], a[120:123], v[216:219], v[56:59]\n"
"ds_read_b64_tr_b16 a[120:121], v241 offset:256\n"
"v_mfma_f32_16x16x32_bf16 v[188:191], a[100:103], a[32:35], v[188:191]\n"
"ds_read_b64_tr_b16 a[122:123], v241 offset:1280\n"
"v_mfma_f32_16x16x32_bf16 v[76:79], a[124:127], v[216:219], v[76:79]\n"
"ds_read_b64_tr_b16 a[124:125], v242 offset:256\n"
"v_mfma_f32_16x16x32_bf16 v[184:187], a[88:91], a[52:55], v[184:187]\n"
"ds_read_b64_tr_b16 a[126:127], v242 offset:1280\n"
"v_mfma_f32_16x16x32_bf16 v[96:99], a[128:131], v[216:219], v[96:99]\n"
"ds_read_b64_tr_b16 a[128:129], v241 offset:512\n"
"v_mfma_f32_16x16x32_bf16 v[188:191], a[104:107], a[52:55], v[188:191]\n"
"ds_read_b64_tr_b16 a[130:131], v241 offset:1536\n"
"v_mfma_f32_16x16x32_bf16 v[116:119], a[132:135], v[216:219], v[116:119]\n"
"ds_read_b64_tr_b16 a[132:133], v242 offset:512\n"
"v_mfma_f32_16x16x32_bf16 v[184:187], a[92:95], a[72:75], v[184:187]\n"
"ds_read_b64_tr_b16 a[134:135], v242 offset:1536\n"
"v_mfma_f32_16x16x32_bf16 v[136:139], a[136:139], v[216:219], v[136:139]\n"
"ds_read_b64_tr_b16 a[136:137], v241 offset:768\n"
"v_mfma_f32_16x16x32_bf16 v[188:191], a[108:111], a[72:75], v[188:191]\n"
"ds_read_b64_tr_b16 a[138:139], v241 offset:1792\n"
"v_mfma_f32_16x16x32_bf16 v[156:159], a[140:143], v[216:219], v[156:159]\n"
"ds_read_b64_tr_b16 a[140:141], v242 offset:768\n"
"v_mfma_f32_16x16x32_bf16 a[160:163], a[164:167], v[216:219], a[160:163]\n"
"ds_read_b64_tr_b16 a[142:143], v242 offset:1792\n"
"v_mfma_f32_16x16x32_bf16 v[192:195], a[80:83], a[16:19], v[236:239]\n"
"s_waitcnt lgkmcnt(12)\n"
"ds_read_b128 a[80:83], v240 offset:41216\n"
"v_mfma_f32_16x16x32_bf16 v[0:3], a[112:115], v[200:203], v[0:3]\n"
"v_exp_f32 v168, v168\n"
"v_mfma_f32_16x16x32_bf16 v[196:199], a[96:99], a[16:19], v[236:239]\n"
"ds_read_b128 a[96:99], v240 offset:42240\n"
"v_mfma_f32_16x16x32_bf16 v[20:23], a[116:119], v[200:203], v[20:23]\n"
"v_exp_f32 v169, v169\n"
"v_mfma_f32_16x16x32_bf16 v[192:195], a[84:87], a[36:39], v[192:195]\n"
"s_waitcnt lgkmcnt(10)\n"
"ds_read_b128 a[84:87], v240 offset:41472\n"
"v_mfma_f32_16x16x32_bf16 v[40:43], a[120:123], v[200:203], v[40:43]\n"
"v_exp_f32 v170, v170\n"
"v_mfma_f32_16x16x32_bf16 v[196:199], a[100:103], a[36:39], v[196:199]\n"
"ds_read_b128 a[100:103], v240 offset:42496\n"
"v_mfma_f32_16x16x32_bf16 v[60:63], a[124:127], v[200:203], v[60:63]\n"
"v_exp_f32 v171, v171\n"
"v_mfma_f32_16x16x32_bf16 v[192:195], a[88:91], a[56:59], v[192:195]\n"
"s_waitcnt lgkmcnt(8)\n"
"ds_read_b128 a[88:91], v240 offset:41728\n"
"v_mfma_f32_16x16x32_bf16 v[80:83], a[128:131], v[200:203], v[80:83]\n"
"v_cvt_pk_bf16_f32 v204, v168, v169\n"
"v_mfma_f32_16x16x32_bf16 v[196:199], a[104:107], a[56:59], v[196:199]\n"
"ds_read_b128 a[104:107], v240 offset:42752\n"
"s_add_u32 m0, s48, 86144\n"
"v_mfma_f32_16x16x32_bf16 v[100:103], a[132:135], v[200:203], v[100:103]\n"
"s_add_u32 s52, s81, 131072\n"
"v_exp_f32 v172, v172\n"
"v_mfma_f32_16x16x32_bf16 v[192:195], a[92:95], a[76:79], v[192:195]\n"
"s_waitcnt lgkmcnt(6)\n"
"v_exp_f32 v173, v173\n"
"v_mfma_f32_16x16x32_bf16 v[120:123], a[136:139], v[200:203], v[120:123]\n"
"ds_read_b128 a[92:95], v240 offset:41984\n"
"v_cvt_pk_bf16_f32 v205, v170, v171\n"
"v_mfma_f32_16x16x32_bf16 v[196:199], a[108:111], a[76:79], v[196:199]\n"
"v_exp_f32 v174, v174\n"
"v_exp_f32 v175, v175\n"
"v_cvt_pk_bf16_f32 v206, v172, v173\n"
"v_cvt_pk_bf16_f32 v207, v174, v175\n"
"v_mfma_f32_16x16x32_bf16 v[140:143], a[140:143], v[200:203], v[140:143]\n"
"ds_read_b128 a[108:111], v240 offset:43008\n"
"v_mfma_f32_16x16x32_bf16 a[144:147], a[164:167], v[200:203], a[144:147]\n"
"s_waitcnt lgkmcnt(4)\n"
"v_exp_f32 v176, v176\n"
"v_mfma_f32_16x16x32_bf16 v[160:163], a[80:83], a[0:3], v[220:223]\n"
"v_exp_f32 v177, v177\n"
"v_mfma_f32_16x16x32_bf16 v[4:7], a[112:115], v[204:207], v[4:7]\n"
"v_exp_f32 v178, v178\n"
"v_mfma_f32_16x16x32_bf16 v[164:167], a[96:99], a[0:3], v[220:223]\n"
"v_exp_f32 v179, v179\n"
"v_mfma_f32_16x16x32_bf16 v[24:27], a[116:119], v[204:207], v[24:27]\n"
"buffer_load_dwordx4 v243, s[40:43], s81 offen nt lds\n"
"v_cvt_pk_bf16_f32 v208, v176, v177\n"
"v_mfma_f32_16x16x32_bf16 v[160:163], a[84:87], a[20:23], v[160:163]\n"
"v_exp_f32 v180, v180\n"
"v_mfma_f32_16x16x32_bf16 v[44:47], a[120:123], v[204:207], v[44:47]\n"
"v_exp_f32 v181, v181\n"
"v_mfma_f32_16x16x32_bf16 v[164:167], a[100:103], a[20:23], v[164:167]\n"
"buffer_load_dwordx4 v243, s[40:43], s81 offen offset:1024 nt lds\n"
"v_cvt_pk_bf16_f32 v209, v178, v179\n"
"v_mfma_f32_16x16x32_bf16 v[64:67], a[124:127], v[204:207], v[64:67]\n"
"s_waitcnt lgkmcnt(0)\n"
"v_exp_f32 v182, v182\n"
"v_mfma_f32_16x16x32_bf16 v[160:163], a[88:91], a[40:43], v[160:163]\n"
"s_add_u32 m0, s48, 94336\n"
"v_exp_f32 v183, v183\n"
"v_mfma_f32_16x16x32_bf16 v[84:87], a[128:131], v[204:207], v[84:87]\n"
"s_add_u32 s81, s81, 8192\n"
"v_cvt_pk_bf16_f32 v210, v180, v181\n"
"v_cvt_pk_bf16_f32 v211, v182, v183\n"
"v_mfma_f32_16x16x32_bf16 v[164:167], a[104:107], a[40:43], v[164:167]\n"
"v_exp_f32 v184, v184\n"
"v_mfma_f32_16x16x32_bf16 v[104:107], a[132:135], v[204:207], v[104:107]\n"
"v_exp_f32 v185, v185\n"
"v_mfma_f32_16x16x32_bf16 v[160:163], a[92:95], a[60:63], v[160:163]\n"
"v_exp_f32 v186, v186\n"
"v_mfma_f32_16x16x32_bf16 v[124:127], a[136:139], v[204:207], v[124:127]\n"
"v_exp_f32 v187, v187\n"
"v_mfma_f32_16x16x32_bf16 v[164:167], a[108:111], a[60:63], v[164:167]\n"
"buffer_load_dwordx4 v243, s[40:43], s52 offen nt lds\n"
"v_cvt_pk_bf16_f32 v212, v184, v185\n"
"v_mfma_f32_16x16x32_bf16 v[144:147], a[140:143], v[204:207], v[144:147]\n"
"v_exp_f32 v188, v188\n"
"v_mfma_f32_16x16x32_bf16 a[148:151], a[164:167], v[204:207], a[148:151]\n"
"v_exp_f32 v189, v189\n"
"v_mfma_f32_16x16x32_bf16 v[168:171], a[80:83], a[4:7], v[224:227]\n"
"buffer_load_dwordx4 v243, s[40:43], s52 offen offset:1024 nt lds\n"
"v_cvt_pk_bf16_f32 v213, v186, v187\n"
"v_mfma_f32_16x16x32_bf16 v[8:11], a[112:115], v[208:211], v[8:11]\n"
"s_and_b32 s81, s81, 131071\n"
"v_exp_f32 v190, v190\n"
"v_mfma_f32_16x16x32_bf16 v[172:175], a[96:99], a[4:7], v[224:227]\n"
"v_exp_f32 v191, v191\n"
"v_mfma_f32_16x16x32_bf16 v[28:31], a[116:119], v[208:211], v[28:31]\n"
"v_cvt_pk_bf16_f32 v214, v188, v189\n"
"v_cvt_pk_bf16_f32 v215, v190, v191\n"
"v_mfma_f32_16x16x32_bf16 v[168:171], a[84:87], a[24:27], v[168:171]\n"
"s_add_u32 m0, s48, 106752\n"
"v_exp_f32 v192, v192\n"
"v_mfma_f32_16x16x32_bf16 v[48:51], a[120:123], v[208:211], v[48:51]\n"
"s_add_u32 s52, s95, 131072\n"
"v_exp_f32 v193, v193\n"
"v_mfma_f32_16x16x32_bf16 v[172:175], a[100:103], a[24:27], v[172:175]\n"
"v_exp_f32 v194, v194\n"
"v_mfma_f32_16x16x32_bf16 v[68:71], a[124:127], v[208:211], v[68:71]\n"
"v_exp_f32 v195, v195\n"
"v_mfma_f32_16x16x32_bf16 v[168:171], a[88:91], a[44:47], v[168:171]\n"
"buffer_load_dwordx4 v244, s[60:63], s95 offen lds\n"
"v_cvt_pk_bf16_f32 v216, v192, v193\n"
"v_mfma_f32_16x16x32_bf16 v[88:91], a[128:131], v[208:211], v[88:91]\n"
"v_exp_f32 v196, v196\n"
"v_mfma_f32_16x16x32_bf16 v[172:175], a[104:107], a[44:47], v[172:175]\n"
"v_exp_f32 v197, v197\n"
"v_mfma_f32_16x16x32_bf16 v[108:111], a[132:135], v[208:211], v[108:111]\n"
"buffer_load_dwordx4 v244, s[60:63], s95 offen offset:1024 lds\n"
"v_cvt_pk_bf16_f32 v217, v194, v195\n"
"v_mfma_f32_16x16x32_bf16 v[168:171], a[92:95], a[64:67], v[168:171]\n"
"v_exp_f32 v198, v198\n"
"v_mfma_f32_16x16x32_bf16 v[128:131], a[136:139], v[208:211], v[128:131]\n"
"v_exp_f32 v199, v199\n"
"v_mfma_f32_16x16x32_bf16 v[172:175], a[108:111], a[64:67], v[172:175]\n"
"v_cvt_pk_bf16_f32 v218, v196, v197\n"
"v_cvt_pk_bf16_f32 v219, v198, v199\n"
"v_mfma_f32_16x16x32_bf16 v[148:151], a[140:143], v[208:211], v[148:151]\n"
"v_exp_f32 v160, v160\n"
"v_mfma_f32_16x16x32_bf16 a[152:155], a[164:167], v[208:211], a[152:155]\n"
"v_exp_f32 v161, v161\n"
"v_mfma_f32_16x16x32_bf16 v[176:179], a[80:83], a[8:11], v[228:231]\n"
"v_exp_f32 v162, v162\n"
"v_mfma_f32_16x16x32_bf16 v[12:15], a[112:115], v[212:215], v[12:15]\n"
"s_add_u32 m0, s48, 114944\n"
"v_exp_f32 v163, v163\n"
"v_mfma_f32_16x16x32_bf16 v[180:183], a[96:99], a[8:11], v[228:231]\n"
"s_add_u32 s95, s95, 8192\n"
"v_cvt_pk_bf16_f32 v200, v160, v161\n"
"v_mfma_f32_16x16x32_bf16 v[32:35], a[116:119], v[212:215], v[32:35]\n"
"v_exp_f32 v164, v164\n"
"v_mfma_f32_16x16x32_bf16 v[176:179], a[84:87], a[28:31], v[176:179]\n"
"v_exp_f32 v165, v165\n"
"v_mfma_f32_16x16x32_bf16 v[52:55], a[120:123], v[212:215], v[52:55]\n"
"buffer_load_dwordx4 v244, s[60:63], s52 offen lds\n"
"v_cvt_pk_bf16_f32 v201, v162, v163\n"
"v_mfma_f32_16x16x32_bf16 v[180:183], a[100:103], a[28:31], v[180:183]\n"
"v_exp_f32 v166, v166\n"
"v_mfma_f32_16x16x32_bf16 v[72:75], a[124:127], v[212:215], v[72:75]\n"
"v_exp_f32 v167, v167\n"
"v_mfma_f32_16x16x32_bf16 v[176:179], a[88:91], a[48:51], v[176:179]\n"
"v_cvt_pk_bf16_f32 v202, v164, v165\n"
"v_cvt_pk_bf16_f32 v203, v166, v167\n"
"v_mfma_f32_16x16x32_bf16 v[92:95], a[128:131], v[212:215], v[92:95]\n"
"buffer_load_dwordx4 v244, s[60:63], s52 offen offset:1024 lds\n"
"s_and_b32 s95, s95, 131071\n"
"v_mfma_f32_16x16x32_bf16 v[180:183], a[104:107], a[48:51], v[180:183]\n"
"v_mfma_f32_16x16x32_bf16 v[112:115], a[132:135], v[212:215], v[112:115]\n"
"v_mfma_f32_16x16x32_bf16 v[176:179], a[92:95], a[68:71], v[176:179]\n"
"v_mfma_f32_16x16x32_bf16 v[132:135], a[136:139], v[212:215], v[132:135]\n"
"v_mfma_f32_16x16x32_bf16 v[180:183], a[108:111], a[68:71], v[180:183]\n"
"v_mfma_f32_16x16x32_bf16 v[152:155], a[140:143], v[212:215], v[152:155]\n"
"v_mfma_f32_16x16x32_bf16 a[156:159], a[164:167], v[212:215], a[156:159]\n"
"s_waitcnt vmcnt(16) lgkmcnt(0)\n"
"s_barrier\n"
"v_mfma_f32_16x16x32_bf16 v[184:187], a[80:83], a[12:15], v[232:235]\n"
"v_mfma_f32_16x16x32_bf16 v[16:19], a[112:115], v[216:219], v[16:19]\n"
"ds_read_b64_tr_b16 a[112:113], v241 offset:32768\n"
"v_mfma_f32_16x16x32_bf16 v[188:191], a[96:99], a[12:15], v[232:235]\n"
"ds_read_b64_tr_b16 a[114:115], v241 offset:33792\n"
"v_mfma_f32_16x16x32_bf16 v[36:39], a[116:119], v[216:219], v[36:39]\n"
"ds_read_b64_tr_b16 a[116:117], v242 offset:32768\n"
"v_mfma_f32_16x16x32_bf16 v[184:187], a[84:87], a[32:35], v[184:187]\n"
"ds_read_b64_tr_b16 a[118:119], v242 offset:33792\n"
"v_mfma_f32_16x16x32_bf16 v[56:59], a[120:123], v[216:219], v[56:59]\n"
"ds_read_b64_tr_b16 a[120:121], v241 offset:33024\n"
"v_mfma_f32_16x16x32_bf16 v[188:191], a[100:103], a[32:35], v[188:191]\n"
"ds_read_b64_tr_b16 a[122:123], v241 offset:34048\n"
"v_mfma_f32_16x16x32_bf16 v[76:79], a[124:127], v[216:219], v[76:79]\n"
"ds_read_b64_tr_b16 a[124:125], v242 offset:33024\n"
"v_mfma_f32_16x16x32_bf16 v[184:187], a[88:91], a[52:55], v[184:187]\n"
"ds_read_b64_tr_b16 a[126:127], v242 offset:34048\n"
"v_mfma_f32_16x16x32_bf16 v[96:99], a[128:131], v[216:219], v[96:99]\n"
"ds_read_b64_tr_b16 a[128:129], v241 offset:33280\n"
"v_mfma_f32_16x16x32_bf16 v[188:191], a[104:107], a[52:55], v[188:191]\n"
"ds_read_b64_tr_b16 a[130:131], v241 offset:34304\n"
"v_mfma_f32_16x16x32_bf16 v[116:119], a[132:135], v[216:219], v[116:119]\n"
"ds_read_b64_tr_b16 a[132:133], v242 offset:33280\n"
"v_mfma_f32_16x16x32_bf16 v[184:187], a[92:95], a[72:75], v[184:187]\n"
"ds_read_b64_tr_b16 a[134:135], v242 offset:34304\n"
"v_mfma_f32_16x16x32_bf16 v[136:139], a[136:139], v[216:219], v[136:139]\n"
"ds_read_b64_tr_b16 a[136:137], v241 offset:33536\n"
"v_mfma_f32_16x16x32_bf16 v[188:191], a[108:111], a[72:75], v[188:191]\n"
"ds_read_b64_tr_b16 a[138:139], v241 offset:34560\n"
"v_mfma_f32_16x16x32_bf16 v[156:159], a[140:143], v[216:219], v[156:159]\n"
"ds_read_b64_tr_b16 a[140:141], v242 offset:33536\n"
"v_mfma_f32_16x16x32_bf16 a[160:163], a[164:167], v[216:219], a[160:163]\n"
"ds_read_b64_tr_b16 a[142:143], v242 offset:34560\n"
"v_add_u32 v240, 4294868992, v240\n"
"v_mfma_f32_16x16x32_bf16 v[192:195], a[80:83], a[16:19], v[236:239]\n"
"s_waitcnt lgkmcnt(12)\n"
"ds_read_b128 a[80:83], v240 offset:16384\n"
"v_mfma_f32_16x16x32_bf16 v[0:3], a[112:115], v[200:203], v[0:3]\n"
"v_exp_f32 v168, v168\n"
"v_mfma_f32_16x16x32_bf16 v[196:199], a[96:99], a[16:19], v[236:239]\n"
"ds_read_b128 a[96:99], v240 offset:17408\n"
"v_mfma_f32_16x16x32_bf16 v[20:23], a[116:119], v[200:203], v[20:23]\n"
"v_exp_f32 v169, v169\n"
"v_mfma_f32_16x16x32_bf16 v[192:195], a[84:87], a[36:39], v[192:195]\n"
"s_waitcnt lgkmcnt(10)\n"
"ds_read_b128 a[84:87], v240 offset:16640\n"
"v_mfma_f32_16x16x32_bf16 v[40:43], a[120:123], v[200:203], v[40:43]\n"
"v_exp_f32 v170, v170\n"
"v_mfma_f32_16x16x32_bf16 v[196:199], a[100:103], a[36:39], v[196:199]\n"
"ds_read_b128 a[100:103], v240 offset:17664\n"
"v_mfma_f32_16x16x32_bf16 v[60:63], a[124:127], v[200:203], v[60:63]\n"
"v_exp_f32 v171, v171\n"
"v_mfma_f32_16x16x32_bf16 v[192:195], a[88:91], a[56:59], v[192:195]\n"
"s_waitcnt lgkmcnt(8)\n"
"ds_read_b128 a[88:91], v240 offset:16896\n"
"v_mfma_f32_16x16x32_bf16 v[80:83], a[128:131], v[200:203], v[80:83]\n"
"v_cvt_pk_bf16_f32 v204, v168, v169\n"
"v_mfma_f32_16x16x32_bf16 v[196:199], a[104:107], a[56:59], v[196:199]\n"
"ds_read_b128 a[104:107], v240 offset:17920\n"
"s_add_u32 m0, s48, 123136\n"
"v_mfma_f32_16x16x32_bf16 v[100:103], a[132:135], v[200:203], v[100:103]\n"
"s_add_u32 s52, s95, 131072\n"
"v_exp_f32 v172, v172\n"
"v_mfma_f32_16x16x32_bf16 v[192:195], a[92:95], a[76:79], v[192:195]\n"
"s_waitcnt lgkmcnt(6)\n"
"v_exp_f32 v173, v173\n"
"v_mfma_f32_16x16x32_bf16 v[120:123], a[136:139], v[200:203], v[120:123]\n"
"ds_read_b128 a[92:95], v240 offset:17152\n"
"v_cvt_pk_bf16_f32 v205, v170, v171\n"
"v_mfma_f32_16x16x32_bf16 v[196:199], a[108:111], a[76:79], v[196:199]\n"
"v_exp_f32 v174, v174\n"
"v_exp_f32 v175, v175\n"
"v_cvt_pk_bf16_f32 v206, v172, v173\n"
"v_cvt_pk_bf16_f32 v207, v174, v175\n"
"v_mfma_f32_16x16x32_bf16 v[140:143], a[140:143], v[200:203], v[140:143]\n"
"ds_read_b128 a[108:111], v240 offset:18176\n"
"v_mfma_f32_16x16x32_bf16 a[144:147], a[164:167], v[200:203], a[144:147]\n"
"s_waitcnt lgkmcnt(4)\n"
"v_exp_f32 v176, v176\n"
"v_mfma_f32_16x16x32_bf16 v[160:163], a[80:83], a[0:3], v[220:223]\n"
"v_exp_f32 v177, v177\n"
"v_mfma_f32_16x16x32_bf16 v[4:7], a[112:115], v[204:207], v[4:7]\n"
"v_exp_f32 v178, v178\n"
"v_mfma_f32_16x16x32_bf16 v[164:167], a[96:99], a[0:3], v[220:223]\n"
"v_exp_f32 v179, v179\n"
"v_mfma_f32_16x16x32_bf16 v[24:27], a[116:119], v[204:207], v[24:27]\n"
"buffer_load_dwordx4 v244, s[60:63], s95 offen lds\n"
"v_cvt_pk_bf16_f32 v208, v176, v177\n"
"v_mfma_f32_16x16x32_bf16 v[160:163], a[84:87], a[20:23], v[160:163]\n"
"v_exp_f32 v180, v180\n"
"v_mfma_f32_16x16x32_bf16 v[44:47], a[120:123], v[204:207], v[44:47]\n"
"v_exp_f32 v181, v181\n"
"v_mfma_f32_16x16x32_bf16 v[164:167], a[100:103], a[20:23], v[164:167]\n"
"buffer_load_dwordx4 v244, s[60:63], s95 offen offset:1024 lds\n"
"v_cvt_pk_bf16_f32 v209, v178, v179\n"
"v_mfma_f32_16x16x32_bf16 v[64:67], a[124:127], v[204:207], v[64:67]\n"
"s_waitcnt lgkmcnt(0)\n"
"v_exp_f32 v182, v182\n"
"v_mfma_f32_16x16x32_bf16 v[160:163], a[88:91], a[40:43], v[160:163]\n"
"s_add_u32 m0, s48, 131328\n"
"v_exp_f32 v183, v183\n"
"v_mfma_f32_16x16x32_bf16 v[84:87], a[128:131], v[204:207], v[84:87]\n"
"s_add_u32 s95, s95, 8192\n"
"v_cvt_pk_bf16_f32 v210, v180, v181\n"
"v_cvt_pk_bf16_f32 v211, v182, v183\n"
"v_mfma_f32_16x16x32_bf16 v[164:167], a[104:107], a[40:43], v[164:167]\n"
"v_exp_f32 v184, v184\n"
"v_mfma_f32_16x16x32_bf16 v[104:107], a[132:135], v[204:207], v[104:107]\n"
"v_exp_f32 v185, v185\n"
"v_mfma_f32_16x16x32_bf16 v[160:163], a[92:95], a[60:63], v[160:163]\n"
"v_exp_f32 v186, v186\n"
"v_mfma_f32_16x16x32_bf16 v[124:127], a[136:139], v[204:207], v[124:127]\n"
"v_exp_f32 v187, v187\n"
"v_mfma_f32_16x16x32_bf16 v[164:167], a[108:111], a[60:63], v[164:167]\n"
"buffer_load_dwordx4 v244, s[60:63], s52 offen lds\n"
"v_cvt_pk_bf16_f32 v212, v184, v185\n"
"v_mfma_f32_16x16x32_bf16 v[144:147], a[140:143], v[204:207], v[144:147]\n"
"v_exp_f32 v188, v188\n"
"v_mfma_f32_16x16x32_bf16 a[148:151], a[164:167], v[204:207], a[148:151]\n"
"v_exp_f32 v189, v189\n"
"v_mfma_f32_16x16x32_bf16 v[168:171], a[80:83], a[4:7], v[224:227]\n"
"buffer_load_dwordx4 v244, s[60:63], s52 offen offset:1024 lds\n"
"v_cvt_pk_bf16_f32 v213, v186, v187\n"
"v_mfma_f32_16x16x32_bf16 v[8:11], a[112:115], v[208:211], v[8:11]\n"
"s_and_b32 s95, s95, 131071\n"
"v_exp_f32 v190, v190\n"
"v_mfma_f32_16x16x32_bf16 v[172:175], a[96:99], a[4:7], v[224:227]\n"
"v_exp_f32 v191, v191\n"
"v_mfma_f32_16x16x32_bf16 v[28:31], a[116:119], v[208:211], v[28:31]\n"
"v_cvt_pk_bf16_f32 v214, v188, v189\n"
"v_cvt_pk_bf16_f32 v215, v190, v191\n"
"v_mfma_f32_16x16x32_bf16 v[168:171], a[84:87], a[24:27], v[168:171]\n"
"v_exp_f32 v192, v192\n"
"v_mfma_f32_16x16x32_bf16 v[48:51], a[120:123], v[208:211], v[48:51]\n"
"v_exp_f32 v193, v193\n"
"v_mfma_f32_16x16x32_bf16 v[172:175], a[100:103], a[24:27], v[172:175]\n"
"v_exp_f32 v194, v194\n"
"v_mfma_f32_16x16x32_bf16 v[68:71], a[124:127], v[208:211], v[68:71]\n"
"v_exp_f32 v195, v195\n"
"v_mfma_f32_16x16x32_bf16 v[168:171], a[88:91], a[44:47], v[168:171]\n"
"v_cvt_pk_bf16_f32 v216, v192, v193\n"
"v_mfma_f32_16x16x32_bf16 v[88:91], a[128:131], v[208:211], v[88:91]\n"
"v_exp_f32 v196, v196\n"
"v_mfma_f32_16x16x32_bf16 v[172:175], a[104:107], a[44:47], v[172:175]\n"
"v_exp_f32 v197, v197\n"
"v_mfma_f32_16x16x32_bf16 v[108:111], a[132:135], v[208:211], v[108:111]\n"
"v_cvt_pk_bf16_f32 v217, v194, v195\n"
"v_mfma_f32_16x16x32_bf16 v[168:171], a[92:95], a[64:67], v[168:171]\n"
"v_exp_f32 v198, v198\n"
"v_mfma_f32_16x16x32_bf16 v[128:131], a[136:139], v[208:211], v[128:131]\n"
"v_exp_f32 v199, v199\n"
"v_mfma_f32_16x16x32_bf16 v[172:175], a[108:111], a[64:67], v[172:175]\n"
"v_cvt_pk_bf16_f32 v218, v196, v197\n"
"v_cvt_pk_bf16_f32 v219, v198, v199\n"
"v_mfma_f32_16x16x32_bf16 v[148:151], a[140:143], v[208:211], v[148:151]\n"
"v_exp_f32 v160, v160\n"
"v_mfma_f32_16x16x32_bf16 a[152:155], a[164:167], v[208:211], a[152:155]\n"
"v_exp_f32 v161, v161\n"
"v_mfma_f32_16x16x32_bf16 v[176:179], a[80:83], a[8:11], v[228:231]\n"
"v_exp_f32 v162, v162\n"
"v_mfma_f32_16x16x32_bf16 v[12:15], a[112:115], v[212:215], v[12:15]\n"
"v_exp_f32 v163, v163\n"
"v_mfma_f32_16x16x32_bf16 v[180:183], a[96:99], a[8:11], v[228:231]\n"
"v_cvt_pk_bf16_f32 v200, v160, v161\n"
"v_mfma_f32_16x16x32_bf16 v[32:35], a[116:119], v[212:215], v[32:35]\n"
"v_exp_f32 v164, v164\n"
"v_mfma_f32_16x16x32_bf16 v[176:179], a[84:87], a[28:31], v[176:179]\n"
"v_exp_f32 v165, v165\n"
"v_mfma_f32_16x16x32_bf16 v[52:55], a[120:123], v[212:215], v[52:55]\n"
"v_cvt_pk_bf16_f32 v201, v162, v163\n"
"v_mfma_f32_16x16x32_bf16 v[180:183], a[100:103], a[28:31], v[180:183]\n"
"v_exp_f32 v166, v166\n"
"v_mfma_f32_16x16x32_bf16 v[72:75], a[124:127], v[212:215], v[72:75]\n"
"v_exp_f32 v167, v167\n"
"v_mfma_f32_16x16x32_bf16 v[176:179], a[88:91], a[48:51], v[176:179]\n"
"v_cvt_pk_bf16_f32 v202, v164, v165\n"
"v_cvt_pk_bf16_f32 v203, v166, v167\n"
"v_mfma_f32_16x16x32_bf16 v[92:95], a[128:131], v[212:215], v[92:95]\n"
"v_mfma_f32_16x16x32_bf16 v[180:183], a[104:107], a[48:51], v[180:183]\n"
"v_mfma_f32_16x16x32_bf16 v[112:115], a[132:135], v[212:215], v[112:115]\n"
"v_mfma_f32_16x16x32_bf16 v[176:179], a[92:95], a[68:71], v[176:179]\n"
"v_mfma_f32_16x16x32_bf16 v[132:135], a[136:139], v[212:215], v[132:135]\n"
"v_mfma_f32_16x16x32_bf16 v[180:183], a[108:111], a[68:71], v[180:183]\n"
"v_mfma_f32_16x16x32_bf16 v[152:155], a[140:143], v[212:215], v[152:155]\n"
"v_mfma_f32_16x16x32_bf16 a[156:159], a[164:167], v[212:215], a[156:159]\n"
"L_ace_%=:\n"
"s_cmp_eq_u32 s51, 1\n"
"s_cbranch_scc1 L_t1_%=\n"
"s_cmp_eq_u32 s51, 3\n"
"s_cbranch_scc1 L_t3_%=\n"
"s_waitcnt vmcnt(12) lgkmcnt(0)\n"
"s_barrier\n"
"v_add_u32 v241, 49152, v241\n"
"v_add_u32 v242, 49152, v242\n"
"v_mfma_f32_16x16x32_bf16 v[184:187], a[80:83], a[12:15], v[232:235]\n"
"s_add_u32 s40, s40, 262144\n"
"s_addc_u32 s41, s41, 0\n"
"s_sub_u32 s42, s42, 262144\n"
"v_mfma_f32_16x16x32_bf16 v[16:19], a[112:115], v[216:219], v[16:19]\n"
"ds_read_b64_tr_b16 a[112:113], v241 offset:20608\n"
"v_mfma_f32_16x16x32_bf16 v[188:191], a[96:99], a[12:15], v[232:235]\n"
"ds_read_b64_tr_b16 a[114:115], v241 offset:21632\n"
"v_mfma_f32_16x16x32_bf16 v[36:39], a[116:119], v[216:219], v[36:39]\n"
"ds_read_b64_tr_b16 a[116:117], v242 offset:20608\n"
"v_mfma_f32_16x16x32_bf16 v[184:187], a[84:87], a[32:35], v[184:187]\n"
"ds_read_b64_tr_b16 a[118:119], v242 offset:21632\n"
"v_mfma_f32_16x16x32_bf16 v[56:59], a[120:123], v[216:219], v[56:59]\n"
"ds_read_b64_tr_b16 a[120:121], v241 offset:20864\n"
"v_mfma_f32_16x16x32_bf16 v[188:191], a[100:103], a[32:35], v[188:191]\n"
"ds_read_b64_tr_b16 a[122:123], v241 offset:21888\n"
"v_mfma_f32_16x16x32_bf16 v[76:79], a[124:127], v[216:219], v[76:79]\n"
"ds_read_b64_tr_b16 a[124:125], v242 offset:20864\n"
"v_mfma_f32_16x16x32_bf16 v[184:187], a[88:91], a[52:55], v[184:187]\n"
"ds_read_b64_tr_b16 a[126:127], v242 offset:21888\n"
"v_mfma_f32_16x16x32_bf16 v[96:99], a[128:131], v[216:219], v[96:99]\n"
"ds_read_b64_tr_b16 a[128:129], v241 offset:21120\n"
"v_mfma_f32_16x16x32_bf16 v[188:191], a[104:107], a[52:55], v[188:191]\n"
"ds_read_b64_tr_b16 a[130:131], v241 offset:22144\n"
"v_mfma_f32_16x16x32_bf16 v[116:119], a[132:135], v[216:219], v[116:119]\n"
"s_add_u32 s60, s60, 262144\n"
"s_addc_u32 s61, s61, 0\n"
"s_sub_u32 s62, s62, 262144\n"
"v_mfma_f32_16x16x32_bf16 v[184:187], a[92:95], a[72:75], v[184:187]\n"
"s_add_u32 s36, s36, 512\n"
"s_addc_u32 s37, s37, 0\n"
"s_sub_u32 s38, s38, 512\n"
"s_mov_b32 s70, 0\n"
"s_mov_b32 s74, 0\n"
"s_sub_u32 s78, 8, %[hg]\n"
"s_min_u32 s78, s78, 0\n"
"s_mul_i32 s78, s78, 655360\n"
"s_mov_b32 s51, 1\n"
"v_mfma_f32_16x16x32_bf16 v[136:139], a[136:139], v[216:219], v[136:139]\n"
"ds_read_b64_tr_b16 a[132:133], v242 offset:21120\n"
"ds_read_b64_tr_b16 a[134:135], v242 offset:22144\n"
"v_mfma_f32_16x16x32_bf16 v[188:191], a[108:111], a[72:75], v[188:191]\n"
"ds_read_b64_tr_b16 a[136:137], v241 offset:21376\n"
"s_add_u32 s52, s91, 0\n"
"v_mfma_f32_16x16x32_bf16 v[156:159], a[140:143], v[216:219], v[156:159]\n"
"ds_read_b64_tr_b16 a[138:139], v241 offset:22400\n"
"v_mfma_f32_16x16x32_bf16 a[160:163], a[164:167], v[216:219], a[160:163]\n"
"ds_read_b64_tr_b16 a[140:141], v242 offset:21376\n"
"v_mfma_f32_16x16x32_bf16 v[192:195], a[80:83], a[16:19], v[236:239]\n"
"s_waitcnt lgkmcnt(11)\n"
"ds_read_b64_tr_b16 a[142:143], v242 offset:22400\n"
"v_add_u32 v240, 49152, v240\n"
"v_mfma_f32_16x16x32_bf16 v[0:3], a[112:115], v[200:203], v[0:3]\n"
"ds_read_b128 a[80:83], v240 offset:4224\n"
"v_mfma_f32_16x16x32_bf16 v[196:199], a[96:99], a[16:19], v[236:239]\n"
"s_mov_b32 exec_lo, 4294967295\n"
"s_mov_b32 exec_hi, 255\n"
"buffer_load_dword v254, v252, s[36:39], s52 offen\n"
"s_mov_b64 exec, -1\n"
"v_mfma_f32_16x16x32_bf16 v[20:23], a[116:119], v[200:203], v[20:23]\n"
"ds_read_b128 a[96:99], v240 offset:5248\n"
"v_mfma_f32_16x16x32_bf16 v[192:195], a[84:87], a[36:39], v[192:195]\n"
"s_waitcnt lgkmcnt(10)\n"
"ds_read_b128 a[84:87], v240 offset:4480\n"
"v_mfma_f32_16x16x32_bf16 v[40:43], a[120:123], v[200:203], v[40:43]\n"
"v_exp_f32 v168, v168\n"
"v_mfma_f32_16x16x32_bf16 v[196:199], a[100:103], a[36:39], v[196:199]\n"
"ds_read_b128 a[100:103], v240 offset:5504\n"
"v_mfma_f32_16x16x32_bf16 v[60:63], a[124:127], v[200:203], v[60:63]\n"
"v_exp_f32 v169, v169\n"
"v_mfma_f32_16x16x32_bf16 v[192:195], a[88:91], a[56:59], v[192:195]\n"
"s_waitcnt lgkmcnt(8)\n"
"ds_read_b128 a[88:91], v240 offset:4736\n"
"v_mfma_f32_16x16x32_bf16 v[80:83], a[128:131], v[200:203], v[80:83]\n"
"v_exp_f32 v170, v170\n"
"v_mfma_f32_16x16x32_bf16 v[196:199], a[104:107], a[56:59], v[196:199]\n"
"ds_read_b128 a[104:107], v240 offset:5760\n"
"v_mfma_f32_16x16x32_bf16 v[100:103], a[132:135], v[200:203], v[100:103]\n"
"v_exp_f32 v171, v171\n"
"v_mfma_f32_16x16x32_bf16 v[192:195], a[92:95], a[76:79], v[192:195]\n"
"s_waitcnt lgkmcnt(6)\n"
"v_cvt_pk_bf16_f32 v204, v168, v169\n"
"v_mfma_f32_16x16x32_bf16 v[120:123], a[136:139], v[200:203], v[120:123]\n"
"v_exp_f32 v172, v172\n"
"v_mfma_f32_16x16x32_bf16 v[196:199], a[108:111], a[76:79], v[196:199]\n"
"ds_read_b128 a[92:95], v240 offset:4992\n"
"v_exp_f32 v173, v173\n"
"v_cvt_pk_bf16_f32 v205, v170, v171\n"
"v_exp_f32 v174, v174\n"
"v_exp_f32 v175, v175\n"
"v_cvt_pk_bf16_f32 v206, v172, v173\n"
"v_cvt_pk_bf16_f32 v207, v174, v175\n"
"v_mfma_f32_16x16x32_bf16 v[140:143], a[140:143], v[200:203], v[140:143]\n"
"ds_read_b128 a[108:111], v240 offset:6016\n"
"v_mfma_f32_16x16x32_bf16 a[144:147], a[164:167], v[200:203], a[144:147]\n"
"s_waitcnt lgkmcnt(4)\n"
"v_exp_f32 v176, v176\n"
"v_mfma_f32_16x16x32_bf16 v[160:163], a[80:83], a[0:3], v[220:223]\n"
"v_exp_f32 v177, v177\n"
"v_mfma_f32_16x16x32_bf16 v[4:7], a[112:115], v[204:207], v[4:7]\n"
"v_exp_f32 v178, v178\n"
"v_mfma_f32_16x16x32_bf16 v[164:167], a[96:99], a[0:3], v[220:223]\n"
"v_exp_f32 v179, v179\n"
"v_mfma_f32_16x16x32_bf16 v[24:27], a[116:119], v[204:207], v[24:27]\n"
"v_cvt_pk_bf16_f32 v208, v176, v177\n"
"v_mfma_f32_16x16x32_bf16 v[160:163], a[84:87], a[20:23], v[160:163]\n"
"v_exp_f32 v180, v180\n"
"v_mfma_f32_16x16x32_bf16 v[44:47], a[120:123], v[204:207], v[44:47]\n"
"v_exp_f32 v181, v181\n"
"v_mfma_f32_16x16x32_bf16 v[164:167], a[100:103], a[20:23], v[164:167]\n"
"v_cvt_pk_bf16_f32 v209, v178, v179\n"
"v_mfma_f32_16x16x32_bf16 v[64:67], a[124:127], v[204:207], v[64:67]\n"
"s_waitcnt lgkmcnt(0)\n"
"v_exp_f32 v182, v182\n"
"v_mfma_f32_16x16x32_bf16 v[160:163], a[88:91], a[40:43], v[160:163]\n"
"v_exp_f32 v183, v183\n"
"v_mfma_f32_16x16x32_bf16 v[84:87], a[128:131], v[204:207], v[84:87]\n"
"v_cvt_pk_bf16_f32 v210, v180, v181\n"
"v_cvt_pk_bf16_f32 v211, v182, v183\n"
"v_mfma_f32_16x16x32_bf16 v[164:167], a[104:107], a[40:43], v[164:167]\n"
"v_exp_f32 v184, v184\n"
"v_mfma_f32_16x16x32_bf16 v[104:107], a[132:135], v[204:207], v[104:107]\n"
"v_exp_f32 v185, v185\n"
"v_mfma_f32_16x16x32_bf16 v[160:163], a[92:95], a[60:63], v[160:163]\n"
"v_exp_f32 v186, v186\n"
"v_mfma_f32_16x16x32_bf16 v[124:127], a[136:139], v[204:207], v[124:127]\n"
"v_exp_f32 v187, v187\n"
"v_mfma_f32_16x16x32_bf16 v[164:167], a[108:111], a[60:63], v[164:167]\n"
"v_cvt_pk_bf16_f32 v212, v184, v185\n"
"v_mfma_f32_16x16x32_bf16 v[144:147], a[140:143], v[204:207], v[144:147]\n"
"v_exp_f32 v188, v188\n"
"v_mfma_f32_16x16x32_bf16 a[148:151], a[164:167], v[204:207], a[148:151]\n"
"v_exp_f32 v189, v189\n"
"v_mfma_f32_16x16x32_bf16 v[168:171], a[80:83], a[4:7], v[224:227]\n"
"v_cvt_pk_bf16_f32 v213, v186, v187\n"
"v_mfma_f32_16x16x32_bf16 v[8:11], a[112:115], v[208:211], v[8:11]\n"
"v_exp_f32 v190, v190\n"
"v_mfma_f32_16x16x32_bf16 v[172:175], a[96:99], a[4:7], v[224:227]\n"
"v_exp_f32 v191, v191\n"
"v_mfma_f32_16x16x32_bf16 v[28:31], a[116:119], v[208:211], v[28:31]\n"
"v_cvt_pk_bf16_f32 v214, v188, v189\n"
"v_cvt_pk_bf16_f32 v215, v190, v191\n"
"v_mfma_f32_16x16x32_bf16 v[168:171], a[84:87], a[24:27], v[168:171]\n"
"v_exp_f32 v192, v192\n"
"v_mfma_f32_16x16x32_bf16 v[48:51], a[120:123], v[208:211], v[48:51]\n"
"v_exp_f32 v193, v193\n"
"v_mfma_f32_16x16x32_bf16 v[172:175], a[100:103], a[24:27], v[172:175]\n"
"v_exp_f32 v194, v194\n"
"v_mfma_f32_16x16x32_bf16 v[68:71], a[124:127], v[208:211], v[68:71]\n"
"v_exp_f32 v195, v195\n"
"v_mfma_f32_16x16x32_bf16 v[168:171], a[88:91], a[44:47], v[168:171]\n"
"v_cvt_pk_bf16_f32 v216, v192, v193\n"
"v_mfma_f32_16x16x32_bf16 v[88:91], a[128:131], v[208:211], v[88:91]\n"
"v_exp_f32 v196, v196\n"
"v_mfma_f32_16x16x32_bf16 v[172:175], a[104:107], a[44:47], v[172:175]\n"
"v_exp_f32 v197, v197\n"
"v_mfma_f32_16x16x32_bf16 v[108:111], a[132:135], v[208:211], v[108:111]\n"
"v_cvt_pk_bf16_f32 v217, v194, v195\n"
"v_mfma_f32_16x16x32_bf16 v[168:171], a[92:95], a[64:67], v[168:171]\n"
"v_exp_f32 v198, v198\n"
"v_mfma_f32_16x16x32_bf16 v[128:131], a[136:139], v[208:211], v[128:131]\n"
"v_exp_f32 v199, v199\n"
"v_mfma_f32_16x16x32_bf16 v[172:175], a[108:111], a[64:67], v[172:175]\n"
"v_cvt_pk_bf16_f32 v218, v196, v197\n"
"v_cvt_pk_bf16_f32 v219, v198, v199\n"
"v_mfma_f32_16x16x32_bf16 v[148:151], a[140:143], v[208:211], v[148:151]\n"
"v_exp_f32 v160, v160\n"
"v_mfma_f32_16x16x32_bf16 a[152:155], a[164:167], v[208:211], a[152:155]\n"
"v_exp_f32 v161, v161\n"
"v_mfma_f32_16x16x32_bf16 v[176:179], a[80:83], a[8:11], v[228:231]\n"
"v_exp_f32 v162, v162\n"
"v_mfma_f32_16x16x32_bf16 v[12:15], a[112:115], v[212:215], v[12:15]\n"
"v_exp_f32 v163, v163\n"
"v_mfma_f32_16x16x32_bf16 v[180:183], a[96:99], a[8:11], v[228:231]\n"
"s_add_u32 m0, s49, 32896\n"
"s_mov_b32 s52, 0\n"
"v_cvt_pk_bf16_f32 v200, v160, v161\n"
"v_mfma_f32_16x16x32_bf16 v[32:35], a[116:119], v[212:215], v[32:35]\n"
"v_exp_f32 v164, v164\n"
"v_mfma_f32_16x16x32_bf16 v[176:179], a[84:87], a[28:31], v[176:179]\n"
"v_exp_f32 v165, v165\n"
"v_mfma_f32_16x16x32_bf16 v[52:55], a[120:123], v[212:215], v[52:55]\n"
"buffer_load_dwordx4 v246, s[84:87], s52 offen lds\n"
"v_cvt_pk_bf16_f32 v201, v162, v163\n"
"v_mfma_f32_16x16x32_bf16 v[180:183], a[100:103], a[28:31], v[180:183]\n"
"v_exp_f32 v166, v166\n"
"v_mfma_f32_16x16x32_bf16 v[72:75], a[124:127], v[212:215], v[72:75]\n"
"v_exp_f32 v167, v167\n"
"v_mfma_f32_16x16x32_bf16 v[176:179], a[88:91], a[48:51], v[176:179]\n"
"s_add_u32 m0, s49, 36992\n"
"v_cvt_pk_bf16_f32 v202, v164, v165\n"
"v_cvt_pk_bf16_f32 v203, v166, v167\n"
"v_mfma_f32_16x16x32_bf16 v[92:95], a[128:131], v[212:215], v[92:95]\n"
"s_mov_b32 s52, 40960\n"
"buffer_load_dwordx4 v246, s[84:87], s52 offen lds\n"
"v_mfma_f32_16x16x32_bf16 v[180:183], a[104:107], a[48:51], v[180:183]\n"
"v_mfma_f32_16x16x32_bf16 v[112:115], a[132:135], v[212:215], v[112:115]\n"
"v_add_u32 v241, 49152, v241\n"
"v_add_u32 v242, 49152, v242\n"
"v_mfma_f32_16x16x32_bf16 v[176:179], a[92:95], a[68:71], v[176:179]\n"
"v_mfma_f32_16x16x32_bf16 v[132:135], a[136:139], v[212:215], v[132:135]\n"
"v_mfma_f32_16x16x32_bf16 v[180:183], a[108:111], a[68:71], v[180:183]\n"
"s_add_u32 m0, s49, 41088\n"
"s_mov_b32 s52, 81920\n"
"v_mfma_f32_16x16x32_bf16 v[152:155], a[140:143], v[212:215], v[152:155]\n"
"buffer_load_dwordx4 v246, s[84:87], s52 offen lds\n"
"v_mfma_f32_16x16x32_bf16 a[156:159], a[164:167], v[212:215], a[156:159]\n"
"s_waitcnt vmcnt(8) lgkmcnt(0)\n"
"s_barrier\n"
"v_mfma_f32_16x16x32_bf16 v[184:187], a[80:83], a[12:15], v[232:235]\n"
"v_mfma_f32_16x16x32_bf16 v[16:19], a[112:115], v[216:219], v[16:19]\n"
"ds_read_b64_tr_b16 a[112:113], v241 offset:8448\n"
"v_mfma_f32_16x16x32_bf16 v[188:191], a[96:99], a[12:15], v[232:235]\n"
"ds_read_b64_tr_b16 a[114:115], v241 offset:9472\n"
"v_mfma_f32_16x16x32_bf16 v[36:39], a[116:119], v[216:219], v[36:39]\n"
"ds_read_b64_tr_b16 a[116:117], v242 offset:8448\n"
"s_add_u32 m0, s49, 45184\n"
"v_mfma_f32_16x16x32_bf16 v[184:187], a[84:87], a[32:35], v[184:187]\n"
"ds_read_b64_tr_b16 a[118:119], v242 offset:9472\n"
"s_mov_b32 s52, 122880\n"
"v_mfma_f32_16x16x32_bf16 v[56:59], a[120:123], v[216:219], v[56:59]\n"
"ds_read_b64_tr_b16 a[120:121], v241 offset:8704\n"
"v_mfma_f32_16x16x32_bf16 v[188:191], a[100:103], a[32:35], v[188:191]\n"
"ds_read_b64_tr_b16 a[122:123], v241 offset:9728\n"
"v_mfma_f32_16x16x32_bf16 v[76:79], a[124:127], v[216:219], v[76:79]\n"
"ds_read_b64_tr_b16 a[124:125], v242 offset:8704\n"
"v_mfma_f32_16x16x32_bf16 v[184:187], a[88:91], a[52:55], v[184:187]\n"
"ds_read_b64_tr_b16 a[126:127], v242 offset:9728\n"
"v_mfma_f32_16x16x32_bf16 v[96:99], a[128:131], v[216:219], v[96:99]\n"
"ds_read_b64_tr_b16 a[128:129], v241 offset:8960\n"
"v_mfma_f32_16x16x32_bf16 v[188:191], a[104:107], a[52:55], v[188:191]\n"
"ds_read_b64_tr_b16 a[130:131], v241 offset:9984\n"
"v_mfma_f32_16x16x32_bf16 v[116:119], a[132:135], v[216:219], v[116:119]\n"
"ds_read_b64_tr_b16 a[132:133], v242 offset:8960\n"
"v_mfma_f32_16x16x32_bf16 v[184:187], a[92:95], a[72:75], v[184:187]\n"
"ds_read_b64_tr_b16 a[134:135], v242 offset:9984\n"
"v_mfma_f32_16x16x32_bf16 v[136:139], a[136:139], v[216:219], v[136:139]\n"
"ds_read_b64_tr_b16 a[136:137], v241 offset:9216\n"
"v_mfma_f32_16x16x32_bf16 v[188:191], a[108:111], a[72:75], v[188:191]\n"
"ds_read_b64_tr_b16 a[138:139], v241 offset:10240\n"
"v_mfma_f32_16x16x32_bf16 v[156:159], a[140:143], v[216:219], v[156:159]\n"
"ds_read_b64_tr_b16 a[140:141], v242 offset:9216\n"
"v_mfma_f32_16x16x32_bf16 a[160:163], a[164:167], v[216:219], a[160:163]\n"
"ds_read_b64_tr_b16 a[142:143], v242 offset:10240\n"
"v_mfma_f32_16x16x32_bf16 v[192:195], a[80:83], a[16:19], v[236:239]\n"
"s_waitcnt lgkmcnt(12)\n"
"ds_read_b128 a[80:83], v240 offset:36992\n"
"v_mfma_f32_16x16x32_bf16 v[0:3], a[112:115], v[200:203], v[0:3]\n"
"v_exp_f32 v168, v168\n"
"v_mfma_f32_16x16x32_bf16 v[196:199], a[96:99], a[16:19], v[236:239]\n"
"ds_read_b128 a[96:99], v240 offset:38016\n"
"v_mfma_f32_16x16x32_bf16 v[20:23], a[116:119], v[200:203], v[20:23]\n"
"v_exp_f32 v169, v169\n"
"v_mfma_f32_16x16x32_bf16 v[192:195], a[84:87], a[36:39], v[192:195]\n"
"s_waitcnt lgkmcnt(10)\n"
"ds_read_b128 a[84:87], v240 offset:37248\n"
"v_mfma_f32_16x16x32_bf16 v[40:43], a[120:123], v[200:203], v[40:43]\n"
"v_exp_f32 v170, v170\n"
"v_mfma_f32_16x16x32_bf16 v[196:199], a[100:103], a[36:39], v[196:199]\n"
"ds_read_b128 a[100:103], v240 offset:38272\n"
"v_mfma_f32_16x16x32_bf16 v[60:63], a[124:127], v[200:203], v[60:63]\n"
"v_exp_f32 v171, v171\n"
"v_mfma_f32_16x16x32_bf16 v[192:195], a[88:91], a[56:59], v[192:195]\n"
"s_waitcnt lgkmcnt(8)\n"
"ds_read_b128 a[88:91], v240 offset:37504\n"
"v_mfma_f32_16x16x32_bf16 v[80:83], a[128:131], v[200:203], v[80:83]\n"
"buffer_load_dwordx4 v246, s[84:87], s52 offen lds\n"
"v_cvt_pk_bf16_f32 v204, v168, v169\n"
"v_mfma_f32_16x16x32_bf16 v[196:199], a[104:107], a[56:59], v[196:199]\n"
"ds_read_b128 a[104:107], v240 offset:38528\n"
"s_add_u32 m0, s49, 49280\n"
"v_mfma_f32_16x16x32_bf16 v[100:103], a[132:135], v[200:203], v[100:103]\n"
"s_mov_b32 s52, 163840\n"
"v_exp_f32 v172, v172\n"
"v_mfma_f32_16x16x32_bf16 v[192:195], a[92:95], a[76:79], v[192:195]\n"
"s_waitcnt lgkmcnt(6)\n"
"v_exp_f32 v173, v173\n"
"v_mfma_f32_16x16x32_bf16 v[120:123], a[136:139], v[200:203], v[120:123]\n"
"ds_read_b128 a[92:95], v240 offset:37760\n"
"v_cvt_pk_bf16_f32 v205, v170, v171\n"
"v_mfma_f32_16x16x32_bf16 v[196:199], a[108:111], a[76:79], v[196:199]\n"
"v_exp_f32 v174, v174\n"
"v_exp_f32 v175, v175\n"
"v_cvt_pk_bf16_f32 v206, v172, v173\n"
"v_cvt_pk_bf16_f32 v207, v174, v175\n"
"v_mfma_f32_16x16x32_bf16 v[140:143], a[140:143], v[200:203], v[140:143]\n"
"ds_read_b128 a[108:111], v240 offset:38784\n"
"v_mfma_f32_16x16x32_bf16 a[144:147], a[164:167], v[200:203], a[144:147]\n"
"s_waitcnt lgkmcnt(4)\n"
"v_exp_f32 v176, v176\n"
"v_mfma_f32_16x16x32_bf16 v[160:163], a[80:83], a[0:3], v[220:223]\n"
"v_exp_f32 v177, v177\n"
"v_mfma_f32_16x16x32_bf16 v[4:7], a[112:115], v[204:207], v[4:7]\n"
"v_exp_f32 v178, v178\n"
"v_mfma_f32_16x16x32_bf16 v[164:167], a[96:99], a[0:3], v[220:223]\n"
"v_exp_f32 v179, v179\n"
"v_mfma_f32_16x16x32_bf16 v[24:27], a[116:119], v[204:207], v[24:27]\n"
"buffer_load_dwordx4 v246, s[84:87], s52 offen lds\n"
"v_cvt_pk_bf16_f32 v208, v176, v177\n"
"v_mfma_f32_16x16x32_bf16 v[160:163], a[84:87], a[20:23], v[160:163]\n"
"s_add_u32 m0, s54, 0\n"
"v_exp_f32 v180, v180\n"
"v_mfma_f32_16x16x32_bf16 v[44:47], a[120:123], v[204:207], v[44:47]\n"
"s_mov_b32 s52, 0\n"
"v_exp_f32 v181, v181\n"
"v_mfma_f32_16x16x32_bf16 v[164:167], a[100:103], a[20:23], v[164:167]\n"
"buffer_load_dwordx4 v245, s[36:39], s52 offen lds\n"
"v_cvt_pk_bf16_f32 v209, v178, v179\n"
"v_mfma_f32_16x16x32_bf16 v[64:67], a[124:127], v[204:207], v[64:67]\n"
"s_waitcnt lgkmcnt(0)\n"
"v_exp_f32 v182, v182\n"
"v_mfma_f32_16x16x32_bf16 v[160:163], a[88:91], a[40:43], v[160:163]\n"
"s_add_u32 m0, s54, 1024\n"
"v_exp_f32 v183, v183\n"
"v_mfma_f32_16x16x32_bf16 v[84:87], a[128:131], v[204:207], v[84:87]\n"
"s_mov_b32 s52, 32768\n"
"v_cvt_pk_bf16_f32 v210, v180, v181\n"
"v_cvt_pk_bf16_f32 v211, v182, v183\n"
"v_mfma_f32_16x16x32_bf16 v[164:167], a[104:107], a[40:43], v[164:167]\n"
"v_exp_f32 v184, v184\n"
"v_mfma_f32_16x16x32_bf16 v[104:107], a[132:135], v[204:207], v[104:107]\n"
"v_exp_f32 v185, v185\n"
"v_mfma_f32_16x16x32_bf16 v[160:163], a[92:95], a[60:63], v[160:163]\n"
"v_exp_f32 v186, v186\n"
"v_mfma_f32_16x16x32_bf16 v[124:127], a[136:139], v[204:207], v[124:127]\n"
"v_exp_f32 v187, v187\n"
"v_mfma_f32_16x16x32_bf16 v[164:167], a[108:111], a[60:63], v[164:167]\n"
"buffer_load_dwordx4 v245, s[36:39], s52 offen lds\n"
"v_cvt_pk_bf16_f32 v212, v184, v185\n"
"v_mfma_f32_16x16x32_bf16 v[144:147], a[140:143], v[204:207], v[144:147]\n"
"s_add_u32 m0, s54, 8224\n"
"v_exp_f32 v188, v188\n"
"v_mfma_f32_16x16x32_bf16 a[148:151], a[164:167], v[204:207], a[148:151]\n"
"s_mov_b32 s52, 256\n"
"v_exp_f32 v189, v189\n"
"v_mfma_f32_16x16x32_bf16 v[168:171], a[80:83], a[4:7], v[224:227]\n"
"buffer_load_dwordx4 v245, s[36:39], s52 offen lds\n"
"v_cvt_pk_bf16_f32 v213, v186, v187\n"
"v_mfma_f32_16x16x32_bf16 v[8:11], a[112:115], v[208:211], v[8:11]\n"
"s_add_u32 m0, s54, 9248\n"
"v_exp_f32 v190, v190\n"
"v_mfma_f32_16x16x32_bf16 v[172:175], a[96:99], a[4:7], v[224:227]\n"
"s_mov_b32 s52, 33024\n"
"v_exp_f32 v191, v191\n"
"v_mfma_f32_16x16x32_bf16 v[28:31], a[116:119], v[208:211], v[28:31]\n"
"v_cvt_pk_bf16_f32 v214, v188, v189\n"
"v_cvt_pk_bf16_f32 v215, v190, v191\n"
"v_mfma_f32_16x16x32_bf16 v[168:171], a[84:87], a[24:27], v[168:171]\n"
"buffer_load_dwordx4 v245, s[36:39], s52 offen lds\n"
"s_add_u32 m0, s54, 16448\n"
"v_mfma_f32_16x16x32_bf16 v[48:51], a[120:123], v[208:211], v[48:51]\n"
"s_mov_b32 s52, 262144\n"
"buffer_load_dwordx4 v245, s[36:39], s52 offen lds\n"
"v_mfma_f32_16x16x32_bf16 v[172:175], a[100:103], a[24:27], v[172:175]\n"
"s_add_u32 m0, s54, 17472\n"
"s_mov_b32 s52, 294912\n"
"v_mfma_f32_16x16x32_bf16 v[68:71], a[124:127], v[208:211], v[68:71]\n"
"buffer_load_dwordx4 v245, s[36:39], s52 offen lds\n"
"s_add_u32 m0, s54, 24672\n"
"v_mfma_f32_16x16x32_bf16 v[168:171], a[88:91], a[44:47], v[168:171]\n"
"s_mov_b32 s52, 262400\n"
"buffer_load_dwordx4 v245, s[36:39], s52 offen lds\n"
"v_mfma_f32_16x16x32_bf16 v[88:91], a[128:131], v[208:211], v[88:91]\n"
"s_add_u32 m0, s54, 25696\n"
"s_mov_b32 s52, 295168\n"
"v_mfma_f32_16x16x32_bf16 v[172:175], a[104:107], a[44:47], v[172:175]\n"
"buffer_load_dwordx4 v245, s[36:39], s52 offen lds\n"
"v_mfma_f32_16x16x32_bf16 v[108:111], a[132:135], v[208:211], v[108:111]\n"
"v_exp_f32 v192, v192\n"
"v_mfma_f32_16x16x32_bf16 v[168:171], a[92:95], a[64:67], v[168:171]\n"
"v_exp_f32 v193, v193\n"
"v_mfma_f32_16x16x32_bf16 v[128:131], a[136:139], v[208:211], v[128:131]\n"
"v_exp_f32 v194, v194\n"
"v_mfma_f32_16x16x32_bf16 v[172:175], a[108:111], a[64:67], v[172:175]\n"
"v_exp_f32 v195, v195\n"
"v_mfma_f32_16x16x32_bf16 v[148:151], a[140:143], v[208:211], v[148:151]\n"
"v_cvt_pk_bf16_f32 v216, v192, v193\n"
"v_mfma_f32_16x16x32_bf16 a[152:155], a[164:167], v[208:211], a[152:155]\n"
"v_exp_f32 v196, v196\n"
"v_mfma_f32_16x16x32_bf16 v[176:179], a[80:83], a[8:11], v[228:231]\n"
"v_exp_f32 v197, v197\n"
"v_mfma_f32_16x16x32_bf16 v[12:15], a[112:115], v[212:215], v[12:15]\n"
"v_cvt_pk_bf16_f32 v217, v194, v195\n"
"v_mfma_f32_16x16x32_bf16 v[180:183], a[96:99], a[8:11], v[228:231]\n"
"v_exp_f32 v198, v198\n"
"v_mfma_f32_16x16x32_bf16 v[32:35], a[116:119], v[212:215], v[32:35]\n"
"v_exp_f32 v199, v199\n"
"v_mfma_f32_16x16x32_bf16 v[176:179], a[84:87], a[28:31], v[176:179]\n"
"v_cvt_pk_bf16_f32 v218, v196, v197\n"
"v_cvt_pk_bf16_f32 v219, v198, v199\n"
"v_mfma_f32_16x16x32_bf16 v[52:55], a[120:123], v[212:215], v[52:55]\n"
"v_exp_f32 v160, v160\n"
"v_mfma_f32_16x16x32_bf16 v[180:183], a[100:103], a[28:31], v[180:183]\n"
"v_exp_f32 v161, v161\n"
"v_mfma_f32_16x16x32_bf16 v[72:75], a[124:127], v[212:215], v[72:75]\n"
"v_exp_f32 v162, v162\n"
"v_mfma_f32_16x16x32_bf16 v[176:179], a[88:91], a[48:51], v[176:179]\n"
"v_exp_f32 v163, v163\n"
"v_mfma_f32_16x16x32_bf16 v[92:95], a[128:131], v[212:215], v[92:95]\n"
"v_cvt_pk_bf16_f32 v200, v160, v161\n"
"v_mfma_f32_16x16x32_bf16 v[180:183], a[104:107], a[48:51], v[180:183]\n"
"v_exp_f32 v164, v164\n"
"v_mfma_f32_16x16x32_bf16 v[112:115], a[132:135], v[212:215], v[112:115]\n"
"v_exp_f32 v165, v165\n"
"v_mfma_f32_16x16x32_bf16 v[176:179], a[92:95], a[68:71], v[176:179]\n"
"v_cvt_pk_bf16_f32 v201, v162, v163\n"
"v_mfma_f32_16x16x32_bf16 v[132:135], a[136:139], v[212:215], v[132:135]\n"
"v_exp_f32 v166, v166\n"
"v_mfma_f32_16x16x32_bf16 v[180:183], a[108:111], a[68:71], v[180:183]\n"
"v_exp_f32 v167, v167\n"
"v_cvt_pk_bf16_f32 v202, v164, v165\n"
"v_cvt_pk_bf16_f32 v203, v166, v167\n"
"v_mfma_f32_16x16x32_bf16 v[152:155], a[140:143], v[212:215], v[152:155]\n"
"v_mfma_f32_16x16x32_bf16 a[156:159], a[164:167], v[212:215], a[156:159]\n"
"s_waitcnt vmcnt(0) lgkmcnt(0)\n"
"s_barrier\n"
"v_mfma_f32_16x16x32_bf16 v[184:187], a[80:83], a[12:15], v[232:235]\n"
"v_mfma_f32_16x16x32_bf16 v[16:19], a[112:115], v[216:219], v[16:19]\n"
"ds_read_b64_tr_b16 a[112:113], v241 offset:24832\n"
"s_add_u32 m0, s49, 86272\n"
"v_mfma_f32_16x16x32_bf16 v[188:191], a[96:99], a[12:15], v[232:235]\n"
"ds_read_b64_tr_b16 a[114:115], v241 offset:25856\n"
"s_mov_b32 s52, 128\n"
"v_mfma_f32_16x16x32_bf16 v[36:39], a[116:119], v[216:219], v[36:39]\n"
"ds_read_b64_tr_b16 a[116:117], v242 offset:24832\n"
"v_mfma_f32_16x16x32_bf16 v[184:187], a[84:87], a[32:35], v[184:187]\n"
"ds_read_b64_tr_b16 a[118:119], v242 offset:25856\n"
"v_mfma_f32_16x16x32_bf16 v[56:59], a[120:123], v[216:219], v[56:59]\n"
"ds_read_b64_tr_b16 a[120:121], v241 offset:25088\n"
"v_mfma_f32_16x16x32_bf16 v[188:191], a[100:103], a[32:35], v[188:191]\n"
"ds_read_b64_tr_b16 a[122:123], v241 offset:26112\n"
"v_mfma_f32_16x16x32_bf16 v[76:79], a[124:127], v[216:219], v[76:79]\n"
"ds_read_b64_tr_b16 a[124:125], v242 offset:25088\n"
"v_mfma_f32_16x16x32_bf16 v[184:187], a[88:91], a[52:55], v[184:187]\n"
"ds_read_b64_tr_b16 a[126:127], v242 offset:26112\n"
"v_mfma_f32_16x16x32_bf16 v[96:99], a[128:131], v[216:219], v[96:99]\n"
"ds_read_b64_tr_b16 a[128:129], v241 offset:25344\n"
"v_mfma_f32_16x16x32_bf16 v[188:191], a[104:107], a[52:55], v[188:191]\n"
"ds_read_b64_tr_b16 a[130:131], v241 offset:26368\n"
"v_mfma_f32_16x16x32_bf16 v[116:119], a[132:135], v[216:219], v[116:119]\n"
"ds_read_b64_tr_b16 a[132:133], v242 offset:25344\n"
"v_mfma_f32_16x16x32_bf16 v[184:187], a[92:95], a[72:75], v[184:187]\n"
"ds_read_b64_tr_b16 a[134:135], v242 offset:26368\n"
"v_mfma_f32_16x16x32_bf16 v[136:139], a[136:139], v[216:219], v[136:139]\n"
"ds_read_b64_tr_b16 a[136:137], v241 offset:25600\n"
"v_mfma_f32_16x16x32_bf16 v[188:191], a[108:111], a[72:75], v[188:191]\n"
"ds_read_b64_tr_b16 a[138:139], v241 offset:26624\n"
"v_mfma_f32_16x16x32_bf16 v[156:159], a[140:143], v[216:219], v[156:159]\n"
"ds_read_b64_tr_b16 a[140:141], v242 offset:25600\n"
"v_mfma_f32_16x16x32_bf16 a[160:163], a[164:167], v[216:219], a[160:163]\n"
"ds_read_b64_tr_b16 a[142:143], v242 offset:26624\n"
"v_mfma_f32_16x16x32_bf16 v[192:195], a[80:83], a[16:19], v[236:239]\n"
"s_waitcnt lgkmcnt(12)\n"
"v_exp_f32 v168, v168\n"
"v_exp_f32 v169, v169\n"
"v_mfma_f32_16x16x32_bf16 v[0:3], a[112:115], v[200:203], v[0:3]\n"
"v_exp_f32 v170, v170\n"
"v_exp_f32 v171, v171\n"
"v_mfma_f32_16x16x32_bf16 v[196:199], a[96:99], a[16:19], v[236:239]\n"
"buffer_load_dwordx4 v246, s[84:87], s52 offen lds\n"
"v_cvt_pk_bf16_f32 v204, v168, v169\n"
"v_exp_f32 v172, v172\n"
"v_mfma_f32_16x16x32_bf16 v[20:23], a[116:119], v[200:203], v[20:23]\n"
"s_add_u32 m0, s49, 90368\n"
"s_mov_b32 s52, 41088\n"
"v_exp_f32 v173, v173\n"
"v_cvt_pk_bf16_f32 v205, v170, v171\n"
"v_mfma_f32_16x16x32_bf16 v[192:195], a[84:87], a[36:39], v[192:195]\n"
"s_waitcnt lgkmcnt(8)\n"
"v_exp_f32 v174, v174\n"
"v_exp_f32 v175, v175\n"
"v_mfma_f32_16x16x32_bf16 v[40:43], a[120:123], v[200:203], v[40:43]\n"
"v_cvt_pk_bf16_f32 v206, v172, v173\n"
"v_cvt_pk_bf16_f32 v207, v174, v175\n"
"v_exp_f32 v176, v176\n"
"v_mfma_f32_16x16x32_bf16 v[196:199], a[100:103], a[36:39], v[196:199]\n"
"v_exp_f32 v177, v177\n"
"v_exp_f32 v178, v178\n"
"v_mfma_f32_16x16x32_bf16 v[60:63], a[124:127], v[200:203], v[60:63]\n"
"buffer_load_dwordx4 v246, s[84:87], s52 offen lds\n"
"v_exp_f32 v179, v179\n"
"v_cvt_pk_bf16_f32 v208, v176, v177\n"
"v_mfma_f32_16x16x32_bf16 v[192:195], a[88:91], a[56:59], v[192:195]\n"
"s_waitcnt lgkmcnt(4)\n"
"v_exp_f32 v180, v180\n"
"v_exp_f32 v181, v181\n"
"v_mfma_f32_16x16x32_bf16 v[80:83], a[128:131], v[200:203], v[80:83]\n"
"s_add_u32 m0, s49, 94464\n"
"s_mov_b32 s52, 82048\n"
"v_cvt_pk_bf16_f32 v209, v178, v179\n"
"v_exp_f32 v182, v182\n"
"v_mfma_f32_16x16x32_bf16 v[196:199], a[104:107], a[56:59], v[196:199]\n"
"v_exp_f32 v183, v183\n"
"v_cvt_pk_bf16_f32 v210, v180, v181\n"
"v_cvt_pk_bf16_f32 v211, v182, v183\n"
"v_mfma_f32_16x16x32_bf16 v[100:103], a[132:135], v[200:203], v[100:103]\n"
"v_exp_f32 v184, v184\n"
"v_exp_f32 v185, v185\n"
"v_mfma_f32_16x16x32_bf16 v[192:195], a[92:95], a[76:79], v[192:195]\n"
"s_waitcnt lgkmcnt(0)\n"
"v_exp_f32 v186, v186\n"
"v_exp_f32 v187, v187\n"
"v_mfma_f32_16x16x32_bf16 v[120:123], a[136:139], v[200:203], v[120:123]\n"
"buffer_load_dwordx4 v246, s[84:87], s52 offen lds\n"
"v_cvt_pk_bf16_f32 v212, v184, v185\n"
"v_exp_f32 v188, v188\n"
"v_mfma_f32_16x16x32_bf16 v[196:199], a[108:111], a[76:79], v[196:199]\n"
"s_add_u32 m0, s49, 98560\n"
"s_mov_b32 s52, 123008\n"
"v_exp_f32 v189, v189\n"
"v_cvt_pk_bf16_f32 v213, v186, v187\n"
"v_mfma_f32_16x16x32_bf16 v[140:143], a[140:143], v[200:203], v[140:143]\n"
"v_exp_f32 v190, v190\n"
"v_exp_f32 v191, v191\n"
"v_mfma_f32_16x16x32_bf16 a[144:147], a[164:167], v[200:203], a[144:147]\n"
"buffer_load_dwordx4 v246, s[84:87], s52 offen lds\n"
"s_add_u32 m0, s49, 102656\n"
"v_cvt_pk_bf16_f32 v214, v188, v189\n"
"v_cvt_pk_bf16_f32 v215, v190, v191\n"
"v_mfma_f32_16x16x32_bf16 v[4:7], a[112:115], v[204:207], v[4:7]\n"
"s_mov_b32 s52, 163968\n"
"v_add_u32 v224, 0, v247\n"
"v_add_u32 v230, 32896, v248\n"
"v_add_u32 v225, 53376, v247\n"
"v_mfma_f32_16x16x32_bf16 v[24:27], a[116:119], v[204:207], v[24:27]\n"
"buffer_load_dwordx4 v246, s[84:87], s52 offen lds\n"
"v_add_u32 v231, 86272, v248\n"
"v_add_u32 v226, 106752, v247\n"
"v_add_u32 v232, 139648, v248\n"
"v_mfma_f32_16x16x32_bf16 v[44:47], a[120:123], v[204:207], v[44:47]\n"
"s_add_u32 m0, s54, 53376\n"
"s_mov_b32 s52, 524288\n"
"buffer_load_dwordx4 v245, s[36:39], s52 offen lds\n"
"s_add_u32 m0, s54, 54400\n"
"v_mfma_f32_16x16x32_bf16 v[64:67], a[124:127], v[204:207], v[64:67]\n"
"s_mov_b32 s52, 557056\n"
"buffer_load_dwordx4 v245, s[36:39], s52 offen lds\n"
"s_add_u32 m0, s54, 61600\n"
"s_mov_b32 s52, 524544\n"
"v_mfma_f32_16x16x32_bf16 v[84:87], a[128:131], v[204:207], v[84:87]\n"
"v_exp_f32 v192, v192\n"
"v_exp_f32 v193, v193\n"
"v_mfma_f32_16x16x32_bf16 v[104:107], a[132:135], v[204:207], v[104:107]\n"
"v_exp_f32 v194, v194\n"
"v_exp_f32 v195, v195\n"
"v_mfma_f32_16x16x32_bf16 v[124:127], a[136:139], v[204:207], v[124:127]\n"
"buffer_load_dwordx4 v245, s[36:39], s52 offen lds\n"
"v_cvt_pk_bf16_f32 v216, v192, v193\n"
"v_exp_f32 v196, v196\n"
"v_mfma_f32_16x16x32_bf16 v[144:147], a[140:143], v[204:207], v[144:147]\n"
"s_add_u32 m0, s54, 62624\n"
"s_mov_b32 s52, 557312\n"
"v_exp_f32 v197, v197\n"
"v_cvt_pk_bf16_f32 v217, v194, v195\n"
"v_mfma_f32_16x16x32_bf16 a[148:151], a[164:167], v[204:207], a[148:151]\n"
"v_exp_f32 v198, v198\n"
"v_exp_f32 v199, v199\n"
"v_mfma_f32_16x16x32_bf16 v[8:11], a[112:115], v[208:211], v[8:11]\n"
"ds_read_b128 a[32:35], v230 offset:0\n"
"v_cvt_pk_bf16_f32 v218, v196, v197\n"
"v_cvt_pk_bf16_f32 v219, v198, v199\n"
"v_accvgpr_read_b32 v255, a144\n"
"v_mfma_f32_16x16x32_bf16 v[28:31], a[116:119], v[208:211], v[28:31]\n"
"ds_read_b128 a[36:39], v230 offset:2048\n"
"buffer_load_dwordx4 v245, s[36:39], s52 offen lds\n"
"s_add_u32 m0, s54, 69824\n"
"v_mfma_f32_16x16x32_bf16 v[48:51], a[120:123], v[208:211], v[48:51]\n"
"ds_read_b128 a[40:43], v230 offset:4096\n"
"s_mov_b32 s52, 786432\n"
"buffer_load_dwordx4 v245, s[36:39], s52 offen lds\n"
"v_mfma_f32_16x16x32_bf16 v[68:71], a[124:127], v[208:211], v[68:71]\n"
"ds_read_b128 a[44:47], v230 offset:6144\n"
"s_add_u32 m0, s54, 70848\n"
"s_mov_b32 s52, 819200\n"
"v_cmp_nge_f32 vcc, s53, v255\n"
"v_mfma_f32_16x16x32_bf16 v[88:91], a[128:131], v[208:211], v[88:91]\n"
"ds_read_b128 a[48:51], v230 offset:8192\n"
"buffer_load_dwordx4 v245, s[36:39], s52 offen lds\n"
"s_add_u32 m0, s54, 78048\n"
"v_mfma_f32_16x16x32_bf16 v[108:111], a[132:135], v[208:211], v[108:111]\n"
"ds_read_b64_tr_b16 a[0:1], v224 offset:0\n"
"s_mov_b32 s52, 786688\n"
"buffer_load_dwordx4 v245, s[36:39], s52 offen lds\n"
"v_mfma_f32_16x16x32_bf16 v[128:131], a[136:139], v[208:211], v[128:131]\n"
"ds_read_b64_tr_b16 a[2:3], v224 offset:1024\n"
"s_add_u32 m0, s54, 79072\n"
"s_mov_b32 s52, 819456\n"
"v_mfma_f32_16x16x32_bf16 v[148:151], a[140:143], v[208:211], v[148:151]\n"
"ds_read_b64_tr_b16 a[4:5], v224 offset:8\n"
"buffer_load_dwordx4 v245, s[36:39], s52 offen lds\n"
"v_mfma_f32_16x16x32_bf16 a[152:155], a[164:167], v[208:211], a[152:155]\n"
"ds_read_b64_tr_b16 a[6:7], v224 offset:1032\n"
"v_rcp_f32 v255, v255\n"
"s_or_b64 s[58:59], s[58:59], vcc\n"
"v_mfma_f32_16x16x32_bf16 v[12:15], a[112:115], v[212:215], v[12:15]\n"
"ds_read_b64_tr_b16 a[8:9], v224 offset:256\n"
"v_mul_f32 v0, v255, v0\n"
"v_mul_f32 v1, v255, v1\n"
"v_mul_f32 v2, v255, v2\n"
"v_mfma_f32_16x16x32_bf16 v[32:35], a[116:119], v[212:215], v[32:35]\n"
"ds_read_b64_tr_b16 a[10:11], v224 offset:1280\n"
"v_mul_f32 v3, v255, v3\n"
"v_mul_f32 v20, v255, v20\n"
"v_mul_f32 v21, v255, v21\n"
"v_mfma_f32_16x16x32_bf16 v[52:55], a[120:123], v[212:215], v[52:55]\n"
"ds_read_b64_tr_b16 a[12:13], v224 offset:264\n"
"v_mul_f32 v22, v255, v22\n"
"v_mul_f32 v23, v255, v23\n"
"v_cvt_pk_bf16_f32 v0, v0, v1\n"
"v_mfma_f32_16x16x32_bf16 v[72:75], a[124:127], v[212:215], v[72:75]\n"
"ds_read_b64_tr_b16 a[14:15], v224 offset:1288\n"
"v_cvt_pk_bf16_f32 v1, v2, v3\n"
"v_cvt_pk_bf16_f32 v2, v20, v21\n"
"v_cvt_pk_bf16_f32 v3, v22, v23\n"
"v_mfma_f32_16x16x32_bf16 v[92:95], a[128:131], v[212:215], v[92:95]\n"
"ds_read_b64_tr_b16 a[16:17], v224 offset:512\n"
"v_accvgpr_write_b32 a168, v0\n"
"v_accvgpr_write_b32 a169, v1\n"
"v_accvgpr_write_b32 a170, v2\n"
"v_mfma_f32_16x16x32_bf16 v[112:115], a[132:135], v[212:215], v[112:115]\n"
"ds_read_b64_tr_b16 a[18:19], v224 offset:1536\n"
"v_accvgpr_write_b32 a171, v3\n"
"v_mul_f32 v40, v255, v40\n"
"v_mul_f32 v41, v255, v41\n"
"v_mfma_f32_16x16x32_bf16 v[132:135], a[136:139], v[212:215], v[132:135]\n"
"ds_read_b64_tr_b16 a[20:21], v224 offset:520\n"
"v_mul_f32 v42, v255, v42\n"
"v_mul_f32 v43, v255, v43\n"
"v_mul_f32 v60, v255, v60\n"
"v_mfma_f32_16x16x32_bf16 v[152:155], a[140:143], v[212:215], v[152:155]\n"
"ds_read_b64_tr_b16 a[22:23], v224 offset:1544\n"
"v_mul_f32 v61, v255, v61\n"
"v_mul_f32 v62, v255, v62\n"
"v_mul_f32 v63, v255, v63\n"
"v_mfma_f32_16x16x32_bf16 a[156:159], a[164:167], v[212:215], a[156:159]\n"
"ds_read_b64_tr_b16 a[24:25], v224 offset:768\n"
"v_cvt_pk_bf16_f32 v40, v40, v41\n"
"v_cvt_pk_bf16_f32 v41, v42, v43\n"
"v_cvt_pk_bf16_f32 v42, v60, v61\n"
"v_mfma_f32_16x16x32_bf16 v[16:19], a[112:115], v[216:219], v[16:19]\n"
"ds_read_b64_tr_b16 a[26:27], v224 offset:1792\n"
"v_cvt_pk_bf16_f32 v43, v62, v63\n"
"v_accvgpr_write_b32 a188, v40\n"
"v_accvgpr_write_b32 a189, v41\n"
"v_mfma_f32_16x16x32_bf16 v[36:39], a[116:119], v[216:219], v[36:39]\n"
"ds_read_b64_tr_b16 a[28:29], v224 offset:776\n"
"v_accvgpr_write_b32 a190, v42\n"
"v_accvgpr_write_b32 a191, v43\n"
"v_mul_f32 v80, v255, v80\n"
"v_mfma_f32_16x16x32_bf16 v[56:59], a[120:123], v[216:219], v[56:59]\n"
"ds_read_b64_tr_b16 a[30:31], v224 offset:1800\n"
"v_add_u32 v240, 4294918144, v240\n"
"v_add_u32 v241, 4294868992, v241\n"
"v_add_u32 v242, 4294868992, v242\n"
"v_mfma_f32_16x16x32_bf16 v[76:79], a[124:127], v[216:219], v[76:79]\n"
"v_mul_f32 v81, v255, v81\n"
"v_mul_f32 v82, v255, v82\n"
"v_mul_f32 v83, v255, v83\n"
"v_mfma_f32_16x16x32_bf16 v[96:99], a[128:131], v[216:219], v[96:99]\n"
"v_mul_f32 v100, v255, v100\n"
"v_mul_f32 v101, v255, v101\n"
"v_mul_f32 v102, v255, v102\n"
"v_mfma_f32_16x16x32_bf16 v[116:119], a[132:135], v[216:219], v[116:119]\n"
"v_mul_f32 v103, v255, v103\n"
"v_cvt_pk_bf16_f32 v80, v80, v81\n"
"v_cvt_pk_bf16_f32 v81, v82, v83\n"
"v_mfma_f32_16x16x32_bf16 v[136:139], a[136:139], v[216:219], v[136:139]\n"
"v_cvt_pk_bf16_f32 v82, v100, v101\n"
"v_cvt_pk_bf16_f32 v83, v102, v103\n"
"v_accvgpr_write_b32 a208, v80\n"
"v_mfma_f32_16x16x32_bf16 v[156:159], a[140:143], v[216:219], v[156:159]\n"
"v_accvgpr_write_b32 a209, v81\n"
"v_accvgpr_write_b32 a210, v82\n"
"v_accvgpr_write_b32 a211, v83\n"
"v_mfma_f32_16x16x32_bf16 a[160:163], a[164:167], v[216:219], a[160:163]\n"
"s_waitcnt lgkmcnt(0)\n"
"s_barrier\n"
"v_mul_f32 v120, v255, v120\n"
"v_mul_f32 v121, v255, v121\n"
"v_mul_f32 v122, v255, v122\n"
"v_mul_f32 v123, v255, v123\n"
"v_pk_mul_f32 v[140:141], v[140:141], v[254:255] op_sel:[0,1] op_sel_hi:[1,1]\n"
"v_pk_mul_f32 v[142:143], v[142:143], v[254:255] op_sel:[0,1] op_sel_hi:[1,1]\n"
"v_cvt_pk_bf16_f32 v120, v120, v121\n"
"v_cvt_pk_bf16_f32 v121, v122, v123\n"
"v_cvt_pk_bf16_f32 v122, v140, v141\n"
"v_cvt_pk_bf16_f32 v123, v142, v143\n"
"v_accvgpr_write_b32 a228, v120\n"
"v_accvgpr_write_b32 a229, v121\n"
"v_accvgpr_write_b32 a230, v122\n"
"v_accvgpr_write_b32 a231, v123\n"
"v_mfma_f32_16x16x32_bf16 v[0:3], a[0:3], a[32:35], 0\n"
"s_add_u32 m0, s49, 139648\n"
"v_accvgpr_read_b32 v255, a148\n"
"v_cmp_nge_f32 vcc, s53, v255\n"
"v_rcp_f32 v255, v255\n"
"v_mfma_f32_16x16x32_bf16 v[20:23], a[4:7], a[32:35], 0\n"
"s_or_b64 s[58:59], s[58:59], vcc\n"
"s_mov_b32 s52, 256\n"
"v_mul_f32 v4, v255, v4\n"
"v_mul_f32 v5, v255, v5\n"
"v_mul_f32 v6, v255, v6\n"
"v_mfma_f32_16x16x32_bf16 v[40:43], a[8:11], a[32:35], 0\n"
"ds_read_b128 v[192:195], v230 offset:512\n"
"v_mul_f32 v7, v255, v7\n"
"v_mul_f32 v24, v255, v24\n"
"v_mul_f32 v25, v255, v25\n"
"v_mfma_f32_16x16x32_bf16 v[60:63], a[12:15], a[32:35], 0\n"
"ds_read_b128 v[196:199], v230 offset:2560\n"
"v_mul_f32 v26, v255, v26\n"
"v_mul_f32 v27, v255, v27\n"
"v_cvt_pk_bf16_f32 v4, v4, v5\n"
"v_mfma_f32_16x16x32_bf16 v[80:83], a[16:19], a[32:35], 0\n"
"ds_read_b128 v[200:203], v230 offset:4608\n"
"v_cvt_pk_bf16_f32 v5, v6, v7\n"
"v_cvt_pk_bf16_f32 v6, v24, v25\n"
"v_cvt_pk_bf16_f32 v7, v26, v27\n"
"v_mfma_f32_16x16x32_bf16 v[100:103], a[20:23], a[32:35], 0\n"
"buffer_load_dwordx4 v246, s[84:87], s52 offen lds\n"
"v_accvgpr_write_b32 a172, v4\n"
"v_accvgpr_write_b32 a173, v5\n"
"v_accvgpr_write_b32 a174, v6\n"
"v_mfma_f32_16x16x32_bf16 v[120:123], a[24:27], a[32:35], 0\n"
"v_accvgpr_write_b32 a175, v7\n"
"v_mul_f32 v44, v255, v44\n"
"v_mul_f32 v45, v255, v45\n"
"v_pk_mul_f32 v[46:47], v[46:47], v[254:255] op_sel:[0,1] op_sel_hi:[1,1]\n"
"v_pk_mul_f32 v[64:65], v[64:65], v[254:255] op_sel:[0,1] op_sel_hi:[1,1]\n"
"v_pk_mul_f32 v[66:67], v[66:67], v[254:255] op_sel:[0,1] op_sel_hi:[1,1]\n"
"v_cvt_pk_bf16_f32 v44, v44, v45\n"
"v_cvt_pk_bf16_f32 v45, v46, v47\n"
"v_cvt_pk_bf16_f32 v46, v64, v65\n"
"v_cvt_pk_bf16_f32 v47, v66, v67\n"
"v_accvgpr_write_b32 a192, v44\n"
"v_accvgpr_write_b32 a193, v45\n"
"v_accvgpr_write_b32 a194, v46\n"
"v_accvgpr_write_b32 a195, v47\n"
"v_pk_mul_f32 v[84:85], v[84:85], v[254:255] op_sel:[0,1] op_sel_hi:[1,1]\n"
"v_pk_mul_f32 v[86:87], v[86:87], v[254:255] op_sel:[0,1] op_sel_hi:[1,1]\n"
"v_pk_mul_f32 v[104:105], v[104:105], v[254:255] op_sel:[0,1] op_sel_hi:[1,1]\n"
"v_pk_mul_f32 v[106:107], v[106:107], v[254:255] op_sel:[0,1] op_sel_hi:[1,1]\n"
"v_cvt_pk_bf16_f32 v84, v84, v85\n"
"v_cvt_pk_bf16_f32 v85, v86, v87\n"
"v_cvt_pk_bf16_f32 v86, v104, v105\n"
"v_cvt_pk_bf16_f32 v87, v106, v107\n"
"v_accvgpr_write_b32 a212, v84\n"
"v_accvgpr_write_b32 a213, v85\n"
"v_accvgpr_write_b32 a214, v86\n"
"v_accvgpr_write_b32 a215, v87\n"
"v_pk_mul_f32 v[124:125], v[124:125], v[254:255] op_sel:[0,1] op_sel_hi:[1,1]\n"
"v_pk_mul_f32 v[126:127], v[126:127], v[254:255] op_sel:[0,1] op_sel_hi:[1,1]\n"
"v_pk_mul_f32 v[144:145], v[144:145], v[254:255] op_sel:[0,1] op_sel_hi:[1,1]\n"
"v_pk_mul_f32 v[146:147], v[146:147], v[254:255] op_sel:[0,1] op_sel_hi:[1,1]\n"
"v_cvt_pk_bf16_f32 v124, v124, v125\n"
"v_cvt_pk_bf16_f32 v125, v126, v127\n"
"v_cvt_pk_bf16_f32 v126, v144, v145\n"
"v_cvt_pk_bf16_f32 v127, v146, v147\n"
"v_accvgpr_write_b32 a232, v124\n"
"v_accvgpr_write_b32 a233, v125\n"
"v_accvgpr_write_b32 a234, v126\n"
"v_accvgpr_write_b32 a235, v127\n"
"v_mfma_f32_16x16x32_bf16 v[140:143], a[28:31], a[32:35], 0\n"
"s_add_u32 m0, s49, 143744\n"
"v_accvgpr_read_b32 v255, a152\n"
"v_cmp_nge_f32 vcc, s53, v255\n"
"v_rcp_f32 v255, v255\n"
"v_mfma_f32_16x16x32_bf16 v[4:7], a[0:3], a[36:39], 0\n"
"s_or_b64 s[58:59], s[58:59], vcc\n"
"s_mov_b32 s52, 41216\n"
"v_mul_f32 v8, v255, v8\n"
"v_mul_f32 v9, v255, v9\n"
"v_mul_f32 v10, v255, v10\n"
"v_mfma_f32_16x16x32_bf16 v[24:27], a[4:7], a[36:39], 0\n"
"ds_read_b128 v[204:207], v230 offset:6656\n"
"v_mul_f32 v11, v255, v11\n"
"v_mul_f32 v28, v255, v28\n"
"v_mul_f32 v29, v255, v29\n"
"v_mfma_f32_16x16x32_bf16 v[44:47], a[8:11], a[36:39], 0\n"
"ds_read_b128 v[208:211], v230 offset:8704\n"
"v_mul_f32 v30, v255, v30\n"
"v_mul_f32 v31, v255, v31\n"
"v_cvt_pk_bf16_f32 v8, v8, v9\n"
"v_mfma_f32_16x16x32_bf16 v[64:67], a[12:15], a[36:39], 0\n"
"ds_read_b64_tr_b16 v[160:161], v224 offset:16448\n"
"v_cvt_pk_bf16_f32 v9, v10, v11\n"
"v_cvt_pk_bf16_f32 v10, v28, v29\n"
"v_cvt_pk_bf16_f32 v11, v30, v31\n"
"v_mfma_f32_16x16x32_bf16 v[84:87], a[16:19], a[36:39], 0\n"
"ds_read_b64_tr_b16 v[162:163], v224 offset:17472\n"
"v_accvgpr_write_b32 a176, v8\n"
"v_accvgpr_write_b32 a177, v9\n"
"v_accvgpr_write_b32 a178, v10\n"
"v_mfma_f32_16x16x32_bf16 v[104:107], a[20:23], a[36:39], 0\n"
"ds_read_b64_tr_b16 v[164:165], v224 offset:16456\n"
"v_accvgpr_write_b32 a179, v11\n"
"v_mul_f32 v48, v255, v48\n"
"v_mul_f32 v49, v255, v49\n"
"v_mfma_f32_16x16x32_bf16 v[124:127], a[24:27], a[36:39], 0\n"
"v_mul_f32 v50, v255, v50\n"
"v_mul_f32 v51, v255, v51\n"
"v_mul_f32 v68, v255, v68\n"
"v_mul_f32 v69, v255, v69\n"
"v_pk_mul_f32 v[70:71], v[70:71], v[254:255] op_sel:[0,1] op_sel_hi:[1,1]\n"
"v_cvt_pk_bf16_f32 v48, v48, v49\n"
"v_cvt_pk_bf16_f32 v49, v50, v51\n"
"v_cvt_pk_bf16_f32 v50, v68, v69\n"
"v_cvt_pk_bf16_f32 v51, v70, v71\n"
"v_accvgpr_write_b32 a196, v48\n"
"v_accvgpr_write_b32 a197, v49\n"
"v_accvgpr_write_b32 a198, v50\n"
"v_accvgpr_write_b32 a199, v51\n"
"v_pk_mul_f32 v[88:89], v[88:89], v[254:255] op_sel:[0,1] op_sel_hi:[1,1]\n"
"v_pk_mul_f32 v[90:91], v[90:91], v[254:255] op_sel:[0,1] op_sel_hi:[1,1]\n"
"v_pk_mul_f32 v[108:109], v[108:109], v[254:255] op_sel:[0,1] op_sel_hi:[1,1]\n"
"v_pk_mul_f32 v[110:111], v[110:111], v[254:255] op_sel:[0,1] op_sel_hi:[1,1]\n"
"v_cvt_pk_bf16_f32 v88, v88, v89\n"
"v_cvt_pk_bf16_f32 v89, v90, v91\n"
"v_cvt_pk_bf16_f32 v90, v108, v109\n"
"v_cvt_pk_bf16_f32 v91, v110, v111\n"
"v_accvgpr_write_b32 a216, v88\n"
"v_accvgpr_write_b32 a217, v89\n"
"v_accvgpr_write_b32 a218, v90\n"
"v_accvgpr_write_b32 a219, v91\n"
"v_pk_mul_f32 v[128:129], v[128:129], v[254:255] op_sel:[0,1] op_sel_hi:[1,1]\n"
"v_pk_mul_f32 v[130:131], v[130:131], v[254:255] op_sel:[0,1] op_sel_hi:[1,1]\n"
"v_pk_mul_f32 v[148:149], v[148:149], v[254:255] op_sel:[0,1] op_sel_hi:[1,1]\n"
"v_pk_mul_f32 v[150:151], v[150:151], v[254:255] op_sel:[0,1] op_sel_hi:[1,1]\n"
"v_cvt_pk_bf16_f32 v128, v128, v129\n"
"v_cvt_pk_bf16_f32 v129, v130, v131\n"
"v_cvt_pk_bf16_f32 v130, v148, v149\n"
"v_cvt_pk_bf16_f32 v131, v150, v151\n"
"v_accvgpr_write_b32 a236, v128\n"
"v_accvgpr_write_b32 a237, v129\n"
"v_accvgpr_write_b32 a238, v130\n"
"v_accvgpr_write_b32 a239, v131\n"
"v_mfma_f32_16x16x32_bf16 v[144:147], a[28:31], a[36:39], 0\n"
"v_accvgpr_read_b32 v255, a156\n"
"v_cmp_nge_f32 vcc, s53, v255\n"
"v_rcp_f32 v255, v255\n"
"v_mfma_f32_16x16x32_bf16 v[8:11], a[0:3], a[40:43], 0\n"
"s_or_b64 s[58:59], s[58:59], vcc\n"
"v_mul_f32 v12, v255, v12\n"
"v_mul_f32 v13, v255, v13\n"
"v_mul_f32 v14, v255, v14\n"
"v_mfma_f32_16x16x32_bf16 v[28:31], a[4:7], a[40:43], 0\n"
"ds_read_b64_tr_b16 v[166:167], v224 offset:17480\n"
"v_mul_f32 v15, v255, v15\n"
"v_mul_f32 v32, v255, v32\n"
"v_mul_f32 v33, v255, v33\n"
"v_mfma_f32_16x16x32_bf16 v[48:51], a[8:11], a[40:43], 0\n"
"ds_read_b64_tr_b16 v[168:169], v224 offset:16704\n"
"v_mul_f32 v34, v255, v34\n"
"v_mul_f32 v35, v255, v35\n"
"v_cvt_pk_bf16_f32 v12, v12, v13\n"
"v_mfma_f32_16x16x32_bf16 v[68:71], a[12:15], a[40:43], 0\n"
"ds_read_b64_tr_b16 v[170:171], v224 offset:17728\n"
"v_cvt_pk_bf16_f32 v13, v14, v15\n"
"v_cvt_pk_bf16_f32 v14, v32, v33\n"
"v_cvt_pk_bf16_f32 v15, v34, v35\n"
"v_mfma_f32_16x16x32_bf16 v[88:91], a[16:19], a[40:43], 0\n"
"ds_read_b64_tr_b16 v[172:173], v224 offset:16712\n"
"v_accvgpr_write_b32 a180, v12\n"
"v_accvgpr_write_b32 a181, v13\n"
"v_accvgpr_write_b32 a182, v14\n"
"v_mfma_f32_16x16x32_bf16 v[108:111], a[20:23], a[40:43], 0\n"
"ds_read_b64_tr_b16 v[174:175], v224 offset:17736\n"
"v_accvgpr_write_b32 a183, v15\n"
"v_mul_f32 v52, v255, v52\n"
"v_mul_f32 v53, v255, v53\n"
"v_mfma_f32_16x16x32_bf16 v[128:131], a[24:27], a[40:43], 0\n"
"v_mul_f32 v54, v255, v54\n"
"v_mul_f32 v55, v255, v55\n"
"v_mul_f32 v72, v255, v72\n"
"v_mul_f32 v73, v255, v73\n"
"v_pk_mul_f32 v[74:75], v[74:75], v[254:255] op_sel:[0,1] op_sel_hi:[1,1]\n"
"v_cvt_pk_bf16_f32 v52, v52, v53\n"
"v_cvt_pk_bf16_f32 v53, v54, v55\n"
"v_cvt_pk_bf16_f32 v54, v72, v73\n"
"v_cvt_pk_bf16_f32 v55, v74, v75\n"
"v_accvgpr_write_b32 a200, v52\n"
"v_accvgpr_write_b32 a201, v53\n"
"v_accvgpr_write_b32 a202, v54\n"
"v_accvgpr_write_b32 a203, v55\n"
"v_pk_mul_f32 v[92:93], v[92:93], v[254:255] op_sel:[0,1] op_sel_hi:[1,1]\n"
"v_pk_mul_f32 v[94:95], v[94:95], v[254:255] op_sel:[0,1] op_sel_hi:[1,1]\n"
"v_pk_mul_f32 v[112:113], v[112:113], v[254:255] op_sel:[0,1] op_sel_hi:[1,1]\n"
"v_pk_mul_f32 v[114:115], v[114:115], v[254:255] op_sel:[0,1] op_sel_hi:[1,1]\n"
"v_cvt_pk_bf16_f32 v92, v92, v93\n"
"v_cvt_pk_bf16_f32 v93, v94, v95\n"
"v_cvt_pk_bf16_f32 v94, v112, v113\n"
"v_cvt_pk_bf16_f32 v95, v114, v115\n"
"v_accvgpr_write_b32 a220, v92\n"
"v_accvgpr_write_b32 a221, v93\n"
"v_accvgpr_write_b32 a222, v94\n"
"v_accvgpr_write_b32 a223, v95\n"
"v_pk_mul_f32 v[132:133], v[132:133], v[254:255] op_sel:[0,1] op_sel_hi:[1,1]\n"
"v_pk_mul_f32 v[134:135], v[134:135], v[254:255] op_sel:[0,1] op_sel_hi:[1,1]\n"
"v_pk_mul_f32 v[152:153], v[152:153], v[254:255] op_sel:[0,1] op_sel_hi:[1,1]\n"
"v_pk_mul_f32 v[154:155], v[154:155], v[254:255] op_sel:[0,1] op_sel_hi:[1,1]\n"
"v_cvt_pk_bf16_f32 v132, v132, v133\n"
"v_cvt_pk_bf16_f32 v133, v134, v135\n"
"v_cvt_pk_bf16_f32 v134, v152, v153\n"
"v_cvt_pk_bf16_f32 v135, v154, v155\n"
"v_accvgpr_write_b32 a240, v132\n"
"v_accvgpr_write_b32 a241, v133\n"
"v_accvgpr_write_b32 a242, v134\n"
"v_accvgpr_write_b32 a243, v135\n"
"v_mfma_f32_16x16x32_bf16 v[148:151], a[28:31], a[40:43], 0\n"
"v_accvgpr_read_b32 v255, a160\n"
"v_cmp_nge_f32 vcc, s53, v255\n"
"v_rcp_f32 v255, v255\n"
"v_mfma_f32_16x16x32_bf16 v[12:15], a[0:3], a[44:47], 0\n"
"s_or_b64 s[58:59], s[58:59], vcc\n"
"v_mul_f32 v16, v255, v16\n"
"v_mul_f32 v17, v255, v17\n"
"v_mul_f32 v18, v255, v18\n"
"v_mfma_f32_16x16x32_bf16 v[32:35], a[4:7], a[44:47], 0\n"
"ds_read_b64_tr_b16 v[176:177], v224 offset:16960\n"
"v_mul_f32 v19, v255, v19\n"
"v_mul_f32 v36, v255, v36\n"
"v_mul_f32 v37, v255, v37\n"
"v_mfma_f32_16x16x32_bf16 v[52:55], a[8:11], a[44:47], 0\n"
"ds_read_b64_tr_b16 v[178:179], v224 offset:17984\n"
"v_mul_f32 v38, v255, v38\n"
"v_mul_f32 v39, v255, v39\n"
"v_cvt_pk_bf16_f32 v16, v16, v17\n"
"v_mfma_f32_16x16x32_bf16 v[72:75], a[12:15], a[44:47], 0\n"
"ds_read_b64_tr_b16 v[180:181], v224 offset:16968\n"
"v_cvt_pk_bf16_f32 v17, v18, v19\n"
"v_cvt_pk_bf16_f32 v18, v36, v37\n"
"v_cvt_pk_bf16_f32 v19, v38, v39\n"
"v_mfma_f32_16x16x32_bf16 v[92:95], a[16:19], a[44:47], 0\n"
"ds_read_b64_tr_b16 v[182:183], v224 offset:17992\n"
"v_accvgpr_write_b32 a184, v16\n"
"v_accvgpr_write_b32 a185, v17\n"
"v_accvgpr_write_b32 a186, v18\n"
"v_mfma_f32_16x16x32_bf16 v[112:115], a[20:23], a[44:47], 0\n"
"ds_read_b64_tr_b16 v[184:185], v224 offset:17216\n"
"v_accvgpr_write_b32 a187, v19\n"
"v_mul_f32 v56, v255, v56\n"
"v_mul_f32 v57, v255, v57\n"
"v_mfma_f32_16x16x32_bf16 v[132:135], a[24:27], a[44:47], 0\n"
"v_mul_f32 v58, v255, v58\n"
"v_mul_f32 v59, v255, v59\n"
"v_mul_f32 v76, v255, v76\n"
"v_mul_f32 v77, v255, v77\n"
"v_pk_mul_f32 v[78:79], v[78:79], v[254:255] op_sel:[0,1] op_sel_hi:[1,1]\n"
"v_cvt_pk_bf16_f32 v56, v56, v57\n"
"v_cvt_pk_bf16_f32 v57, v58, v59\n"
"v_cvt_pk_bf16_f32 v58, v76, v77\n"
"v_cvt_pk_bf16_f32 v59, v78, v79\n"
"v_accvgpr_write_b32 a204, v56\n"
"v_accvgpr_write_b32 a205, v57\n"
"v_accvgpr_write_b32 a206, v58\n"
"v_accvgpr_write_b32 a207, v59\n"
"v_pk_mul_f32 v[96:97], v[96:97], v[254:255] op_sel:[0,1] op_sel_hi:[1,1]\n"
"v_pk_mul_f32 v[98:99], v[98:99], v[254:255] op_sel:[0,1] op_sel_hi:[1,1]\n"
"v_pk_mul_f32 v[116:117], v[116:117], v[254:255] op_sel:[0,1] op_sel_hi:[1,1]\n"
"v_pk_mul_f32 v[118:119], v[118:119], v[254:255] op_sel:[0,1] op_sel_hi:[1,1]\n"
"v_cvt_pk_bf16_f32 v96, v96, v97\n"
"v_cvt_pk_bf16_f32 v97, v98, v99\n"
"v_cvt_pk_bf16_f32 v98, v116, v117\n"
"v_cvt_pk_bf16_f32 v99, v118, v119\n"
"v_accvgpr_write_b32 a224, v96\n"
"v_accvgpr_write_b32 a225, v97\n"
"v_accvgpr_write_b32 a226, v98\n"
"v_accvgpr_write_b32 a227, v99\n"
"v_pk_mul_f32 v[136:137], v[136:137], v[254:255] op_sel:[0,1] op_sel_hi:[1,1]\n"
"v_pk_mul_f32 v[138:139], v[138:139], v[254:255] op_sel:[0,1] op_sel_hi:[1,1]\n"
"v_pk_mul_f32 v[156:157], v[156:157], v[254:255] op_sel:[0,1] op_sel_hi:[1,1]\n"
"v_pk_mul_f32 v[158:159], v[158:159], v[254:255] op_sel:[0,1] op_sel_hi:[1,1]\n"
"v_cvt_pk_bf16_f32 v136, v136, v137\n"
"v_cvt_pk_bf16_f32 v137, v138, v139\n"
"v_cvt_pk_bf16_f32 v138, v156, v157\n"
"v_cvt_pk_bf16_f32 v139, v158, v159\n"
"v_accvgpr_write_b32 a244, v136\n"
"v_accvgpr_write_b32 a245, v137\n"
"v_accvgpr_write_b32 a246, v138\n"
"v_accvgpr_write_b32 a247, v139\n"
"v_mfma_f32_16x16x32_bf16 v[152:155], a[28:31], a[44:47], 0\n"
"ds_read_b64_tr_b16 v[186:187], v224 offset:18240\n"
"ds_read_b64_tr_b16 v[188:189], v224 offset:17224\n"
"v_mfma_f32_16x16x32_bf16 v[16:19], a[0:3], a[48:51], 0\n"
"ds_read_b64_tr_b16 v[190:191], v224 offset:18248\n"
"buffer_load_dwordx4 v246, s[84:87], s52 offen lds\n"
"s_add_u32 m0, s49, 147840\n"
"v_mfma_f32_16x16x32_bf16 v[36:39], a[4:7], a[48:51], 0\n"
"s_mov_b32 s52, 82176\n"
"buffer_load_dwordx4 v246, s[84:87], s52 offen lds\n"
"s_add_u32 m0, s49, 151936\n"
"s_mov_b32 s52, 123136\n"
"v_mfma_f32_16x16x32_bf16 v[56:59], a[8:11], a[48:51], 0\n"
"buffer_load_dwordx4 v246, s[84:87], s52 offen lds\n"
"s_add_u32 m0, s49, 156032\n"
"s_mov_b32 s52, 164096\n"
"v_mfma_f32_16x16x32_bf16 v[76:79], a[12:15], a[48:51], 0\n"
"buffer_load_dwordx4 v246, s[84:87], s52 offen lds\n"
"s_add_u32 m0, s54, 106752\n"
"s_mov_b32 s52, 1048576\n"
"buffer_load_dwordx4 v245, s[36:39], s52 offen lds\n"
"s_add_u32 m0, s54, 107776\n"
"s_mov_b32 s52, 1081344\n"
"buffer_load_dwordx4 v245, s[36:39], s52 offen lds\n"
"s_add_u32 m0, s54, 114976\n"
"s_mov_b32 s52, 1048832\n"
"buffer_load_dwordx4 v245, s[36:39], s52 offen lds\n"
"v_mfma_f32_16x16x32_bf16 v[96:99], a[16:19], a[48:51], 0\n"
"s_add_u32 s52, s90, 640\n"
"v_mfma_f32_16x16x32_bf16 v[116:119], a[20:23], a[48:51], 0\n"
"s_mov_b32 exec_lo, 1023\n"
"s_mov_b32 exec_hi, 0\n"
"buffer_load_dword v254, v251, s[84:87], s52 offen\n"
"s_mov_b64 exec, -1\n"
"v_mfma_f32_16x16x32_bf16 v[136:139], a[24:27], a[48:51], 0\n"
"s_add_u32 s52, s91, 2621440\n"
"v_mfma_f32_16x16x32_bf16 v[156:159], a[28:31], a[48:51], 0\n"
"s_mov_b32 exec_lo, 255\n"
"s_mov_b32 exec_hi, 0\n"
"buffer_load_dword v254, v252, s[36:39], s52 offen\n"
"s_mov_b64 exec, -1\n"
"s_branch L_qpa_%=\n"
"L_qpbq_%=:\n"
"s_waitcnt vmcnt(18) lgkmcnt(0)\n"
"s_barrier\n"
"v_mfma_f32_16x16x32_bf16 v[0:3], v[160:163], v[192:195], v[0:3]\n"
"s_sub_u32 s36, s36, 512\n"
"s_subb_u32 s37, s37, 0\n"
"s_add_u32 s38, s38, 512\n"
"v_mfma_f32_16x16x32_bf16 v[4:7], v[160:163], v[196:199], v[4:7]\n"
"s_mov_b32 s98, 0\n"
"s_mov_b32 s51, 3\n"
"v_mfma_f32_16x16x32_bf16 v[8:11], v[160:163], v[200:203], v[8:11]\n"
"ds_read_b128 a[32:35], v232 offset:0\n"
"v_mfma_f32_16x16x32_bf16 v[12:15], v[160:163], v[204:207], v[12:15]\n"
"ds_read_b128 a[36:39], v232 offset:2048\n"
"v_mfma_f32_16x16x32_bf16 v[16:19], v[160:163], v[208:211], v[16:19]\n"
"ds_read_b128 a[40:43], v232 offset:4096\n"
"v_mfma_f32_16x16x32_bf16 v[20:23], v[164:167], v[192:195], v[20:23]\n"
"ds_read_b128 a[44:47], v232 offset:6144\n"
"v_mfma_f32_16x16x32_bf16 v[24:27], v[164:167], v[196:199], v[24:27]\n"
"ds_read_b128 a[48:51], v232 offset:8192\n"
"s_add_u32 m0, s49, 86272\n"
"v_mfma_f32_16x16x32_bf16 v[28:31], v[164:167], v[200:203], v[28:31]\n"
"ds_read_b64_tr_b16 a[0:1], v226 offset:0\n"
"s_mov_b32 s52, 128\n"
"v_mfma_f32_16x16x32_bf16 v[32:35], v[164:167], v[204:207], v[32:35]\n"
"buffer_load_dwordx4 v246, s[84:87], s52 offen lds\n"
"v_mfma_f32_16x16x32_bf16 v[36:39], v[164:167], v[208:211], v[36:39]\n"
"ds_read_b64_tr_b16 a[2:3], v226 offset:1024\n"
"v_mfma_f32_16x16x32_bf16 v[40:43], v[168:171], v[192:195], v[40:43]\n"
"v_mfma_f32_16x16x32_bf16 v[44:47], v[168:171], v[196:199], v[44:47]\n"
"ds_read_b64_tr_b16 a[4:5], v226 offset:8\n"
"v_mfma_f32_16x16x32_bf16 v[48:51], v[168:171], v[200:203], v[48:51]\n"
"ds_read_b64_tr_b16 a[6:7], v226 offset:1032\n"
"s_add_u32 m0, s49, 90368\n"
"v_mfma_f32_16x16x32_bf16 v[52:55], v[168:171], v[204:207], v[52:55]\n"
"s_mov_b32 s52, 41088\n"
"buffer_load_dwordx4 v246, s[84:87], s52 offen lds\n"
"v_mfma_f32_16x16x32_bf16 v[56:59], v[168:171], v[208:211], v[56:59]\n"
"ds_read_b64_tr_b16 a[8:9], v226 offset:256\n"
"v_mfma_f32_16x16x32_bf16 v[60:63], v[172:175], v[192:195], v[60:63]\n"
"ds_read_b64_tr_b16 a[10:11], v226 offset:1280\n"
"v_mfma_f32_16x16x32_bf16 v[64:67], v[172:175], v[196:199], v[64:67]\n"
"v_mfma_f32_16x16x32_bf16 v[68:71], v[172:175], v[200:203], v[68:71]\n"
"ds_read_b64_tr_b16 a[12:13], v226 offset:264\n"
"v_mfma_f32_16x16x32_bf16 v[72:75], v[172:175], v[204:207], v[72:75]\n"
"ds_read_b64_tr_b16 a[14:15], v226 offset:1288\n"
"s_add_u32 m0, s49, 94464\n"
"v_mfma_f32_16x16x32_bf16 v[76:79], v[172:175], v[208:211], v[76:79]\n"
"s_mov_b32 s52, 82048\n"
"buffer_load_dwordx4 v246, s[84:87], s52 offen lds\n"
"v_mfma_f32_16x16x32_bf16 v[80:83], v[176:179], v[192:195], v[80:83]\n"
"ds_read_b64_tr_b16 a[16:17], v226 offset:512\n"
"v_mfma_f32_16x16x32_bf16 v[84:87], v[176:179], v[196:199], v[84:87]\n"
"v_mfma_f32_16x16x32_bf16 v[88:91], v[176:179], v[200:203], v[88:91]\n"
"ds_read_b64_tr_b16 a[18:19], v226 offset:1536\n"
"v_mfma_f32_16x16x32_bf16 v[92:95], v[176:179], v[204:207], v[92:95]\n"
"ds_read_b64_tr_b16 a[20:21], v226 offset:520\n"
"v_mfma_f32_16x16x32_bf16 v[96:99], v[176:179], v[208:211], v[96:99]\n"
"s_add_u32 m0, s49, 98560\n"
"s_mov_b32 s52, 123008\n"
"v_mfma_f32_16x16x32_bf16 v[100:103], v[180:183], v[192:195], v[100:103]\n"
"ds_read_b64_tr_b16 a[22:23], v226 offset:1544\n"
"v_mfma_f32_16x16x32_bf16 v[104:107], v[180:183], v[196:199], v[104:107]\n"
"ds_read_b64_tr_b16 a[24:25], v226 offset:768\n"
"v_mfma_f32_16x16x32_bf16 v[108:111], v[180:183], v[200:203], v[108:111]\n"
"buffer_load_dwordx4 v246, s[84:87], s52 offen lds\n"
"v_mfma_f32_16x16x32_bf16 v[112:115], v[180:183], v[204:207], v[112:115]\n"
"ds_read_b64_tr_b16 a[26:27], v226 offset:1792\n"
"v_mfma_f32_16x16x32_bf16 v[116:119], v[180:183], v[208:211], v[116:119]\n"
"ds_read_b64_tr_b16 a[28:29], v226 offset:776\n"
"v_mfma_f32_16x16x32_bf16 v[120:123], v[184:187], v[192:195], v[120:123]\n"
"s_add_u32 m0, s49, 102656\n"
"s_mov_b32 s52, 163968\n"
"v_mfma_f32_16x16x32_bf16 v[124:127], v[184:187], v[196:199], v[124:127]\n"
"ds_read_b64_tr_b16 a[30:31], v226 offset:1800\n"
"v_mfma_f32_16x16x32_bf16 v[128:131], v[184:187], v[200:203], v[128:131]\n"
"buffer_load_dwordx4 v246, s[84:87], s52 offen lds\n"
"v_mfma_f32_16x16x32_bf16 v[132:135], v[184:187], v[204:207], v[132:135]\n"
"v_mfma_f32_16x16x32_bf16 v[136:139], v[184:187], v[208:211], v[136:139]\n"
"v_mfma_f32_16x16x32_bf16 v[140:143], v[188:191], v[192:195], v[140:143]\n"
"v_mfma_f32_16x16x32_bf16 v[144:147], v[188:191], v[196:199], v[144:147]\n"
"s_add_u32 m0, s54, 53376\n"
"s_mov_b32 s52, 524288\n"
"v_mfma_f32_16x16x32_bf16 v[148:151], v[188:191], v[200:203], v[148:151]\n"
"buffer_load_dwordx4 v245, s[36:39], s52 offen lds\n"
"v_mfma_f32_16x16x32_bf16 v[152:155], v[188:191], v[204:207], v[152:155]\n"
"v_mfma_f32_16x16x32_bf16 v[156:159], v[188:191], v[208:211], v[156:159]\n"
"s_waitcnt lgkmcnt(0)\n"
"v_mfma_f32_16x16x32_bf16 v[0:3], a[0:3], a[32:35], v[0:3]\n"
"ds_read_b128 v[192:195], v232 offset:512\n"
"v_mfma_f32_16x16x32_bf16 v[4:7], a[0:3], a[36:39], v[4:7]\n"
"ds_read_b128 v[196:199], v232 offset:2560\n"
"v_mfma_f32_16x16x32_bf16 v[8:11], a[0:3], a[40:43], v[8:11]\n"
"s_add_u32 m0, s54, 54400\n"
"s_mov_b32 s52, 557056\n"
"v_mfma_f32_16x16x32_bf16 v[12:15], a[0:3], a[44:47], v[12:15]\n"
"ds_read_b128 v[200:203], v232 offset:4608\n"
"v_mfma_f32_16x16x32_bf16 v[16:19], a[0:3], a[48:51], v[16:19]\n"
"ds_read_b128 v[204:207], v232 offset:6656\n"
"v_mfma_f32_16x16x32_bf16 v[20:23], a[4:7], a[32:35], v[20:23]\n"
"buffer_load_dwordx4 v245, s[36:39], s52 offen lds\n"
"v_mfma_f32_16x16x32_bf16 v[24:27], a[4:7], a[36:39], v[24:27]\n"
"ds_read_b128 v[208:211], v232 offset:8704\n"
"v_mfma_f32_16x16x32_bf16 v[28:31], a[4:7], a[40:43], v[28:31]\n"
"ds_read_b64_tr_b16 v[160:161], v226 offset:16448\n"
"v_mfma_f32_16x16x32_bf16 v[32:35], a[4:7], a[44:47], v[32:35]\n"
"s_add_u32 m0, s54, 61600\n"
"s_mov_b32 s52, 524544\n"
"v_mfma_f32_16x16x32_bf16 v[36:39], a[4:7], a[48:51], v[36:39]\n"
"ds_read_b64_tr_b16 v[162:163], v226 offset:17472\n"
"v_mfma_f32_16x16x32_bf16 v[40:43], a[8:11], a[32:35], v[40:43]\n"
"buffer_load_dwordx4 v245, s[36:39], s52 offen lds\n"
"v_mfma_f32_16x16x32_bf16 v[44:47], a[8:11], a[36:39], v[44:47]\n"
"ds_read_b64_tr_b16 v[164:165], v226 offset:16456\n"
"v_mfma_f32_16x16x32_bf16 v[48:51], a[8:11], a[40:43], v[48:51]\n"
"ds_read_b64_tr_b16 v[166:167], v226 offset:17480\n"
"v_mfma_f32_16x16x32_bf16 v[52:55], a[8:11], a[44:47], v[52:55]\n"
"v_mfma_f32_16x16x32_bf16 v[56:59], a[8:11], a[48:51], v[56:59]\n"
"ds_read_b64_tr_b16 v[168:169], v226 offset:16704\n"
"s_add_u32 m0, s54, 62624\n"
"v_mfma_f32_16x16x32_bf16 v[60:63], a[12:15], a[32:35], v[60:63]\n"
"ds_read_b64_tr_b16 v[170:171], v226 offset:17728\n"
"s_mov_b32 s52, 557312\n"
"v_mfma_f32_16x16x32_bf16 v[64:67], a[12:15], a[36:39], v[64:67]\n"
"buffer_load_dwordx4 v245, s[36:39], s52 offen lds\n"
"v_mfma_f32_16x16x32_bf16 v[68:71], a[12:15], a[40:43], v[68:71]\n"
"ds_read_b64_tr_b16 v[172:173], v226 offset:16712\n"
"v_mfma_f32_16x16x32_bf16 v[72:75], a[12:15], a[44:47], v[72:75]\n"
"ds_read_b64_tr_b16 v[174:175], v226 offset:17736\n"
"v_mfma_f32_16x16x32_bf16 v[76:79], a[12:15], a[48:51], v[76:79]\n"
"v_mfma_f32_16x16x32_bf16 v[80:83], a[16:19], a[32:35], v[80:83]\n"
"ds_read_b64_tr_b16 v[176:177], v226 offset:16960\n"
"s_add_u32 m0, s54, 69824\n"
"v_mfma_f32_16x16x32_bf16 v[84:87], a[16:19], a[36:39], v[84:87]\n"
"s_mov_b32 s52, 786432\n"
"buffer_load_dwordx4 v245, s[36:39], s52 offen lds\n"
"v_mfma_f32_16x16x32_bf16 v[88:91], a[16:19], a[40:43], v[88:91]\n"
"ds_read_b64_tr_b16 v[178:179], v226 offset:17984\n"
"v_mfma_f32_16x16x32_bf16 v[92:95], a[16:19], a[44:47], v[92:95]\n"
"ds_read_b64_tr_b16 v[180:181], v226 offset:16968\n"
"v_mfma_f32_16x16x32_bf16 v[96:99], a[16:19], a[48:51], v[96:99]\n"
"v_mfma_f32_16x16x32_bf16 v[100:103], a[20:23], a[32:35], v[100:103]\n"
"ds_read_b64_tr_b16 v[182:183], v226 offset:17992\n"
"v_mfma_f32_16x16x32_bf16 v[104:107], a[20:23], a[36:39], v[104:107]\n"
"ds_read_b64_tr_b16 v[184:185], v226 offset:17216\n"
"s_add_u32 m0, s54, 70848\n"
"v_mfma_f32_16x16x32_bf16 v[108:111], a[20:23], a[40:43], v[108:111]\n"
"s_mov_b32 s52, 819200\n"
"buffer_load_dwordx4 v245, s[36:39], s52 offen lds\n"
"v_mfma_f32_16x16x32_bf16 v[112:115], a[20:23], a[44:47], v[112:115]\n"
"ds_read_b64_tr_b16 v[186:187], v226 offset:18240\n"
"v_mfma_f32_16x16x32_bf16 v[116:119], a[20:23], a[48:51], v[116:119]\n"
"ds_read_b64_tr_b16 v[188:189], v226 offset:17224\n"
"v_mfma_f32_16x16x32_bf16 v[120:123], a[24:27], a[32:35], v[120:123]\n"
"v_mfma_f32_16x16x32_bf16 v[124:127], a[24:27], a[36:39], v[124:127]\n"
"ds_read_b64_tr_b16 v[190:191], v226 offset:18248\n"
"v_mfma_f32_16x16x32_bf16 v[128:131], a[24:27], a[40:43], v[128:131]\n"
"s_add_u32 m0, s54, 78048\n"
"s_mov_b32 s52, 786688\n"
"v_mfma_f32_16x16x32_bf16 v[132:135], a[24:27], a[44:47], v[132:135]\n"
"buffer_load_dwordx4 v245, s[36:39], s52 offen lds\n"
"v_mfma_f32_16x16x32_bf16 v[136:139], a[24:27], a[48:51], v[136:139]\n"
"v_mfma_f32_16x16x32_bf16 v[140:143], a[28:31], a[32:35], v[140:143]\n"
"v_mfma_f32_16x16x32_bf16 v[144:147], a[28:31], a[36:39], v[144:147]\n"
"v_mfma_f32_16x16x32_bf16 v[148:151], a[28:31], a[40:43], v[148:151]\n"
"v_mfma_f32_16x16x32_bf16 v[152:155], a[28:31], a[44:47], v[152:155]\n"
"s_add_u32 m0, s54, 79072\n"
"s_mov_b32 s52, 819456\n"
"v_mfma_f32_16x16x32_bf16 v[156:159], a[28:31], a[48:51], v[156:159]\n"
"s_waitcnt vmcnt(14) lgkmcnt(0)\n"
"s_barrier\n"
"v_mfma_f32_16x16x32_bf16 v[0:3], v[160:163], v[192:195], v[0:3]\n"
"buffer_load_dwordx4 v245, s[36:39], s52 offen lds\n"
"v_mfma_f32_16x16x32_bf16 v[4:7], v[160:163], v[196:199], v[4:7]\n"
"ds_read_b128 a[32:35], v230 offset:0\n"
"v_mfma_f32_16x16x32_bf16 v[8:11], v[160:163], v[200:203], v[8:11]\n"
"ds_read_b128 a[36:39], v230 offset:2048\n"
"v_mfma_f32_16x16x32_bf16 v[12:15], v[160:163], v[204:207], v[12:15]\n"
"ds_read_b128 a[40:43], v230 offset:4096\n"
"v_mfma_f32_16x16x32_bf16 v[16:19], v[160:163], v[208:211], v[16:19]\n"
"ds_read_b128 a[44:47], v230 offset:6144\n"
"v_mfma_f32_16x16x32_bf16 v[20:23], v[164:167], v[192:195], v[20:23]\n"
"v_mfma_f32_16x16x32_bf16 v[24:27], v[164:167], v[196:199], v[24:27]\n"
"ds_read_b128 a[48:51], v230 offset:8192\n"
"v_mfma_f32_16x16x32_bf16 v[28:31], v[164:167], v[200:203], v[28:31]\n"
"ds_read_b64_tr_b16 a[0:1], v224 offset:0\n"
"v_mfma_f32_16x16x32_bf16 v[32:35], v[164:167], v[204:207], v[32:35]\n"
"v_mfma_f32_16x16x32_bf16 v[36:39], v[164:167], v[208:211], v[36:39]\n"
"ds_read_b64_tr_b16 a[2:3], v224 offset:1024\n"
"v_mfma_f32_16x16x32_bf16 v[40:43], v[168:171], v[192:195], v[40:43]\n"
"v_mfma_f32_16x16x32_bf16 v[44:47], v[168:171], v[196:199], v[44:47]\n"
"ds_read_b64_tr_b16 a[4:5], v224 offset:8\n"
"v_mfma_f32_16x16x32_bf16 v[48:51], v[168:171], v[200:203], v[48:51]\n"
"ds_read_b64_tr_b16 a[6:7], v224 offset:1032\n"
"v_mfma_f32_16x16x32_bf16 v[52:55], v[168:171], v[204:207], v[52:55]\n"
"v_mfma_f32_16x16x32_bf16 v[56:59], v[168:171], v[208:211], v[56:59]\n"
"ds_read_b64_tr_b16 a[8:9], v224 offset:256\n"
"v_mfma_f32_16x16x32_bf16 v[60:63], v[172:175], v[192:195], v[60:63]\n"
"ds_read_b64_tr_b16 a[10:11], v224 offset:1280\n"
"v_mfma_f32_16x16x32_bf16 v[64:67], v[172:175], v[196:199], v[64:67]\n"
"v_mfma_f32_16x16x32_bf16 v[68:71], v[172:175], v[200:203], v[68:71]\n"
"ds_read_b64_tr_b16 a[12:13], v224 offset:264\n"
"v_mfma_f32_16x16x32_bf16 v[72:75], v[172:175], v[204:207], v[72:75]\n"
"ds_read_b64_tr_b16 a[14:15], v224 offset:1288\n"
"v_mfma_f32_16x16x32_bf16 v[76:79], v[172:175], v[208:211], v[76:79]\n"
"v_mfma_f32_16x16x32_bf16 v[80:83], v[176:179], v[192:195], v[80:83]\n"
"ds_read_b64_tr_b16 a[16:17], v224 offset:512\n"
"v_mfma_f32_16x16x32_bf16 v[84:87], v[176:179], v[196:199], v[84:87]\n"
"v_mfma_f32_16x16x32_bf16 v[88:91], v[176:179], v[200:203], v[88:91]\n"
"ds_read_b64_tr_b16 a[18:19], v224 offset:1536\n"
"v_mfma_f32_16x16x32_bf16 v[92:95], v[176:179], v[204:207], v[92:95]\n"
"ds_read_b64_tr_b16 a[20:21], v224 offset:520\n"
"v_mfma_f32_16x16x32_bf16 v[96:99], v[176:179], v[208:211], v[96:99]\n"
"v_mfma_f32_16x16x32_bf16 v[100:103], v[180:183], v[192:195], v[100:103]\n"
"ds_read_b64_tr_b16 a[22:23], v224 offset:1544\n"
"v_mfma_f32_16x16x32_bf16 v[104:107], v[180:183], v[196:199], v[104:107]\n"
"ds_read_b64_tr_b16 a[24:25], v224 offset:768\n"
"v_mfma_f32_16x16x32_bf16 v[108:111], v[180:183], v[200:203], v[108:111]\n"
"v_mfma_f32_16x16x32_bf16 v[112:115], v[180:183], v[204:207], v[112:115]\n"
"ds_read_b64_tr_b16 a[26:27], v224 offset:1792\n"
"v_mfma_f32_16x16x32_bf16 v[116:119], v[180:183], v[208:211], v[116:119]\n"
"ds_read_b64_tr_b16 a[28:29], v224 offset:776\n"
"v_mfma_f32_16x16x32_bf16 v[120:123], v[184:187], v[192:195], v[120:123]\n"
"v_mfma_f32_16x16x32_bf16 v[124:127], v[184:187], v[196:199], v[124:127]\n"
"ds_read_b64_tr_b16 a[30:31], v224 offset:1800\n"
"v_mfma_f32_16x16x32_bf16 v[128:131], v[184:187], v[200:203], v[128:131]\n"
"v_mfma_f32_16x16x32_bf16 v[132:135], v[184:187], v[204:207], v[132:135]\n"
"v_mfma_f32_16x16x32_bf16 v[136:139], v[184:187], v[208:211], v[136:139]\n"
"v_mfma_f32_16x16x32_bf16 v[140:143], v[188:191], v[192:195], v[140:143]\n"
"v_mfma_f32_16x16x32_bf16 v[144:147], v[188:191], v[196:199], v[144:147]\n"
"v_mfma_f32_16x16x32_bf16 v[148:151], v[188:191], v[200:203], v[148:151]\n"
"v_mfma_f32_16x16x32_bf16 v[152:155], v[188:191], v[204:207], v[152:155]\n"
"v_mfma_f32_16x16x32_bf16 v[156:159], v[188:191], v[208:211], v[156:159]\n"
"s_waitcnt lgkmcnt(0)\n"
"v_mfma_f32_16x16x32_bf16 v[0:3], a[0:3], a[32:35], v[0:3]\n"
"ds_read_b128 v[192:195], v230 offset:512\n"
"v_mfma_f32_16x16x32_bf16 v[4:7], a[0:3], a[36:39], v[4:7]\n"
"ds_read_b128 v[196:199], v230 offset:2560\n"
"v_mfma_f32_16x16x32_bf16 v[8:11], a[0:3], a[40:43], v[8:11]\n"
"v_mfma_f32_16x16x32_bf16 v[12:15], a[0:3], a[44:47], v[12:15]\n"
"ds_read_b128 v[200:203], v230 offset:4608\n"
"v_mfma_f32_16x16x32_bf16 v[16:19], a[0:3], a[48:51], v[16:19]\n"
"ds_read_b128 v[204:207], v230 offset:6656\n"
"v_mfma_f32_16x16x32_bf16 v[20:23], a[4:7], a[32:35], v[20:23]\n"
"v_mfma_f32_16x16x32_bf16 v[24:27], a[4:7], a[36:39], v[24:27]\n"
"ds_read_b128 v[208:211], v230 offset:8704\n"
"v_mfma_f32_16x16x32_bf16 v[28:31], a[4:7], a[40:43], v[28:31]\n"
"ds_read_b64_tr_b16 v[160:161], v224 offset:16448\n"
"v_mfma_f32_16x16x32_bf16 v[32:35], a[4:7], a[44:47], v[32:35]\n"
"v_mfma_f32_16x16x32_bf16 v[36:39], a[4:7], a[48:51], v[36:39]\n"
"ds_read_b64_tr_b16 v[162:163], v224 offset:17472\n"
"v_mfma_f32_16x16x32_bf16 v[40:43], a[8:11], a[32:35], v[40:43]\n"
"v_mfma_f32_16x16x32_bf16 v[44:47], a[8:11], a[36:39], v[44:47]\n"
"ds_read_b64_tr_b16 v[164:165], v224 offset:16456\n"
"v_mfma_f32_16x16x32_bf16 v[48:51], a[8:11], a[40:43], v[48:51]\n"
"ds_read_b64_tr_b16 v[166:167], v224 offset:17480\n"
"v_mfma_f32_16x16x32_bf16 v[52:55], a[8:11], a[44:47], v[52:55]\n"
"v_mfma_f32_16x16x32_bf16 v[56:59], a[8:11], a[48:51], v[56:59]\n"
"ds_read_b64_tr_b16 v[168:169], v224 offset:16704\n"
"v_mfma_f32_16x16x32_bf16 v[60:63], a[12:15], a[32:35], v[60:63]\n"
"ds_read_b64_tr_b16 v[170:171], v224 offset:17728\n"
"v_mfma_f32_16x16x32_bf16 v[64:67], a[12:15], a[36:39], v[64:67]\n"
"v_mfma_f32_16x16x32_bf16 v[68:71], a[12:15], a[40:43], v[68:71]\n"
"ds_read_b64_tr_b16 v[172:173], v224 offset:16712\n"
"v_mfma_f32_16x16x32_bf16 v[72:75], a[12:15], a[44:47], v[72:75]\n"
"ds_read_b64_tr_b16 v[174:175], v224 offset:17736\n"
"v_mfma_f32_16x16x32_bf16 v[76:79], a[12:15], a[48:51], v[76:79]\n"
"v_mfma_f32_16x16x32_bf16 v[80:83], a[16:19], a[32:35], v[80:83]\n"
"ds_read_b64_tr_b16 v[176:177], v224 offset:16960\n"
"v_mfma_f32_16x16x32_bf16 v[84:87], a[16:19], a[36:39], v[84:87]\n"
"v_mfma_f32_16x16x32_bf16 v[88:91], a[16:19], a[40:43], v[88:91]\n"
"ds_read_b64_tr_b16 v[178:179], v224 offset:17984\n"
"v_mfma_f32_16x16x32_bf16 v[92:95], a[16:19], a[44:47], v[92:95]\n"
"ds_read_b64_tr_b16 v[180:181], v224 offset:16968\n"
"v_mfma_f32_16x16x32_bf16 v[96:99], a[16:19], a[48:51], v[96:99]\n"
"v_mfma_f32_16x16x32_bf16 v[100:103], a[20:23], a[32:35], v[100:103]\n"
"ds_read_b64_tr_b16 v[182:183], v224 offset:17992\n"
"v_mfma_f32_16x16x32_bf16 v[104:107], a[20:23], a[36:39], v[104:107]\n"
"ds_read_b64_tr_b16 v[184:185], v224 offset:17216\n"
"v_mfma_f32_16x16x32_bf16 v[108:111], a[20:23], a[40:43], v[108:111]\n"
"v_mfma_f32_16x16x32_bf16 v[112:115], a[20:23], a[44:47], v[112:115]\n"
"ds_read_b64_tr_b16 v[186:187], v224 offset:18240\n"
"v_mfma_f32_16x16x32_bf16 v[116:119], a[20:23], a[48:51], v[116:119]\n"
"ds_read_b64_tr_b16 v[188:189], v224 offset:17224\n"
"v_mfma_f32_16x16x32_bf16 v[120:123], a[24:27], a[32:35], v[120:123]\n"
"v_mfma_f32_16x16x32_bf16 v[124:127], a[24:27], a[36:39], v[124:127]\n"
"ds_read_b64_tr_b16 v[190:191], v224 offset:18248\n"
"v_mfma_f32_16x16x32_bf16 v[128:131], a[24:27], a[40:43], v[128:131]\n"
"v_mfma_f32_16x16x32_bf16 v[132:135], a[24:27], a[44:47], v[132:135]\n"
"v_mfma_f32_16x16x32_bf16 v[136:139], a[24:27], a[48:51], v[136:139]\n"
"v_mfma_f32_16x16x32_bf16 v[140:143], a[28:31], a[32:35], v[140:143]\n"
"v_mfma_f32_16x16x32_bf16 v[144:147], a[28:31], a[36:39], v[144:147]\n"
"v_mfma_f32_16x16x32_bf16 v[148:151], a[28:31], a[40:43], v[148:151]\n"
"v_mfma_f32_16x16x32_bf16 v[152:155], a[28:31], a[44:47], v[152:155]\n"
"v_mfma_f32_16x16x32_bf16 v[156:159], a[28:31], a[48:51], v[156:159]\n"
"s_waitcnt lgkmcnt(0)\n"
"s_barrier\n"
"v_mfma_f32_16x16x32_bf16 v[0:3], v[160:163], v[192:195], v[0:3]\n"
"s_add_u32 m0, s49, 32896\n"
"s_mov_b32 s52, 0\n"
"buffer_load_dwordx4 v246, s[84:87], s52 offen lds\n"
"v_mfma_f32_16x16x32_bf16 v[20:23], v[164:167], v[192:195], v[20:23]\n"
"v_mfma_f32_16x16x32_bf16 v[40:43], v[168:171], v[192:195], v[40:43]\n"
"v_mfma_f32_16x16x32_bf16 v[60:63], v[172:175], v[192:195], v[60:63]\n"
"s_add_u32 m0, s49, 36992\n"
"s_mov_b32 s52, 40960\n"
"buffer_load_dwordx4 v246, s[84:87], s52 offen lds\n"
"v_mfma_f32_16x16x32_bf16 v[80:83], v[176:179], v[192:195], v[80:83]\n"
"v_mfma_f32_16x16x32_bf16 v[100:103], v[180:183], v[192:195], v[100:103]\n"
"v_mfma_f32_16x16x32_bf16 v[120:123], v[184:187], v[192:195], v[120:123]\n"
"s_add_u32 m0, s49, 41088\n"
"s_mov_b32 s52, 81920\n"
"buffer_load_dwordx4 v246, s[84:87], s52 offen lds\n"
"v_mfma_f32_16x16x32_bf16 v[140:143], v[188:191], v[192:195], v[140:143]\n"
"v_mfma_f32_16x16x32_bf16 v[4:7], v[160:163], v[196:199], v[4:7]\n"
"v_mfma_f32_16x16x32_bf16 v[24:27], v[164:167], v[196:199], v[24:27]\n"
"s_add_u32 m0, s49, 45184\n"
"s_mov_b32 s52, 122880\n"
"buffer_load_dwordx4 v246, s[84:87], s52 offen lds\n"
"v_mfma_f32_16x16x32_bf16 v[44:47], v[168:171], v[196:199], v[44:47]\n"
"v_mul_f32 v0, s50, v0\n"
"v_mul_f32 v1, s50, v1\n"
"v_mul_f32 v2, s50, v2\n"
"v_mfma_f32_16x16x32_bf16 v[64:67], v[172:175], v[196:199], v[64:67]\n"
"v_mul_f32 v3, s50, v3\n"
"v_mul_f32 v20, s50, v20\n"
"v_mul_f32 v21, s50, v21\n"
"v_mfma_f32_16x16x32_bf16 v[84:87], v[176:179], v[196:199], v[84:87]\n"
"s_add_u32 m0, s49, 49280\n"
"s_mov_b32 s52, 163840\n"
"buffer_load_dwordx4 v246, s[84:87], s52 offen lds\n"
"v_mul_f32 v22, s50, v22\n"
"v_mfma_f32_16x16x32_bf16 v[104:107], v[180:183], v[196:199], v[104:107]\n"
"v_mul_f32 v23, s50, v23\n"
"v_cvt_pk_bf16_f32 v212, v0, v1\n"
"v_cvt_pk_bf16_f32 v213, v2, v3\n"
"v_mfma_f32_16x16x32_bf16 v[124:127], v[184:187], v[196:199], v[124:127]\n"
"v_cvt_pk_bf16_f32 v214, v20, v21\n"
"v_cvt_pk_bf16_f32 v215, v22, v23\n"
"v_accvgpr_write_b32 a168, v212\n"
"v_mfma_f32_16x16x32_bf16 v[144:147], v[188:191], v[196:199], v[144:147]\n"
"s_add_u32 m0, s54, 0\n"
"s_mov_b32 s52, 0\n"
"buffer_load_dwordx4 v245, s[36:39], s52 offen lds\n"
"v_accvgpr_write_b32 a169, v213\n"
"v_mfma_f32_16x16x32_bf16 v[8:11], v[160:163], v[200:203], v[8:11]\n"
"v_accvgpr_write_b32 a170, v214\n"
"v_accvgpr_write_b32 a171, v215\n"
"v_mul_f32 v40, s50, v40\n"
"v_mfma_f32_16x16x32_bf16 v[28:31], v[164:167], v[200:203], v[28:31]\n"
"v_mul_f32 v41, s50, v41\n"
"v_mul_f32 v42, s50, v42\n"
"v_mul_f32 v43, s50, v43\n"
"v_mfma_f32_16x16x32_bf16 v[48:51], v[168:171], v[200:203], v[48:51]\n"
"s_add_u32 m0, s54, 1024\n"
"s_mov_b32 s52, 32768\n"
"buffer_load_dwordx4 v245, s[36:39], s52 offen lds\n"
"v_mul_f32 v60, s50, v60\n"
"v_mfma_f32_16x16x32_bf16 v[68:71], v[172:175], v[200:203], v[68:71]\n"
"v_mul_f32 v61, s50, v61\n"
"v_mul_f32 v62, s50, v62\n"
"v_mul_f32 v63, s50, v63\n"
"v_mfma_f32_16x16x32_bf16 v[88:91], v[176:179], v[200:203], v[88:91]\n"
"v_cvt_pk_bf16_f32 v212, v40, v41\n"
"v_cvt_pk_bf16_f32 v213, v42, v43\n"
"v_cvt_pk_bf16_f32 v214, v60, v61\n"
"v_mfma_f32_16x16x32_bf16 v[108:111], v[180:183], v[200:203], v[108:111]\n"
"s_add_u32 m0, s54, 8224\n"
"s_mov_b32 s52, 256\n"
"buffer_load_dwordx4 v245, s[36:39], s52 offen lds\n"
"v_cvt_pk_bf16_f32 v215, v62, v63\n"
"v_mfma_f32_16x16x32_bf16 v[128:131], v[184:187], v[200:203], v[128:131]\n"
"v_accvgpr_write_b32 a188, v212\n"
"v_accvgpr_write_b32 a189, v213\n"
"v_accvgpr_write_b32 a190, v214\n"
"v_mfma_f32_16x16x32_bf16 v[148:151], v[188:191], v[200:203], v[148:151]\n"
"v_accvgpr_write_b32 a191, v215\n"
"v_mul_f32 v80, s50, v80\n"
"v_mul_f32 v81, s50, v81\n"
"v_mfma_f32_16x16x32_bf16 v[12:15], v[160:163], v[204:207], v[12:15]\n"
"s_add_u32 m0, s54, 9248\n"
"s_mov_b32 s52, 33024\n"
"buffer_load_dwordx4 v245, s[36:39], s52 offen lds\n"
"v_mul_f32 v82, s50, v82\n"
"v_mfma_f32_16x16x32_bf16 v[32:35], v[164:167], v[204:207], v[32:35]\n"
"v_mul_f32 v83, s50, v83\n"
"v_mul_f32 v100, s50, v100\n"
"v_mul_f32 v101, s50, v101\n"
"v_mfma_f32_16x16x32_bf16 v[52:55], v[168:171], v[204:207], v[52:55]\n"
"v_mul_f32 v102, s50, v102\n"
"v_mul_f32 v103, s50, v103\n"
"v_cvt_pk_bf16_f32 v212, v80, v81\n"
"v_mfma_f32_16x16x32_bf16 v[72:75], v[172:175], v[204:207], v[72:75]\n"
"s_add_u32 m0, s54, 16448\n"
"s_mov_b32 s52, 262144\n"
"buffer_load_dwordx4 v245, s[36:39], s52 offen lds\n"
"v_cvt_pk_bf16_f32 v213, v82, v83\n"
"v_mfma_f32_16x16x32_bf16 v[92:95], v[176:179], v[204:207], v[92:95]\n"
"v_cvt_pk_bf16_f32 v214, v100, v101\n"
"v_cvt_pk_bf16_f32 v215, v102, v103\n"
"v_accvgpr_write_b32 a208, v212\n"
"v_mfma_f32_16x16x32_bf16 v[112:115], v[180:183], v[204:207], v[112:115]\n"
"v_accvgpr_write_b32 a209, v213\n"
"v_accvgpr_write_b32 a210, v214\n"
"v_accvgpr_write_b32 a211, v215\n"
"v_mfma_f32_16x16x32_bf16 v[132:135], v[184:187], v[204:207], v[132:135]\n"
"s_add_u32 m0, s54, 17472\n"
"s_mov_b32 s52, 294912\n"
"buffer_load_dwordx4 v245, s[36:39], s52 offen lds\n"
"v_mul_f32 v120, s50, v120\n"
"v_mfma_f32_16x16x32_bf16 v[152:155], v[188:191], v[204:207], v[152:155]\n"
"v_mul_f32 v121, s50, v121\n"
"v_mul_f32 v122, s50, v122\n"
"v_mul_f32 v123, s50, v123\n"
"v_mfma_f32_16x16x32_bf16 v[16:19], v[160:163], v[208:211], v[16:19]\n"
"v_mul_f32 v140, s50, v140\n"
"v_mul_f32 v141, s50, v141\n"
"v_mul_f32 v142, s50, v142\n"
"v_mfma_f32_16x16x32_bf16 v[36:39], v[164:167], v[208:211], v[36:39]\n"
"s_add_u32 m0, s54, 24672\n"
"s_mov_b32 s52, 262400\n"
"buffer_load_dwordx4 v245, s[36:39], s52 offen lds\n"
"v_mul_f32 v143, s50, v143\n"
"v_mfma_f32_16x16x32_bf16 v[56:59], v[168:171], v[208:211], v[56:59]\n"
"v_cvt_pk_bf16_f32 v212, v120, v121\n"
"v_cvt_pk_bf16_f32 v213, v122, v123\n"
"v_cvt_pk_bf16_f32 v214, v140, v141\n"
"v_mfma_f32_16x16x32_bf16 v[76:79], v[172:175], v[208:211], v[76:79]\n"
"v_cvt_pk_bf16_f32 v215, v142, v143\n"
"v_accvgpr_write_b32 a228, v212\n"
"v_accvgpr_write_b32 a229, v213\n"
"v_mfma_f32_16x16x32_bf16 v[96:99], v[176:179], v[208:211], v[96:99]\n"
"s_add_u32 m0, s54, 25696\n"
"s_mov_b32 s52, 295168\n"
"buffer_load_dwordx4 v245, s[36:39], s52 offen lds\n"
"v_accvgpr_write_b32 a230, v214\n"
"v_mfma_f32_16x16x32_bf16 v[116:119], v[180:183], v[208:211], v[116:119]\n"
"v_accvgpr_write_b32 a231, v215\n"
"v_mul_f32 v4, s50, v4\n"
"v_mul_f32 v5, s50, v5\n"
"v_mfma_f32_16x16x32_bf16 v[136:139], v[184:187], v[208:211], v[136:139]\n"
"v_mul_f32 v6, s50, v6\n"
"v_mul_f32 v7, s50, v7\n"
"v_mul_f32 v24, s50, v24\n"
"v_mfma_f32_16x16x32_bf16 v[156:159], v[188:191], v[208:211], v[156:159]\n"
"v_mul_f32 v25, s50, v25\n"
"v_mul_f32 v26, s50, v26\n"
"v_mul_f32 v27, s50, v27\n"
"v_cvt_pk_bf16_f32 v212, v4, v5\n"
"v_cvt_pk_bf16_f32 v213, v6, v7\n"
"v_cvt_pk_bf16_f32 v214, v24, v25\n"
"v_cvt_pk_bf16_f32 v215, v26, v27\n"
"v_accvgpr_write_b32 a172, v212\n"
"v_accvgpr_write_b32 a173, v213\n"
"v_accvgpr_write_b32 a174, v214\n"
"v_accvgpr_write_b32 a175, v215\n"
"v_pk_mul_f32 v[44:45], v[44:45], s[50:51] op_sel_hi:[1,0]\n"
"v_pk_mul_f32 v[46:47], v[46:47], s[50:51] op_sel_hi:[1,0]\n"
"v_pk_mul_f32 v[64:65], v[64:65], s[50:51] op_sel_hi:[1,0]\n"
"v_pk_mul_f32 v[66:67], v[66:67], s[50:51] op_sel_hi:[1,0]\n"
"v_cvt_pk_bf16_f32 v212, v44, v45\n"
"v_cvt_pk_bf16_f32 v213, v46, v47\n"
"v_cvt_pk_bf16_f32 v214, v64, v65\n"
"v_cvt_pk_bf16_f32 v215, v66, v67\n"
"v_accvgpr_write_b32 a192, v212\n"
"v_accvgpr_write_b32 a193, v213\n"
"v_accvgpr_write_b32 a194, v214\n"
"v_accvgpr_write_b32 a195, v215\n"
"v_pk_mul_f32 v[84:85], v[84:85], s[50:51] op_sel_hi:[1,0]\n"
"v_pk_mul_f32 v[86:87], v[86:87], s[50:51] op_sel_hi:[1,0]\n"
"s_waitcnt vmcnt(0) lgkmcnt(0)\n"
"s_barrier\n"
"ds_read_b128 a[32:35], v230 offset:0\n"
"ds_read_b128 a[36:39], v230 offset:2048\n"
"ds_read_b128 a[40:43], v230 offset:4096\n"
"ds_read_b128 a[44:47], v230 offset:6144\n"
"ds_read_b128 a[48:51], v230 offset:8192\n"
"ds_read_b64_tr_b16 a[0:1], v224 offset:0\n"
"ds_read_b64_tr_b16 a[2:3], v224 offset:1024\n"
"ds_read_b64_tr_b16 a[4:5], v224 offset:8\n"
"ds_read_b64_tr_b16 a[6:7], v224 offset:1032\n"
"ds_read_b64_tr_b16 a[8:9], v224 offset:256\n"
"ds_read_b64_tr_b16 a[10:11], v224 offset:1280\n"
"ds_read_b64_tr_b16 a[12:13], v224 offset:264\n"
"ds_read_b64_tr_b16 a[14:15], v224 offset:1288\n"
"ds_read_b64_tr_b16 a[16:17], v224 offset:512\n"
"ds_read_b64_tr_b16 a[18:19], v224 offset:1536\n"
"ds_read_b64_tr_b16 a[20:21], v224 offset:520\n"
"ds_read_b64_tr_b16 a[22:23], v224 offset:1544\n"
"ds_read_b64_tr_b16 a[24:25], v224 offset:768\n"
"ds_read_b64_tr_b16 a[26:27], v224 offset:1792\n"
"ds_read_b64_tr_b16 a[28:29], v224 offset:776\n"
"ds_read_b64_tr_b16 a[30:31], v224 offset:1800\n"
"v_pk_mul_f32 v[104:105], v[104:105], s[50:51] op_sel_hi:[1,0]\n"
"v_pk_mul_f32 v[106:107], v[106:107], s[50:51] op_sel_hi:[1,0]\n"
"v_cvt_pk_bf16_f32 v212, v84, v85\n"
"v_cvt_pk_bf16_f32 v213, v86, v87\n"
"v_cvt_pk_bf16_f32 v214, v104, v105\n"
"v_cvt_pk_bf16_f32 v215, v106, v107\n"
"v_accvgpr_write_b32 a212, v212\n"
"v_accvgpr_write_b32 a213, v213\n"
"v_accvgpr_write_b32 a214, v214\n"
"v_accvgpr_write_b32 a215, v215\n"
"v_pk_mul_f32 v[124:125], v[124:125], s[50:51] op_sel_hi:[1,0]\n"
"v_pk_mul_f32 v[126:127], v[126:127], s[50:51] op_sel_hi:[1,0]\n"
"v_pk_mul_f32 v[144:145], v[144:145], s[50:51] op_sel_hi:[1,0]\n"
"v_pk_mul_f32 v[146:147], v[146:147], s[50:51] op_sel_hi:[1,0]\n"
"v_cvt_pk_bf16_f32 v212, v124, v125\n"
"v_cvt_pk_bf16_f32 v213, v126, v127\n"
"v_cvt_pk_bf16_f32 v214, v144, v145\n"
"v_cvt_pk_bf16_f32 v215, v146, v147\n"
"v_accvgpr_write_b32 a232, v212\n"
"v_accvgpr_write_b32 a233, v213\n"
"v_accvgpr_write_b32 a234, v214\n"
"v_accvgpr_write_b32 a235, v215\n"
"s_waitcnt lgkmcnt(0)\n"
"v_mfma_f32_16x16x32_bf16 v[0:3], a[0:3], a[32:35], 0\n"
"s_add_u32 m0, s49, 139648\n"
"s_mov_b32 s52, 256\n"
"v_mul_f32 v8, s50, v8\n"
"v_mul_f32 v9, s50, v9\n"
"v_mul_f32 v10, s50, v10\n"
"v_mfma_f32_16x16x32_bf16 v[20:23], a[4:7], a[32:35], 0\n"
"buffer_load_dwordx4 v246, s[84:87], s52 offen lds\n"
"v_mul_f32 v11, s50, v11\n"
"v_mul_f32 v28, s50, v28\n"
"v_mul_f32 v29, s50, v29\n"
"v_mfma_f32_16x16x32_bf16 v[40:43], a[8:11], a[32:35], 0\n"
"ds_read_b128 v[192:195], v230 offset:512\n"
"v_mul_f32 v30, s50, v30\n"
"v_mul_f32 v31, s50, v31\n"
"v_cvt_pk_bf16_f32 v212, v8, v9\n"
"v_mfma_f32_16x16x32_bf16 v[60:63], a[12:15], a[32:35], 0\n"
"ds_read_b128 v[196:199], v230 offset:2560\n"
"v_cvt_pk_bf16_f32 v213, v10, v11\n"
"v_cvt_pk_bf16_f32 v214, v28, v29\n"
"v_cvt_pk_bf16_f32 v215, v30, v31\n"
"v_mfma_f32_16x16x32_bf16 v[80:83], a[16:19], a[32:35], 0\n"
"ds_read_b128 v[200:203], v230 offset:4608\n"
"v_accvgpr_write_b32 a176, v212\n"
"v_accvgpr_write_b32 a177, v213\n"
"v_accvgpr_write_b32 a178, v214\n"
"v_mfma_f32_16x16x32_bf16 v[100:103], a[20:23], a[32:35], 0\n"
"s_add_u32 m0, s49, 143744\n"
"s_mov_b32 s52, 41216\n"
"v_accvgpr_write_b32 a179, v215\n"
"v_mul_f32 v48, s50, v48\n"
"v_mul_f32 v49, s50, v49\n"
"v_mfma_f32_16x16x32_bf16 v[120:123], a[24:27], a[32:35], 0\n"
"ds_read_b128 v[204:207], v230 offset:6656\n"
"v_mul_f32 v50, s50, v50\n"
"v_mul_f32 v51, s50, v51\n"
"v_mul_f32 v68, s50, v68\n"
"v_mfma_f32_16x16x32_bf16 v[140:143], a[28:31], a[32:35], 0\n"
"ds_read_b128 v[208:211], v230 offset:8704\n"
"v_mul_f32 v69, s50, v69\n"
"v_mul_f32 v70, s50, v70\n"
"v_mul_f32 v71, s50, v71\n"
"v_mfma_f32_16x16x32_bf16 v[4:7], a[0:3], a[36:39], 0\n"
"buffer_load_dwordx4 v246, s[84:87], s52 offen lds\n"
"v_cvt_pk_bf16_f32 v212, v48, v49\n"
"v_cvt_pk_bf16_f32 v213, v50, v51\n"
"v_cvt_pk_bf16_f32 v214, v68, v69\n"
"v_mfma_f32_16x16x32_bf16 v[24:27], a[4:7], a[36:39], 0\n"
"ds_read_b64_tr_b16 v[160:161], v224 offset:16448\n"
"v_cvt_pk_bf16_f32 v215, v70, v71\n"
"v_accvgpr_write_b32 a196, v212\n"
"v_accvgpr_write_b32 a197, v213\n"
"v_mfma_f32_16x16x32_bf16 v[44:47], a[8:11], a[36:39], 0\n"
"ds_read_b64_tr_b16 v[162:163], v224 offset:17472\n"
"v_accvgpr_write_b32 a198, v214\n"
"v_accvgpr_write_b32 a199, v215\n"
"v_mul_f32 v88, s50, v88\n"
"v_mfma_f32_16x16x32_bf16 v[64:67], a[12:15], a[36:39], 0\n"
"s_add_u32 m0, s49, 147840\n"
"s_mov_b32 s52, 82176\n"
"v_mul_f32 v89, s50, v89\n"
"v_mul_f32 v90, s50, v90\n"
"v_mul_f32 v91, s50, v91\n"
"v_mfma_f32_16x16x32_bf16 v[84:87], a[16:19], a[36:39], 0\n"
"ds_read_b64_tr_b16 v[164:165], v224 offset:16456\n"
"v_mul_f32 v108, s50, v108\n"
"v_mul_f32 v109, s50, v109\n"
"v_mul_f32 v110, s50, v110\n"
"v_mfma_f32_16x16x32_bf16 v[104:107], a[20:23], a[36:39], 0\n"
"ds_read_b64_tr_b16 v[166:167], v224 offset:17480\n"
"v_mul_f32 v111, s50, v111\n"
"v_cvt_pk_bf16_f32 v212, v88, v89\n"
"v_cvt_pk_bf16_f32 v213, v90, v91\n"
"v_mfma_f32_16x16x32_bf16 v[124:127], a[24:27], a[36:39], 0\n"
"v_cvt_pk_bf16_f32 v214, v108, v109\n"
"v_cvt_pk_bf16_f32 v215, v110, v111\n"
"v_accvgpr_write_b32 a216, v212\n"
"v_accvgpr_write_b32 a217, v213\n"
"v_accvgpr_write_b32 a218, v214\n"
"v_accvgpr_write_b32 a219, v215\n"
"v_pk_mul_f32 v[128:129], v[128:129], s[50:51] op_sel_hi:[1,0]\n"
"v_pk_mul_f32 v[130:131], v[130:131], s[50:51] op_sel_hi:[1,0]\n"
"v_pk_mul_f32 v[148:149], v[148:149], s[50:51] op_sel_hi:[1,0]\n"
"v_pk_mul_f32 v[150:151], v[150:151], s[50:51] op_sel_hi:[1,0]\n"
"v_cvt_pk_bf16_f32 v212, v128, v129\n"
"v_cvt_pk_bf16_f32 v213, v130, v131\n"
"v_cvt_pk_bf16_f32 v214, v148, v149\n"
"v_cvt_pk_bf16_f32 v215, v150, v151\n"
"v_accvgpr_write_b32 a236, v212\n"
"v_accvgpr_write_b32 a237, v213\n"
"v_accvgpr_write_b32 a238, v214\n"
"v_accvgpr_write_b32 a239, v215\n"
"v_mfma_f32_16x16x32_bf16 v[144:147], a[28:31], a[36:39], 0\n"
"ds_read_b64_tr_b16 v[168:169], v224 offset:16704\n"
"v_mul_f32 v12, s50, v12\n"
"v_mul_f32 v13, s50, v13\n"
"v_mul_f32 v14, s50, v14\n"
"v_mfma_f32_16x16x32_bf16 v[8:11], a[0:3], a[40:43], 0\n"
"ds_read_b64_tr_b16 v[170:171], v224 offset:17728\n"
"v_mul_f32 v15, s50, v15\n"
"v_mul_f32 v32, s50, v32\n"
"v_mul_f32 v33, s50, v33\n"
"v_mfma_f32_16x16x32_bf16 v[28:31], a[4:7], a[40:43], 0\n"
"buffer_load_dwordx4 v246, s[84:87], s52 offen lds\n"
"v_mul_f32 v34, s50, v34\n"
"v_mul_f32 v35, s50, v35\n"
"v_cvt_pk_bf16_f32 v212, v12, v13\n"
"v_mfma_f32_16x16x32_bf16 v[48:51], a[8:11], a[40:43], 0\n"
"ds_read_b64_tr_b16 v[172:173], v224 offset:16712\n"
"v_cvt_pk_bf16_f32 v213, v14, v15\n"
"v_cvt_pk_bf16_f32 v214, v32, v33\n"
"v_cvt_pk_bf16_f32 v215, v34, v35\n"
"v_mfma_f32_16x16x32_bf16 v[68:71], a[12:15], a[40:43], 0\n"
"ds_read_b64_tr_b16 v[174:175], v224 offset:17736\n"
"v_accvgpr_write_b32 a180, v212\n"
"v_accvgpr_write_b32 a181, v213\n"
"v_accvgpr_write_b32 a182, v214\n"
"v_mfma_f32_16x16x32_bf16 v[88:91], a[16:19], a[40:43], 0\n"
"s_add_u32 m0, s49, 151936\n"
"s_mov_b32 s52, 123136\n"
"v_accvgpr_write_b32 a183, v215\n"
"v_mul_f32 v52, s50, v52\n"
"v_mul_f32 v53, s50, v53\n"
"v_mfma_f32_16x16x32_bf16 v[108:111], a[20:23], a[40:43], 0\n"
"ds_read_b64_tr_b16 v[176:177], v224 offset:16960\n"
"v_mul_f32 v54, s50, v54\n"
"v_mul_f32 v55, s50, v55\n"
"v_mul_f32 v72, s50, v72\n"
"v_mfma_f32_16x16x32_bf16 v[128:131], a[24:27], a[40:43], 0\n"
"v_mul_f32 v73, s50, v73\n"
"v_mul_f32 v74, s50, v74\n"
"v_mul_f32 v75, s50, v75\n"
"v_cvt_pk_bf16_f32 v212, v52, v53\n"
"v_cvt_pk_bf16_f32 v213, v54, v55\n"
"v_cvt_pk_bf16_f32 v214, v72, v73\n"
"v_cvt_pk_bf16_f32 v215, v74, v75\n"
"v_accvgpr_write_b32 a200, v212\n"
"v_accvgpr_write_b32 a201, v213\n"
"v_accvgpr_write_b32 a202, v214\n"
"v_accvgpr_write_b32 a203, v215\n"
"v_pk_mul_f32 v[92:93], v[92:93], s[50:51] op_sel_hi:[1,0]\n"
"v_pk_mul_f32 v[94:95], v[94:95], s[50:51] op_sel_hi:[1,0]\n"
"v_pk_mul_f32 v[112:113], v[112:113], s[50:51] op_sel_hi:[1,0]\n"
"v_pk_mul_f32 v[114:115], v[114:115], s[50:51] op_sel_hi:[1,0]\n"
"v_cvt_pk_bf16_f32 v212, v92, v93\n"
"v_cvt_pk_bf16_f32 v213, v94, v95\n"
"v_cvt_pk_bf16_f32 v214, v112, v113\n"
"v_cvt_pk_bf16_f32 v215, v114, v115\n"
"v_accvgpr_write_b32 a220, v212\n"
"v_accvgpr_write_b32 a221, v213\n"
"v_accvgpr_write_b32 a222, v214\n"
"v_accvgpr_write_b32 a223, v215\n"
"v_pk_mul_f32 v[132:133], v[132:133], s[50:51] op_sel_hi:[1,0]\n"
"v_pk_mul_f32 v[134:135], v[134:135], s[50:51] op_sel_hi:[1,0]\n"
"v_pk_mul_f32 v[152:153], v[152:153], s[50:51] op_sel_hi:[1,0]\n"
"v_pk_mul_f32 v[154:155], v[154:155], s[50:51] op_sel_hi:[1,0]\n"
"v_cvt_pk_bf16_f32 v212, v132, v133\n"
"v_cvt_pk_bf16_f32 v213, v134, v135\n"
"v_cvt_pk_bf16_f32 v214, v152, v153\n"
"v_cvt_pk_bf16_f32 v215, v154, v155\n"
"v_accvgpr_write_b32 a240, v212\n"
"v_accvgpr_write_b32 a241, v213\n"
"v_accvgpr_write_b32 a242, v214\n"
"v_accvgpr_write_b32 a243, v215\n"
"v_mfma_f32_16x16x32_bf16 v[148:151], a[28:31], a[40:43], 0\n"
"ds_read_b64_tr_b16 v[178:179], v224 offset:17984\n"
"v_mul_f32 v16, s50, v16\n"
"v_mul_f32 v17, s50, v17\n"
"v_mul_f32 v18, s50, v18\n"
"v_mfma_f32_16x16x32_bf16 v[12:15], a[0:3], a[44:47], 0\n"
"ds_read_b64_tr_b16 v[180:181], v224 offset:16968\n"
"v_mul_f32 v19, s50, v19\n"
"v_mul_f32 v36, s50, v36\n"
"v_mul_f32 v37, s50, v37\n"
"v_mfma_f32_16x16x32_bf16 v[32:35], a[4:7], a[44:47], 0\n"
"ds_read_b64_tr_b16 v[182:183], v224 offset:17992\n"
"v_mul_f32 v38, s50, v38\n"
"v_mul_f32 v39, s50, v39\n"
"v_cvt_pk_bf16_f32 v212, v16, v17\n"
"v_mfma_f32_16x16x32_bf16 v[52:55], a[8:11], a[44:47], 0\n"
"buffer_load_dwordx4 v246, s[84:87], s52 offen lds\n"
"v_cvt_pk_bf16_f32 v213, v18, v19\n"
"v_cvt_pk_bf16_f32 v214, v36, v37\n"
"v_cvt_pk_bf16_f32 v215, v38, v39\n"
"v_mfma_f32_16x16x32_bf16 v[72:75], a[12:15], a[44:47], 0\n"
"ds_read_b64_tr_b16 v[184:185], v224 offset:17216\n"
"v_accvgpr_write_b32 a184, v212\n"
"v_accvgpr_write_b32 a185, v213\n"
"v_accvgpr_write_b32 a186, v214\n"
"v_mfma_f32_16x16x32_bf16 v[92:95], a[16:19], a[44:47], 0\n"
"ds_read_b64_tr_b16 v[186:187], v224 offset:18240\n"
"v_accvgpr_write_b32 a187, v215\n"
"v_mul_f32 v56, s50, v56\n"
"v_mul_f32 v57, s50, v57\n"
"v_mfma_f32_16x16x32_bf16 v[112:115], a[20:23], a[44:47], 0\n"
"s_add_u32 m0, s49, 156032\n"
"s_mov_b32 s52, 164096\n"
"v_mul_f32 v58, s50, v58\n"
"v_mul_f32 v59, s50, v59\n"
"v_mul_f32 v76, s50, v76\n"
"v_mfma_f32_16x16x32_bf16 v[132:135], a[24:27], a[44:47], 0\n"
"v_mul_f32 v77, s50, v77\n"
"v_mul_f32 v78, s50, v78\n"
"v_mul_f32 v79, s50, v79\n"
"v_cvt_pk_bf16_f32 v212, v56, v57\n"
"v_cvt_pk_bf16_f32 v213, v58, v59\n"
"v_cvt_pk_bf16_f32 v214, v76, v77\n"
"v_cvt_pk_bf16_f32 v215, v78, v79\n"
"v_accvgpr_write_b32 a204, v212\n"
"v_accvgpr_write_b32 a205, v213\n"
"v_accvgpr_write_b32 a206, v214\n"
"v_accvgpr_write_b32 a207, v215\n"
"v_pk_mul_f32 v[96:97], v[96:97], s[50:51] op_sel_hi:[1,0]\n"
"v_pk_mul_f32 v[98:99], v[98:99], s[50:51] op_sel_hi:[1,0]\n"
"v_pk_mul_f32 v[116:117], v[116:117], s[50:51] op_sel_hi:[1,0]\n"
"v_pk_mul_f32 v[118:119], v[118:119], s[50:51] op_sel_hi:[1,0]\n"
"v_cvt_pk_bf16_f32 v212, v96, v97\n"
"v_cvt_pk_bf16_f32 v213, v98, v99\n"
"v_cvt_pk_bf16_f32 v214, v116, v117\n"
"v_cvt_pk_bf16_f32 v215, v118, v119\n"
"v_accvgpr_write_b32 a224, v212\n"
"v_accvgpr_write_b32 a225, v213\n"
"v_accvgpr_write_b32 a226, v214\n"
"v_accvgpr_write_b32 a227, v215\n"
"v_pk_mul_f32 v[136:137], v[136:137], s[50:51] op_sel_hi:[1,0]\n"
"v_pk_mul_f32 v[138:139], v[138:139], s[50:51] op_sel_hi:[1,0]\n"
"v_pk_mul_f32 v[156:157], v[156:157], s[50:51] op_sel_hi:[1,0]\n"
"v_pk_mul_f32 v[158:159], v[158:159], s[50:51] op_sel_hi:[1,0]\n"
"v_cvt_pk_bf16_f32 v212, v136, v137\n"
"v_cvt_pk_bf16_f32 v213, v138, v139\n"
"v_cvt_pk_bf16_f32 v214, v156, v157\n"
"v_cvt_pk_bf16_f32 v215, v158, v159\n"
"v_accvgpr_write_b32 a244, v212\n"
"v_accvgpr_write_b32 a245, v213\n"
"v_accvgpr_write_b32 a246, v214\n"
"v_accvgpr_write_b32 a247, v215\n"
"v_mfma_f32_16x16x32_bf16 v[152:155], a[28:31], a[44:47], 0\n"
"ds_read_b64_tr_b16 v[188:189], v224 offset:17224\n"
"ds_read_b64_tr_b16 v[190:191], v224 offset:18248\n"
"v_mfma_f32_16x16x32_bf16 v[16:19], a[0:3], a[48:51], 0\n"
"buffer_load_dwordx4 v246, s[84:87], s52 offen lds\n"
"s_add_u32 m0, s54, 106752\n"
"s_mov_b32 s52, 1048576\n"
"v_mfma_f32_16x16x32_bf16 v[36:39], a[4:7], a[48:51], 0\n"
"buffer_load_dwordx4 v245, s[36:39], s52 offen lds\n"
"s_add_u32 m0, s54, 107776\n"
"s_mov_b32 s52, 1081344\n"
"v_mfma_f32_16x16x32_bf16 v[56:59], a[8:11], a[48:51], 0\n"
"buffer_load_dwordx4 v245, s[36:39], s52 offen lds\n"
"s_add_u32 m0, s54, 114976\n"
"s_mov_b32 s52, 1048832\n"
"v_mfma_f32_16x16x32_bf16 v[76:79], a[12:15], a[48:51], 0\n"
"buffer_load_dwordx4 v245, s[36:39], s52 offen lds\n"
"s_add_u32 s52, s90, 640\n"
"v_mfma_f32_16x16x32_bf16 v[96:99], a[16:19], a[48:51], 0\n"
"s_mov_b32 exec_lo, 1023\n"
"s_mov_b32 exec_hi, 0\n"
"buffer_load_dword v254, v251, s[84:87], s52 offen\n"
"s_mov_b64 exec, -1\n"
"v_mfma_f32_16x16x32_bf16 v[116:119], a[20:23], a[48:51], 0\n"
"s_add_u32 s52, s91, 2621440\n"
"v_mfma_f32_16x16x32_bf16 v[136:139], a[24:27], a[48:51], 0\n"
"s_mov_b32 exec_lo, 255\n"
"s_mov_b32 exec_hi, 0\n"
"buffer_load_dword v254, v252, s[36:39], s52 offen\n"
"s_mov_b64 exec, -1\n"
"v_mfma_f32_16x16x32_bf16 v[156:159], a[28:31], a[48:51], 0\n"
"s_branch L_qpa_%=\n"
"L_t3_%=:\n"
"s_waitcnt vmcnt(12) lgkmcnt(0)\n"
"s_barrier\n"
"v_add_u32 v241, 49152, v241\n"
"v_add_u32 v242, 49152, v242\n"
"v_mfma_f32_16x16x32_bf16 v[184:187], a[80:83], a[12:15], v[232:235]\n"
"s_add_u32 s40, s40, 262144\n"
"s_addc_u32 s41, s41, 0\n"
"s_sub_u32 s42, s42, 262144\n"
"v_mfma_f32_16x16x32_bf16 v[16:19], a[112:115], v[216:219], v[16:19]\n"
"ds_read_b64_tr_b16 a[112:113], v241 offset:20608\n"
"v_mfma_f32_16x16x32_bf16 v[188:191], a[96:99], a[12:15], v[232:235]\n"
"ds_read_b64_tr_b16 a[114:115], v241 offset:21632\n"
"v_mfma_f32_16x16x32_bf16 v[36:39], a[116:119], v[216:219], v[36:39]\n"
"ds_read_b64_tr_b16 a[116:117], v242 offset:20608\n"
"v_mfma_f32_16x16x32_bf16 v[184:187], a[84:87], a[32:35], v[184:187]\n"
"ds_read_b64_tr_b16 a[118:119], v242 offset:21632\n"
"v_mfma_f32_16x16x32_bf16 v[56:59], a[120:123], v[216:219], v[56:59]\n"
"ds_read_b64_tr_b16 a[120:121], v241 offset:20864\n"
"v_mfma_f32_16x16x32_bf16 v[188:191], a[100:103], a[32:35], v[188:191]\n"
"ds_read_b64_tr_b16 a[122:123], v241 offset:21888\n"
"v_mfma_f32_16x16x32_bf16 v[76:79], a[124:127], v[216:219], v[76:79]\n"
"ds_read_b64_tr_b16 a[124:125], v242 offset:20864\n"
"v_mfma_f32_16x16x32_bf16 v[184:187], a[88:91], a[52:55], v[184:187]\n"
"ds_read_b64_tr_b16 a[126:127], v242 offset:21888\n"
"v_mfma_f32_16x16x32_bf16 v[96:99], a[128:131], v[216:219], v[96:99]\n"
"ds_read_b64_tr_b16 a[128:129], v241 offset:21120\n"
"v_mfma_f32_16x16x32_bf16 v[188:191], a[104:107], a[52:55], v[188:191]\n"
"ds_read_b64_tr_b16 a[130:131], v241 offset:22144\n"
"v_mfma_f32_16x16x32_bf16 v[116:119], a[132:135], v[216:219], v[116:119]\n"
"s_add_u32 s60, s60, 262144\n"
"s_addc_u32 s61, s61, 0\n"
"s_sub_u32 s62, s62, 262144\n"
"v_mfma_f32_16x16x32_bf16 v[184:187], a[92:95], a[72:75], v[184:187]\n"
"s_mov_b32 s78, 0\n"
"s_mov_b32 s51, 1\n"
"v_mfma_f32_16x16x32_bf16 v[136:139], a[136:139], v[216:219], v[136:139]\n"
"ds_read_b64_tr_b16 a[132:133], v242 offset:21120\n"
"ds_read_b64_tr_b16 a[134:135], v242 offset:22144\n"
"v_mfma_f32_16x16x32_bf16 v[188:191], a[108:111], a[72:75], v[188:191]\n"
"ds_read_b64_tr_b16 a[136:137], v241 offset:21376\n"
"s_add_u32 m0, s48, 16384\n"
"v_mfma_f32_16x16x32_bf16 v[156:159], a[140:143], v[216:219], v[156:159]\n"
"ds_read_b64_tr_b16 a[138:139], v241 offset:22400\n"
"s_add_u32 s52, s81, 131072\n"
"v_mfma_f32_16x16x32_bf16 a[160:163], a[164:167], v[216:219], a[160:163]\n"
"ds_read_b64_tr_b16 a[140:141], v242 offset:21376\n"
"v_mfma_f32_16x16x32_bf16 v[192:195], a[80:83], a[16:19], v[236:239]\n"
"s_waitcnt lgkmcnt(11)\n"
"ds_read_b64_tr_b16 a[142:143], v242 offset:22400\n"
"v_add_u32 v240, 49152, v240\n"
"v_mfma_f32_16x16x32_bf16 v[0:3], a[112:115], v[200:203], v[0:3]\n"
"ds_read_b128 a[80:83], v240 offset:4224\n"
"v_mfma_f32_16x16x32_bf16 v[196:199], a[96:99], a[16:19], v[236:239]\n"
"ds_read_b128 a[96:99], v240 offset:5248\n"
"v_mfma_f32_16x16x32_bf16 v[20:23], a[116:119], v[200:203], v[20:23]\n"
"v_exp_f32 v168, v168\n"
"v_mfma_f32_16x16x32_bf16 v[192:195], a[84:87], a[36:39], v[192:195]\n"
"s_waitcnt lgkmcnt(10)\n"
"ds_read_b128 a[84:87], v240 offset:4480\n"
"v_mfma_f32_16x16x32_bf16 v[40:43], a[120:123], v[200:203], v[40:43]\n"
"v_exp_f32 v169, v169\n"
"v_mfma_f32_16x16x32_bf16 v[196:199], a[100:103], a[36:39], v[196:199]\n"
"ds_read_b128 a[100:103], v240 offset:5504\n"
"v_mfma_f32_16x16x32_bf16 v[60:63], a[124:127], v[200:203], v[60:63]\n"
"v_exp_f32 v170, v170\n"
"v_mfma_f32_16x16x32_bf16 v[192:195], a[88:91], a[56:59], v[192:195]\n"
"s_waitcnt lgkmcnt(8)\n"
"ds_read_b128 a[88:91], v240 offset:4736\n"
"v_mfma_f32_16x16x32_bf16 v[80:83], a[128:131], v[200:203], v[80:83]\n"
"v_exp_f32 v171, v171\n"
"v_mfma_f32_16x16x32_bf16 v[196:199], a[104:107], a[56:59], v[196:199]\n"
"ds_read_b128 a[104:107], v240 offset:5760\n"
"v_cvt_pk_bf16_f32 v204, v168, v169\n"
"v_mfma_f32_16x16x32_bf16 v[100:103], a[132:135], v[200:203], v[100:103]\n"
"v_exp_f32 v172, v172\n"
"v_mfma_f32_16x16x32_bf16 v[192:195], a[92:95], a[76:79], v[192:195]\n"
"s_waitcnt lgkmcnt(6)\n"
"v_exp_f32 v173, v173\n"
"v_mfma_f32_16x16x32_bf16 v[120:123], a[136:139], v[200:203], v[120:123]\n"
"ds_read_b128 a[92:95], v240 offset:4992\n"
"v_cvt_pk_bf16_f32 v205, v170, v171\n"
"v_mfma_f32_16x16x32_bf16 v[196:199], a[108:111], a[76:79], v[196:199]\n"
"v_exp_f32 v174, v174\n"
"v_exp_f32 v175, v175\n"
"v_cvt_pk_bf16_f32 v206, v172, v173\n"
"v_cvt_pk_bf16_f32 v207, v174, v175\n"
"v_mfma_f32_16x16x32_bf16 v[140:143], a[140:143], v[200:203], v[140:143]\n"
"ds_read_b128 a[108:111], v240 offset:6016\n"
"v_mfma_f32_16x16x32_bf16 a[144:147], a[164:167], v[200:203], a[144:147]\n"
"s_waitcnt lgkmcnt(4)\n"
"v_exp_f32 v176, v176\n"
"v_mfma_f32_16x16x32_bf16 v[160:163], a[80:83], a[0:3], v[220:223]\n"
"v_exp_f32 v177, v177\n"
"v_mfma_f32_16x16x32_bf16 v[4:7], a[112:115], v[204:207], v[4:7]\n"
"v_exp_f32 v178, v178\n"
"v_mfma_f32_16x16x32_bf16 v[164:167], a[96:99], a[0:3], v[220:223]\n"
"v_exp_f32 v179, v179\n"
"v_mfma_f32_16x16x32_bf16 v[24:27], a[116:119], v[204:207], v[24:27]\n"
"buffer_load_dwordx4 v243, s[40:43], s81 offen nt lds\n"
"v_cvt_pk_bf16_f32 v208, v176, v177\n"
"v_mfma_f32_16x16x32_bf16 v[160:163], a[84:87], a[20:23], v[160:163]\n"
"v_exp_f32 v180, v180\n"
"v_mfma_f32_16x16x32_bf16 v[44:47], a[120:123], v[204:207], v[44:47]\n"
"v_exp_f32 v181, v181\n"
"v_mfma_f32_16x16x32_bf16 v[164:167], a[100:103], a[20:23], v[164:167]\n"
"buffer_load_dwordx4 v243, s[40:43], s81 offen offset:1024 nt lds\n"
"v_cvt_pk_bf16_f32 v209, v178, v179\n"
"v_mfma_f32_16x16x32_bf16 v[64:67], a[124:127], v[204:207], v[64:67]\n"
"s_waitcnt lgkmcnt(0)\n"
"v_exp_f32 v182, v182\n"
"v_mfma_f32_16x16x32_bf16 v[160:163], a[88:91], a[40:43], v[160:163]\n"
"s_add_u32 m0, s48, 24576\n"
"v_exp_f32 v183, v183\n"
"v_mfma_f32_16x16x32_bf16 v[84:87], a[128:131], v[204:207], v[84:87]\n"
"s_add_u32 s81, s81, 8192\n"
"v_cvt_pk_bf16_f32 v210, v180, v181\n"
"v_cvt_pk_bf16_f32 v211, v182, v183\n"
"v_mfma_f32_16x16x32_bf16 v[164:167], a[104:107], a[40:43], v[164:167]\n"
"v_exp_f32 v184, v184\n"
"v_mfma_f32_16x16x32_bf16 v[104:107], a[132:135], v[204:207], v[104:107]\n"
"v_exp_f32 v185, v185\n"
"v_mfma_f32_16x16x32_bf16 v[160:163], a[92:95], a[60:63], v[160:163]\n"
"v_exp_f32 v186, v186\n"
"v_mfma_f32_16x16x32_bf16 v[124:127], a[136:139], v[204:207], v[124:127]\n"
"v_exp_f32 v187, v187\n"
"v_mfma_f32_16x16x32_bf16 v[164:167], a[108:111], a[60:63], v[164:167]\n"
"buffer_load_dwordx4 v243, s[40:43], s52 offen nt lds\n"
"v_cvt_pk_bf16_f32 v212, v184, v185\n"
"v_mfma_f32_16x16x32_bf16 v[144:147], a[140:143], v[204:207], v[144:147]\n"
"v_exp_f32 v188, v188\n"
"v_mfma_f32_16x16x32_bf16 a[148:151], a[164:167], v[204:207], a[148:151]\n"
"v_exp_f32 v189, v189\n"
"v_mfma_f32_16x16x32_bf16 v[168:171], a[80:83], a[4:7], v[224:227]\n"
"buffer_load_dwordx4 v243, s[40:43], s52 offen offset:1024 nt lds\n"
"v_cvt_pk_bf16_f32 v213, v186, v187\n"
"v_mfma_f32_16x16x32_bf16 v[8:11], a[112:115], v[208:211], v[8:11]\n"
"s_and_b32 s81, s81, 131071\n"
"v_exp_f32 v190, v190\n"
"v_mfma_f32_16x16x32_bf16 v[172:175], a[96:99], a[4:7], v[224:227]\n"
"s_add_u32 m0, s48, 32768\n"
"v_exp_f32 v191, v191\n"
"v_mfma_f32_16x16x32_bf16 v[28:31], a[116:119], v[208:211], v[28:31]\n"
"s_add_u32 s52, s95, 131072\n"
"v_cvt_pk_bf16_f32 v214, v188, v189\n"
"v_cvt_pk_bf16_f32 v215, v190, v191\n"
"v_mfma_f32_16x16x32_bf16 v[168:171], a[84:87], a[24:27], v[168:171]\n"
"v_exp_f32 v192, v192\n"
"v_mfma_f32_16x16x32_bf16 v[48:51], a[120:123], v[208:211], v[48:51]\n"
"v_exp_f32 v193, v193\n"
"v_mfma_f32_16x16x32_bf16 v[172:175], a[100:103], a[24:27], v[172:175]\n"
"v_exp_f32 v194, v194\n"
"v_mfma_f32_16x16x32_bf16 v[68:71], a[124:127], v[208:211], v[68:71]\n"
"v_exp_f32 v195, v195\n"
"v_mfma_f32_16x16x32_bf16 v[168:171], a[88:91], a[44:47], v[168:171]\n"
"buffer_load_dwordx4 v244, s[60:63], s95 offen lds\n"
"v_cvt_pk_bf16_f32 v216, v192, v193\n"
"v_mfma_f32_16x16x32_bf16 v[88:91], a[128:131], v[208:211], v[88:91]\n"
"v_exp_f32 v196, v196\n"
"v_mfma_f32_16x16x32_bf16 v[172:175], a[104:107], a[44:47], v[172:175]\n"
"v_exp_f32 v197, v197\n"
"v_mfma_f32_16x16x32_bf16 v[108:111], a[132:135], v[208:211], v[108:111]\n"
"buffer_load_dwordx4 v244, s[60:63], s95 offen offset:1024 lds\n"
"v_cvt_pk_bf16_f32 v217, v194, v195\n"
"v_mfma_f32_16x16x32_bf16 v[168:171], a[92:95], a[64:67], v[168:171]\n"
"v_exp_f32 v198, v198\n"
"v_mfma_f32_16x16x32_bf16 v[128:131], a[136:139], v[208:211], v[128:131]\n"
"v_exp_f32 v199, v199\n"
"v_mfma_f32_16x16x32_bf16 v[172:175], a[108:111], a[64:67], v[172:175]\n"
"v_cvt_pk_bf16_f32 v218, v196, v197\n"
"v_cvt_pk_bf16_f32 v219, v198, v199\n"
"v_mfma_f32_16x16x32_bf16 v[148:151], a[140:143], v[208:211], v[148:151]\n"
"v_exp_f32 v160, v160\n"
"v_mfma_f32_16x16x32_bf16 a[152:155], a[164:167], v[208:211], a[152:155]\n"
"v_exp_f32 v161, v161\n"
"v_mfma_f32_16x16x32_bf16 v[176:179], a[80:83], a[8:11], v[228:231]\n"
"v_exp_f32 v162, v162\n"
"v_mfma_f32_16x16x32_bf16 v[12:15], a[112:115], v[212:215], v[12:15]\n"
"v_exp_f32 v163, v163\n"
"v_mfma_f32_16x16x32_bf16 v[180:183], a[96:99], a[8:11], v[228:231]\n"
"v_cvt_pk_bf16_f32 v200, v160, v161\n"
"v_mfma_f32_16x16x32_bf16 v[32:35], a[116:119], v[212:215], v[32:35]\n"
"v_exp_f32 v164, v164\n"
"v_mfma_f32_16x16x32_bf16 v[176:179], a[84:87], a[28:31], v[176:179]\n"
"v_exp_f32 v165, v165\n"
"v_mfma_f32_16x16x32_bf16 v[52:55], a[120:123], v[212:215], v[52:55]\n"
"s_add_u32 m0, s48, 40960\n"
"s_add_u32 s95, s95, 8192\n"
"v_cvt_pk_bf16_f32 v201, v162, v163\n"
"v_mfma_f32_16x16x32_bf16 v[180:183], a[100:103], a[28:31], v[180:183]\n"
"v_exp_f32 v166, v166\n"
"v_mfma_f32_16x16x32_bf16 v[72:75], a[124:127], v[212:215], v[72:75]\n"
"v_exp_f32 v167, v167\n"
"v_mfma_f32_16x16x32_bf16 v[176:179], a[88:91], a[48:51], v[176:179]\n"
"v_cvt_pk_bf16_f32 v202, v164, v165\n"
"v_cvt_pk_bf16_f32 v203, v166, v167\n"
"v_mfma_f32_16x16x32_bf16 v[92:95], a[128:131], v[212:215], v[92:95]\n"
"buffer_load_dwordx4 v244, s[60:63], s52 offen lds\n"
"v_mfma_f32_16x16x32_bf16 v[180:183], a[104:107], a[48:51], v[180:183]\n"
"buffer_load_dwordx4 v244, s[60:63], s52 offen offset:1024 lds\n"
"s_and_b32 s95, s95, 131071\n"
"v_mfma_f32_16x16x32_bf16 v[112:115], a[132:135], v[212:215], v[112:115]\n"
"v_add_u32 v241, 49152, v241\n"
"v_add_u32 v242, 49152, v242\n"
"v_mfma_f32_16x16x32_bf16 v[176:179], a[92:95], a[68:71], v[176:179]\n"
"v_mfma_f32_16x16x32_bf16 v[132:135], a[136:139], v[212:215], v[132:135]\n"
"v_mfma_f32_16x16x32_bf16 v[180:183], a[108:111], a[68:71], v[180:183]\n"
"v_mfma_f32_16x16x32_bf16 v[152:155], a[140:143], v[212:215], v[152:155]\n"
"v_mfma_f32_16x16x32_bf16 a[156:159], a[164:167], v[212:215], a[156:159]\n"
"s_waitcnt vmcnt(12) lgkmcnt(0)\n"
"s_barrier\n"
"v_mfma_f32_16x16x32_bf16 v[184:187], a[80:83], a[12:15], v[232:235]\n"
"v_mfma_f32_16x16x32_bf16 v[16:19], a[112:115], v[216:219], v[16:19]\n"
"ds_read_b64_tr_b16 a[112:113], v241 offset:8448\n"
"v_mfma_f32_16x16x32_bf16 v[188:191], a[96:99], a[12:15], v[232:235]\n"
"ds_read_b64_tr_b16 a[114:115], v241 offset:9472\n"
"v_mfma_f32_16x16x32_bf16 v[36:39], a[116:119], v[216:219], v[36:39]\n"
"ds_read_b64_tr_b16 a[116:117], v242 offset:8448\n"
"v_mfma_f32_16x16x32_bf16 v[184:187], a[84:87], a[32:35], v[184:187]\n"
"ds_read_b64_tr_b16 a[118:119], v242 offset:9472\n"
"v_mfma_f32_16x16x32_bf16 v[56:59], a[120:123], v[216:219], v[56:59]\n"
"ds_read_b64_tr_b16 a[120:121], v241 offset:8704\n"
"v_mfma_f32_16x16x32_bf16 v[188:191], a[100:103], a[32:35], v[188:191]\n"
"ds_read_b64_tr_b16 a[122:123], v241 offset:9728\n"
"v_mfma_f32_16x16x32_bf16 v[76:79], a[124:127], v[216:219], v[76:79]\n"
"ds_read_b64_tr_b16 a[124:125], v242 offset:8704\n"
"v_mfma_f32_16x16x32_bf16 v[184:187], a[88:91], a[52:55], v[184:187]\n"
"ds_read_b64_tr_b16 a[126:127], v242 offset:9728\n"
"s_add_u32 m0, s48, 0\n"
"v_mfma_f32_16x16x32_bf16 v[96:99], a[128:131], v[216:219], v[96:99]\n"
"ds_read_b64_tr_b16 a[128:129], v241 offset:8960\n"
"s_add_u32 s52, s81, 131072\n"
"v_mfma_f32_16x16x32_bf16 v[188:191], a[104:107], a[52:55], v[188:191]\n"
"ds_read_b64_tr_b16 a[130:131], v241 offset:9984\n"
"v_mfma_f32_16x16x32_bf16 v[116:119], a[132:135], v[216:219], v[116:119]\n"
"ds_read_b64_tr_b16 a[132:133], v242 offset:8960\n"
"v_mfma_f32_16x16x32_bf16 v[184:187], a[92:95], a[72:75], v[184:187]\n"
"ds_read_b64_tr_b16 a[134:135], v242 offset:9984\n"
"v_mfma_f32_16x16x32_bf16 v[136:139], a[136:139], v[216:219], v[136:139]\n"
"ds_read_b64_tr_b16 a[136:137], v241 offset:9216\n"
"v_mfma_f32_16x16x32_bf16 v[188:191], a[108:111], a[72:75], v[188:191]\n"
"ds_read_b64_tr_b16 a[138:139], v241 offset:10240\n"
"v_mfma_f32_16x16x32_bf16 v[156:159], a[140:143], v[216:219], v[156:159]\n"
"ds_read_b64_tr_b16 a[140:141], v242 offset:9216\n"
"v_mfma_f32_16x16x32_bf16 a[160:163], a[164:167], v[216:219], a[160:163]\n"
"ds_read_b64_tr_b16 a[142:143], v242 offset:10240\n"
"v_mfma_f32_16x16x32_bf16 v[192:195], a[80:83], a[16:19], v[236:239]\n"
"s_waitcnt lgkmcnt(12)\n"
"ds_read_b128 a[80:83], v240 offset:36992\n"
"v_mfma_f32_16x16x32_bf16 v[0:3], a[112:115], v[200:203], v[0:3]\n"
"v_exp_f32 v168, v168\n"
"v_mfma_f32_16x16x32_bf16 v[196:199], a[96:99], a[16:19], v[236:239]\n"
"ds_read_b128 a[96:99], v240 offset:38016\n"
"v_mfma_f32_16x16x32_bf16 v[20:23], a[116:119], v[200:203], v[20:23]\n"
"v_exp_f32 v169, v169\n"
"v_mfma_f32_16x16x32_bf16 v[192:195], a[84:87], a[36:39], v[192:195]\n"
"s_waitcnt lgkmcnt(10)\n"
"ds_read_b128 a[84:87], v240 offset:37248\n"
"v_mfma_f32_16x16x32_bf16 v[40:43], a[120:123], v[200:203], v[40:43]\n"
"v_exp_f32 v170, v170\n"
"v_mfma_f32_16x16x32_bf16 v[196:199], a[100:103], a[36:39], v[196:199]\n"
"ds_read_b128 a[100:103], v240 offset:38272\n"
"v_mfma_f32_16x16x32_bf16 v[60:63], a[124:127], v[200:203], v[60:63]\n"
"v_exp_f32 v171, v171\n"
"v_mfma_f32_16x16x32_bf16 v[192:195], a[88:91], a[56:59], v[192:195]\n"
"s_waitcnt lgkmcnt(8)\n"
"ds_read_b128 a[88:91], v240 offset:37504\n"
"v_mfma_f32_16x16x32_bf16 v[80:83], a[128:131], v[200:203], v[80:83]\n"
"buffer_load_dwordx4 v243, s[40:43], s81 offen nt lds\n"
"v_cvt_pk_bf16_f32 v204, v168, v169\n"
"v_mfma_f32_16x16x32_bf16 v[196:199], a[104:107], a[56:59], v[196:199]\n"
"ds_read_b128 a[104:107], v240 offset:38528\n"
"v_mfma_f32_16x16x32_bf16 v[100:103], a[132:135], v[200:203], v[100:103]\n"
"v_exp_f32 v172, v172\n"
"v_mfma_f32_16x16x32_bf16 v[192:195], a[92:95], a[76:79], v[192:195]\n"
"s_waitcnt lgkmcnt(6)\n"
"v_exp_f32 v173, v173\n"
"v_mfma_f32_16x16x32_bf16 v[120:123], a[136:139], v[200:203], v[120:123]\n"
"ds_read_b128 a[92:95], v240 offset:37760\n"
"v_cvt_pk_bf16_f32 v205, v170, v171\n"
"v_mfma_f32_16x16x32_bf16 v[196:199], a[108:111], a[76:79], v[196:199]\n"
"v_exp_f32 v174, v174\n"
"v_exp_f32 v175, v175\n"
"v_cvt_pk_bf16_f32 v206, v172, v173\n"
"v_cvt_pk_bf16_f32 v207, v174, v175\n"
"v_mfma_f32_16x16x32_bf16 v[140:143], a[140:143], v[200:203], v[140:143]\n"
"ds_read_b128 a[108:111], v240 offset:38784\n"
"v_mfma_f32_16x16x32_bf16 a[144:147], a[164:167], v[200:203], a[144:147]\n"
"s_waitcnt lgkmcnt(4)\n"
"v_exp_f32 v176, v176\n"
"v_mfma_f32_16x16x32_bf16 v[160:163], a[80:83], a[0:3], v[220:223]\n"
"v_exp_f32 v177, v177\n"
"v_mfma_f32_16x16x32_bf16 v[4:7], a[112:115], v[204:207], v[4:7]\n"
"v_exp_f32 v178, v178\n"
"v_mfma_f32_16x16x32_bf16 v[164:167], a[96:99], a[0:3], v[220:223]\n"
"v_exp_f32 v179, v179\n"
"v_mfma_f32_16x16x32_bf16 v[24:27], a[116:119], v[204:207], v[24:27]\n"
"buffer_load_dwordx4 v243, s[40:43], s81 offen offset:1024 nt lds\n"
"v_cvt_pk_bf16_f32 v208, v176, v177\n"
"v_mfma_f32_16x16x32_bf16 v[160:163], a[84:87], a[20:23], v[160:163]\n"
"s_add_u32 m0, s48, 8192\n"
"v_exp_f32 v180, v180\n"
"v_mfma_f32_16x16x32_bf16 v[44:47], a[120:123], v[204:207], v[44:47]\n"
"s_add_u32 s81, s81, 8192\n"
"v_exp_f32 v181, v181\n"
"v_mfma_f32_16x16x32_bf16 v[164:167], a[100:103], a[20:23], v[164:167]\n"
"buffer_load_dwordx4 v243, s[40:43], s52 offen nt lds\n"
"v_cvt_pk_bf16_f32 v209, v178, v179\n"
"v_mfma_f32_16x16x32_bf16 v[64:67], a[124:127], v[204:207], v[64:67]\n"
"s_waitcnt lgkmcnt(0)\n"
"v_exp_f32 v182, v182\n"
"v_mfma_f32_16x16x32_bf16 v[160:163], a[88:91], a[40:43], v[160:163]\n"
"v_exp_f32 v183, v183\n"
"v_mfma_f32_16x16x32_bf16 v[84:87], a[128:131], v[204:207], v[84:87]\n"
"v_cvt_pk_bf16_f32 v210, v180, v181\n"
"v_cvt_pk_bf16_f32 v211, v182, v183\n"
"v_mfma_f32_16x16x32_bf16 v[164:167], a[104:107], a[40:43], v[164:167]\n"
"v_exp_f32 v184, v184\n"
"v_mfma_f32_16x16x32_bf16 v[104:107], a[132:135], v[204:207], v[104:107]\n"
"v_exp_f32 v185, v185\n"
"v_mfma_f32_16x16x32_bf16 v[160:163], a[92:95], a[60:63], v[160:163]\n"
"v_exp_f32 v186, v186\n"
"v_mfma_f32_16x16x32_bf16 v[124:127], a[136:139], v[204:207], v[124:127]\n"
"v_exp_f32 v187, v187\n"
"v_mfma_f32_16x16x32_bf16 v[164:167], a[108:111], a[60:63], v[164:167]\n"
"buffer_load_dwordx4 v243, s[40:43], s52 offen offset:1024 nt lds\n"
"v_cvt_pk_bf16_f32 v212, v184, v185\n"
"v_mfma_f32_16x16x32_bf16 v[144:147], a[140:143], v[204:207], v[144:147]\n"
"s_and_b32 s81, s81, 131071\n"
"v_exp_f32 v188, v188\n"
"v_mfma_f32_16x16x32_bf16 a[148:151], a[164:167], v[204:207], a[148:151]\n"
"v_exp_f32 v189, v189\n"
"v_mfma_f32_16x16x32_bf16 v[168:171], a[80:83], a[4:7], v[224:227]\n"
"v_cvt_pk_bf16_f32 v213, v186, v187\n"
"v_accvgpr_mov_b32 a0, a168\n"
"v_mfma_f32_16x16x32_bf16 v[8:11], a[112:115], v[208:211], v[8:11]\n"
"v_exp_f32 v190, v190\n"
"v_mfma_f32_16x16x32_bf16 v[172:175], a[96:99], a[4:7], v[224:227]\n"
"v_exp_f32 v191, v191\n"
"v_mfma_f32_16x16x32_bf16 v[28:31], a[116:119], v[208:211], v[28:31]\n"
"v_cvt_pk_bf16_f32 v214, v188, v189\n"
"v_cvt_pk_bf16_f32 v215, v190, v191\n"
"v_mfma_f32_16x16x32_bf16 v[168:171], a[84:87], a[24:27], v[168:171]\n"
"v_exp_f32 v192, v192\n"
"v_mfma_f32_16x16x32_bf16 v[48:51], a[120:123], v[208:211], v[48:51]\n"
"v_exp_f32 v193, v193\n"
"v_mfma_f32_16x16x32_bf16 v[172:175], a[100:103], a[24:27], v[172:175]\n"
"v_exp_f32 v194, v194\n"
"v_mfma_f32_16x16x32_bf16 v[68:71], a[124:127], v[208:211], v[68:71]\n"
"v_exp_f32 v195, v195\n"
"v_mfma_f32_16x16x32_bf16 v[168:171], a[88:91], a[44:47], v[168:171]\n"
"v_cvt_pk_bf16_f32 v216, v192, v193\n"
"v_accvgpr_mov_b32 a1, a169\n"
"v_mfma_f32_16x16x32_bf16 v[88:91], a[128:131], v[208:211], v[88:91]\n"
"v_exp_f32 v196, v196\n"
"v_mfma_f32_16x16x32_bf16 v[172:175], a[104:107], a[44:47], v[172:175]\n"
"v_exp_f32 v197, v197\n"
"v_mfma_f32_16x16x32_bf16 v[108:111], a[132:135], v[208:211], v[108:111]\n"
"v_cvt_pk_bf16_f32 v217, v194, v195\n"
"v_accvgpr_mov_b32 a2, a170\n"
"v_mfma_f32_16x16x32_bf16 v[168:171], a[92:95], a[64:67], v[168:171]\n"
"v_exp_f32 v198, v198\n"
"v_mfma_f32_16x16x32_bf16 v[128:131], a[136:139], v[208:211], v[128:131]\n"
"v_exp_f32 v199, v199\n"
"v_mfma_f32_16x16x32_bf16 v[172:175], a[108:111], a[64:67], v[172:175]\n"
"v_cvt_pk_bf16_f32 v218, v196, v197\n"
"v_cvt_pk_bf16_f32 v219, v198, v199\n"
"v_mfma_f32_16x16x32_bf16 v[148:151], a[140:143], v[208:211], v[148:151]\n"
"v_exp_f32 v160, v160\n"
"v_mfma_f32_16x16x32_bf16 a[152:155], a[164:167], v[208:211], a[152:155]\n"
"v_exp_f32 v161, v161\n"
"v_mfma_f32_16x16x32_bf16 v[176:179], a[80:83], a[8:11], v[228:231]\n"
"v_exp_f32 v162, v162\n"
"v_mfma_f32_16x16x32_bf16 v[12:15], a[112:115], v[212:215], v[12:15]\n"
"v_exp_f32 v163, v163\n"
"v_mfma_f32_16x16x32_bf16 v[180:183], a[96:99], a[8:11], v[228:231]\n"
"v_cvt_pk_bf16_f32 v200, v160, v161\n"
"v_accvgpr_mov_b32 a3, a171\n"
"v_mfma_f32_16x16x32_bf16 v[32:35], a[116:119], v[212:215], v[32:35]\n"
"v_exp_f32 v164, v164\n"
"v_mfma_f32_16x16x32_bf16 v[176:179], a[84:87], a[28:31], v[176:179]\n"
"v_exp_f32 v165, v165\n"
"v_mfma_f32_16x16x32_bf16 v[52:55], a[120:123], v[212:215], v[52:55]\n"
"v_cvt_pk_bf16_f32 v201, v162, v163\n"
"v_accvgpr_mov_b32 a20, a188\n"
"v_mfma_f32_16x16x32_bf16 v[180:183], a[100:103], a[28:31], v[180:183]\n"
"v_exp_f32 v166, v166\n"
"v_mfma_f32_16x16x32_bf16 v[72:75], a[124:127], v[212:215], v[72:75]\n"
"v_exp_f32 v167, v167\n"
"v_mfma_f32_16x16x32_bf16 v[176:179], a[88:91], a[48:51], v[176:179]\n"
"v_cvt_pk_bf16_f32 v202, v164, v165\n"
"v_cvt_pk_bf16_f32 v203, v166, v167\n"
"v_mfma_f32_16x16x32_bf16 v[92:95], a[128:131], v[212:215], v[92:95]\n"
"v_accvgpr_mov_b32 a21, a189\n"
"v_accvgpr_mov_b32 a22, a190\n"
"v_mfma_f32_16x16x32_bf16 v[180:183], a[104:107], a[48:51], v[180:183]\n"
"v_accvgpr_mov_b32 a23, a191\n"
"v_accvgpr_mov_b32 a40, a208\n"
"v_mfma_f32_16x16x32_bf16 v[112:115], a[132:135], v[212:215], v[112:115]\n"
"v_accvgpr_mov_b32 a41, a209\n"
"v_accvgpr_mov_b32 a42, a210\n"
"v_mfma_f32_16x16x32_bf16 v[176:179], a[92:95], a[68:71], v[176:179]\n"
"v_accvgpr_mov_b32 a43, a211\n"
"v_accvgpr_mov_b32 a60, a228\n"
"v_mfma_f32_16x16x32_bf16 v[132:135], a[136:139], v[212:215], v[132:135]\n"
"v_accvgpr_mov_b32 a61, a229\n"
"v_accvgpr_mov_b32 a62, a230\n"
"v_mfma_f32_16x16x32_bf16 v[180:183], a[108:111], a[68:71], v[180:183]\n"
"v_accvgpr_mov_b32 a63, a231\n"
"v_accvgpr_mov_b32 a4, a172\n"
"v_mfma_f32_16x16x32_bf16 v[152:155], a[140:143], v[212:215], v[152:155]\n"
"v_accvgpr_mov_b32 a5, a173\n"
"v_accvgpr_mov_b32 a6, a174\n"
"v_mfma_f32_16x16x32_bf16 a[156:159], a[164:167], v[212:215], a[156:159]\n"
"s_waitcnt vmcnt(0) lgkmcnt(0)\n"
"s_barrier\n"
"v_accvgpr_mov_b32 a7, a175\n"
"v_mfma_f32_16x16x32_bf16 v[184:187], a[80:83], a[12:15], v[232:235]\n"
"v_accvgpr_mov_b32 a24, a192\n"
"v_accvgpr_mov_b32 a25, a193\n"
"v_mfma_f32_16x16x32_bf16 v[16:19], a[112:115], v[216:219], v[16:19]\n"
"ds_read_b64_tr_b16 a[112:113], v241 offset:24832\n"
"s_add_u32 m0, s48, 106752\n"
"v_mfma_f32_16x16x32_bf16 v[188:191], a[96:99], a[12:15], v[232:235]\n"
"ds_read_b64_tr_b16 a[114:115], v241 offset:25856\n"
"s_add_u32 s52, s81, 131072\n"
"v_mfma_f32_16x16x32_bf16 v[36:39], a[116:119], v[216:219], v[36:39]\n"
"ds_read_b64_tr_b16 a[116:117], v242 offset:24832\n"
"v_accvgpr_mov_b32 a26, a194\n"
"v_mfma_f32_16x16x32_bf16 v[184:187], a[84:87], a[32:35], v[184:187]\n"
"ds_read_b64_tr_b16 a[118:119], v242 offset:25856\n"
"v_accvgpr_mov_b32 a27, a195\n"
"v_mfma_f32_16x16x32_bf16 v[56:59], a[120:123], v[216:219], v[56:59]\n"
"ds_read_b64_tr_b16 a[120:121], v241 offset:25088\n"
"v_accvgpr_mov_b32 a44, a212\n"
"v_mfma_f32_16x16x32_bf16 v[188:191], a[100:103], a[32:35], v[188:191]\n"
"ds_read_b64_tr_b16 a[122:123], v241 offset:26112\n"
"v_accvgpr_mov_b32 a45, a213\n"
"v_mfma_f32_16x16x32_bf16 v[76:79], a[124:127], v[216:219], v[76:79]\n"
"ds_read_b64_tr_b16 a[124:125], v242 offset:25088\n"
"v_accvgpr_mov_b32 a46, a214\n"
"v_mfma_f32_16x16x32_bf16 v[184:187], a[88:91], a[52:55], v[184:187]\n"
"ds_read_b64_tr_b16 a[126:127], v242 offset:26112\n"
"v_accvgpr_mov_b32 a47, a215\n"
"v_mfma_f32_16x16x32_bf16 v[96:99], a[128:131], v[216:219], v[96:99]\n"
"ds_read_b64_tr_b16 a[128:129], v241 offset:25344\n"
"v_accvgpr_mov_b32 a64, a232\n"
"v_mfma_f32_16x16x32_bf16 v[188:191], a[104:107], a[52:55], v[188:191]\n"
"ds_read_b64_tr_b16 a[130:131], v241 offset:26368\n"
"v_accvgpr_mov_b32 a65, a233\n"
"v_mfma_f32_16x16x32_bf16 v[116:119], a[132:135], v[216:219], v[116:119]\n"
"ds_read_b64_tr_b16 a[132:133], v242 offset:25344\n"
"v_accvgpr_mov_b32 a66, a234\n"
"v_mfma_f32_16x16x32_bf16 v[184:187], a[92:95], a[72:75], v[184:187]\n"
"ds_read_b64_tr_b16 a[134:135], v242 offset:26368\n"
"v_accvgpr_mov_b32 a67, a235\n"
"v_mfma_f32_16x16x32_bf16 v[136:139], a[136:139], v[216:219], v[136:139]\n"
"ds_read_b64_tr_b16 a[136:137], v241 offset:25600\n"
"v_accvgpr_mov_b32 a8, a176\n"
"v_mfma_f32_16x16x32_bf16 v[188:191], a[108:111], a[72:75], v[188:191]\n"
"ds_read_b64_tr_b16 a[138:139], v241 offset:26624\n"
"v_accvgpr_mov_b32 a9, a177\n"
"v_mfma_f32_16x16x32_bf16 v[156:159], a[140:143], v[216:219], v[156:159]\n"
"ds_read_b64_tr_b16 a[140:141], v242 offset:25600\n"
"v_accvgpr_mov_b32 a10, a178\n"
"v_mfma_f32_16x16x32_bf16 a[160:163], a[164:167], v[216:219], a[160:163]\n"
"ds_read_b64_tr_b16 a[142:143], v242 offset:26624\n"
"v_add_u32 v240, 4294918144, v240\n"
"v_mfma_f32_16x16x32_bf16 v[192:195], a[80:83], a[16:19], v[236:239]\n"
"s_waitcnt lgkmcnt(12)\n"
"ds_read_b128 a[80:83], v240 offset:16384\n"
"v_exp_f32 v168, v168\n"
"v_mfma_f32_16x16x32_bf16 v[0:3], a[112:115], v[200:203], v[0:3]\n"
"v_exp_f32 v169, v169\n"
"v_exp_f32 v170, v170\n"
"v_mfma_f32_16x16x32_bf16 v[196:199], a[96:99], a[16:19], v[236:239]\n"
"ds_read_b128 a[96:99], v240 offset:17408\n"
"v_exp_f32 v171, v171\n"
"v_cvt_pk_bf16_f32 v204, v168, v169\n"
"v_mfma_f32_16x16x32_bf16 v[20:23], a[116:119], v[200:203], v[20:23]\n"
"v_exp_f32 v172, v172\n"
"v_exp_f32 v173, v173\n"
"v_mfma_f32_16x16x32_bf16 v[192:195], a[84:87], a[36:39], v[192:195]\n"
"s_waitcnt lgkmcnt(10)\n"
"ds_read_b128 a[84:87], v240 offset:16640\n"
"v_cvt_pk_bf16_f32 v205, v170, v171\n"
"v_accvgpr_mov_b32 a11, a179\n"
"v_mfma_f32_16x16x32_bf16 v[40:43], a[120:123], v[200:203], v[40:43]\n"
"v_exp_f32 v174, v174\n"
"v_exp_f32 v175, v175\n"
"v_mfma_f32_16x16x32_bf16 v[196:199], a[100:103], a[36:39], v[196:199]\n"
"ds_read_b128 a[100:103], v240 offset:17664\n"
"v_cvt_pk_bf16_f32 v206, v172, v173\n"
"v_cvt_pk_bf16_f32 v207, v174, v175\n"
"v_accvgpr_mov_b32 a28, a196\n"
"v_mfma_f32_16x16x32_bf16 v[60:63], a[124:127], v[200:203], v[60:63]\n"
"buffer_load_dwordx4 v243, s[40:43], s81 offen nt lds\n"
"buffer_load_dwordx4 v243, s[40:43], s81 offen offset:1024 nt lds\n"
"s_add_u32 m0, s48, 114944\n"
"v_mfma_f32_16x16x32_bf16 v[192:195], a[88:91], a[56:59], v[192:195]\n"
"s_waitcnt lgkmcnt(8)\n"
"ds_read_b128 a[88:91], v240 offset:16896\n"
"s_add_u32 s81, s81, 8192\n"
"v_accvgpr_mov_b32 a29, a197\n"
"v_mfma_f32_16x16x32_bf16 v[80:83], a[128:131], v[200:203], v[80:83]\n"
"buffer_load_dwordx4 v243, s[40:43], s52 offen nt lds\n"
"buffer_load_dwordx4 v243, s[40:43], s52 offen offset:1024 nt lds\n"
"s_and_b32 s81, s81, 131071\n"
"v_mfma_f32_16x16x32_bf16 v[196:199], a[104:107], a[56:59], v[196:199]\n"
"ds_read_b128 a[104:107], v240 offset:17920\n"
"v_exp_f32 v176, v176\n"
"v_accvgpr_mov_b32 a30, a198\n"
"v_mfma_f32_16x16x32_bf16 v[100:103], a[132:135], v[200:203], v[100:103]\n"
"v_exp_f32 v177, v177\n"
"v_exp_f32 v178, v178\n"
"v_mfma_f32_16x16x32_bf16 v[192:195], a[92:95], a[76:79], v[192:195]\n"
"s_waitcnt lgkmcnt(6)\n"
"ds_read_b128 a[92:95], v240 offset:17152\n"
"v_exp_f32 v179, v179\n"
"v_mfma_f32_16x16x32_bf16 v[120:123], a[136:139], v[200:203], v[120:123]\n"
"v_cvt_pk_bf16_f32 v208, v176, v177\n"
"v_exp_f32 v180, v180\n"
"v_accvgpr_mov_b32 a31, a199\n"
"v_mfma_f32_16x16x32_bf16 v[196:199], a[108:111], a[76:79], v[196:199]\n"
"ds_read_b128 a[108:111], v240 offset:18176\n"
"v_exp_f32 v181, v181\n"
"v_cvt_pk_bf16_f32 v209, v178, v179\n"
"v_mfma_f32_16x16x32_bf16 v[140:143], a[140:143], v[200:203], v[140:143]\n"
"v_exp_f32 v182, v182\n"
"v_exp_f32 v183, v183\n"
"v_mfma_f32_16x16x32_bf16 a[144:147], a[164:167], v[200:203], a[144:147]\n"
"s_waitcnt lgkmcnt(4)\n"
"s_waitcnt lgkmcnt(0)\n"
"s_barrier\n"
"v_cvt_pk_bf16_f32 v210, v180, v181\n"
"v_cvt_pk_bf16_f32 v211, v182, v183\n"
"v_mfma_f32_16x16x32_bf16 v[160:163], a[80:83], a[0:3], 0\n"
"s_add_u32 m0, s48, 123136\n"
"v_exp_f32 v184, v184\n"
"v_exp_f32 v185, v185\n"
"v_mfma_f32_16x16x32_bf16 v[4:7], a[112:115], v[204:207], v[4:7]\n"
"s_add_u32 s52, s95, 131072\n"
"v_exp_f32 v186, v186\n"
"v_exp_f32 v187, v187\n"
"v_mfma_f32_16x16x32_bf16 v[164:167], a[96:99], a[0:3], 0\n"
"v_cvt_pk_bf16_f32 v212, v184, v185\n"
"v_exp_f32 v188, v188\n"
"v_accvgpr_mov_b32 a48, a216\n"
"v_mfma_f32_16x16x32_bf16 v[24:27], a[116:119], v[204:207], v[24:27]\n"
"v_exp_f32 v189, v189\n"
"v_cvt_pk_bf16_f32 v213, v186, v187\n"
"v_accvgpr_mov_b32 a49, a217\n"
"v_mfma_f32_16x16x32_bf16 v[160:163], a[84:87], a[20:23], v[160:163]\n"
"v_exp_f32 v190, v190\n"
"v_exp_f32 v191, v191\n"
"v_mfma_f32_16x16x32_bf16 v[44:47], a[120:123], v[204:207], v[44:47]\n"
"buffer_load_dwordx4 v244, s[60:63], s95 offen lds\n"
"v_cvt_pk_bf16_f32 v214, v188, v189\n"
"v_cvt_pk_bf16_f32 v215, v190, v191\n"
"v_accvgpr_mov_b32 a50, a218\n"
"v_mfma_f32_16x16x32_bf16 v[164:167], a[100:103], a[20:23], v[164:167]\n"
"buffer_load_dwordx4 v244, s[60:63], s95 offen offset:1024 lds\n"
"v_accvgpr_mov_b32 a51, a219\n"
"v_accvgpr_mov_b32 a68, a236\n"
"v_accvgpr_mov_b32 a69, a237\n"
"v_mfma_f32_16x16x32_bf16 v[64:67], a[124:127], v[204:207], v[64:67]\n"
"s_waitcnt lgkmcnt(0)\n"
"v_accvgpr_mov_b32 a70, a238\n"
"v_accvgpr_mov_b32 a71, a239\n"
"v_exp_f32 v192, v192\n"
"v_mfma_f32_16x16x32_bf16 v[160:163], a[88:91], a[40:43], v[160:163]\n"
"v_exp_f32 v193, v193\n"
"v_exp_f32 v194, v194\n"
"v_mfma_f32_16x16x32_bf16 v[84:87], a[128:131], v[204:207], v[84:87]\n"
"v_exp_f32 v195, v195\n"
"v_cvt_pk_bf16_f32 v216, v192, v193\n"
"v_accvgpr_mov_b32 a12, a180\n"
"v_mfma_f32_16x16x32_bf16 v[164:167], a[104:107], a[40:43], v[164:167]\n"
"v_exp_f32 v196, v196\n"
"v_exp_f32 v197, v197\n"
"v_mfma_f32_16x16x32_bf16 v[104:107], a[132:135], v[204:207], v[104:107]\n"
"v_cvt_pk_bf16_f32 v217, v194, v195\n"
"v_exp_f32 v198, v198\n"
"v_accvgpr_mov_b32 a13, a181\n"
"v_mfma_f32_16x16x32_bf16 v[160:163], a[92:95], a[60:63], v[160:163]\n"
"s_add_u32 m0, s48, 131328\n"
"v_exp_f32 v199, v199\n"
"v_cvt_pk_bf16_f32 v218, v196, v197\n"
"v_cvt_pk_bf16_f32 v219, v198, v199\n"
"v_mfma_f32_16x16x32_bf16 v[124:127], a[136:139], v[204:207], v[124:127]\n"
"s_add_u32 s95, s95, 8192\n"
"v_accvgpr_mov_b32 a14, a182\n"
"v_accvgpr_mov_b32 a15, a183\n"
"v_accvgpr_mov_b32 a32, a200\n"
"v_mfma_f32_16x16x32_bf16 v[164:167], a[108:111], a[60:63], v[164:167]\n"
"buffer_load_dwordx4 v244, s[60:63], s52 offen lds\n"
"v_accvgpr_mov_b32 a33, a201\n"
"v_accvgpr_mov_b32 a34, a202\n"
"v_accvgpr_mov_b32 a35, a203\n"
"v_mfma_f32_16x16x32_bf16 v[144:147], a[140:143], v[204:207], v[144:147]\n"
"buffer_load_dwordx4 v244, s[60:63], s52 offen offset:1024 lds\n"
"v_accvgpr_mov_b32 a52, a220\n"
"v_accvgpr_mov_b32 a53, a221\n"
"v_accvgpr_mov_b32 a54, a222\n"
"v_mfma_f32_16x16x32_bf16 a[148:151], a[164:167], v[204:207], a[148:151]\n"
"s_and_b32 s95, s95, 131071\n"
"v_accvgpr_mov_b32 a55, a223\n"
"v_accvgpr_mov_b32 a72, a240\n"
"v_accvgpr_mov_b32 a73, a241\n"
"v_mfma_f32_16x16x32_bf16 v[168:171], a[80:83], a[4:7], 0\n"
"v_accvgpr_mov_b32 a74, a242\n"
"v_accvgpr_mov_b32 a75, a243\n"
"v_accvgpr_mov_b32 a16, a184\n"
"v_mfma_f32_16x16x32_bf16 v[8:11], a[112:115], v[208:211], v[8:11]\n"
"v_accvgpr_mov_b32 a17, a185\n"
"v_accvgpr_mov_b32 a18, a186\n"
"v_accvgpr_mov_b32 a19, a187\n"
"v_mfma_f32_16x16x32_bf16 v[172:175], a[96:99], a[4:7], 0\n"
"v_accvgpr_mov_b32 a36, a204\n"
"v_accvgpr_mov_b32 a37, a205\n"
"v_accvgpr_mov_b32 a38, a206\n"
"v_mfma_f32_16x16x32_bf16 v[28:31], a[116:119], v[208:211], v[28:31]\n"
"v_accvgpr_mov_b32 a39, a207\n"
"v_accvgpr_mov_b32 a56, a224\n"
"v_accvgpr_mov_b32 a57, a225\n"
"v_mfma_f32_16x16x32_bf16 v[168:171], a[84:87], a[24:27], v[168:171]\n"
"v_accvgpr_mov_b32 a58, a226\n"
"v_accvgpr_mov_b32 a59, a227\n"
"v_accvgpr_mov_b32 a76, a244\n"
"v_mfma_f32_16x16x32_bf16 v[48:51], a[120:123], v[208:211], v[48:51]\n"
"v_accvgpr_mov_b32 a77, a245\n"
"v_accvgpr_mov_b32 a78, a246\n"
"v_accvgpr_mov_b32 a79, a247\n"
"v_mfma_f32_16x16x32_bf16 v[172:175], a[100:103], a[24:27], v[172:175]\n"
"v_max3_f32 v220, v160, v161, v162\n"
"v_max3_f32 v221, v163, v164, v165\n"
"v_max3_f32 v220, v220, v166, v167\n"
"v_mfma_f32_16x16x32_bf16 v[68:71], a[124:127], v[208:211], v[68:71]\n"
"s_add_u32 m0, s48, 139520\n"
"s_add_u32 s52, s81, 131072\n"
"buffer_load_dwordx4 v243, s[40:43], s81 offen nt lds\n"
"v_max_f32 v220, v220, v221\n"
"v_mfma_f32_16x16x32_bf16 v[168:171], a[88:91], a[44:47], v[168:171]\n"
"buffer_load_dwordx4 v243, s[40:43], s81 offen offset:1024 nt lds\n"
"v_mov_b32 v221, v220\n"
"s_nop 0\n"
"s_nop 0\n"
"v_permlane32_swap_b32 v220, v221\n"
"v_max_f32 v220, v220, v221\n"
"v_mfma_f32_16x16x32_bf16 v[88:91], a[128:131], v[208:211], v[88:91]\n"
"v_mov_b32 v221, v220\n"
"s_nop 0\n"
"s_nop 0\n"
"v_permlane16_swap_b32 v220, v221\n"
"v_max_f32 v221, v220, v221\n"
"v_mfma_f32_16x16x32_bf16 v[172:175], a[104:107], a[44:47], v[172:175]\n"
"v_sub_f32 v220, 0, v221\n"
"v_add_f32 v160, v160, v220\n"
"v_add_f32 v161, v161, v220\n"
"v_mfma_f32_16x16x32_bf16 v[108:111], a[132:135], v[208:211], v[108:111]\n"
"v_add_f32 v162, v162, v220\n"
"v_add_f32 v163, v163, v220\n"
"v_add_f32 v164, v164, v220\n"
"v_mfma_f32_16x16x32_bf16 v[168:171], a[92:95], a[64:67], v[168:171]\n"
"v_add_f32 v165, v165, v220\n"
"v_add_f32 v166, v166, v220\n"
"v_add_f32 v167, v167, v220\n"
"v_mfma_f32_16x16x32_bf16 v[128:131], a[136:139], v[208:211], v[128:131]\n"
"v_mov_b32 v221, v220\n"
"v_mov_b32 v222, v220\n"
"v_mov_b32 v223, v220\n"
"v_mfma_f32_16x16x32_bf16 v[172:175], a[108:111], a[64:67], v[172:175]\n"
"v_exp_f32 v160, v160\n"
"v_exp_f32 v161, v161\n"
"v_mfma_f32_16x16x32_bf16 v[148:151], a[140:143], v[208:211], v[148:151]\n"
"v_exp_f32 v162, v162\n"
"v_exp_f32 v163, v163\n"
"v_mfma_f32_16x16x32_bf16 a[152:155], a[164:167], v[208:211], a[152:155]\n"
"v_cvt_pk_bf16_f32 v200, v160, v161\n"
"v_exp_f32 v164, v164\n"
"v_accvgpr_read_b32 v255, a144\n"
"v_mfma_f32_16x16x32_bf16 v[176:179], a[80:83], a[8:11], 0\n"
"v_exp_f32 v165, v165\n"
"v_cvt_pk_bf16_f32 v201, v162, v163\n"
"v_cmp_nge_f32 vcc, s53, v255\n"
"v_mfma_f32_16x16x32_bf16 v[12:15], a[112:115], v[212:215], v[12:15]\n"
"v_exp_f32 v166, v166\n"
"v_exp_f32 v167, v167\n"
"v_mfma_f32_16x16x32_bf16 v[180:183], a[96:99], a[8:11], 0\n"
"s_add_u32 m0, s48, 147712\n"
"s_add_u32 s81, s81, 8192\n"
"buffer_load_dwordx4 v243, s[40:43], s52 offen nt lds\n"
"v_cvt_pk_bf16_f32 v202, v164, v165\n"
"v_mfma_f32_16x16x32_bf16 v[32:35], a[116:119], v[212:215], v[32:35]\n"
"buffer_load_dwordx4 v243, s[40:43], s52 offen offset:1024 nt lds\n"
"s_and_b32 s81, s81, 131071\n"
"v_cvt_pk_bf16_f32 v203, v166, v167\n"
"v_mfma_f32_16x16x32_bf16 v[176:179], a[84:87], a[28:31], v[176:179]\n"
"v_rcp_f32 v255, v255\n"
"s_or_b64 s[58:59], s[58:59], vcc\n"
"v_mul_f32 v0, v255, v0\n"
"v_mfma_f32_16x16x32_bf16 v[52:55], a[120:123], v[212:215], v[52:55]\n"
"v_mul_f32 v1, v255, v1\n"
"v_mul_f32 v2, v255, v2\n"
"v_mul_f32 v3, v255, v3\n"
"v_mfma_f32_16x16x32_bf16 v[180:183], a[100:103], a[28:31], v[180:183]\n"
"v_mul_f32 v20, v255, v20\n"
"v_mul_f32 v21, v255, v21\n"
"v_mul_f32 v22, v255, v22\n"
"v_mfma_f32_16x16x32_bf16 v[72:75], a[124:127], v[212:215], v[72:75]\n"
"v_mul_f32 v23, v255, v23\n"
"v_cvt_pk_bf16_f32 v0, v0, v1\n"
"v_cvt_pk_bf16_f32 v1, v2, v3\n"
"v_mfma_f32_16x16x32_bf16 v[176:179], a[88:91], a[48:51], v[176:179]\n"
"v_cvt_pk_bf16_f32 v2, v20, v21\n"
"v_cvt_pk_bf16_f32 v3, v22, v23\n"
"v_accvgpr_write_b32 a168, v0\n"
"v_mfma_f32_16x16x32_bf16 v[92:95], a[128:131], v[212:215], v[92:95]\n"
"v_accvgpr_write_b32 a169, v1\n"
"v_accvgpr_write_b32 a170, v2\n"
"v_accvgpr_write_b32 a171, v3\n"
"v_mfma_f32_16x16x32_bf16 v[180:183], a[104:107], a[48:51], v[180:183]\n"
"v_mul_f32 v40, v255, v40\n"
"v_mul_f32 v41, v255, v41\n"
"v_mul_f32 v42, v255, v42\n"
"v_mfma_f32_16x16x32_bf16 v[112:115], a[132:135], v[212:215], v[112:115]\n"
"v_mul_f32 v43, v255, v43\n"
"v_mul_f32 v60, v255, v60\n"
"v_mul_f32 v61, v255, v61\n"
"v_mfma_f32_16x16x32_bf16 v[176:179], a[92:95], a[68:71], v[176:179]\n"
"v_mul_f32 v62, v255, v62\n"
"v_mul_f32 v63, v255, v63\n"
"v_cvt_pk_bf16_f32 v40, v40, v41\n"
"v_mfma_f32_16x16x32_bf16 v[132:135], a[136:139], v[212:215], v[132:135]\n"
"v_add_u32 v241, 4294868992, v241\n"
"v_add_u32 v242, 4294868992, v242\n"
"v_cvt_pk_bf16_f32 v41, v42, v43\n"
"v_mfma_f32_16x16x32_bf16 v[180:183], a[108:111], a[68:71], v[180:183]\n"
"v_cvt_pk_bf16_f32 v42, v60, v61\n"
"v_cvt_pk_bf16_f32 v43, v62, v63\n"
"v_accvgpr_write_b32 a188, v40\n"
"v_mfma_f32_16x16x32_bf16 v[152:155], a[140:143], v[212:215], v[152:155]\n"
"v_accvgpr_write_b32 a189, v41\n"
"v_accvgpr_write_b32 a190, v42\n"
"v_accvgpr_write_b32 a191, v43\n"
"v_mfma_f32_16x16x32_bf16 a[156:159], a[164:167], v[212:215], a[156:159]\n"
"v_mul_f32 v80, v255, v80\n"
"v_mul_f32 v81, v255, v81\n"
"v_mul_f32 v82, v255, v82\n"
"v_mfma_f32_16x16x32_bf16 v[184:187], a[80:83], a[12:15], 0\n"
"v_mul_f32 v83, v255, v83\n"
"v_mul_f32 v100, v255, v100\n"
"v_mul_f32 v101, v255, v101\n"
"v_mfma_f32_16x16x32_bf16 v[16:19], a[112:115], v[216:219], v[16:19]\n"
"ds_read_b64_tr_b16 a[112:113], v241 offset:32768\n"
"v_mul_f32 v102, v255, v102\n"
"v_mul_f32 v103, v255, v103\n"
"v_cvt_pk_bf16_f32 v80, v80, v81\n"
"v_mfma_f32_16x16x32_bf16 v[188:191], a[96:99], a[12:15], 0\n"
"ds_read_b64_tr_b16 a[114:115], v241 offset:33792\n"
"v_cvt_pk_bf16_f32 v81, v82, v83\n"
"v_cvt_pk_bf16_f32 v82, v100, v101\n"
"v_cvt_pk_bf16_f32 v83, v102, v103\n"
"v_mfma_f32_16x16x32_bf16 v[36:39], a[116:119], v[216:219], v[36:39]\n"
"ds_read_b64_tr_b16 a[116:117], v242 offset:32768\n"
"v_accvgpr_write_b32 a208, v80\n"
"v_accvgpr_write_b32 a209, v81\n"
"v_accvgpr_write_b32 a210, v82\n"
"v_mfma_f32_16x16x32_bf16 v[184:187], a[84:87], a[32:35], v[184:187]\n"
"ds_read_b64_tr_b16 a[118:119], v242 offset:33792\n"
"v_accvgpr_write_b32 a211, v83\n"
"v_mul_f32 v120, v255, v120\n"
"v_mul_f32 v121, v255, v121\n"
"v_mfma_f32_16x16x32_bf16 v[56:59], a[120:123], v[216:219], v[56:59]\n"
"ds_read_b64_tr_b16 a[120:121], v241 offset:33024\n"
"v_mul_f32 v122, v255, v122\n"
"v_mul_f32 v123, v255, v123\n"
"v_mul_f32 v140, v255, v140\n"
"v_mfma_f32_16x16x32_bf16 v[188:191], a[100:103], a[32:35], v[188:191]\n"
"ds_read_b64_tr_b16 a[122:123], v241 offset:34048\n"
"v_mul_f32 v141, v255, v141\n"
"v_mul_f32 v142, v255, v142\n"
"v_mul_f32 v143, v255, v143\n"
"v_mfma_f32_16x16x32_bf16 v[76:79], a[124:127], v[216:219], v[76:79]\n"
"ds_read_b64_tr_b16 a[124:125], v242 offset:33024\n"
"v_cvt_pk_bf16_f32 v120, v120, v121\n"
"v_cvt_pk_bf16_f32 v121, v122, v123\n"
"v_cvt_pk_bf16_f32 v122, v140, v141\n"
"v_mfma_f32_16x16x32_bf16 v[184:187], a[88:91], a[52:55], v[184:187]\n"
"ds_read_b64_tr_b16 a[126:127], v242 offset:34048\n"
"v_cvt_pk_bf16_f32 v123, v142, v143\n"
"v_accvgpr_write_b32 a228, v120\n"
"v_accvgpr_write_b32 a229, v121\n"
"v_mfma_f32_16x16x32_bf16 v[96:99], a[128:131], v[216:219], v[96:99]\n"
"ds_read_b64_tr_b16 a[128:129], v241 offset:33280\n"
"v_accvgpr_write_b32 a230, v122\n"
"v_accvgpr_write_b32 a231, v123\n"
"v_max3_f32 v224, v168, v169, v170\n"
"v_mfma_f32_16x16x32_bf16 v[188:191], a[104:107], a[52:55], v[188:191]\n"
"ds_read_b64_tr_b16 a[130:131], v241 offset:34304\n"
"v_max3_f32 v225, v171, v172, v173\n"
"v_max3_f32 v224, v224, v174, v175\n"
"v_max_f32 v224, v224, v225\n"
"v_mfma_f32_16x16x32_bf16 v[116:119], a[132:135], v[216:219], v[116:119]\n"
"ds_read_b64_tr_b16 a[132:133], v242 offset:33280\n"
"v_mov_b32 v225, v224\n"
"s_nop 0\n"
"s_nop 0\n"
"v_permlane32_swap_b32 v224, v225\n"
"v_max_f32 v224, v224, v225\n"
"v_mfma_f32_16x16x32_bf16 v[184:187], a[92:95], a[72:75], v[184:187]\n"
"ds_read_b64_tr_b16 a[134:135], v242 offset:34304\n"
"v_mov_b32 v225, v224\n"
"s_nop 0\n"
"s_nop 0\n"
"v_permlane16_swap_b32 v224, v225\n"
"v_max_f32 v225, v224, v225\n"
"v_mfma_f32_16x16x32_bf16 v[136:139], a[136:139], v[216:219], v[136:139]\n"
"ds_read_b64_tr_b16 a[136:137], v241 offset:33536\n"
"v_sub_f32 v224, 0, v225\n"
"v_add_f32 v168, v168, v224\n"
"v_add_f32 v169, v169, v224\n"
"v_mfma_f32_16x16x32_bf16 v[188:191], a[108:111], a[72:75], v[188:191]\n"
"ds_read_b64_tr_b16 a[138:139], v241 offset:34560\n"
"v_add_f32 v170, v170, v224\n"
"v_add_f32 v171, v171, v224\n"
"v_add_f32 v172, v172, v224\n"
"v_mfma_f32_16x16x32_bf16 v[156:159], a[140:143], v[216:219], v[156:159]\n"
"ds_read_b64_tr_b16 a[140:141], v242 offset:33536\n"
"v_add_f32 v173, v173, v224\n"
"v_add_f32 v174, v174, v224\n"
"v_add_f32 v175, v175, v224\n"
"v_mfma_f32_16x16x32_bf16 a[160:163], a[164:167], v[216:219], a[160:163]\n"
"ds_read_b64_tr_b16 a[142:143], v242 offset:34560\n"
"v_mov_b32 v225, v224\n"
"v_mov_b32 v226, v224\n"
"v_mov_b32 v227, v224\n"
"v_mfma_f32_16x16x32_bf16 v[192:195], a[80:83], a[16:19], 0\n"
"ds_read_b128 a[80:83], v240 offset:0\n"
"v_exp_f32 v168, v168\n"
"v_accvgpr_read_b32 v255, a148\n"
"v_mfma_f32_16x16x32_bf16 v[196:199], a[96:99], a[16:19], 0\n"
"ds_read_b128 a[96:99], v240 offset:1024\n"
"v_exp_f32 v169, v169\n"
"v_cmp_nge_f32 vcc, s53, v255\n"
"v_mfma_f32_16x16x32_bf16 v[192:195], a[84:87], a[36:39], v[192:195]\n"
"ds_read_b128 a[84:87], v240 offset:256\n"
"v_exp_f32 v170, v170\n"
"v_mfma_f32_16x16x32_bf16 v[196:199], a[100:103], a[36:39], v[196:199]\n"
"ds_read_b128 a[100:103], v240 offset:1280\n"
"v_exp_f32 v171, v171\n"
"v_cvt_pk_bf16_f32 v204, v168, v169\n"
"v_mfma_f32_16x16x32_bf16 v[192:195], a[88:91], a[56:59], v[192:195]\n"
"ds_read_b128 a[88:91], v240 offset:512\n"
"v_exp_f32 v172, v172\n"
"v_mfma_f32_16x16x32_bf16 v[196:199], a[104:107], a[56:59], v[196:199]\n"
"ds_read_b128 a[104:107], v240 offset:1536\n"
"v_exp_f32 v173, v173\n"
"v_cvt_pk_bf16_f32 v205, v170, v171\n"
"v_mfma_f32_16x16x32_bf16 v[192:195], a[92:95], a[76:79], v[192:195]\n"
"ds_read_b128 a[92:95], v240 offset:768\n"
"v_exp_f32 v174, v174\n"
"v_mfma_f32_16x16x32_bf16 v[196:199], a[108:111], a[76:79], v[196:199]\n"
"s_waitcnt lgkmcnt(7)\n"
"ds_read_b128 a[108:111], v240 offset:1792\n"
"v_exp_f32 v175, v175\n"
"v_mfma_f32_16x16x32_bf16 v[0:3], a[112:115], v[200:203], 0\n"
"v_cvt_pk_bf16_f32 v206, v172, v173\n"
"v_cvt_pk_bf16_f32 v207, v174, v175\n"
"v_rcp_f32 v255, v255\n"
"v_mfma_f32_16x16x32_bf16 v[20:23], a[116:119], v[200:203], 0\n"
"s_or_b64 s[58:59], s[58:59], vcc\n"
"v_mul_f32 v4, v255, v4\n"
"v_mul_f32 v5, v255, v5\n"
"v_mul_f32 v6, v255, v6\n"
"v_mfma_f32_16x16x32_bf16 v[40:43], a[120:123], v[200:203], 0\n"
"v_mul_f32 v7, v255, v7\n"
"v_mul_f32 v24, v255, v24\n"
"v_mul_f32 v25, v255, v25\n"
"v_mfma_f32_16x16x32_bf16 v[60:63], a[124:127], v[200:203], 0\n"
"v_mul_f32 v26, v255, v26\n"
"v_mul_f32 v27, v255, v27\n"
"v_cvt_pk_bf16_f32 v4, v4, v5\n"
"v_mfma_f32_16x16x32_bf16 v[80:83], a[128:131], v[200:203], 0\n"
"v_cvt_pk_bf16_f32 v5, v6, v7\n"
"v_cvt_pk_bf16_f32 v6, v24, v25\n"
"v_cvt_pk_bf16_f32 v7, v26, v27\n"
"v_mfma_f32_16x16x32_bf16 v[100:103], a[132:135], v[200:203], 0\n"
"v_accvgpr_write_b32 a172, v4\n"
"v_accvgpr_write_b32 a173, v5\n"
"v_accvgpr_write_b32 a174, v6\n"
"v_mfma_f32_16x16x32_bf16 v[120:123], a[136:139], v[200:203], 0\n"
"v_accvgpr_write_b32 a175, v7\n"
"v_mul_f32 v44, v255, v44\n"
"v_mul_f32 v45, v255, v45\n"
"v_mfma_f32_16x16x32_bf16 v[140:143], a[140:143], v[200:203], 0\n"
"s_waitcnt lgkmcnt(0)\n"
"s_barrier\n"
"v_mul_f32 v46, v255, v46\n"
"v_mul_f32 v47, v255, v47\n"
"v_mul_f32 v64, v255, v64\n"
"v_mul_f32 v65, v255, v65\n"
"v_pk_mul_f32 v[66:67], v[66:67], v[254:255] op_sel:[0,1] op_sel_hi:[1,1]\n"
"v_cvt_pk_bf16_f32 v44, v44, v45\n"
"v_cvt_pk_bf16_f32 v45, v46, v47\n"
"v_cvt_pk_bf16_f32 v46, v64, v65\n"
"v_cvt_pk_bf16_f32 v47, v66, v67\n"
"v_accvgpr_write_b32 a192, v44\n"
"v_accvgpr_write_b32 a193, v45\n"
"v_accvgpr_write_b32 a194, v46\n"
"v_accvgpr_write_b32 a195, v47\n"
"v_pk_mul_f32 v[84:85], v[84:85], v[254:255] op_sel:[0,1] op_sel_hi:[1,1]\n"
"v_pk_mul_f32 v[86:87], v[86:87], v[254:255] op_sel:[0,1] op_sel_hi:[1,1]\n"
"v_pk_mul_f32 v[104:105], v[104:105], v[254:255] op_sel:[0,1] op_sel_hi:[1,1]\n"
"v_pk_mul_f32 v[106:107], v[106:107], v[254:255] op_sel:[0,1] op_sel_hi:[1,1]\n"
"v_cvt_pk_bf16_f32 v84, v84, v85\n"
"v_cvt_pk_bf16_f32 v85, v86, v87\n"
"v_cvt_pk_bf16_f32 v86, v104, v105\n"
"v_cvt_pk_bf16_f32 v87, v106, v107\n"
"v_accvgpr_write_b32 a212, v84\n"
"v_accvgpr_write_b32 a213, v85\n"
"v_accvgpr_write_b32 a214, v86\n"
"v_accvgpr_write_b32 a215, v87\n"
"v_pk_mul_f32 v[124:125], v[124:125], v[254:255] op_sel:[0,1] op_sel_hi:[1,1]\n"
"v_pk_mul_f32 v[126:127], v[126:127], v[254:255] op_sel:[0,1] op_sel_hi:[1,1]\n"
"v_pk_mul_f32 v[144:145], v[144:145], v[254:255] op_sel:[0,1] op_sel_hi:[1,1]\n"
"v_pk_mul_f32 v[146:147], v[146:147], v[254:255] op_sel:[0,1] op_sel_hi:[1,1]\n"
"v_cvt_pk_bf16_f32 v124, v124, v125\n"
"v_cvt_pk_bf16_f32 v125, v126, v127\n"
"v_cvt_pk_bf16_f32 v126, v144, v145\n"
"v_cvt_pk_bf16_f32 v127, v146, v147\n"
"v_accvgpr_write_b32 a232, v124\n"
"v_accvgpr_write_b32 a233, v125\n"
"v_accvgpr_write_b32 a234, v126\n"
"v_accvgpr_write_b32 a235, v127\n"
"v_mfma_f32_16x16x32_bf16 a[144:147], a[164:167], v[200:203], 0\n"
"s_waitcnt lgkmcnt(4)\n"
"s_add_u32 m0, s48, 0\n"
"v_add_u32 v240, 49152, v240\n"
"v_add_u32 v241, 49152, v241\n"
"v_add_u32 v242, 49152, v242\n"
"v_mfma_f32_16x16x32_bf16 v[160:163], a[80:83], a[0:3], v[220:223]\n"
"s_add_u32 s52, s95, 131072\n"
"buffer_load_dwordx4 v244, s[60:63], s95 offen lds\n"
"buffer_load_dwordx4 v244, s[60:63], s95 offen offset:1024 lds\n"
"v_mfma_f32_16x16x32_bf16 v[4:7], a[112:115], v[204:207], 0\n"
"s_add_u32 m0, s48, 8192\n"
"s_add_u32 s95, s95, 8192\n"
"buffer_load_dwordx4 v244, s[60:63], s52 offen lds\n"
"v_max3_f32 v228, v176, v177, v178\n"
"v_mfma_f32_16x16x32_bf16 v[164:167], a[96:99], a[0:3], v[220:223]\n"
"buffer_load_dwordx4 v244, s[60:63], s52 offen offset:1024 lds\n"
"s_and_b32 s95, s95, 131071\n"
"v_max3_f32 v229, v179, v180, v181\n"
"v_max3_f32 v228, v228, v182, v183\n"
"v_mfma_f32_16x16x32_bf16 v[24:27], a[116:119], v[204:207], 0\n"
"s_add_u32 m0, s48, 16384\n"
"s_add_u32 s52, s81, 131072\n"
"v_max_f32 v228, v228, v229\n"
"v_mov_b32 v229, v228\n"
"s_nop 0\n"
"s_nop 0\n"
"v_permlane32_swap_b32 v228, v229\n"
"v_mfma_f32_16x16x32_bf16 v[160:163], a[84:87], a[20:23], v[160:163]\n"
"buffer_load_dwordx4 v243, s[40:43], s81 offen nt lds\n"
"v_max_f32 v228, v228, v229\n"
"v_mov_b32 v229, v228\n"
"s_nop 0\n"
"s_nop 0\n"
"v_permlane16_swap_b32 v228, v229\n"
"v_mfma_f32_16x16x32_bf16 v[44:47], a[120:123], v[204:207], 0\n"
"buffer_load_dwordx4 v243, s[40:43], s81 offen offset:1024 nt lds\n"
"v_max_f32 v229, v228, v229\n"
"v_sub_f32 v228, 0, v229\n"
"v_add_f32 v176, v176, v228\n"
"v_mfma_f32_16x16x32_bf16 v[164:167], a[100:103], a[20:23], v[164:167]\n"
"v_add_f32 v177, v177, v228\n"
"v_add_f32 v178, v178, v228\n"
"v_add_f32 v179, v179, v228\n"
"v_mfma_f32_16x16x32_bf16 v[64:67], a[124:127], v[204:207], 0\n"
"s_waitcnt lgkmcnt(0)\n"
"v_add_f32 v180, v180, v228\n"
"v_add_f32 v181, v181, v228\n"
"v_add_f32 v182, v182, v228\n"
"v_mfma_f32_16x16x32_bf16 v[160:163], a[88:91], a[40:43], v[160:163]\n"
"v_add_f32 v183, v183, v228\n"
"v_mov_b32 v229, v228\n"
"v_mov_b32 v230, v228\n"
"v_mfma_f32_16x16x32_bf16 v[84:87], a[128:131], v[204:207], 0\n"
"v_mov_b32 v231, v228\n"
"v_exp_f32 v176, v176\n"
"v_accvgpr_read_b32 v255, a152\n"
"v_mfma_f32_16x16x32_bf16 v[164:167], a[104:107], a[40:43], v[164:167]\n"
"v_exp_f32 v177, v177\n"
"v_exp_f32 v178, v178\n"
"v_mfma_f32_16x16x32_bf16 v[104:107], a[132:135], v[204:207], 0\n"
"v_exp_f32 v179, v179\n"
"v_cvt_pk_bf16_f32 v208, v176, v177\n"
"v_cmp_nge_f32 vcc, s53, v255\n"
"v_mfma_f32_16x16x32_bf16 v[160:163], a[92:95], a[60:63], v[160:163]\n"
"v_exp_f32 v180, v180\n"
"v_exp_f32 v181, v181\n"
"v_mfma_f32_16x16x32_bf16 v[124:127], a[136:139], v[204:207], 0\n"
"v_cvt_pk_bf16_f32 v209, v178, v179\n"
"v_exp_f32 v182, v182\n"
"v_mfma_f32_16x16x32_bf16 v[164:167], a[108:111], a[60:63], v[164:167]\n"
"v_exp_f32 v183, v183\n"
"v_cvt_pk_bf16_f32 v210, v180, v181\n"
"v_cvt_pk_bf16_f32 v211, v182, v183\n"
"v_mfma_f32_16x16x32_bf16 v[144:147], a[140:143], v[204:207], 0\n"
"v_rcp_f32 v255, v255\n"
"s_or_b64 s[58:59], s[58:59], vcc\n"
"v_mul_f32 v8, v255, v8\n"
"v_mul_f32 v9, v255, v9\n"
"v_pk_mul_f32 v[10:11], v[10:11], v[254:255] op_sel:[0,1] op_sel_hi:[1,1]\n"
"v_pk_mul_f32 v[28:29], v[28:29], v[254:255] op_sel:[0,1] op_sel_hi:[1,1]\n"
"v_pk_mul_f32 v[30:31], v[30:31], v[254:255] op_sel:[0,1] op_sel_hi:[1,1]\n"
"v_cvt_pk_bf16_f32 v8, v8, v9\n"
"v_cvt_pk_bf16_f32 v9, v10, v11\n"
"v_cvt_pk_bf16_f32 v10, v28, v29\n"
"v_cvt_pk_bf16_f32 v11, v30, v31\n"
"v_accvgpr_write_b32 a176, v8\n"
"v_accvgpr_write_b32 a177, v9\n"
"v_accvgpr_write_b32 a178, v10\n"
"v_accvgpr_write_b32 a179, v11\n"
"v_pk_mul_f32 v[48:49], v[48:49], v[254:255] op_sel:[0,1] op_sel_hi:[1,1]\n"
"v_pk_mul_f32 v[50:51], v[50:51], v[254:255] op_sel:[0,1] op_sel_hi:[1,1]\n"
"v_pk_mul_f32 v[68:69], v[68:69], v[254:255] op_sel:[0,1] op_sel_hi:[1,1]\n"
"v_pk_mul_f32 v[70:71], v[70:71], v[254:255] op_sel:[0,1] op_sel_hi:[1,1]\n"
"v_cvt_pk_bf16_f32 v48, v48, v49\n"
"v_cvt_pk_bf16_f32 v49, v50, v51\n"
"v_cvt_pk_bf16_f32 v50, v68, v69\n"
"v_cvt_pk_bf16_f32 v51, v70, v71\n"
"v_accvgpr_write_b32 a196, v48\n"
"v_accvgpr_write_b32 a197, v49\n"
"v_accvgpr_write_b32 a198, v50\n"
"v_accvgpr_write_b32 a199, v51\n"
"v_pk_mul_f32 v[88:89], v[88:89], v[254:255] op_sel:[0,1] op_sel_hi:[1,1]\n"
"v_pk_mul_f32 v[90:91], v[90:91], v[254:255] op_sel:[0,1] op_sel_hi:[1,1]\n"
"v_pk_mul_f32 v[108:109], v[108:109], v[254:255] op_sel:[0,1] op_sel_hi:[1,1]\n"
"v_pk_mul_f32 v[110:111], v[110:111], v[254:255] op_sel:[0,1] op_sel_hi:[1,1]\n"
"v_cvt_pk_bf16_f32 v88, v88, v89\n"
"v_cvt_pk_bf16_f32 v89, v90, v91\n"
"v_cvt_pk_bf16_f32 v90, v108, v109\n"
"v_cvt_pk_bf16_f32 v91, v110, v111\n"
"v_accvgpr_write_b32 a216, v88\n"
"v_accvgpr_write_b32 a217, v89\n"
"v_accvgpr_write_b32 a218, v90\n"
"v_accvgpr_write_b32 a219, v91\n"
"v_pk_mul_f32 v[128:129], v[128:129], v[254:255] op_sel:[0,1] op_sel_hi:[1,1]\n"
"v_pk_mul_f32 v[130:131], v[130:131], v[254:255] op_sel:[0,1] op_sel_hi:[1,1]\n"
"v_pk_mul_f32 v[148:149], v[148:149], v[254:255] op_sel:[0,1] op_sel_hi:[1,1]\n"
"v_pk_mul_f32 v[150:151], v[150:151], v[254:255] op_sel:[0,1] op_sel_hi:[1,1]\n"
"v_cvt_pk_bf16_f32 v128, v128, v129\n"
"v_cvt_pk_bf16_f32 v129, v130, v131\n"
"v_cvt_pk_bf16_f32 v130, v148, v149\n"
"v_cvt_pk_bf16_f32 v131, v150, v151\n"
"v_accvgpr_write_b32 a236, v128\n"
"v_accvgpr_write_b32 a237, v129\n"
"v_accvgpr_write_b32 a238, v130\n"
"v_accvgpr_write_b32 a239, v131\n"
"v_mfma_f32_16x16x32_bf16 a[148:151], a[164:167], v[204:207], 0\n"
"s_add_u32 m0, s48, 24576\n"
"s_add_u32 s81, s81, 8192\n"
"v_max3_f32 v232, v184, v185, v186\n"
"v_max3_f32 v233, v187, v188, v189\n"
"v_max3_f32 v232, v232, v190, v191\n"
"v_mfma_f32_16x16x32_bf16 v[168:171], a[80:83], a[4:7], v[224:227]\n"
"buffer_load_dwordx4 v243, s[40:43], s52 offen nt lds\n"
"v_max_f32 v232, v232, v233\n"
"v_mov_b32 v233, v232\n"
"s_nop 0\n"
"s_nop 0\n"
"v_permlane32_swap_b32 v232, v233\n"
"v_mfma_f32_16x16x32_bf16 v[8:11], a[112:115], v[208:211], 0\n"
"buffer_load_dwordx4 v243, s[40:43], s52 offen offset:1024 nt lds\n"
"v_max_f32 v232, v232, v233\n"
"v_mov_b32 v233, v232\n"
"s_nop 0\n"
"s_nop 0\n"
"v_permlane16_swap_b32 v232, v233\n"
"v_mfma_f32_16x16x32_bf16 v[172:175], a[96:99], a[4:7], v[224:227]\n"
"s_and_b32 s81, s81, 131071\n"
"v_max_f32 v233, v232, v233\n"
"v_sub_f32 v232, 0, v233\n"
"v_add_f32 v184, v184, v232\n"
"v_mfma_f32_16x16x32_bf16 v[28:31], a[116:119], v[208:211], 0\n"
"v_add_f32 v185, v185, v232\n"
"v_add_f32 v186, v186, v232\n"
"v_add_f32 v187, v187, v232\n"
"v_mfma_f32_16x16x32_bf16 v[168:171], a[84:87], a[24:27], v[168:171]\n"
"v_add_f32 v188, v188, v232\n"
"v_add_f32 v189, v189, v232\n"
"v_add_f32 v190, v190, v232\n"
"v_mfma_f32_16x16x32_bf16 v[48:51], a[120:123], v[208:211], 0\n"
"v_add_f32 v191, v191, v232\n"
"v_mov_b32 v233, v232\n"
"v_mov_b32 v234, v232\n"
"v_mfma_f32_16x16x32_bf16 v[172:175], a[100:103], a[24:27], v[172:175]\n"
"v_mov_b32 v235, v232\n"
"v_exp_f32 v184, v184\n"
"v_accvgpr_read_b32 v255, a156\n"
"v_mfma_f32_16x16x32_bf16 v[68:71], a[124:127], v[208:211], 0\n"
"v_exp_f32 v185, v185\n"
"v_exp_f32 v186, v186\n"
"v_mfma_f32_16x16x32_bf16 v[168:171], a[88:91], a[44:47], v[168:171]\n"
"v_exp_f32 v187, v187\n"
"v_cvt_pk_bf16_f32 v212, v184, v185\n"
"v_cmp_nge_f32 vcc, s53, v255\n"
"v_mfma_f32_16x16x32_bf16 v[88:91], a[128:131], v[208:211], 0\n"
"v_exp_f32 v188, v188\n"
"v_exp_f32 v189, v189\n"
"v_mfma_f32_16x16x32_bf16 v[172:175], a[104:107], a[44:47], v[172:175]\n"
"v_cvt_pk_bf16_f32 v213, v186, v187\n"
"v_exp_f32 v190, v190\n"
"v_mfma_f32_16x16x32_bf16 v[108:111], a[132:135], v[208:211], 0\n"
"v_exp_f32 v191, v191\n"
"v_cvt_pk_bf16_f32 v214, v188, v189\n"
"v_cvt_pk_bf16_f32 v215, v190, v191\n"
"v_mfma_f32_16x16x32_bf16 v[168:171], a[92:95], a[64:67], v[168:171]\n"
"s_add_u32 m0, s48, 32768\n"
"v_rcp_f32 v255, v255\n"
"s_or_b64 s[58:59], s[58:59], vcc\n"
"v_mul_f32 v12, v255, v12\n"
"v_mfma_f32_16x16x32_bf16 v[128:131], a[136:139], v[208:211], 0\n"
"s_add_u32 s52, s95, 131072\n"
"v_mul_f32 v13, v255, v13\n"
"v_mul_f32 v14, v255, v14\n"
"v_mul_f32 v15, v255, v15\n"
"v_mfma_f32_16x16x32_bf16 v[172:175], a[108:111], a[64:67], v[172:175]\n"
"buffer_load_dwordx4 v244, s[60:63], s95 offen lds\n"
"v_mul_f32 v32, v255, v32\n"
"v_mul_f32 v33, v255, v33\n"
"v_mul_f32 v34, v255, v34\n"
"v_mfma_f32_16x16x32_bf16 v[148:151], a[140:143], v[208:211], 0\n"
"v_mul_f32 v35, v255, v35\n"
"v_cvt_pk_bf16_f32 v12, v12, v13\n"
"v_cvt_pk_bf16_f32 v13, v14, v15\n"
"v_cvt_pk_bf16_f32 v14, v32, v33\n"
"v_cvt_pk_bf16_f32 v15, v34, v35\n"
"v_accvgpr_write_b32 a180, v12\n"
"v_accvgpr_write_b32 a181, v13\n"
"v_accvgpr_write_b32 a182, v14\n"
"v_accvgpr_write_b32 a183, v15\n"
"v_pk_mul_f32 v[52:53], v[52:53], v[254:255] op_sel:[0,1] op_sel_hi:[1,1]\n"
"v_pk_mul_f32 v[54:55], v[54:55], v[254:255] op_sel:[0,1] op_sel_hi:[1,1]\n"
"v_pk_mul_f32 v[72:73], v[72:73], v[254:255] op_sel:[0,1] op_sel_hi:[1,1]\n"
"v_pk_mul_f32 v[74:75], v[74:75], v[254:255] op_sel:[0,1] op_sel_hi:[1,1]\n"
"v_cvt_pk_bf16_f32 v52, v52, v53\n"
"v_cvt_pk_bf16_f32 v53, v54, v55\n"
"v_cvt_pk_bf16_f32 v54, v72, v73\n"
"v_cvt_pk_bf16_f32 v55, v74, v75\n"
"v_accvgpr_write_b32 a200, v52\n"
"v_accvgpr_write_b32 a201, v53\n"
"v_accvgpr_write_b32 a202, v54\n"
"v_accvgpr_write_b32 a203, v55\n"
"v_pk_mul_f32 v[92:93], v[92:93], v[254:255] op_sel:[0,1] op_sel_hi:[1,1]\n"
"v_pk_mul_f32 v[94:95], v[94:95], v[254:255] op_sel:[0,1] op_sel_hi:[1,1]\n"
"v_pk_mul_f32 v[112:113], v[112:113], v[254:255] op_sel:[0,1] op_sel_hi:[1,1]\n"
"v_pk_mul_f32 v[114:115], v[114:115], v[254:255] op_sel:[0,1] op_sel_hi:[1,1]\n"
"v_cvt_pk_bf16_f32 v92, v92, v93\n"
"v_cvt_pk_bf16_f32 v93, v94, v95\n"
"v_cvt_pk_bf16_f32 v94, v112, v113\n"
"v_cvt_pk_bf16_f32 v95, v114, v115\n"
"v_accvgpr_write_b32 a220, v92\n"
"v_accvgpr_write_b32 a221, v93\n"
"v_accvgpr_write_b32 a222, v94\n"
"v_accvgpr_write_b32 a223, v95\n"
"v_pk_mul_f32 v[132:133], v[132:133], v[254:255] op_sel:[0,1] op_sel_hi:[1,1]\n"
"v_pk_mul_f32 v[134:135], v[134:135], v[254:255] op_sel:[0,1] op_sel_hi:[1,1]\n"
"v_pk_mul_f32 v[152:153], v[152:153], v[254:255] op_sel:[0,1] op_sel_hi:[1,1]\n"
"v_pk_mul_f32 v[154:155], v[154:155], v[254:255] op_sel:[0,1] op_sel_hi:[1,1]\n"
"v_cvt_pk_bf16_f32 v132, v132, v133\n"
"v_cvt_pk_bf16_f32 v133, v134, v135\n"
"v_cvt_pk_bf16_f32 v134, v152, v153\n"
"v_cvt_pk_bf16_f32 v135, v154, v155\n"
"v_accvgpr_write_b32 a240, v132\n"
"v_accvgpr_write_b32 a241, v133\n"
"v_accvgpr_write_b32 a242, v134\n"
"v_accvgpr_write_b32 a243, v135\n"
"v_mfma_f32_16x16x32_bf16 a[152:155], a[164:167], v[208:211], 0\n"
"buffer_load_dwordx4 v244, s[60:63], s95 offen offset:1024 lds\n"
"v_max3_f32 v236, v192, v193, v194\n"
"v_max3_f32 v237, v195, v196, v197\n"
"v_max3_f32 v236, v236, v198, v199\n"
"v_mfma_f32_16x16x32_bf16 v[176:179], a[80:83], a[8:11], v[228:231]\n"
"v_max_f32 v236, v236, v237\n"
"v_mov_b32 v237, v236\n"
"s_nop 0\n"
"s_nop 0\n"
"v_permlane32_swap_b32 v236, v237\n"
"v_mfma_f32_16x16x32_bf16 v[12:15], a[112:115], v[212:215], 0\n"
"v_max_f32 v236, v236, v237\n"
"v_mov_b32 v237, v236\n"
"s_nop 0\n"
"s_nop 0\n"
"v_permlane16_swap_b32 v236, v237\n"
"v_mfma_f32_16x16x32_bf16 v[180:183], a[96:99], a[8:11], v[228:231]\n"
"v_max_f32 v237, v236, v237\n"
"v_sub_f32 v236, 0, v237\n"
"v_add_f32 v192, v192, v236\n"
"v_mfma_f32_16x16x32_bf16 v[32:35], a[116:119], v[212:215], 0\n"
"v_add_f32 v193, v193, v236\n"
"v_add_f32 v194, v194, v236\n"
"v_add_f32 v195, v195, v236\n"
"v_mfma_f32_16x16x32_bf16 v[176:179], a[84:87], a[28:31], v[176:179]\n"
"v_add_f32 v196, v196, v236\n"
"v_add_f32 v197, v197, v236\n"
"v_add_f32 v198, v198, v236\n"
"v_mfma_f32_16x16x32_bf16 v[52:55], a[120:123], v[212:215], 0\n"
"v_add_f32 v199, v199, v236\n"
"v_mov_b32 v237, v236\n"
"v_mov_b32 v238, v236\n"
"v_mfma_f32_16x16x32_bf16 v[180:183], a[100:103], a[28:31], v[180:183]\n"
"v_mov_b32 v239, v236\n"
"v_exp_f32 v192, v192\n"
"v_accvgpr_read_b32 v255, a160\n"
"v_mfma_f32_16x16x32_bf16 v[72:75], a[124:127], v[212:215], 0\n"
"v_exp_f32 v193, v193\n"
"v_exp_f32 v194, v194\n"
"v_mfma_f32_16x16x32_bf16 v[176:179], a[88:91], a[48:51], v[176:179]\n"
"v_exp_f32 v195, v195\n"
"v_cvt_pk_bf16_f32 v216, v192, v193\n"
"v_cmp_nge_f32 vcc, s53, v255\n"
"v_mfma_f32_16x16x32_bf16 v[92:95], a[128:131], v[212:215], 0\n"
"s_add_u32 m0, s48, 40960\n"
"v_exp_f32 v196, v196\n"
"v_exp_f32 v197, v197\n"
"v_mfma_f32_16x16x32_bf16 v[180:183], a[104:107], a[48:51], v[180:183]\n"
"s_add_u32 s95, s95, 8192\n"
"v_cvt_pk_bf16_f32 v217, v194, v195\n"
"v_exp_f32 v198, v198\n"
"v_mfma_f32_16x16x32_bf16 v[112:115], a[132:135], v[212:215], 0\n"
"v_exp_f32 v199, v199\n"
"v_cvt_pk_bf16_f32 v218, v196, v197\n"
"v_cvt_pk_bf16_f32 v219, v198, v199\n"
"v_mfma_f32_16x16x32_bf16 v[176:179], a[92:95], a[68:71], v[176:179]\n"
"v_exp_f32 v160, v160\n"
"v_exp_f32 v161, v161\n"
"v_mfma_f32_16x16x32_bf16 v[132:135], a[136:139], v[212:215], 0\n"
"v_exp_f32 v162, v162\n"
"v_exp_f32 v163, v163\n"
"v_mfma_f32_16x16x32_bf16 v[180:183], a[108:111], a[68:71], v[180:183]\n"
"v_cvt_pk_bf16_f32 v200, v160, v161\n"
"v_exp_f32 v164, v164\n"
"v_exp_f32 v165, v165\n"
"v_cvt_pk_bf16_f32 v201, v162, v163\n"
"v_exp_f32 v166, v166\n"
"v_exp_f32 v167, v167\n"
"v_cvt_pk_bf16_f32 v202, v164, v165\n"
"v_cvt_pk_bf16_f32 v203, v166, v167\n"
"v_mfma_f32_16x16x32_bf16 v[152:155], a[140:143], v[212:215], 0\n"
"buffer_load_dwordx4 v244, s[60:63], s52 offen lds\n"
"buffer_load_dwordx4 v244, s[60:63], s52 offen offset:1024 lds\n"
"s_and_b32 s95, s95, 131071\n"
"v_add_u32 v241, 49152, v241\n"
"v_add_u32 v242, 49152, v242\n"
"v_rcp_f32 v255, v255\n"
"s_or_b64 s[58:59], s[58:59], vcc\n"
"v_pk_mul_f32 v[16:17], v[16:17], v[254:255] op_sel:[0,1] op_sel_hi:[1,1]\n"
"v_pk_mul_f32 v[18:19], v[18:19], v[254:255] op_sel:[0,1] op_sel_hi:[1,1]\n"
"v_pk_mul_f32 v[36:37], v[36:37], v[254:255] op_sel:[0,1] op_sel_hi:[1,1]\n"
"v_pk_mul_f32 v[38:39], v[38:39], v[254:255] op_sel:[0,1] op_sel_hi:[1,1]\n"
"v_cvt_pk_bf16_f32 v16, v16, v17\n"
"v_cvt_pk_bf16_f32 v17, v18, v19\n"
"v_cvt_pk_bf16_f32 v18, v36, v37\n"
"v_cvt_pk_bf16_f32 v19, v38, v39\n"
"v_accvgpr_write_b32 a184, v16\n"
"v_accvgpr_write_b32 a185, v17\n"
"v_accvgpr_write_b32 a186, v18\n"
"v_accvgpr_write_b32 a187, v19\n"
"v_pk_mul_f32 v[56:57], v[56:57], v[254:255] op_sel:[0,1] op_sel_hi:[1,1]\n"
"v_pk_mul_f32 v[58:59], v[58:59], v[254:255] op_sel:[0,1] op_sel_hi:[1,1]\n"
"v_pk_mul_f32 v[76:77], v[76:77], v[254:255] op_sel:[0,1] op_sel_hi:[1,1]\n"
"v_pk_mul_f32 v[78:79], v[78:79], v[254:255] op_sel:[0,1] op_sel_hi:[1,1]\n"
"v_cvt_pk_bf16_f32 v56, v56, v57\n"
"v_cvt_pk_bf16_f32 v57, v58, v59\n"
"v_cvt_pk_bf16_f32 v58, v76, v77\n"
"v_cvt_pk_bf16_f32 v59, v78, v79\n"
"v_accvgpr_write_b32 a204, v56\n"
"v_accvgpr_write_b32 a205, v57\n"
"v_accvgpr_write_b32 a206, v58\n"
"v_accvgpr_write_b32 a207, v59\n"
"v_pk_mul_f32 v[96:97], v[96:97], v[254:255] op_sel:[0,1] op_sel_hi:[1,1]\n"
"v_pk_mul_f32 v[98:99], v[98:99], v[254:255] op_sel:[0,1] op_sel_hi:[1,1]\n"
"v_pk_mul_f32 v[116:117], v[116:117], v[254:255] op_sel:[0,1] op_sel_hi:[1,1]\n"
"v_pk_mul_f32 v[118:119], v[118:119], v[254:255] op_sel:[0,1] op_sel_hi:[1,1]\n"
"v_cvt_pk_bf16_f32 v96, v96, v97\n"
"v_cvt_pk_bf16_f32 v97, v98, v99\n"
"v_cvt_pk_bf16_f32 v98, v116, v117\n"
"v_cvt_pk_bf16_f32 v99, v118, v119\n"
"v_accvgpr_write_b32 a224, v96\n"
"v_accvgpr_write_b32 a225, v97\n"
"v_accvgpr_write_b32 a226, v98\n"
"v_accvgpr_write_b32 a227, v99\n"
"v_pk_mul_f32 v[136:137], v[136:137], v[254:255] op_sel:[0,1] op_sel_hi:[1,1]\n"
"v_pk_mul_f32 v[138:139], v[138:139], v[254:255] op_sel:[0,1] op_sel_hi:[1,1]\n"
"v_pk_mul_f32 v[156:157], v[156:157], v[254:255] op_sel:[0,1] op_sel_hi:[1,1]\n"
"v_pk_mul_f32 v[158:159], v[158:159], v[254:255] op_sel:[0,1] op_sel_hi:[1,1]\n"
"v_cvt_pk_bf16_f32 v136, v136, v137\n"
"v_cvt_pk_bf16_f32 v137, v138, v139\n"
"v_cvt_pk_bf16_f32 v138, v156, v157\n"
"v_cvt_pk_bf16_f32 v139, v158, v159\n"
"v_accvgpr_write_b32 a244, v136\n"
"v_accvgpr_write_b32 a245, v137\n"
"v_accvgpr_write_b32 a246, v138\n"
"v_accvgpr_write_b32 a247, v139\n"
"v_mfma_f32_16x16x32_bf16 a[156:159], a[164:167], v[212:215], 0\n"
"s_waitcnt vmcnt(16) lgkmcnt(0)\n"
"s_barrier\n"
"v_mfma_f32_16x16x32_bf16 v[184:187], a[80:83], a[12:15], v[232:235]\n"
"v_mfma_f32_16x16x32_bf16 v[16:19], a[112:115], v[216:219], 0\n"
"ds_read_b64_tr_b16 a[112:113], v241 offset:24832\n"
"v_mfma_f32_16x16x32_bf16 v[188:191], a[96:99], a[12:15], v[232:235]\n"
"ds_read_b64_tr_b16 a[114:115], v241 offset:25856\n"
"v_mfma_f32_16x16x32_bf16 v[36:39], a[116:119], v[216:219], 0\n"
"ds_read_b64_tr_b16 a[116:117], v242 offset:24832\n"
"v_mfma_f32_16x16x32_bf16 v[184:187], a[84:87], a[32:35], v[184:187]\n"
"ds_read_b64_tr_b16 a[118:119], v242 offset:25856\n"
"v_mfma_f32_16x16x32_bf16 v[56:59], a[120:123], v[216:219], 0\n"
"ds_read_b64_tr_b16 a[120:121], v241 offset:25088\n"
"v_mfma_f32_16x16x32_bf16 v[188:191], a[100:103], a[32:35], v[188:191]\n"
"ds_read_b64_tr_b16 a[122:123], v241 offset:26112\n"
"v_mfma_f32_16x16x32_bf16 v[76:79], a[124:127], v[216:219], 0\n"
"ds_read_b64_tr_b16 a[124:125], v242 offset:25088\n"
"v_mfma_f32_16x16x32_bf16 v[184:187], a[88:91], a[52:55], v[184:187]\n"
"ds_read_b64_tr_b16 a[126:127], v242 offset:26112\n"
"v_mfma_f32_16x16x32_bf16 v[96:99], a[128:131], v[216:219], 0\n"
"ds_read_b64_tr_b16 a[128:129], v241 offset:25344\n"
"v_mfma_f32_16x16x32_bf16 v[188:191], a[104:107], a[52:55], v[188:191]\n"
"ds_read_b64_tr_b16 a[130:131], v241 offset:26368\n"
"v_mfma_f32_16x16x32_bf16 v[116:119], a[132:135], v[216:219], 0\n"
"ds_read_b64_tr_b16 a[132:133], v242 offset:25344\n"
"v_mfma_f32_16x16x32_bf16 v[184:187], a[92:95], a[72:75], v[184:187]\n"
"ds_read_b64_tr_b16 a[134:135], v242 offset:26368\n"
"v_mfma_f32_16x16x32_bf16 v[136:139], a[136:139], v[216:219], 0\n"
"ds_read_b64_tr_b16 a[136:137], v241 offset:25600\n"
"v_mfma_f32_16x16x32_bf16 v[188:191], a[108:111], a[72:75], v[188:191]\n"
"ds_read_b64_tr_b16 a[138:139], v241 offset:26624\n"
"v_mfma_f32_16x16x32_bf16 v[156:159], a[140:143], v[216:219], 0\n"
"ds_read_b64_tr_b16 a[140:141], v242 offset:25600\n"
"v_mfma_f32_16x16x32_bf16 a[160:163], a[164:167], v[216:219], 0\n"
"ds_read_b64_tr_b16 a[142:143], v242 offset:26624\n"
"s_branch L_ac_%=\n"
"L_sp0_%=:\n"
"s_cmp_lg_u32 s69, 0\n"
"s_cbranch_scc1 L_ok0_%=\n"
"s_mov_b32 s78, 0\n"
"L_sl0_%=:\n"
"s_sleep 1\n"
"global_load_dwordx2 v[254:255], v252, s[60:61] sc1\n"
"s_waitcnt vmcnt(0)\n"
"v_cmp_gt_i32 s[76:77], 0, v255\n"
"v_and_b32 v255, 2147483647, v255\n"
"v_cmp_eq_u64 vcc, s[62:63], v[254:255]\n"
"s_and_b32 s59, s76, 31\n"
"s_nop 0\n"
"s_and_b32 s77, vcc_lo, 31\n"
"s_cmp_eq_u32 s77, 31\n"
"s_cbranch_scc1 L_ok0_%=\n"
"s_add_u32 s78, s78, 1\n"
"s_cmp_lt_u32 s78, 60000\n"
"s_cbranch_scc1 L_sl0_%=\n"
"s_mov_b32 s69, 2\n"
"s_branch L_ok0_%=\n"
"L_sp1_%=:\n"
"s_cmp_lg_u32 s69, 0\n"
"s_cbranch_scc1 L_ok1_%=\n"
"s_mov_b32 s78, 0\n"
"L_sl1_%=:\n"
"s_sleep 1\n"
"global_load_dwordx2 v[254:255], v252, s[60:61] sc1\n"
"s_waitcnt vmcnt(0)\n"
"v_cmp_gt_i32 s[76:77], 0, v255\n"
"v_and_b32 v255, 2147483647, v255\n"
"v_cmp_eq_u64 vcc, s[62:63], v[254:255]\n"
"s_and_b32 s59, s76, 96\n"
"s_nop 0\n"
"s_and_b32 s77, vcc_lo, 96\n"
"s_cmp_eq_u32 s77, 96\n"
"s_cbranch_scc1 L_ok1_%=\n"
"s_add_u32 s78, s78, 1\n"
"s_cmp_lt_u32 s78, 60000\n"
"s_cbranch_scc1 L_sl1_%=\n"
"s_mov_b32 s69, 2\n"
"s_branch L_ok1_%=\n"
"L_lsp_%=:\n"
"s_mov_b32 s78, 0\n"
"s_cmp_lg_u32 s69, 0\n"
"s_cbranch_scc1 L_lgo_%=\n"
"L_lsl_%=:\n"
"s_sleep 3\n"
"global_load_dwordx2 v[190:191], v252, s[60:61] sc1\n"
"s_waitcnt vmcnt(2)\n"
"v_cmp_gt_i32 s[76:77], 0, v193\n"
"v_and_b32 v193, 2147483647, v193\n"
"v_cmp_eq_u64 vcc, s[62:63], v[192:193]\n"
"s_and_b32 s59, s76, 65280\n"
"s_nop 0\n"
"s_and_b32 s77, vcc_lo, 65280\n"
"s_cmp_eq_u32 s77, 65280\n"
"s_cbranch_scc1 L_lgo_%=\n"
"s_sleep 3\n"
"global_load_dwordx2 v[192:193], v252, s[60:61] sc1\n"
"s_waitcnt vmcnt(2)\n"
"v_cmp_gt_i32 s[76:77], 0, v195\n"
"v_and_b32 v195, 2147483647, v195\n"
"v_cmp_eq_u64 vcc, s[62:63], v[194:195]\n"
"s_and_b32 s59, s76, 65280\n"
"s_nop 0\n"
"s_and_b32 s77, vcc_lo, 65280\n"
"s_cmp_eq_u32 s77, 65280\n"
"s_cbranch_scc1 L_lgo_%=\n"
"s_sleep 3\n"
"global_load_dwordx2 v[194:195], v252, s[60:61] sc1\n"
"s_waitcnt vmcnt(2)\n"
"v_cmp_gt_i32 s[76:77], 0, v191\n"
"v_and_b32 v191, 2147483647, v191\n"
"v_cmp_eq_u64 vcc, s[62:63], v[190:191]\n"
"s_and_b32 s59, s76, 65280\n"
"s_nop 0\n"
"s_and_b32 s77, vcc_lo, 65280\n"
"s_cmp_eq_u32 s77, 65280\n"
"s_cbranch_scc1 L_lgo_%=\n"
"s_add_u32 s78, s78, 1\n"
"s_cmp_lt_u32 s78, 40000\n"
"s_cbranch_scc1 L_lsl_%=\n"
"s_mov_b32 s69, 2\n"
"s_branch L_lgo_%=\n"
"L_t1_%=:\n"
"s_waitcnt vmcnt(12) lgkmcnt(0)\n"
"s_barrier\n"
"v_add_u32 v241, 49152, v241\n"
"v_add_u32 v242, 49152, v242\n"
"v_mfma_f32_16x16x32_bf16 v[184:187], a[80:83], a[12:15], v[232:235]\n"
"s_lshl_b32 s79, %[hg], 2\n"
"s_add_u32 s79, s79, 4\n"
"s_mov_b32 s72, 1985229328\n"
"v_mfma_f32_16x16x32_bf16 v[16:19], a[112:115], v[216:219], v[16:19]\n"
"ds_read_b64_tr_b16 a[112:113], v241 offset:20608\n"
"s_mov_b32 s73, 1985229328\n"
"v_mfma_f32_16x16x32_bf16 v[188:191], a[96:99], a[12:15], v[232:235]\n"
"ds_read_b64_tr_b16 a[114:115], v241 offset:21632\n"
"s_lshr_b64 s[72:73], s[72:73], s79\n"
"v_mfma_f32_16x16x32_bf16 v[36:39], a[116:119], v[216:219], v[36:39]\n"
"ds_read_b64_tr_b16 a[116:117], v242 offset:20608\n"
"s_lshl_b32 s73, %[hg], 4\n"
"v_mfma_f32_16x16x32_bf16 v[184:187], a[84:87], a[32:35], v[184:187]\n"
"ds_read_b64_tr_b16 a[118:119], v242 offset:21632\n"
"s_or_b32 s73, s73, 9\n"
"v_mfma_f32_16x16x32_bf16 v[56:59], a[120:123], v[216:219], v[56:59]\n"
"ds_read_b64_tr_b16 a[120:121], v241 offset:20864\n"
"s_and_b32 s72, s72, 268435455\n"
"v_mfma_f32_16x16x32_bf16 v[188:191], a[100:103], a[32:35], v[188:191]\n"
"ds_read_b64_tr_b16 a[122:123], v241 offset:21888\n"
"s_or_b32 s72, s72, 2147483648\n"
"v_mfma_f32_16x16x32_bf16 v[76:79], a[124:127], v[216:219], v[76:79]\n"
"ds_read_b64_tr_b16 a[124:125], v242 offset:20864\n"
"s_and_b32 s56, s72, 15\n"
"v_mfma_f32_16x16x32_bf16 v[184:187], a[88:91], a[52:55], v[184:187]\n"
"ds_read_b64_tr_b16 a[126:127], v242 offset:21888\n"
"s_lshl_b32 s56, s56, 7\n"
"v_mfma_f32_16x16x32_bf16 v[96:99], a[128:131], v[216:219], v[96:99]\n"
"ds_read_b64_tr_b16 a[128:129], v241 offset:21120\n"
"s_mov_b64 s[74:75], s[72:73]\n"
"v_mfma_f32_16x16x32_bf16 v[188:191], a[104:107], a[52:55], v[188:191]\n"
"ds_read_b64_tr_b16 a[130:131], v241 offset:22144\n"
"s_load_dwordx4 s[96:99], %[kp], 40\n"
"v_mfma_f32_16x16x32_bf16 v[116:119], a[132:135], v[216:219], v[116:119]\n"
"ds_read_b64_tr_b16 a[132:133], v242 offset:21120\n"
"s_mul_i32 s70, %[wv], 5120\n"
"v_mfma_f32_16x16x32_bf16 v[184:187], a[92:95], a[72:75], v[184:187]\n"
"ds_read_b64_tr_b16 a[134:135], v242 offset:22144\n"
"s_mul_i32 s68, %[hg], 145152\n"
"v_mfma_f32_16x16x32_bf16 v[136:139], a[136:139], v[216:219], v[136:139]\n"
"ds_read_b64_tr_b16 a[136:137], v241 offset:21376\n"
"s_add_u32 s68, s68, s70\n"
"v_mfma_f32_16x16x32_bf16 v[188:191], a[108:111], a[72:75], v[188:191]\n"
"ds_read_b64_tr_b16 a[138:139], v241 offset:22400\n"
"s_mov_b32 s69, 0\n"
"v_mfma_f32_16x16x32_bf16 v[156:159], a[140:143], v[216:219], v[156:159]\n"
"s_mov_b32 s79, 0\n"
"s_lshr_b32 s92, %[rr], 2\n"
"s_lshl_b32 s92, s92, 2\n"
"v_mfma_f32_16x16x32_bf16 a[160:163], a[164:167], v[216:219], a[160:163]\n"
"s_add_u32 s92, s92, %[wv]\n"
"s_mul_i32 s92, s92, 20480\n"
"v_bfe_u32 v245, v253, 7, 4\n"
"v_mul_u32_u24 v245, 1280, v245\n"
"v_mfma_f32_16x16x32_bf16 v[192:195], a[80:83], a[16:19], v[236:239]\n"
"s_waitcnt lgkmcnt(10)\n"
"ds_read_b64_tr_b16 a[140:141], v242 offset:21376\n"
"ds_read_b64_tr_b16 a[142:143], v242 offset:22400\n"
"s_bfe_u32 s52, s72, 262144\n"
"s_lshl_b32 s52, s52, 7\n"
"s_add_u32 s52, s52, s92\n"
"s_mov_b32 exec_lo, 65535\n"
"s_mov_b32 exec_hi, 0\n"
"buffer_load_dword a248, v245, s[64:67], s52 offen\n"
"s_mov_b64 exec, -1\n"
"s_bfe_u32 s52, s72, 262148\n"
"s_lshl_b32 s52, s52, 7\n"
"s_add_u32 s52, s52, s92\n"
"s_mov_b32 exec_lo, 65535\n"
"s_mov_b32 exec_hi, 0\n"
"buffer_load_dword a248, v245, s[64:67], s52 offen\n"
"s_mov_b64 exec, -1\n"
"s_bfe_u32 s52, s72, 262152\n"
"s_lshl_b32 s52, s52, 7\n"
"s_add_u32 s52, s52, s92\n"
"s_mov_b32 exec_lo, 65535\n"
"s_mov_b32 exec_hi, 0\n"
"buffer_load_dword a248, v245, s[64:67], s52 offen\n"
"s_mov_b64 exec, -1\n"
"v_add_u32 v240, 49152, v240\n"
"v_mfma_f32_16x16x32_bf16 v[0:3], a[112:115], v[200:203], v[0:3]\n"
"ds_read_b128 a[80:83], v240 offset:4224\n"
"v_lshrrev_b32 v253, 3, v253\n"
"v_mfma_f32_16x16x32_bf16 v[196:199], a[96:99], a[16:19], v[236:239]\n"
"ds_read_b128 a[96:99], v240 offset:5248\n"
"v_add_u32 v251, s93, v253\n"
"v_mfma_f32_16x16x32_bf16 v[20:23], a[116:119], v[200:203], v[20:23]\n"
"v_exp_f32 v168, v168\n"
"v_mfma_f32_16x16x32_bf16 v[192:195], a[84:87], a[36:39], v[192:195]\n"
"s_waitcnt lgkmcnt(10)\n"
"ds_read_b128 a[84:87], v240 offset:4480\n"
"v_mfma_f32_16x16x32_bf16 v[40:43], a[120:123], v[200:203], v[40:43]\n"
"v_exp_f32 v169, v169\n"
"v_mfma_f32_16x16x32_bf16 v[196:199], a[100:103], a[36:39], v[196:199]\n"
"ds_read_b128 a[100:103], v240 offset:5504\n"
"v_add_u32 v252, s94, v253\n"
"v_mfma_f32_16x16x32_bf16 v[60:63], a[124:127], v[200:203], v[60:63]\n"
"v_exp_f32 v170, v170\n"
"v_mfma_f32_16x16x32_bf16 v[192:195], a[88:91], a[56:59], v[192:195]\n"
"s_waitcnt lgkmcnt(8)\n"
"ds_read_b128 a[88:91], v240 offset:4736\n"
"v_mfma_f32_16x16x32_bf16 v[80:83], a[128:131], v[200:203], v[80:83]\n"
"v_exp_f32 v171, v171\n"
"v_mfma_f32_16x16x32_bf16 v[196:199], a[104:107], a[56:59], v[196:199]\n"
"ds_read_b128 a[104:107], v240 offset:5760\n"
"v_cvt_pk_bf16_f32 v204, v168, v169\n"
"v_mfma_f32_16x16x32_bf16 v[100:103], a[132:135], v[200:203], v[100:103]\n"
"v_exp_f32 v172, v172\n"
"v_mfma_f32_16x16x32_bf16 v[192:195], a[92:95], a[76:79], v[192:195]\n"
"s_waitcnt lgkmcnt(6)\n"
"v_exp_f32 v173, v173\n"
"v_mfma_f32_16x16x32_bf16 v[120:123], a[136:139], v[200:203], v[120:123]\n"
"ds_read_b128 a[92:95], v240 offset:4992\n"
"v_cvt_pk_bf16_f32 v205, v170, v171\n"
"v_mfma_f32_16x16x32_bf16 v[196:199], a[108:111], a[76:79], v[196:199]\n"
"v_exp_f32 v174, v174\n"
"v_exp_f32 v175, v175\n"
"v_cvt_pk_bf16_f32 v206, v172, v173\n"
"v_cvt_pk_bf16_f32 v207, v174, v175\n"
"v_mfma_f32_16x16x32_bf16 v[140:143], a[140:143], v[200:203], v[140:143]\n"
"ds_read_b128 a[108:111], v240 offset:6016\n"
"v_mfma_f32_16x16x32_bf16 a[144:147], a[164:167], v[200:203], a[144:147]\n"
"s_waitcnt lgkmcnt(4)\n"
"v_exp_f32 v176, v176\n"
"v_mfma_f32_16x16x32_bf16 v[160:163], a[80:83], a[0:3], v[220:223]\n"
"v_exp_f32 v177, v177\n"
"v_mfma_f32_16x16x32_bf16 v[4:7], a[112:115], v[204:207], v[4:7]\n"
"v_exp_f32 v178, v178\n"
"v_mfma_f32_16x16x32_bf16 v[164:167], a[96:99], a[0:3], v[220:223]\n"
"v_exp_f32 v179, v179\n"
"v_mfma_f32_16x16x32_bf16 v[24:27], a[116:119], v[204:207], v[24:27]\n"
"v_cvt_pk_bf16_f32 v208, v176, v177\n"
"v_mfma_f32_16x16x32_bf16 v[160:163], a[84:87], a[20:23], v[160:163]\n"
"v_exp_f32 v180, v180\n"
"v_mfma_f32_16x16x32_bf16 v[44:47], a[120:123], v[204:207], v[44:47]\n"
"v_exp_f32 v181, v181\n"
"v_mfma_f32_16x16x32_bf16 v[164:167], a[100:103], a[20:23], v[164:167]\n"
"v_cvt_pk_bf16_f32 v209, v178, v179\n"
"v_mfma_f32_16x16x32_bf16 v[64:67], a[124:127], v[204:207], v[64:67]\n"
"s_waitcnt lgkmcnt(0)\n"
"v_exp_f32 v182, v182\n"
"v_mfma_f32_16x16x32_bf16 v[160:163], a[88:91], a[40:43], v[160:163]\n"
"v_exp_f32 v183, v183\n"
"v_mfma_f32_16x16x32_bf16 v[84:87], a[128:131], v[204:207], v[84:87]\n"
"v_cvt_pk_bf16_f32 v210, v180, v181\n"
"v_cvt_pk_bf16_f32 v211, v182, v183\n"
"v_mfma_f32_16x16x32_bf16 v[164:167], a[104:107], a[40:43], v[164:167]\n"
"v_exp_f32 v184, v184\n"
"v_mfma_f32_16x16x32_bf16 v[104:107], a[132:135], v[204:207], v[104:107]\n"
"v_exp_f32 v185, v185\n"
"v_mfma_f32_16x16x32_bf16 v[160:163], a[92:95], a[60:63], v[160:163]\n"
"v_exp_f32 v186, v186\n"
"v_mfma_f32_16x16x32_bf16 v[124:127], a[136:139], v[204:207], v[124:127]\n"
"v_exp_f32 v187, v187\n"
"v_mfma_f32_16x16x32_bf16 v[164:167], a[108:111], a[60:63], v[164:167]\n"
"v_cvt_pk_bf16_f32 v212, v184, v185\n"
"v_mfma_f32_16x16x32_bf16 v[144:147], a[140:143], v[204:207], v[144:147]\n"
"v_exp_f32 v188, v188\n"
"v_mfma_f32_16x16x32_bf16 a[148:151], a[164:167], v[204:207], a[148:151]\n"
"s_add_u32 m0, s49, 0\n"
"v_exp_f32 v189, v189\n"
"v_mfma_f32_16x16x32_bf16 v[168:171], a[80:83], a[4:7], v[224:227]\n"
"s_add_u32 s52, s56, 0\n"
"v_cvt_pk_bf16_f32 v213, v186, v187\n"
"v_mfma_f32_16x16x32_bf16 v[8:11], a[112:115], v[208:211], v[8:11]\n"
"v_exp_f32 v190, v190\n"
"v_mfma_f32_16x16x32_bf16 v[172:175], a[96:99], a[4:7], v[224:227]\n"
"v_exp_f32 v191, v191\n"
"v_mfma_f32_16x16x32_bf16 v[28:31], a[116:119], v[208:211], v[28:31]\n"
"v_cvt_pk_bf16_f32 v214, v188, v189\n"
"v_cvt_pk_bf16_f32 v215, v190, v191\n"
"v_mfma_f32_16x16x32_bf16 v[168:171], a[84:87], a[24:27], v[168:171]\n"
"v_exp_f32 v192, v192\n"
"v_mfma_f32_16x16x32_bf16 v[48:51], a[120:123], v[208:211], v[48:51]\n"
"v_exp_f32 v193, v193\n"
"v_mfma_f32_16x16x32_bf16 v[172:175], a[100:103], a[24:27], v[172:175]\n"
"v_exp_f32 v194, v194\n"
"v_mfma_f32_16x16x32_bf16 v[68:71], a[124:127], v[208:211], v[68:71]\n"
"v_exp_f32 v195, v195\n"
"v_mfma_f32_16x16x32_bf16 v[168:171], a[88:91], a[44:47], v[168:171]\n"
"buffer_load_dwordx4 v249, s[64:67], s52 offen lds\n"
"v_cvt_pk_bf16_f32 v216, v192, v193\n"
"v_mfma_f32_16x16x32_bf16 v[88:91], a[128:131], v[208:211], v[88:91]\n"
"s_add_u32 m0, s49, 4096\n"
"v_exp_f32 v196, v196\n"
"v_mfma_f32_16x16x32_bf16 v[172:175], a[104:107], a[44:47], v[172:175]\n"
"s_add_u32 s52, s56, 40960\n"
"v_exp_f32 v197, v197\n"
"v_mfma_f32_16x16x32_bf16 v[108:111], a[132:135], v[208:211], v[108:111]\n"
"buffer_load_dwordx4 v249, s[64:67], s52 offen lds\n"
"v_cvt_pk_bf16_f32 v217, v194, v195\n"
"v_mfma_f32_16x16x32_bf16 v[168:171], a[92:95], a[64:67], v[168:171]\n"
"v_exp_f32 v198, v198\n"
"v_mfma_f32_16x16x32_bf16 v[128:131], a[136:139], v[208:211], v[128:131]\n"
"v_exp_f32 v199, v199\n"
"v_mfma_f32_16x16x32_bf16 v[172:175], a[108:111], a[64:67], v[172:175]\n"
"v_cvt_pk_bf16_f32 v218, v196, v197\n"
"v_cvt_pk_bf16_f32 v219, v198, v199\n"
"v_mfma_f32_16x16x32_bf16 v[148:151], a[140:143], v[208:211], v[148:151]\n"
"s_add_u32 m0, s49, 8192\n"
"v_exp_f32 v160, v160\n"
"v_mfma_f32_16x16x32_bf16 a[152:155], a[164:167], v[208:211], a[152:155]\n"
"s_add_u32 s52, s56, 81920\n"
"v_exp_f32 v161, v161\n"
"v_mfma_f32_16x16x32_bf16 v[176:179], a[80:83], a[8:11], v[228:231]\n"
"v_exp_f32 v162, v162\n"
"v_mfma_f32_16x16x32_bf16 v[12:15], a[112:115], v[212:215], v[12:15]\n"
"v_exp_f32 v163, v163\n"
"v_mfma_f32_16x16x32_bf16 v[180:183], a[96:99], a[8:11], v[228:231]\n"
"buffer_load_dwordx4 v249, s[64:67], s52 offen lds\n"
"v_cvt_pk_bf16_f32 v200, v160, v161\n"
"v_mfma_f32_16x16x32_bf16 v[32:35], a[116:119], v[212:215], v[32:35]\n"
"v_exp_f32 v164, v164\n"
"v_mfma_f32_16x16x32_bf16 v[176:179], a[84:87], a[28:31], v[176:179]\n"
"v_exp_f32 v165, v165\n"
"v_mfma_f32_16x16x32_bf16 v[52:55], a[120:123], v[212:215], v[52:55]\n"
"v_cvt_pk_bf16_f32 v201, v162, v163\n"
"v_mfma_f32_16x16x32_bf16 v[180:183], a[100:103], a[28:31], v[180:183]\n"
"s_add_u32 m0, s49, 12288\n"
"v_exp_f32 v166, v166\n"
"v_mfma_f32_16x16x32_bf16 v[72:75], a[124:127], v[212:215], v[72:75]\n"
"s_add_u32 s52, s56, 122880\n"
"v_exp_f32 v167, v167\n"
"v_mfma_f32_16x16x32_bf16 v[176:179], a[88:91], a[48:51], v[176:179]\n"
"v_cvt_pk_bf16_f32 v202, v164, v165\n"
"v_cvt_pk_bf16_f32 v203, v166, v167\n"
"v_mfma_f32_16x16x32_bf16 v[92:95], a[128:131], v[212:215], v[92:95]\n"
"buffer_load_dwordx4 v249, s[64:67], s52 offen lds\n"
"v_mfma_f32_16x16x32_bf16 v[180:183], a[104:107], a[48:51], v[180:183]\n"
"v_mfma_f32_16x16x32_bf16 v[112:115], a[132:135], v[212:215], v[112:115]\n"
"v_add_u32 v241, 49152, v241\n"
"v_add_u32 v242, 49152, v242\n"
"v_mfma_f32_16x16x32_bf16 v[176:179], a[92:95], a[68:71], v[176:179]\n"
"v_mfma_f32_16x16x32_bf16 v[132:135], a[136:139], v[212:215], v[132:135]\n"
"v_mfma_f32_16x16x32_bf16 v[180:183], a[108:111], a[68:71], v[180:183]\n"
"s_add_u32 m0, s49, 16384\n"
"s_add_u32 s52, s56, 163840\n"
"v_mfma_f32_16x16x32_bf16 v[152:155], a[140:143], v[212:215], v[152:155]\n"
"buffer_load_dwordx4 v249, s[64:67], s52 offen lds\n"
"v_mfma_f32_16x16x32_bf16 a[156:159], a[164:167], v[212:215], a[156:159]\n"
"s_waitcnt vmcnt(12) lgkmcnt(0)\n"
"s_barrier\n"
"v_mfma_f32_16x16x32_bf16 v[184:187], a[80:83], a[12:15], v[232:235]\n"
"v_mfma_f32_16x16x32_bf16 v[16:19], a[112:115], v[216:219], v[16:19]\n"
"ds_read_b64_tr_b16 a[112:113], v241 offset:8448\n"
"v_mfma_f32_16x16x32_bf16 v[188:191], a[96:99], a[12:15], v[232:235]\n"
"ds_read_b64_tr_b16 a[114:115], v241 offset:9472\n"
"v_mfma_f32_16x16x32_bf16 v[36:39], a[116:119], v[216:219], v[36:39]\n"
"ds_read_b64_tr_b16 a[116:117], v242 offset:8448\n"
"v_mfma_f32_16x16x32_bf16 v[184:187], a[84:87], a[32:35], v[184:187]\n"
"ds_read_b64_tr_b16 a[118:119], v242 offset:9472\n"
"v_mfma_f32_16x16x32_bf16 v[56:59], a[120:123], v[216:219], v[56:59]\n"
"ds_read_b64_tr_b16 a[120:121], v241 offset:8704\n"
"s_add_u32 m0, s49, 20480\n"
"v_mfma_f32_16x16x32_bf16 v[188:191], a[100:103], a[32:35], v[188:191]\n"
"ds_read_b64_tr_b16 a[122:123], v241 offset:9728\n"
"s_add_u32 s52, s56, 204800\n"
"v_mfma_f32_16x16x32_bf16 v[76:79], a[124:127], v[216:219], v[76:79]\n"
"ds_read_b64_tr_b16 a[124:125], v242 offset:8704\n"
"v_mfma_f32_16x16x32_bf16 v[184:187], a[88:91], a[52:55], v[184:187]\n"
"ds_read_b64_tr_b16 a[126:127], v242 offset:9728\n"
"v_mfma_f32_16x16x32_bf16 v[96:99], a[128:131], v[216:219], v[96:99]\n"
"ds_read_b64_tr_b16 a[128:129], v241 offset:8960\n"
"v_mfma_f32_16x16x32_bf16 v[188:191], a[104:107], a[52:55], v[188:191]\n"
"ds_read_b64_tr_b16 a[130:131], v241 offset:9984\n"
"v_mfma_f32_16x16x32_bf16 v[116:119], a[132:135], v[216:219], v[116:119]\n"
"ds_read_b64_tr_b16 a[132:133], v242 offset:8960\n"
"v_mfma_f32_16x16x32_bf16 v[184:187], a[92:95], a[72:75], v[184:187]\n"
"ds_read_b64_tr_b16 a[134:135], v242 offset:9984\n"
"v_mfma_f32_16x16x32_bf16 v[136:139], a[136:139], v[216:219], v[136:139]\n"
"ds_read_b64_tr_b16 a[136:137], v241 offset:9216\n"
"v_mfma_f32_16x16x32_bf16 v[188:191], a[108:111], a[72:75], v[188:191]\n"
"ds_read_b64_tr_b16 a[138:139], v241 offset:10240\n"
"v_mfma_f32_16x16x32_bf16 v[156:159], a[140:143], v[216:219], v[156:159]\n"
"ds_read_b64_tr_b16 a[140:141], v242 offset:9216\n"
"v_mfma_f32_16x16x32_bf16 a[160:163], a[164:167], v[216:219], a[160:163]\n"
"ds_read_b64_tr_b16 a[142:143], v242 offset:10240\n"
"v_mfma_f32_16x16x32_bf16 v[192:195], a[80:83], a[16:19], v[236:239]\n"
"s_waitcnt lgkmcnt(12)\n"
"ds_read_b128 a[80:83], v240 offset:36992\n"
"v_mfma_f32_16x16x32_bf16 v[0:3], a[112:115], v[200:203], v[0:3]\n"
"v_exp_f32 v168, v168\n"
"v_mfma_f32_16x16x32_bf16 v[196:199], a[96:99], a[16:19], v[236:239]\n"
"ds_read_b128 a[96:99], v240 offset:38016\n"
"v_mfma_f32_16x16x32_bf16 v[20:23], a[116:119], v[200:203], v[20:23]\n"
"v_exp_f32 v169, v169\n"
"v_mfma_f32_16x16x32_bf16 v[192:195], a[84:87], a[36:39], v[192:195]\n"
"s_waitcnt lgkmcnt(10)\n"
"ds_read_b128 a[84:87], v240 offset:37248\n"
"v_mfma_f32_16x16x32_bf16 v[40:43], a[120:123], v[200:203], v[40:43]\n"
"v_exp_f32 v170, v170\n"
"v_mfma_f32_16x16x32_bf16 v[196:199], a[100:103], a[36:39], v[196:199]\n"
"ds_read_b128 a[100:103], v240 offset:38272\n"
"v_mfma_f32_16x16x32_bf16 v[60:63], a[124:127], v[200:203], v[60:63]\n"
"v_exp_f32 v171, v171\n"
"v_mfma_f32_16x16x32_bf16 v[192:195], a[88:91], a[56:59], v[192:195]\n"
"s_waitcnt lgkmcnt(8)\n"
"ds_read_b128 a[88:91], v240 offset:37504\n"
"v_mfma_f32_16x16x32_bf16 v[80:83], a[128:131], v[200:203], v[80:83]\n"
"buffer_load_dwordx4 v249, s[64:67], s52 offen lds\n"
"v_cvt_pk_bf16_f32 v204, v168, v169\n"
"v_mfma_f32_16x16x32_bf16 v[196:199], a[104:107], a[56:59], v[196:199]\n"
"ds_read_b128 a[104:107], v240 offset:38528\n"
"s_add_u32 m0, s49, 24576\n"
"v_mfma_f32_16x16x32_bf16 v[100:103], a[132:135], v[200:203], v[100:103]\n"
"s_add_u32 s52, s56, 245760\n"
"v_exp_f32 v172, v172\n"
"v_mfma_f32_16x16x32_bf16 v[192:195], a[92:95], a[76:79], v[192:195]\n"
"s_waitcnt lgkmcnt(6)\n"
"v_exp_f32 v173, v173\n"
"v_mfma_f32_16x16x32_bf16 v[120:123], a[136:139], v[200:203], v[120:123]\n"
"ds_read_b128 a[92:95], v240 offset:37760\n"
"v_cvt_pk_bf16_f32 v205, v170, v171\n"
"v_mfma_f32_16x16x32_bf16 v[196:199], a[108:111], a[76:79], v[196:199]\n"
"v_exp_f32 v174, v174\n"
"v_exp_f32 v175, v175\n"
"v_cvt_pk_bf16_f32 v206, v172, v173\n"
"v_cvt_pk_bf16_f32 v207, v174, v175\n"
"v_mfma_f32_16x16x32_bf16 v[140:143], a[140:143], v[200:203], v[140:143]\n"
"ds_read_b128 a[108:111], v240 offset:38784\n"
"v_mfma_f32_16x16x32_bf16 a[144:147], a[164:167], v[200:203], a[144:147]\n"
"s_waitcnt lgkmcnt(4)\n"
"v_exp_f32 v176, v176\n"
"v_mfma_f32_16x16x32_bf16 v[160:163], a[80:83], a[0:3], v[220:223]\n"
"v_exp_f32 v177, v177\n"
"v_mfma_f32_16x16x32_bf16 v[4:7], a[112:115], v[204:207], v[4:7]\n"
"v_exp_f32 v178, v178\n"
"v_mfma_f32_16x16x32_bf16 v[164:167], a[96:99], a[0:3], v[220:223]\n"
"v_exp_f32 v179, v179\n"
"v_mfma_f32_16x16x32_bf16 v[24:27], a[116:119], v[204:207], v[24:27]\n"
"buffer_load_dwordx4 v249, s[64:67], s52 offen lds\n"
"v_cvt_pk_bf16_f32 v208, v176, v177\n"
"v_mfma_f32_16x16x32_bf16 v[160:163], a[84:87], a[20:23], v[160:163]\n"
"s_add_u32 m0, s49, 28672\n"
"v_exp_f32 v180, v180\n"
"v_mfma_f32_16x16x32_bf16 v[44:47], a[120:123], v[204:207], v[44:47]\n"
"s_add_u32 s52, s56, 286720\n"
"v_exp_f32 v181, v181\n"
"v_mfma_f32_16x16x32_bf16 v[164:167], a[100:103], a[20:23], v[164:167]\n"
"buffer_load_dwordx4 v249, s[64:67], s52 offen lds\n"
"v_cvt_pk_bf16_f32 v209, v178, v179\n"
"v_mfma_f32_16x16x32_bf16 v[64:67], a[124:127], v[204:207], v[64:67]\n"
"s_waitcnt lgkmcnt(0)\n"
"v_exp_f32 v182, v182\n"
"v_mfma_f32_16x16x32_bf16 v[160:163], a[88:91], a[40:43], v[160:163]\n"
"s_lshl_b32 s52, %[bb], 11\n"
"v_exp_f32 v183, v183\n"
"v_mfma_f32_16x16x32_bf16 v[84:87], a[128:131], v[204:207], v[84:87]\n"
"v_cvt_pk_bf16_f32 v210, v180, v181\n"
"v_cvt_pk_bf16_f32 v211, v182, v183\n"
"v_mfma_f32_16x16x32_bf16 v[164:167], a[104:107], a[40:43], v[164:167]\n"
"v_exp_f32 v184, v184\n"
"v_mfma_f32_16x16x32_bf16 v[104:107], a[132:135], v[204:207], v[104:107]\n"
"v_exp_f32 v185, v185\n"
"v_mfma_f32_16x16x32_bf16 v[160:163], a[92:95], a[60:63], v[160:163]\n"
"v_exp_f32 v186, v186\n"
"v_mfma_f32_16x16x32_bf16 v[124:127], a[136:139], v[204:207], v[124:127]\n"
"v_exp_f32 v187, v187\n"
"v_mfma_f32_16x16x32_bf16 v[164:167], a[108:111], a[60:63], v[164:167]\n"
"s_add_u32 s60, s96, s52\n"
"s_addc_u32 s61, s97, 0\n"
"v_cvt_pk_bf16_f32 v212, v184, v185\n"
"v_mfma_f32_16x16x32_bf16 v[144:147], a[140:143], v[204:207], v[144:147]\n"
"s_mul_i32 s52, %[bb], 204800\n"
"v_exp_f32 v188, v188\n"
"v_mfma_f32_16x16x32_bf16 a[148:151], a[164:167], v[204:207], a[148:151]\n"
"v_exp_f32 v189, v189\n"
"v_mfma_f32_16x16x32_bf16 v[168:171], a[80:83], a[4:7], v[224:227]\n"
"s_add_u32 s40, s98, s52\n"
"s_addc_u32 s41, s99, 0\n"
"v_cvt_pk_bf16_f32 v213, v186, v187\n"
"v_mfma_f32_16x16x32_bf16 v[8:11], a[112:115], v[208:211], v[8:11]\n"
"s_and_b32 s41, s41, 65535\n"
"v_exp_f32 v190, v190\n"
"v_mfma_f32_16x16x32_bf16 v[172:175], a[96:99], a[4:7], v[224:227]\n"
"s_mov_b32 s42, 204800\n"
"v_exp_f32 v191, v191\n"
"v_mfma_f32_16x16x32_bf16 v[28:31], a[116:119], v[208:211], v[28:31]\n"
"s_lshl_b32 s98, %[hg], 1\n"
"v_cvt_pk_bf16_f32 v214, v188, v189\n"
"v_cvt_pk_bf16_f32 v215, v190, v191\n"
"v_mfma_f32_16x16x32_bf16 v[168:171], a[84:87], a[24:27], v[168:171]\n"
"s_add_u32 s98, s98, 49\n"
"s_mov_b32 s99, 1073741824\n"
"s_mov_b32 s97, 127\n"
"v_mfma_f32_16x16x32_bf16 v[48:51], a[120:123], v[208:211], v[48:51]\n"
"v_exp_f32 v192, v192\n"
"v_mfma_f32_16x16x32_bf16 v[172:175], a[100:103], a[24:27], v[172:175]\n"
"v_exp_f32 v193, v193\n"
"v_mfma_f32_16x16x32_bf16 v[68:71], a[124:127], v[208:211], v[68:71]\n"
"v_exp_f32 v194, v194\n"
"v_mfma_f32_16x16x32_bf16 v[168:171], a[88:91], a[44:47], v[168:171]\n"
"v_exp_f32 v195, v195\n"
"v_mfma_f32_16x16x32_bf16 v[88:91], a[128:131], v[208:211], v[88:91]\n"
"v_cvt_pk_bf16_f32 v216, v192, v193\n"
"v_mfma_f32_16x16x32_bf16 v[172:175], a[104:107], a[44:47], v[172:175]\n"
"v_exp_f32 v196, v196\n"
"v_mfma_f32_16x16x32_bf16 v[108:111], a[132:135], v[208:211], v[108:111]\n"
"v_exp_f32 v197, v197\n"
"v_mfma_f32_16x16x32_bf16 v[168:171], a[92:95], a[64:67], v[168:171]\n"
"v_cvt_pk_bf16_f32 v217, v194, v195\n"
"v_mfma_f32_16x16x32_bf16 v[128:131], a[136:139], v[208:211], v[128:131]\n"
"v_exp_f32 v198, v198\n"
"v_mfma_f32_16x16x32_bf16 v[172:175], a[108:111], a[64:67], v[172:175]\n"
"v_exp_f32 v199, v199\n"
"v_mfma_f32_16x16x32_bf16 v[148:151], a[140:143], v[208:211], v[148:151]\n"
"v_lshrrev_b32 v244, 4, v253\n"
"v_add_u32 v243, %[hg], v244\n"
"v_mfma_f32_16x16x32_bf16 a[152:155], a[164:167], v[208:211], a[152:155]\n"
"v_add_u32 v243, 1, v243\n"
"v_and_b32 v243, 7, v243\n"
"v_mfma_f32_16x16x32_bf16 v[176:179], a[80:83], a[8:11], v[228:231]\n"
"v_lshlrev_b32 v255, 3, v243\n"
"v_cvt_pk_bf16_f32 v218, v196, v197\n"
"v_mfma_f32_16x16x32_bf16 v[12:15], a[112:115], v[212:215], v[12:15]\n"
"global_load_dwordx2 v[254:255], v255, s[60:61] sc1\n"
"v_lshlrev_b32 v243, 7, v243\n"
"v_mfma_f32_16x16x32_bf16 v[180:183], a[96:99], a[8:11], v[228:231]\n"
"v_lshl_add_u32 v244, v244, 3, v243\n"
"v_add_u32 v244, 128, v244\n"
"v_mfma_f32_16x16x32_bf16 v[32:35], a[116:119], v[212:215], v[32:35]\n"
"v_cvt_pk_bf16_f32 v219, v198, v199\n"
"v_mfma_f32_16x16x32_bf16 v[176:179], a[84:87], a[28:31], v[176:179]\n"
"v_exp_f32 v160, v160\n"
"v_mfma_f32_16x16x32_bf16 v[52:55], a[120:123], v[212:215], v[52:55]\n"
"v_exp_f32 v161, v161\n"
"v_mfma_f32_16x16x32_bf16 v[180:183], a[100:103], a[28:31], v[180:183]\n"
"v_exp_f32 v162, v162\n"
"v_mfma_f32_16x16x32_bf16 v[72:75], a[124:127], v[212:215], v[72:75]\n"
"v_exp_f32 v163, v163\n"
"v_mfma_f32_16x16x32_bf16 v[176:179], a[88:91], a[48:51], v[176:179]\n"
"v_cvt_pk_bf16_f32 v200, v160, v161\n"
"v_mfma_f32_16x16x32_bf16 v[92:95], a[128:131], v[212:215], v[92:95]\n"
"v_exp_f32 v164, v164\n"
"v_mfma_f32_16x16x32_bf16 v[180:183], a[104:107], a[48:51], v[180:183]\n"
"v_exp_f32 v165, v165\n"
"v_mfma_f32_16x16x32_bf16 v[112:115], a[132:135], v[212:215], v[112:115]\n"
"v_cvt_pk_bf16_f32 v201, v162, v163\n"
"v_mfma_f32_16x16x32_bf16 v[176:179], a[92:95], a[68:71], v[176:179]\n"
"v_exp_f32 v166, v166\n"
"v_mfma_f32_16x16x32_bf16 v[132:135], a[136:139], v[212:215], v[132:135]\n"
"v_exp_f32 v167, v167\n"
"v_mfma_f32_16x16x32_bf16 v[180:183], a[108:111], a[68:71], v[180:183]\n"
"v_cvt_pk_bf16_f32 v202, v164, v165\n"
"v_cvt_pk_bf16_f32 v203, v166, v167\n"
"v_mfma_f32_16x16x32_bf16 v[152:155], a[140:143], v[212:215], v[152:155]\n"
"v_mfma_f32_16x16x32_bf16 a[156:159], a[164:167], v[212:215], a[156:159]\n"
"s_waitcnt vmcnt(1) lgkmcnt(0)\n"
"s_barrier\n"
"v_mfma_f32_16x16x32_bf16 v[184:187], a[80:83], a[12:15], v[232:235]\n"
"ds_write_b128 v251, a[168:171] offset:0\n"
"v_mfma_f32_16x16x32_bf16 v[16:19], a[112:115], v[216:219], v[16:19]\n"
"ds_read_b64_tr_b16 a[112:113], v241 offset:24832\n"
"v_mfma_f32_16x16x32_bf16 v[188:191], a[96:99], a[12:15], v[232:235]\n"
"ds_read_b64_tr_b16 a[114:115], v241 offset:25856\n"
"v_mfma_f32_16x16x32_bf16 v[36:39], a[116:119], v[216:219], v[36:39]\n"
"ds_write_b128 v251, a[172:175] offset:1024\n"
"v_mfma_f32_16x16x32_bf16 v[184:187], a[84:87], a[32:35], v[184:187]\n"
"ds_read_b64_tr_b16 a[116:117], v242 offset:24832\n"
"v_mfma_f32_16x16x32_bf16 v[56:59], a[120:123], v[216:219], v[56:59]\n"
"ds_read_b64_tr_b16 a[118:119], v242 offset:25856\n"
"v_mfma_f32_16x16x32_bf16 v[188:191], a[100:103], a[32:35], v[188:191]\n"
"ds_read_b64_tr_b16 a[120:121], v241 offset:25088\n"
"v_mfma_f32_16x16x32_bf16 v[76:79], a[124:127], v[216:219], v[76:79]\n"
"ds_read_b64_tr_b16 a[122:123], v241 offset:26112\n"
"v_mfma_f32_16x16x32_bf16 v[184:187], a[88:91], a[52:55], v[184:187]\n"
"ds_write_b128 v251, a[176:179] offset:2048\n"
"v_mfma_f32_16x16x32_bf16 v[96:99], a[128:131], v[216:219], v[96:99]\n"
"ds_read_b64_tr_b16 a[124:125], v242 offset:25088\n"
"ds_read_b64_tr_b16 a[126:127], v242 offset:26112\n"
"v_mfma_f32_16x16x32_bf16 v[188:191], a[104:107], a[52:55], v[188:191]\n"
"ds_read_b64_tr_b16 a[128:129], v241 offset:25344\n"
"v_mfma_f32_16x16x32_bf16 v[116:119], a[132:135], v[216:219], v[116:119]\n"
"ds_read_b64_tr_b16 a[130:131], v241 offset:26368\n"
"v_mfma_f32_16x16x32_bf16 v[184:187], a[92:95], a[72:75], v[184:187]\n"
"ds_write_b128 v251, a[180:183] offset:3072\n"
"v_mfma_f32_16x16x32_bf16 v[136:139], a[136:139], v[216:219], v[136:139]\n"
"ds_read_b64_tr_b16 a[132:133], v242 offset:25344\n"
"ds_read_b64_tr_b16 a[134:135], v242 offset:26368\n"
"v_mfma_f32_16x16x32_bf16 v[188:191], a[108:111], a[72:75], v[188:191]\n"
"ds_read_b64_tr_b16 a[136:137], v241 offset:25600\n"
"v_mfma_f32_16x16x32_bf16 v[156:159], a[140:143], v[216:219], v[156:159]\n"
"ds_read_b64_tr_b16 a[138:139], v241 offset:26624\n"
"v_mfma_f32_16x16x32_bf16 a[160:163], a[164:167], v[216:219], a[160:163]\n"
"ds_write_b128 v251, a[184:187] offset:4096\n"
"v_mfma_f32_16x16x32_bf16 v[192:195], a[80:83], a[16:19], v[236:239]\n"
"s_waitcnt lgkmcnt(13)\n"
"ds_read_b64_tr_b16 a[140:141], v242 offset:25600\n"
"ds_read_b64_tr_b16 a[142:143], v242 offset:26624\n"
"v_mfma_f32_16x16x32_bf16 v[0:3], a[112:115], v[200:203], v[0:3]\n"
"ds_write_b128 v251, a[188:191] offset:5120\n"
"v_exp_f32 v168, v168\n"
"v_mfma_f32_16x16x32_bf16 v[196:199], a[96:99], a[16:19], v[236:239]\n"
"v_exp_f32 v169, v169\n"
"v_exp_f32 v170, v170\n"
"v_mfma_f32_16x16x32_bf16 v[20:23], a[116:119], v[200:203], v[20:23]\n"
"v_exp_f32 v171, v171\n"
"v_cvt_pk_bf16_f32 v204, v168, v169\n"
"v_mfma_f32_16x16x32_bf16 v[192:195], a[84:87], a[36:39], v[192:195]\n"
"s_waitcnt lgkmcnt(11)\n"
"ds_write_b128 v251, a[192:195] offset:6144\n"
"v_exp_f32 v172, v172\n"
"v_mfma_f32_16x16x32_bf16 v[40:43], a[120:123], v[200:203], v[40:43]\n"
"v_exp_f32 v173, v173\n"
"v_cvt_pk_bf16_f32 v205, v170, v171\n"
"v_mfma_f32_16x16x32_bf16 v[196:199], a[100:103], a[36:39], v[196:199]\n"
"v_exp_f32 v174, v174\n"
"v_exp_f32 v175, v175\n"
"v_mfma_f32_16x16x32_bf16 v[60:63], a[124:127], v[200:203], v[60:63]\n"
"ds_write_b128 v251, a[196:199] offset:7168\n"
"v_cvt_pk_bf16_f32 v206, v172, v173\n"
"v_cvt_pk_bf16_f32 v207, v174, v175\n"
"v_mfma_f32_16x16x32_bf16 v[192:195], a[88:91], a[56:59], v[192:195]\n"
"s_waitcnt lgkmcnt(8)\n"
"v_mov_b32 v245, v250\n"
"v_xor_b32 v246, 32, v250\n"
"v_exp_f32 v176, v176\n"
"v_mfma_f32_16x16x32_bf16 v[80:83], a[128:131], v[200:203], v[80:83]\n"
"v_xor_b32 v247, 16384, v245\n"
"v_xor_b32 v248, 16384, v246\n"
"v_exp_f32 v177, v177\n"
"v_mfma_f32_16x16x32_bf16 v[196:199], a[104:107], a[56:59], v[196:199]\n"
"v_exp_f32 v178, v178\n"
"v_exp_f32 v179, v179\n"
"v_mfma_f32_16x16x32_bf16 v[100:103], a[132:135], v[200:203], v[100:103]\n"
"ds_write_b128 v251, a[200:203] offset:8192\n"
"v_cvt_pk_bf16_f32 v208, v176, v177\n"
"v_exp_f32 v180, v180\n"
"v_mfma_f32_16x16x32_bf16 v[192:195], a[92:95], a[76:79], v[192:195]\n"
"s_waitcnt lgkmcnt(4)\n"
"v_exp_f32 v181, v181\n"
"v_cvt_pk_bf16_f32 v209, v178, v179\n"
"v_mfma_f32_16x16x32_bf16 v[120:123], a[136:139], v[200:203], v[120:123]\n"
"v_exp_f32 v182, v182\n"
"v_exp_f32 v183, v183\n"
"v_mfma_f32_16x16x32_bf16 v[196:199], a[108:111], a[76:79], v[196:199]\n"
"ds_write_b128 v251, a[204:207] offset:9216\n"
"v_cvt_pk_bf16_f32 v210, v180, v181\n"
"v_cvt_pk_bf16_f32 v211, v182, v183\n"
"v_mfma_f32_16x16x32_bf16 v[140:143], a[140:143], v[200:203], v[140:143]\n"
"v_exp_f32 v184, v184\n"
"v_exp_f32 v185, v185\n"
"v_mfma_f32_16x16x32_bf16 a[144:147], a[164:167], v[200:203], a[144:147]\n"
"s_waitcnt lgkmcnt(5)\n"
"s_barrier\n"
"v_exp_f32 v186, v186\n"
"v_mfma_f32_16x16x32_bf16 v[4:7], a[112:115], v[204:207], v[4:7]\n"
"s_add_u32 m0, s49, 106752\n"
"s_add_u32 s52, s56, 327680\n"
"v_add_u32 v240, 65536, v245\n"
"v_add_u32 v241, 65536, v246\n"
"v_add_u32 v242, 65536, v247\n"
"v_mfma_f32_16x16x32_bf16 v[24:27], a[116:119], v[204:207], v[24:27]\n"
"ds_write_b128 v251, a[208:211] offset:10240\n"
"v_add_u32 v243, 65536, v248\n"
"v_exp_f32 v187, v187\n"
"v_mfma_f32_16x16x32_bf16 v[44:47], a[120:123], v[204:207], v[44:47]\n"
"buffer_load_dwordx4 v249, s[64:67], s52 offen lds\n"
"v_cvt_pk_bf16_f32 v212, v184, v185\n"
"v_exp_f32 v188, v188\n"
"v_mfma_f32_16x16x32_bf16 v[64:67], a[124:127], v[204:207], v[64:67]\n"
"s_add_u32 m0, s49, 110848\n"
"s_add_u32 s52, s56, 368640\n"
"v_exp_f32 v189, v189\n"
"v_cvt_pk_bf16_f32 v213, v186, v187\n"
"v_mfma_f32_16x16x32_bf16 v[84:87], a[128:131], v[204:207], v[84:87]\n"
"ds_write_b128 v251, a[212:215] offset:11264\n"
"v_exp_f32 v190, v190\n"
"v_accvgpr_read_b32 v233, a144\n"
"v_mfma_f32_16x16x32_bf16 v[104:107], a[132:135], v[204:207], v[104:107]\n"
"v_exp_f32 v191, v191\n"
"v_cvt_pk_bf16_f32 v214, v188, v189\n"
"v_cvt_pk_bf16_f32 v215, v190, v191\n"
"v_mfma_f32_16x16x32_bf16 v[124:127], a[136:139], v[204:207], v[124:127]\n"
"v_exp_f32 v192, v192\n"
"v_exp_f32 v193, v193\n"
"v_mfma_f32_16x16x32_bf16 v[144:147], a[140:143], v[204:207], v[144:147]\n"
"v_exp_f32 v194, v194\n"
"v_exp_f32 v195, v195\n"
"v_mfma_f32_16x16x32_bf16 a[148:151], a[164:167], v[204:207], a[148:151]\n"
"ds_write_b128 v251, a[216:219] offset:12288\n"
"v_cvt_pk_bf16_f32 v216, v192, v193\n"
"v_exp_f32 v196, v196\n"
"v_mfma_f32_16x16x32_bf16 v[8:11], a[112:115], v[208:211], v[8:11]\n"
"v_exp_f32 v197, v197\n"
"v_cvt_pk_bf16_f32 v217, v194, v195\n"
"v_cmp_nge_f32 vcc, s53, v233\n"
"v_mfma_f32_16x16x32_bf16 v[28:31], a[116:119], v[208:211], v[28:31]\n"
"v_exp_f32 v198, v198\n"
"v_exp_f32 v199, v199\n"
"v_mfma_f32_16x16x32_bf16 v[48:51], a[120:123], v[208:211], v[48:51]\n"
"ds_write_b128 v251, a[220:223] offset:13312\n"
"v_cvt_pk_bf16_f32 v218, v196, v197\n"
"v_cvt_pk_bf16_f32 v219, v198, v199\n"
"v_mfma_f32_16x16x32_bf16 v[68:71], a[124:127], v[208:211], v[68:71]\n"
"v_rcp_f32 v233, v233\n"
"s_or_b64 s[58:59], s[58:59], vcc\n"
"v_mul_f32 v0, v233, v0\n"
"v_mfma_f32_16x16x32_bf16 v[88:91], a[128:131], v[208:211], v[88:91]\n"
"buffer_load_dwordx4 v249, s[64:67], s52 offen lds\n"
"v_mul_f32 v1, v233, v1\n"
"v_mul_f32 v2, v233, v2\n"
"v_mul_f32 v3, v233, v3\n"
"v_mfma_f32_16x16x32_bf16 v[108:111], a[132:135], v[208:211], v[108:111]\n"
"s_add_u32 m0, s49, 114944\n"
"s_add_u32 s52, s56, 409600\n"
"v_mul_f32 v20, v233, v20\n"
"v_mul_f32 v21, v233, v21\n"
"v_mul_f32 v22, v233, v22\n"
"v_mfma_f32_16x16x32_bf16 v[128:131], a[136:139], v[208:211], v[128:131]\n"
"ds_write_b128 v251, a[224:227] offset:14336\n"
"v_mul_f32 v23, v233, v23\n"
"v_cvt_pk_bf16_f32 v0, v0, v1\n"
"v_cvt_pk_bf16_f32 v1, v2, v3\n"
"v_mfma_f32_16x16x32_bf16 v[148:151], a[140:143], v[208:211], v[148:151]\n"
"buffer_load_dwordx4 v249, s[64:67], s52 offen lds\n"
"v_cvt_pk_bf16_f32 v2, v20, v21\n"
"v_cvt_pk_bf16_f32 v3, v22, v23\n"
"v_mul_f32 v40, v233, v40\n"
"v_mfma_f32_16x16x32_bf16 a[152:155], a[164:167], v[208:211], a[152:155]\n"
"s_add_u32 m0, s49, 119040\n"
"s_add_u32 s52, s56, 450560\n"
"v_mul_f32 v41, v233, v41\n"
"v_mul_f32 v42, v233, v42\n"
"v_mul_f32 v43, v233, v43\n"
"v_mfma_f32_16x16x32_bf16 v[12:15], a[112:115], v[212:215], v[12:15]\n"
"ds_write_b128 v251, a[228:231] offset:15360\n"
"v_mul_f32 v60, v233, v60\n"
"v_mul_f32 v61, v233, v61\n"
"v_mul_f32 v62, v233, v62\n"
"v_mfma_f32_16x16x32_bf16 v[32:35], a[116:119], v[212:215], v[32:35]\n"
"buffer_load_dwordx4 v249, s[64:67], s52 offen lds\n"
"v_mul_f32 v63, v233, v63\n"
"v_cvt_pk_bf16_f32 v40, v40, v41\n"
"v_cvt_pk_bf16_f32 v41, v42, v43\n"
"v_mfma_f32_16x16x32_bf16 v[52:55], a[120:123], v[212:215], v[52:55]\n"
"s_add_u32 m0, s49, 123136\n"
"s_add_u32 s52, s56, 491520\n"
"v_cvt_pk_bf16_f32 v42, v60, v61\n"
"v_cvt_pk_bf16_f32 v43, v62, v63\n"
"v_mul_f32 v80, v233, v80\n"
"v_mfma_f32_16x16x32_bf16 v[72:75], a[124:127], v[212:215], v[72:75]\n"
"buffer_load_dwordx4 v249, s[64:67], s52 offen lds\n"
"v_mul_f32 v81, v233, v81\n"
"v_mul_f32 v82, v233, v82\n"
"v_mul_f32 v83, v233, v83\n"
"v_mfma_f32_16x16x32_bf16 v[92:95], a[128:131], v[212:215], v[92:95]\n"
"ds_write_b128 v251, a[232:235] offset:16384\n"
"v_mul_f32 v100, v233, v100\n"
"v_mul_f32 v101, v233, v101\n"
"v_mul_f32 v102, v233, v102\n"
"v_mfma_f32_16x16x32_bf16 v[112:115], a[132:135], v[212:215], v[112:115]\n"
"s_add_u32 m0, s49, 127232\n"
"s_add_u32 s52, s56, 532480\n"
"v_mul_f32 v103, v233, v103\n"
"v_cvt_pk_bf16_f32 v80, v80, v81\n"
"v_cvt_pk_bf16_f32 v81, v82, v83\n"
"v_mfma_f32_16x16x32_bf16 v[132:135], a[136:139], v[212:215], v[132:135]\n"
"buffer_load_dwordx4 v249, s[64:67], s52 offen lds\n"
"v_cvt_pk_bf16_f32 v82, v100, v101\n"
"v_cvt_pk_bf16_f32 v83, v102, v103\n"
"v_mul_f32 v120, v233, v120\n"
"v_mfma_f32_16x16x32_bf16 v[152:155], a[140:143], v[212:215], v[152:155]\n"
"ds_write_b128 v251, a[236:239] offset:17408\n"
"v_mul_f32 v121, v233, v121\n"
"v_mul_f32 v122, v233, v122\n"
"v_mul_f32 v123, v233, v123\n"
"v_mfma_f32_16x16x32_bf16 a[156:159], a[164:167], v[212:215], a[156:159]\n"
"v_mul_f32 v140, v233, v140\n"
"v_mul_f32 v141, v233, v141\n"
"v_mul_f32 v142, v233, v142\n"
"v_mul_f32 v143, v233, v143\n"
"v_cvt_pk_bf16_f32 v120, v120, v121\n"
"v_cvt_pk_bf16_f32 v121, v122, v123\n"
"v_cvt_pk_bf16_f32 v122, v140, v141\n"
"v_cvt_pk_bf16_f32 v123, v142, v143\n"
"v_mfma_f32_16x16x32_bf16 v[16:19], a[112:115], v[216:219], v[16:19]\n"
"s_add_u32 m0, s49, 131328\n"
"s_add_u32 s52, s56, 573440\n"
"buffer_load_dwordx4 v249, s[64:67], s52 offen lds\n"
"s_add_u32 m0, s49, 135424\n"
"v_mfma_f32_16x16x32_bf16 v[36:39], a[116:119], v[216:219], v[36:39]\n"
"s_add_u32 s52, s56, 614400\n"
"buffer_load_dwordx4 v249, s[64:67], s52 offen lds\n"
"ds_read_b64_tr_b16 a[80:81], v245 offset:0\n"
"v_mfma_f32_16x16x32_bf16 v[56:59], a[120:123], v[216:219], v[56:59]\n"
"ds_write_b128 v251, a[240:243] offset:18432\n"
"ds_read_b64_tr_b16 a[82:83], v245 offset:2048\n"
"v_accvgpr_read_b32 v234, a148\n"
"v_mfma_f32_16x16x32_bf16 v[76:79], a[124:127], v[216:219], v[76:79]\n"
"ds_read_b64_tr_b16 a[84:85], v246 offset:0\n"
"ds_read_b64_tr_b16 a[86:87], v246 offset:2048\n"
"v_cmp_nge_f32 vcc, s53, v234\n"
"v_mfma_f32_16x16x32_bf16 v[96:99], a[128:131], v[216:219], v[96:99]\n"
"ds_read_b64_tr_b16 a[88:89], v245 offset:4096\n"
"ds_read_b64_tr_b16 a[90:91], v245 offset:6144\n"
"v_mfma_f32_16x16x32_bf16 v[116:119], a[132:135], v[216:219], v[116:119]\n"
"ds_read_b64_tr_b16 a[92:93], v246 offset:4096\n"
"ds_read_b64_tr_b16 a[94:95], v246 offset:6144\n"
"v_mfma_f32_16x16x32_bf16 v[136:139], a[136:139], v[216:219], v[136:139]\n"
"ds_read_b64_tr_b16 a[96:97], v245 offset:8192\n"
"ds_read_b64_tr_b16 a[98:99], v245 offset:10240\n"
"ds_read_b64_tr_b16 a[100:101], v246 offset:8192\n"
"ds_read_b64_tr_b16 a[102:103], v246 offset:10240\n"
"ds_read_b64_tr_b16 a[104:105], v245 offset:12288\n"
"ds_read_b64_tr_b16 a[106:107], v245 offset:14336\n"
"ds_read_b64_tr_b16 a[108:109], v246 offset:12288\n"
"ds_read_b64_tr_b16 a[110:111], v246 offset:14336\n"
"v_mfma_f32_16x16x32_bf16 v[156:159], a[140:143], v[216:219], v[156:159]\n"
"ds_write_b128 v251, a[244:247] offset:19456\n"
"ds_read_b64_tr_b16 a[112:113], v247 offset:0\n"
"v_mfma_f32_16x16x32_bf16 a[160:163], a[164:167], v[216:219], a[160:163]\n"
"ds_read_b64_tr_b16 a[114:115], v247 offset:2048\n"
"ds_read_b64_tr_b16 a[116:117], v248 offset:0\n"
"ds_read_b64_tr_b16 a[118:119], v248 offset:2048\n"
"ds_read_b64_tr_b16 a[120:121], v247 offset:4096\n"
"ds_read_b64_tr_b16 a[122:123], v247 offset:6144\n"
"ds_read_b64_tr_b16 a[124:125], v248 offset:4096\n"
"ds_read_b64_tr_b16 a[126:127], v248 offset:6144\n"
"ds_read_b64_tr_b16 a[128:129], v247 offset:8192\n"
"ds_read_b64_tr_b16 a[130:131], v247 offset:10240\n"
"ds_read_b64_tr_b16 a[132:133], v248 offset:8192\n"
"ds_read_b64_tr_b16 a[134:135], v248 offset:10240\n"
"ds_read_b64_tr_b16 a[136:137], v247 offset:12288\n"
"ds_read_b64_tr_b16 a[138:139], v247 offset:14336\n"
"ds_read_b64_tr_b16 a[140:141], v248 offset:12288\n"
"ds_read_b64_tr_b16 a[142:143], v248 offset:14336\n"
"s_waitcnt vmcnt(0) lgkmcnt(0)\n"
"s_barrier\n"
"v_mfma_f32_16x16x32_bf16 v[160:163], a[80:83], a[168:171], 0\n"
"s_lshr_b64 s[72:73], s[72:73], 4\n"
"v_rcp_f32 v234, v234\n"
"s_or_b64 s[58:59], s[58:59], vcc\n"
"v_mul_f32 v4, v234, v4\n"
"v_mfma_f32_16x16x32_bf16 v[164:167], a[80:83], a[172:175], 0\n"
"ds_read_b128 a[0:3], v252 offset:0\n"
"v_mul_f32 v5, v234, v5\n"
"v_mul_f32 v6, v234, v6\n"
"v_mul_f32 v7, v234, v7\n"
"v_mfma_f32_16x16x32_bf16 v[168:171], a[80:83], a[176:179], 0\n"
"ds_read_b128 a[4:7], v252 offset:1024\n"
"v_mul_f32 v24, v234, v24\n"
"v_mul_f32 v25, v234, v25\n"
"v_mul_f32 v26, v234, v26\n"
"v_mfma_f32_16x16x32_bf16 v[172:175], a[80:83], a[180:183], 0\n"
"ds_read_b128 a[8:11], v252 offset:2048\n"
"v_mul_f32 v27, v234, v27\n"
"v_cvt_pk_bf16_f32 v4, v4, v5\n"
"v_cvt_pk_bf16_f32 v5, v6, v7\n"
"v_mfma_f32_16x16x32_bf16 v[176:179], a[80:83], a[184:187], 0\n"
"ds_read_b64_tr_b16 a[80:81], v240 offset:41216\n"
"v_cvt_pk_bf16_f32 v6, v24, v25\n"
"v_cvt_pk_bf16_f32 v7, v26, v27\n"
"v_mul_f32 v44, v234, v44\n"
"v_mfma_f32_16x16x32_bf16 v[180:183], a[84:87], a[168:171], 0\n"
"ds_read_b64_tr_b16 a[82:83], v240 offset:43264\n"
"v_mul_f32 v45, v234, v45\n"
"v_mul_f32 v46, v234, v46\n"
"v_mul_f32 v47, v234, v47\n"
"v_mfma_f32_16x16x32_bf16 v[184:187], a[84:87], a[172:175], 0\n"
"ds_read_b128 a[12:15], v252 offset:3072\n"
"v_mul_f32 v64, v234, v64\n"
"v_mul_f32 v65, v234, v65\n"
"v_mul_f32 v66, v234, v66\n"
"v_mfma_f32_16x16x32_bf16 v[188:191], a[84:87], a[176:179], 0\n"
"ds_read_b128 a[16:19], v252 offset:4096\n"
"v_mul_f32 v67, v234, v67\n"
"v_cvt_pk_bf16_f32 v44, v44, v45\n"
"v_cvt_pk_bf16_f32 v45, v46, v47\n"
"v_mfma_f32_16x16x32_bf16 v[192:195], a[84:87], a[180:183], 0\n"
"ds_read_b128 a[20:23], v252 offset:5120\n"
"v_cvt_pk_bf16_f32 v46, v64, v65\n"
"v_cvt_pk_bf16_f32 v47, v66, v67\n"
"v_mul_f32 v84, v234, v84\n"
"v_mfma_f32_16x16x32_bf16 v[196:199], a[84:87], a[184:187], 0\n"
"ds_read_b64_tr_b16 a[84:85], v241 offset:41216\n"
"v_mul_f32 v85, v234, v85\n"
"v_mul_f32 v86, v234, v86\n"
"v_mul_f32 v87, v234, v87\n"
"v_mfma_f32_16x16x32_bf16 v[160:163], a[88:91], a[188:191], v[160:163]\n"
"ds_read_b64_tr_b16 a[86:87], v241 offset:43264\n"
"v_mul_f32 v104, v234, v104\n"
"v_mul_f32 v105, v234, v105\n"
"v_mul_f32 v106, v234, v106\n"
"v_mfma_f32_16x16x32_bf16 v[164:167], a[88:91], a[192:195], v[164:167]\n"
"v_mul_f32 v107, v234, v107\n"
"v_cvt_pk_bf16_f32 v84, v84, v85\n"
"v_cvt_pk_bf16_f32 v85, v86, v87\n"
"v_cvt_pk_bf16_f32 v86, v104, v105\n"
"v_cvt_pk_bf16_f32 v87, v106, v107\n"
"v_pk_mul_f32 v[124:125], v[124:125], v[234:235] op_sel_hi:[1,0]\n"
"v_pk_mul_f32 v[126:127], v[126:127], v[234:235] op_sel_hi:[1,0]\n"
"v_pk_mul_f32 v[144:145], v[144:145], v[234:235] op_sel_hi:[1,0]\n"
"v_pk_mul_f32 v[146:147], v[146:147], v[234:235] op_sel_hi:[1,0]\n"
"v_cvt_pk_bf16_f32 v124, v124, v125\n"
"v_cvt_pk_bf16_f32 v125, v126, v127\n"
"v_cvt_pk_bf16_f32 v126, v144, v145\n"
"v_cvt_pk_bf16_f32 v127, v146, v147\n"
"v_mfma_f32_16x16x32_bf16 v[168:171], a[88:91], a[196:199], v[168:171]\n"
"ds_read_b128 a[24:27], v252 offset:6144\n"
"ds_read_b128 a[28:31], v252 offset:7168\n"
"v_accvgpr_read_b32 v235, a152\n"
"v_mfma_f32_16x16x32_bf16 v[172:175], a[88:91], a[200:203], v[172:175]\n"
"ds_read_b128 a[32:35], v252 offset:8192\n"
"ds_read_b128 a[36:39], v252 offset:9216\n"
"v_cmp_nge_f32 vcc, s53, v235\n"
"v_mfma_f32_16x16x32_bf16 v[176:179], a[88:91], a[204:207], v[176:179]\n"
"ds_read_b64_tr_b16 a[88:89], v240 offset:45312\n"
"ds_read_b128 a[40:43], v252 offset:10240\n"
"s_and_b32 s56, s72, 15\n"
"v_mfma_f32_16x16x32_bf16 v[180:183], a[92:95], a[188:191], v[180:183]\n"
"ds_read_b64_tr_b16 a[90:91], v240 offset:47360\n"
"ds_read_b128 a[44:47], v252 offset:11264\n"
"s_lshl_b32 s56, s56, 7\n"
"v_mfma_f32_16x16x32_bf16 v[184:187], a[92:95], a[192:195], v[184:187]\n"
"ds_read_b128 a[48:51], v252 offset:12288\n"
"ds_read_b128 a[52:55], v252 offset:13312\n"
"s_add_u32 m0, s49, 0\n"
"v_mfma_f32_16x16x32_bf16 v[188:191], a[92:95], a[196:199], v[188:191]\n"
"ds_read_b128 a[56:59], v252 offset:14336\n"
"ds_read_b128 a[60:63], v252 offset:15360\n"
"s_add_u32 s52, s56, 0\n"
"v_mfma_f32_16x16x32_bf16 v[192:195], a[92:95], a[200:203], v[192:195]\n"
"ds_read_b128 a[64:67], v252 offset:16384\n"
"ds_read_b128 a[68:71], v252 offset:17408\n"
"v_mfma_f32_16x16x32_bf16 v[196:199], a[92:95], a[204:207], v[196:199]\n"
"ds_read_b64_tr_b16 a[92:93], v241 offset:45312\n"
"ds_read_b128 a[72:75], v252 offset:18432\n"
"v_mfma_f32_16x16x32_bf16 v[160:163], a[96:99], a[208:211], v[160:163]\n"
"ds_read_b64_tr_b16 a[94:95], v241 offset:47360\n"
"ds_read_b128 a[76:79], v252 offset:19456\n"
"v_mfma_f32_16x16x32_bf16 v[164:167], a[96:99], a[212:215], v[164:167]\n"
"v_rcp_f32 v235, v235\n"
"s_or_b64 s[58:59], s[58:59], vcc\n"
"v_mul_f32 v8, v235, v8\n"
"v_mfma_f32_16x16x32_bf16 v[168:171], a[96:99], a[216:219], v[168:171]\n"
"buffer_load_dwordx4 v249, s[64:67], s52 offen lds\n"
"v_mul_f32 v9, v235, v9\n"
"v_mul_f32 v10, v235, v10\n"
"v_mul_f32 v11, v235, v11\n"
"v_mfma_f32_16x16x32_bf16 v[172:175], a[96:99], a[220:223], v[172:175]\n"
"s_add_u32 m0, s49, 4096\n"
"s_add_u32 s52, s56, 40960\n"
"v_mul_f32 v28, v235, v28\n"
"v_mul_f32 v29, v235, v29\n"
"v_mul_f32 v30, v235, v30\n"
"v_mfma_f32_16x16x32_bf16 v[176:179], a[96:99], a[224:227], v[176:179]\n"
"ds_read_b64_tr_b16 a[96:97], v240 offset:49408\n"
"v_mul_f32 v31, v235, v31\n"
"v_cvt_pk_bf16_f32 v8, v8, v9\n"
"v_cvt_pk_bf16_f32 v9, v10, v11\n"
"v_mfma_f32_16x16x32_bf16 v[180:183], a[100:103], a[208:211], v[180:183]\n"
"ds_read_b64_tr_b16 a[98:99], v240 offset:51456\n"
"v_cvt_pk_bf16_f32 v10, v28, v29\n"
"v_cvt_pk_bf16_f32 v11, v30, v31\n"
"v_mul_f32 v48, v235, v48\n"
"v_mfma_f32_16x16x32_bf16 v[184:187], a[100:103], a[212:215], v[184:187]\n"
"buffer_load_dwordx4 v249, s[64:67], s52 offen lds\n"
"v_mul_f32 v49, v235, v49\n"
"v_mul_f32 v50, v235, v50\n"
"v_mul_f32 v51, v235, v51\n"
"v_mfma_f32_16x16x32_bf16 v[188:191], a[100:103], a[216:219], v[188:191]\n"
"s_add_u32 m0, s49, 8192\n"
"s_add_u32 s52, s56, 81920\n"
"v_mul_f32 v68, v235, v68\n"
"v_mul_f32 v69, v235, v69\n"
"v_mul_f32 v70, v235, v70\n"
"v_mfma_f32_16x16x32_bf16 v[192:195], a[100:103], a[220:223], v[192:195]\n"
"s_waitcnt lgkmcnt(2)\n"
"s_barrier\n"
"v_mul_f32 v71, v235, v71\n"
"v_cvt_pk_bf16_f32 v48, v48, v49\n"
"v_cvt_pk_bf16_f32 v49, v50, v51\n"
"v_mfma_f32_16x16x32_bf16 v[196:199], a[100:103], a[224:227], v[196:199]\n"
"ds_read_b64_tr_b16 a[100:101], v241 offset:49408\n"
"v_cvt_pk_bf16_f32 v50, v68, v69\n"
"v_cvt_pk_bf16_f32 v51, v70, v71\n"
"v_mul_f32 v88, v235, v88\n"
"v_mfma_f32_16x16x32_bf16 v[160:163], a[104:107], a[228:231], v[160:163]\n"
"ds_write_b128 v251, v[0:3] offset:0\n"
"v_mul_f32 v89, v235, v89\n"
"v_mul_f32 v90, v235, v90\n"
"v_mul_f32 v91, v235, v91\n"
"v_mfma_f32_16x16x32_bf16 v[164:167], a[104:107], a[232:235], v[164:167]\n"
"ds_read_b64_tr_b16 a[102:103], v241 offset:51456\n"
"v_mul_f32 v108, v235, v108\n"
"v_mul_f32 v109, v235, v109\n"
"v_mul_f32 v110, v235, v110\n"
"v_mfma_f32_16x16x32_bf16 v[168:171], a[104:107], a[236:239], v[168:171]\n"
"v_mul_f32 v111, v235, v111\n"
"v_cvt_pk_bf16_f32 v88, v88, v89\n"
"v_cvt_pk_bf16_f32 v89, v90, v91\n"
"v_cvt_pk_bf16_f32 v90, v108, v109\n"
"v_cvt_pk_bf16_f32 v91, v110, v111\n"
"v_pk_mul_f32 v[128:129], v[128:129], v[234:235] op_sel:[0,1] op_sel_hi:[1,1]\n"
"v_pk_mul_f32 v[130:131], v[130:131], v[234:235] op_sel:[0,1] op_sel_hi:[1,1]\n"
"v_pk_mul_f32 v[148:149], v[148:149], v[234:235] op_sel:[0,1] op_sel_hi:[1,1]\n"
"v_pk_mul_f32 v[150:151], v[150:151], v[234:235] op_sel:[0,1] op_sel_hi:[1,1]\n"
"v_cvt_pk_bf16_f32 v128, v128, v129\n"
"v_cvt_pk_bf16_f32 v129, v130, v131\n"
"v_cvt_pk_bf16_f32 v130, v148, v149\n"
"v_cvt_pk_bf16_f32 v131, v150, v151\n"
"v_mfma_f32_16x16x32_bf16 v[172:175], a[104:107], a[240:243], v[172:175]\n"
"ds_write_b128 v251, v[40:43] offset:5120\n"
"v_accvgpr_read_b32 v236, a156\n"
"v_cmp_nge_f32 vcc, s53, v236\n"
"v_mfma_f32_16x16x32_bf16 v[176:179], a[104:107], a[244:247], v[176:179]\n"
"ds_read_b64_tr_b16 a[104:105], v240 offset:53504\n"
"v_rcp_f32 v236, v236\n"
"s_or_b64 s[58:59], s[58:59], vcc\n"
"v_mfma_f32_16x16x32_bf16 v[180:183], a[108:111], a[228:231], v[180:183]\n"
"ds_read_b64_tr_b16 a[106:107], v240 offset:55552\n"
"v_mul_f32 v12, v236, v12\n"
"v_mul_f32 v13, v236, v13\n"
"v_mul_f32 v14, v236, v14\n"
"v_mfma_f32_16x16x32_bf16 v[184:187], a[108:111], a[232:235], v[184:187]\n"
"ds_write_b128 v251, v[80:83] offset:10240\n"
"v_mul_f32 v15, v236, v15\n"
"v_mul_f32 v32, v236, v32\n"
"v_mul_f32 v33, v236, v33\n"
"v_mfma_f32_16x16x32_bf16 v[188:191], a[108:111], a[236:239], v[188:191]\n"
"buffer_load_dwordx4 v249, s[64:67], s52 offen lds\n"
"v_mul_f32 v34, v236, v34\n"
"v_mul_f32 v35, v236, v35\n"
"v_cvt_pk_bf16_f32 v12, v12, v13\n"
"v_mfma_f32_16x16x32_bf16 v[192:195], a[108:111], a[240:243], v[192:195]\n"
"s_add_u32 m0, s49, 12288\n"
"s_add_u32 s52, s56, 122880\n"
"v_cvt_pk_bf16_f32 v13, v14, v15\n"
"v_cvt_pk_bf16_f32 v14, v32, v33\n"
"v_cvt_pk_bf16_f32 v15, v34, v35\n"
"v_mfma_f32_16x16x32_bf16 v[196:199], a[108:111], a[244:247], v[196:199]\n"
"ds_read_b64_tr_b16 a[108:109], v241 offset:53504\n"
"v_mul_f32 v52, v236, v52\n"
"v_mul_f32 v53, v236, v53\n"
"v_mul_f32 v54, v236, v54\n"
"v_mfma_f32_16x16x32_bf16 v[160:163], a[112:115], a[0:3], v[160:163]\n"
"ds_write_b128 v251, v[120:123] offset:15360\n"
"ds_read_b64_tr_b16 a[110:111], v241 offset:55552\n"
"v_mul_f32 v55, v236, v55\n"
"v_mfma_f32_16x16x32_bf16 v[164:167], a[112:115], a[4:7], v[164:167]\n"
"buffer_load_dwordx4 v249, s[64:67], s52 offen lds\n"
"v_mul_f32 v72, v236, v72\n"
"v_mul_f32 v73, v236, v73\n"
"v_mul_f32 v74, v236, v74\n"
"v_mfma_f32_16x16x32_bf16 v[168:171], a[112:115], a[8:11], v[168:171]\n"
"s_add_u32 m0, s49, 16384\n"
"s_add_u32 s52, s56, 163840\n"
"v_mul_f32 v75, v236, v75\n"
"v_cvt_pk_bf16_f32 v52, v52, v53\n"
"v_cvt_pk_bf16_f32 v53, v54, v55\n"
"v_mfma_f32_16x16x32_bf16 v[172:175], a[112:115], a[12:15], v[172:175]\n"
"ds_write_b128 v251, v[4:7] offset:1024\n"
"v_cvt_pk_bf16_f32 v54, v72, v73\n"
"v_cvt_pk_bf16_f32 v55, v74, v75\n"
"v_mul_f32 v92, v236, v92\n"
"v_mfma_f32_16x16x32_bf16 v[176:179], a[112:115], a[16:19], v[176:179]\n"
"ds_read_b64_tr_b16 a[112:113], v242 offset:41216\n"
"v_mul_f32 v93, v236, v93\n"
"v_mul_f32 v94, v236, v94\n"
"v_mul_f32 v95, v236, v95\n"
"v_mfma_f32_16x16x32_bf16 v[180:183], a[116:119], a[0:3], v[180:183]\n"
"ds_read_b64_tr_b16 a[114:115], v242 offset:43264\n"
"v_mul_f32 v112, v236, v112\n"
"v_mul_f32 v113, v236, v113\n"
"v_mul_f32 v114, v236, v114\n"
"v_mfma_f32_16x16x32_bf16 v[184:187], a[116:119], a[4:7], v[184:187]\n"
"ds_write_b128 v251, v[44:47] offset:6144\n"
"v_mul_f32 v115, v236, v115\n"
"v_cvt_pk_bf16_f32 v92, v92, v93\n"
"v_cvt_pk_bf16_f32 v93, v94, v95\n"
"v_mfma_f32_16x16x32_bf16 v[188:191], a[116:119], a[8:11], v[188:191]\n"
"buffer_load_dwordx4 v249, s[64:67], s52 offen lds\n"
"v_cvt_pk_bf16_f32 v94, v112, v113\n"
"v_cvt_pk_bf16_f32 v95, v114, v115\n"
"v_mul_f32 v132, v236, v132\n"
"v_mfma_f32_16x16x32_bf16 v[192:195], a[116:119], a[12:15], v[192:195]\n"
"s_add_u32 m0, s49, 20480\n"
"s_add_u32 s52, s56, 204800\n"
"v_mul_f32 v133, v236, v133\n"
"v_mul_f32 v134, v236, v134\n"
"v_mul_f32 v135, v236, v135\n"
"v_mfma_f32_16x16x32_bf16 v[196:199], a[116:119], a[16:19], v[196:199]\n"
"ds_read_b64_tr_b16 a[116:117], v243 offset:41216\n"
"v_mul_f32 v152, v236, v152\n"
"v_mul_f32 v153, v236, v153\n"
"v_mul_f32 v154, v236, v154\n"
"v_mfma_f32_16x16x32_bf16 v[160:163], a[120:123], a[20:23], v[160:163]\n"
"ds_write_b128 v251, v[84:87] offset:11264\n"
"v_mul_f32 v155, v236, v155\n"
"v_cvt_pk_bf16_f32 v132, v132, v133\n"
"v_cvt_pk_bf16_f32 v133, v134, v135\n"
"v_mfma_f32_16x16x32_bf16 v[164:167], a[120:123], a[24:27], v[164:167]\n"
"ds_read_b64_tr_b16 a[118:119], v243 offset:43264\n"
"v_cvt_pk_bf16_f32 v134, v152, v153\n"
"v_cvt_pk_bf16_f32 v135, v154, v155\n"
"v_accvgpr_read_b32 v237, a160\n"
"v_mfma_f32_16x16x32_bf16 v[168:171], a[120:123], a[28:31], v[168:171]\n"
"v_cmp_nge_f32 vcc, s53, v237\n"
"v_rcp_f32 v237, v237\n"
"s_nop 0\n"
"s_or_b64 s[58:59], s[58:59], vcc\n"
"v_mfma_f32_16x16x32_bf16 v[172:175], a[120:123], a[32:35], v[172:175]\n"
"ds_write_b128 v251, v[124:127] offset:16384\n"
"v_mul_f32 v16, v237, v16\n"
"v_mul_f32 v17, v237, v17\n"
"v_mul_f32 v18, v237, v18\n"
"v_mfma_f32_16x16x32_bf16 v[176:179], a[120:123], a[36:39], v[176:179]\n"
"ds_read_b64_tr_b16 a[120:121], v242 offset:45312\n"
"v_mul_f32 v19, v237, v19\n"
"v_mul_f32 v36, v237, v36\n"
"v_mul_f32 v37, v237, v37\n"
"v_mfma_f32_16x16x32_bf16 v[180:183], a[124:127], a[20:23], v[180:183]\n"
"ds_read_b64_tr_b16 a[122:123], v242 offset:47360\n"
"v_mul_f32 v38, v237, v38\n"
"v_mul_f32 v39, v237, v39\n"
"v_cvt_pk_bf16_f32 v16, v16, v17\n"
"v_mfma_f32_16x16x32_bf16 v[184:187], a[124:127], a[24:27], v[184:187]\n"
"ds_write_b128 v251, v[8:11] offset:2048\n"
"v_cvt_pk_bf16_f32 v17, v18, v19\n"
"v_cvt_pk_bf16_f32 v18, v36, v37\n"
"v_cvt_pk_bf16_f32 v19, v38, v39\n"
"v_mfma_f32_16x16x32_bf16 v[188:191], a[124:127], a[28:31], v[188:191]\n"
"buffer_load_dwordx4 v249, s[64:67], s52 offen lds\n"
"v_mul_f32 v56, v237, v56\n"
"v_mul_f32 v57, v237, v57\n"
"v_mul_f32 v58, v237, v58\n"
"v_mfma_f32_16x16x32_bf16 v[192:195], a[124:127], a[32:35], v[192:195]\n"
"s_add_u32 m0, s49, 24576\n"
"s_add_u32 s52, s56, 245760\n"
"v_mul_f32 v59, v237, v59\n"
"v_mul_f32 v76, v237, v76\n"
"v_mul_f32 v77, v237, v77\n"
"v_mfma_f32_16x16x32_bf16 v[196:199], a[124:127], a[36:39], v[196:199]\n"
"ds_read_b64_tr_b16 a[124:125], v243 offset:45312\n"
"v_mul_f32 v78, v237, v78\n"
"v_mul_f32 v79, v237, v79\n"
"v_cvt_pk_bf16_f32 v56, v56, v57\n"
"v_mfma_f32_16x16x32_bf16 v[160:163], a[128:131], a[40:43], v[160:163]\n"
"ds_write_b128 v251, v[48:51] offset:7168\n"
"ds_read_b64_tr_b16 a[126:127], v243 offset:47360\n"
"v_cvt_pk_bf16_f32 v57, v58, v59\n"
"v_mfma_f32_16x16x32_bf16 v[164:167], a[128:131], a[44:47], v[164:167]\n"
"buffer_load_dwordx4 v249, s[64:67], s52 offen lds\n"
"v_cvt_pk_bf16_f32 v58, v76, v77\n"
"v_cvt_pk_bf16_f32 v59, v78, v79\n"
"v_mul_f32 v96, v237, v96\n"
"v_mfma_f32_16x16x32_bf16 v[168:171], a[128:131], a[48:51], v[168:171]\n"
"s_add_u32 m0, s49, 28672\n"
"s_add_u32 s52, s56, 286720\n"
"v_mul_f32 v97, v237, v97\n"
"v_mul_f32 v98, v237, v98\n"
"v_mul_f32 v99, v237, v99\n"
"v_mfma_f32_16x16x32_bf16 v[172:175], a[128:131], a[52:55], v[172:175]\n"
"ds_write_b128 v251, v[88:91] offset:12288\n"
"v_mul_f32 v116, v237, v116\n"
"v_mul_f32 v117, v237, v117\n"
"v_mul_f32 v118, v237, v118\n"
"v_mfma_f32_16x16x32_bf16 v[176:179], a[128:131], a[56:59], v[176:179]\n"
"ds_read_b64_tr_b16 a[128:129], v242 offset:49408\n"
"v_mul_f32 v119, v237, v119\n"
"v_cvt_pk_bf16_f32 v96, v96, v97\n"
"v_cvt_pk_bf16_f32 v97, v98, v99\n"
"v_mfma_f32_16x16x32_bf16 v[180:183], a[132:135], a[40:43], v[180:183]\n"
"ds_read_b64_tr_b16 a[130:131], v242 offset:51456\n"
"v_cvt_pk_bf16_f32 v98, v116, v117\n"
"v_cvt_pk_bf16_f32 v99, v118, v119\n"
"v_mul_f32 v136, v237, v136\n"
"v_mfma_f32_16x16x32_bf16 v[184:187], a[132:135], a[44:47], v[184:187]\n"
"ds_write_b128 v251, v[128:131] offset:17408\n"
"v_mul_f32 v137, v237, v137\n"
"v_mul_f32 v138, v237, v138\n"
"v_mul_f32 v139, v237, v139\n"
"v_mfma_f32_16x16x32_bf16 v[188:191], a[132:135], a[48:51], v[188:191]\n"
"buffer_load_dwordx4 v249, s[64:67], s52 offen lds\n"
"v_mul_f32 v156, v237, v156\n"
"v_mul_f32 v157, v237, v157\n"
"v_mul_f32 v158, v237, v158\n"
"v_mfma_f32_16x16x32_bf16 v[192:195], a[132:135], a[52:55], v[192:195]\n"
"v_mul_f32 v159, v237, v159\n"
"v_cvt_pk_bf16_f32 v136, v136, v137\n"
"v_cvt_pk_bf16_f32 v137, v138, v139\n"
"v_mfma_f32_16x16x32_bf16 v[196:199], a[132:135], a[56:59], v[196:199]\n"
"ds_read_b64_tr_b16 a[132:133], v243 offset:49408\n"
"v_cvt_pk_bf16_f32 v138, v156, v157\n"
"v_cvt_pk_bf16_f32 v139, v158, v159\n"
"v_mfma_f32_16x16x32_bf16 v[160:163], a[136:139], a[60:63], v[160:163]\n"
"ds_write_b128 v251, v[12:15] offset:3072\n"
"ds_read_b64_tr_b16 a[134:135], v243 offset:51456\n"
"v_mfma_f32_16x16x32_bf16 v[164:167], a[136:139], a[64:67], v[164:167]\n"
"v_mfma_f32_16x16x32_bf16 v[168:171], a[136:139], a[68:71], v[168:171]\n"
"v_mfma_f32_16x16x32_bf16 v[172:175], a[136:139], a[72:75], v[172:175]\n"
"ds_write_b128 v251, v[52:55] offset:8192\n"
"v_mfma_f32_16x16x32_bf16 v[176:179], a[136:139], a[76:79], v[176:179]\n"
"ds_read_b64_tr_b16 a[136:137], v242 offset:53504\n"
"v_mfma_f32_16x16x32_bf16 v[180:183], a[140:143], a[60:63], v[180:183]\n"
"ds_read_b64_tr_b16 a[138:139], v242 offset:55552\n"
"v_mfma_f32_16x16x32_bf16 v[184:187], a[140:143], a[64:67], v[184:187]\n"
"ds_write_b128 v251, v[92:95] offset:13312\n"
"v_mfma_f32_16x16x32_bf16 v[188:191], a[140:143], a[68:71], v[188:191]\n"
"v_mfma_f32_16x16x32_bf16 v[192:195], a[140:143], a[72:75], v[192:195]\n"
"v_mfma_f32_16x16x32_bf16 v[196:199], a[140:143], a[76:79], v[196:199]\n"
"ds_read_b64_tr_b16 a[140:141], v243 offset:53504\n"
"ds_read_b64_tr_b16 a[142:143], v243 offset:55552\n"
"s_waitcnt vmcnt(0) lgkmcnt(0)\n"
"s_barrier\n"
"v_mfma_f32_16x16x32_bf16 v[160:163], a[80:83], v[0:3], v[160:163]\n"
"ds_write_b128 v251, v[132:135] offset:18432\n"
"s_waitcnt vmcnt(16)\n"
"v_mfma_f32_16x16x32_bf16 v[164:167], a[80:83], v[4:7], v[164:167]\n"
"s_add_u32 m0, s49, 106752\n"
"s_add_u32 s52, s56, 327680\n"
"s_or_b32 s58, s58, s59\n"
"v_mfma_f32_16x16x32_bf16 v[168:171], a[80:83], v[8:11], v[168:171]\n"
"buffer_load_dwordx4 v249, s[64:67], s52 offen lds\n"
"v_and_b32 v255, 2147483647, v255\n"
"v_mfma_f32_16x16x32_bf16 v[172:175], a[80:83], v[12:15], v[172:175]\n"
"ds_write_b128 v251, v[16:19] offset:4096\n"
"v_mfma_f32_16x16x32_bf16 v[176:179], a[80:83], v[16:19], v[176:179]\n"
"ds_read_b64_tr_b16 a[80:81], v245 offset:0\n"
"v_mfma_f32_16x16x32_bf16 v[180:183], a[84:87], v[0:3], v[180:183]\n"
"ds_read_b64_tr_b16 a[82:83], v245 offset:2048\n"
"s_add_u32 m0, s49, 110848\n"
"v_mfma_f32_16x16x32_bf16 v[184:187], a[84:87], v[4:7], v[184:187]\n"
"ds_write_b128 v251, v[56:59] offset:9216\n"
"s_add_u32 s52, s56, 368640\n"
"v_mfma_f32_16x16x32_bf16 v[188:191], a[84:87], v[8:11], v[188:191]\n"
"buffer_load_dwordx4 v249, s[64:67], s52 offen lds\n"
"v_mfma_f32_16x16x32_bf16 v[192:195], a[84:87], v[12:15], v[192:195]\n"
"s_cmp_lg_u32 s58, 0\n"
"s_cselect_b32 s59, 0x80000000, 0\n"
"v_readlane_b32 s62, v254, 7\n"
"v_mfma_f32_16x16x32_bf16 v[196:199], a[84:87], v[16:19], v[196:199]\n"
"ds_read_b64_tr_b16 a[84:85], v246 offset:0\n"
"s_add_u32 m0, s49, 114944\n"
"v_mfma_f32_16x16x32_bf16 v[160:163], a[88:91], v[40:43], v[160:163]\n"
"ds_write_b128 v251, v[96:99] offset:14336\n"
"s_add_u32 s52, s56, 409600\n"
"v_mfma_f32_16x16x32_bf16 v[164:167], a[88:91], v[44:47], v[164:167]\n"
"ds_read_b64_tr_b16 a[86:87], v246 offset:2048\n"
"v_readlane_b32 s63, v255, 7\n"
"v_mfma_f32_16x16x32_bf16 v[168:171], a[88:91], v[48:51], v[168:171]\n"
"buffer_load_dwordx4 v249, s[64:67], s52 offen lds\n"
"v_or_b32 v255, s59, v255\n"
"v_mfma_f32_16x16x32_bf16 v[172:175], a[88:91], v[52:55], v[172:175]\n"
"ds_write_b128 v251, v[136:139] offset:19456\n"
"s_add_u32 m0, s49, 119040\n"
"v_mfma_f32_16x16x32_bf16 v[176:179], a[88:91], v[56:59], v[176:179]\n"
"ds_read_b64_tr_b16 a[88:89], v245 offset:4096\n"
"s_add_u32 s52, s56, 450560\n"
"v_mfma_f32_16x16x32_bf16 v[180:183], a[92:95], v[40:43], v[180:183]\n"
"ds_read_b64_tr_b16 a[90:91], v245 offset:6144\n"
"v_accvgpr_write_b32 a254, v254\n"
"v_mfma_f32_16x16x32_bf16 v[184:187], a[92:95], v[44:47], v[184:187]\n"
"buffer_load_dwordx4 v249, s[64:67], s52 offen lds\n"
"v_accvgpr_write_b32 a255, v255\n"
"v_mfma_f32_16x16x32_bf16 v[188:191], a[92:95], v[48:51], v[188:191]\n"
"s_add_u32 m0, s49, 123136\n"
"s_add_u32 s52, s56, 491520\n"
"s_mov_b32 s59, 0\n"
"v_mfma_f32_16x16x32_bf16 v[192:195], a[92:95], v[52:55], v[192:195]\n"
"s_waitcnt lgkmcnt(2)\n"
"s_barrier\n"
"v_lshrrev_b32 v254, 4, v253\n"
"v_mfma_f32_16x16x32_bf16 v[196:199], a[92:95], v[56:59], v[196:199]\n"
"ds_read_b64_tr_b16 a[92:93], v246 offset:4096\n"
"v_and_b32 v255, 15, v254\n"
"v_mfma_f32_16x16x32_bf16 v[160:163], a[96:99], v[80:83], v[160:163]\n"
"ds_read_b64_tr_b16 a[94:95], v246 offset:6144\n"
"v_lshrrev_b32 v254, 4, v254\n"
"v_mfma_f32_16x16x32_bf16 v[164:167], a[96:99], v[84:87], v[164:167]\n"
"ds_read_b128 v[20:23], v252 offset:0\n"
"v_mul_u32_u24 v250, 1280, v255\n"
"v_mfma_f32_16x16x32_bf16 v[168:171], a[96:99], v[88:91], v[168:171]\n"
"ds_read_b128 v[24:27], v252 offset:1024\n"
"v_and_b32 v251, 1, v254\n"
"v_mfma_f32_16x16x32_bf16 v[172:175], a[96:99], v[92:95], v[172:175]\n"
"ds_read_b128 v[28:31], v252 offset:2048\n"
"v_lshl_add_u32 v250, v251, 5, v250\n"
"v_mfma_f32_16x16x32_bf16 v[176:179], a[96:99], v[96:99], v[176:179]\n"
"ds_read_b64_tr_b16 a[96:97], v245 offset:8192\n"
"v_lshrrev_b32 v251, 1, v254\n"
"v_mfma_f32_16x16x32_bf16 v[180:183], a[100:103], v[80:83], v[180:183]\n"
"ds_read_b64_tr_b16 a[98:99], v245 offset:10240\n"
"v_lshl_add_u32 v250, v251, 4, v250\n"
"v_mfma_f32_16x16x32_bf16 v[184:187], a[100:103], v[84:87], v[184:187]\n"
"ds_read_b128 v[32:35], v252 offset:3072\n"
"v_lshrrev_b32 v251, 2, v255\n"
"v_mfma_f32_16x16x32_bf16 v[188:191], a[100:103], v[88:91], v[188:191]\n"
"ds_read_b128 v[36:39], v252 offset:4096\n"
"v_cmp_eq_u32 vcc, v251, v254\n"
"v_mfma_f32_16x16x32_bf16 v[192:195], a[100:103], v[92:95], v[192:195]\n"
"ds_read_b128 v[60:63], v252 offset:5120\n"
"v_and_b32 v254, 1, v255\n"
"v_mfma_f32_16x16x32_bf16 v[196:199], a[100:103], v[96:99], v[196:199]\n"
"ds_read_b64_tr_b16 a[100:101], v246 offset:8192\n"
"v_lshlrev_b32 v254, 4, v254\n"
"v_mfma_f32_16x16x32_bf16 v[160:163], a[104:107], v[120:123], v[160:163]\n"
"ds_read_b64_tr_b16 a[102:103], v246 offset:10240\n"
"v_mov_b32 v251, 16256\n"
"v_mfma_f32_16x16x32_bf16 v[164:167], a[104:107], v[124:127], v[164:167]\n"
"ds_read_b128 v[64:67], v252 offset:6144\n"
"v_lshlrev_b32 v251, v254, v251\n"
"v_mfma_f32_16x16x32_bf16 v[168:171], a[104:107], v[128:131], v[168:171]\n"
"ds_read_b128 v[68:71], v252 offset:7168\n"
"v_cndmask_b32 v251, 0, v251, vcc\n"
"v_mfma_f32_16x16x32_bf16 v[172:175], a[104:107], v[132:135], v[172:175]\n"
"ds_read_b128 v[72:75], v252 offset:8192\n"
"v_and_b32 v255, 2, v255\n"
"v_mfma_f32_16x16x32_bf16 v[176:179], a[104:107], v[136:139], v[176:179]\n"
"ds_read_b64_tr_b16 a[104:105], v245 offset:12288\n"
"v_cmp_eq_u32 vcc, 0, v255\n"
"v_mfma_f32_16x16x32_bf16 v[180:183], a[108:111], v[120:123], v[180:183]\n"
"ds_read_b64_tr_b16 a[106:107], v245 offset:14336\n"
"v_mfma_f32_16x16x32_bf16 v[184:187], a[108:111], v[124:127], v[184:187]\n"
"ds_read_b128 v[76:79], v252 offset:9216\n"
"v_mfma_f32_16x16x32_bf16 v[188:191], a[108:111], v[128:131], v[188:191]\n"
"ds_read_b128 v[100:103], v252 offset:10240\n"
"v_mfma_f32_16x16x32_bf16 v[192:195], a[108:111], v[132:135], v[192:195]\n"
"ds_read_b128 v[104:107], v252 offset:11264\n"
"v_mfma_f32_16x16x32_bf16 v[196:199], a[108:111], v[136:139], v[196:199]\n"
"s_waitcnt lgkmcnt(11)\n"
"ds_read_b64_tr_b16 a[108:109], v246 offset:12288\n"
"v_mfma_f32_16x16x32_bf16 v[160:163], a[112:115], v[20:23], v[160:163]\n"
"ds_read_b64_tr_b16 a[110:111], v246 offset:14336\n"
"s_and_b32 s57, s74, 15\n"
"v_mfma_f32_16x16x32_bf16 v[164:167], a[112:115], v[24:27], v[164:167]\n"
"ds_read_b128 v[108:111], v252 offset:12288\n"
"s_mul_i32 s80, s57, 7\n"
"v_mfma_f32_16x16x32_bf16 v[168:171], a[112:115], v[28:31], v[168:171]\n"
"ds_read_b128 v[112:115], v252 offset:13312\n"
"s_add_u32 s80, s80, s79\n"
"v_mfma_f32_16x16x32_bf16 v[172:175], a[112:115], v[32:35], v[172:175]\n"
"ds_read_b128 v[116:119], v252 offset:14336\n"
"v_mfma_f32_16x16x32_bf16 v[176:179], a[112:115], v[36:39], v[176:179]\n"
"ds_read_b128 v[140:143], v252 offset:15360\n"
"ds_read_b128 v[144:147], v252 offset:16384\n"
"ds_read_b128 v[148:151], v252 offset:17408\n"
"ds_read_b128 v[152:155], v252 offset:18432\n"
"ds_read_b128 v[156:159], v252 offset:19456\n"
"v_mfma_f32_16x16x32_bf16 v[180:183], a[116:119], v[20:23], v[180:183]\n"
"buffer_load_dwordx4 v249, s[64:67], s52 offen lds\n"
"s_add_u32 m0, s49, 127232\n"
"v_mfma_f32_16x16x32_bf16 v[184:187], a[116:119], v[24:27], v[184:187]\n"
"s_add_u32 s52, s56, 532480\n"
"buffer_load_dwordx4 v249, s[64:67], s52 offen lds\n"
"s_add_u32 m0, s49, 131328\n"
"s_add_u32 s52, s56, 573440\n"
"buffer_load_dwordx4 v249, s[64:67], s52 offen lds\n"
"s_add_u32 m0, s49, 135424\n"
"s_add_u32 s52, s56, 614400\n"
"buffer_load_dwordx4 v249, s[64:67], s52 offen lds\n"
"v_mfma_f32_16x16x32_bf16 v[188:191], a[116:119], v[28:31], v[188:191]\n"
"s_waitcnt lgkmcnt(0)\n"
"s_barrier\n"
"s_lshr_b64 s[72:73], s[72:73], 4\n"
"v_mfma_f32_16x16x32_bf16 v[192:195], a[116:119], v[32:35], v[192:195]\n"
"ds_read_b64_tr_b16 a[112:113], v247 offset:0\n"
"ds_read_b64_tr_b16 a[114:115], v247 offset:2048\n"
"v_mfma_f32_16x16x32_bf16 v[196:199], a[116:119], v[36:39], v[196:199]\n"
"s_waitcnt lgkmcnt(14)\n"
"s_and_b32 s56, s72, 15\n"
"s_lshl_b32 s56, s56, 7\n"
"v_mfma_f32_16x16x32_bf16 v[160:163], a[120:123], v[60:63], v[160:163]\n"
"ds_read_b64_tr_b16 a[116:117], v248 offset:0\n"
"s_add_u32 m0, s49, 32768\n"
"v_mfma_f32_16x16x32_bf16 v[164:167], a[120:123], v[64:67], v[164:167]\n"
"ds_read_b64_tr_b16 a[118:119], v248 offset:2048\n"
"s_add_u32 s52, s56, 0\n"
"v_mfma_f32_16x16x32_bf16 v[168:171], a[120:123], v[68:71], v[168:171]\n"
"s_nop 1\n"
"v_cndmask_b32 v254, 0, v251, vcc\n"
"v_mfma_f32_16x16x32_bf16 v[172:175], a[120:123], v[72:75], v[172:175]\n"
"v_cndmask_b32_e64 v255, v251, 0, vcc\n"
"v_accvgpr_write_b32 a250, v254\n"
"v_mfma_f32_16x16x32_bf16 v[176:179], a[120:123], v[76:79], v[176:179]\n"
"ds_read_b64_tr_b16 a[120:121], v247 offset:4096\n"
"v_accvgpr_write_b32 a251, v255\n"
"v_mfma_f32_16x16x32_bf16 v[180:183], a[124:127], v[60:63], v[180:183]\n"
"ds_read_b64_tr_b16 a[122:123], v247 offset:6144\n"
"v_accvgpr_write_b32 a248, 0\n"
"v_mfma_f32_16x16x32_bf16 v[184:187], a[124:127], v[64:67], v[184:187]\n"
"v_accvgpr_write_b32 a249, 0\n"
"v_accvgpr_write_b32 a252, 0\n"
"v_mfma_f32_16x16x32_bf16 v[188:191], a[124:127], v[68:71], v[188:191]\n"
"s_cmp_lt_u32 s57, 8\n"
"s_cselect_b32 s57, s80, s98\n"
"v_accvgpr_write_b32 a253, 0\n"
"v_mfma_f32_16x16x32_bf16 v[192:195], a[124:127], v[72:75], v[192:195]\n"
"s_mul_i32 s57, s57, 20736\n"
"s_add_u32 s57, s57, s70\n"
"s_add_u32 s80, s57, 4096\n"
"v_mfma_f32_16x16x32_bf16 v[196:199], a[124:127], v[76:79], v[196:199]\n"
"s_waitcnt lgkmcnt(11)\n"
"ds_read_b64_tr_b16 a[124:125], v248 offset:4096\n"
"v_mfma_f32_16x16x32_bf16 v[160:163], a[128:131], v[100:103], v[160:163]\n"
"ds_read_b64_tr_b16 a[126:127], v248 offset:6144\n"
"s_lshr_b64 s[74:75], s[74:75], 4\n"
"v_mfma_f32_16x16x32_bf16 v[164:167], a[128:131], v[104:107], v[164:167]\n"
"s_add_u32 s98, s98, 1\n"
"s_add_u32 s79, s79, 1\n"
"v_lshrrev_b32 v252, 1, v253\n"
"v_mfma_f32_16x16x32_bf16 v[168:171], a[128:131], v[108:111], v[168:171]\n"
"s_cmp_eq_u32 s79, 6\n"
"s_cselect_b32 s96, 31, 0x80\n"
"v_and_b32 v252, 127, v252\n"
"v_mfma_f32_16x16x32_bf16 v[172:175], a[128:131], v[112:115], v[172:175]\n"
"s_lshl_b32 s71, %[hg], 7\n"
"s_add_u32 s71, s71, 128\n"
"v_add_u32 v252, s71, v252\n"
"v_mfma_f32_16x16x32_bf16 v[176:179], a[128:131], v[116:119], v[176:179]\n"
"ds_read_b64_tr_b16 a[128:129], v247 offset:8192\n"
"v_mfma_f32_16x16x32_bf16 v[180:183], a[132:135], v[100:103], v[180:183]\n"
"ds_read_b64_tr_b16 a[130:131], v247 offset:10240\n"
"v_mfma_f32_16x16x32_bf16 v[184:187], a[132:135], v[104:107], v[184:187]\n"
"buffer_load_dwordx4 v249, s[64:67], s52 offen lds\n"
"s_add_u32 m0, s49, 36864\n"
"v_mfma_f32_16x16x32_bf16 v[188:191], a[132:135], v[108:111], v[188:191]\n"
"s_add_u32 s52, s56, 40960\n"
"buffer_load_dwordx4 v249, s[64:67], s52 offen lds\n"
"v_mfma_f32_16x16x32_bf16 v[192:195], a[132:135], v[112:115], v[192:195]\n"
"s_add_u32 m0, s49, 40960\n"
"s_add_u32 s52, s56, 81920\n"
"v_mfma_f32_16x16x32_bf16 v[196:199], a[132:135], v[116:119], v[196:199]\n"
"s_waitcnt lgkmcnt(10)\n"
"ds_read_b64_tr_b16 a[132:133], v248 offset:8192\n"
"v_mfma_f32_16x16x32_bf16 v[160:163], a[136:139], v[140:143], v[160:163]\n"
"ds_read_b64_tr_b16 a[134:135], v248 offset:10240\n"
"v_mfma_f32_16x16x32_bf16 v[164:167], a[136:139], v[144:147], v[164:167]\n"
"buffer_load_dwordx4 v249, s[64:67], s52 offen lds\n"
"s_add_u32 m0, s49, 45056\n"
"v_mfma_f32_16x16x32_bf16 v[168:171], a[136:139], v[148:151], v[168:171]\n"
"s_add_u32 s52, s56, 122880\n"
"buffer_load_dwordx4 v249, s[64:67], s52 offen lds\n"
"v_mfma_f32_16x16x32_bf16 v[172:175], a[136:139], v[152:155], v[172:175]\n"
"s_add_u32 m0, s49, 49152\n"
"s_add_u32 s52, s56, 163840\n"
"v_mfma_f32_16x16x32_bf16 v[176:179], a[136:139], v[156:159], v[176:179]\n"
"ds_read_b64_tr_b16 a[136:137], v247 offset:12288\n"
"v_mfma_f32_16x16x32_bf16 v[180:183], a[140:143], v[140:143], v[180:183]\n"
"ds_read_b64_tr_b16 a[138:139], v247 offset:14336\n"
"v_mfma_f32_16x16x32_bf16 v[184:187], a[140:143], v[144:147], v[184:187]\n"
"buffer_load_dwordx4 v249, s[64:67], s52 offen lds\n"
"s_add_u32 m0, s49, 53248\n"
"v_mfma_f32_16x16x32_bf16 v[188:191], a[140:143], v[148:151], v[188:191]\n"
"s_add_u32 s52, s56, 204800\n"
"buffer_load_dwordx4 v249, s[64:67], s52 offen lds\n"
"v_mfma_f32_16x16x32_bf16 v[192:195], a[140:143], v[152:155], v[192:195]\n"
"s_add_u32 m0, s49, 57344\n"
"s_add_u32 s52, s56, 245760\n"
"v_mfma_f32_16x16x32_bf16 v[196:199], a[140:143], v[156:159], v[196:199]\n"
"buffer_load_dwordx4 v249, s[64:67], s52 offen lds\n"
"s_add_u32 m0, s49, 61440\n"
"s_add_u32 s52, s56, 286720\n"
"buffer_load_dwordx4 v249, s[64:67], s52 offen lds\n"
"ds_read_b64_tr_b16 a[140:141], v248 offset:12288\n"
"ds_read_b64_tr_b16 a[142:143], v248 offset:14336\n"
"L_ol_%=:\n"
"s_waitcnt vmcnt(8)\n"
"s_barrier\n"
"s_waitcnt lgkmcnt(15)\n"
"v_mfma_f32_16x16x32_bf16 v[200:203], a[80:83], a[168:171], 0\n"
"v_mfma_f32_16x16x32_bf16 v[204:207], a[80:83], a[172:175], 0\n"
"v_mfma_f32_16x16x32_bf16 v[208:211], a[80:83], a[176:179], 0\n"
"v_mfma_f32_16x16x32_bf16 v[212:215], a[80:83], a[180:183], 0\n"
"v_mfma_f32_16x16x32_bf16 v[216:219], a[80:83], a[184:187], 0\n"
"ds_read_b64_tr_b16 a[80:81], v240 offset:41216\n"
"v_mfma_f32_16x16x32_bf16 v[220:223], a[84:87], a[168:171], 0\n"
"ds_read_b64_tr_b16 a[82:83], v240 offset:43264\n"
"v_cvt_pk_bf16_f32 v160, v160, v161\n"
"v_mfma_f32_16x16x32_bf16 v[224:227], a[84:87], a[172:175], 0\n"
"v_cvt_pk_bf16_f32 v161, v162, v163\n"
"v_cvt_pk_bf16_f32 v162, v180, v181\n"
"v_mfma_f32_16x16x32_bf16 v[228:231], a[84:87], a[176:179], 0\n"
"v_cvt_pk_bf16_f32 v163, v182, v183\n"
"v_mfma_f32_16x16x32_bf16 v[232:235], a[84:87], a[180:183], 0\n"
"buffer_store_dwordx4 v[160:163], v253, s[44:47], s57 offen sc1\n"
"v_mfma_f32_16x16x32_bf16 v[236:239], a[84:87], a[184:187], 0\n"
"ds_read_b64_tr_b16 a[84:85], v241 offset:41216\n"
"v_mfma_f32_16x16x32_bf16 v[200:203], a[88:91], a[188:191], v[200:203]\n"
"ds_read_b64_tr_b16 a[86:87], v241 offset:43264\n"
"v_cvt_pk_bf16_f32 v164, v164, v165\n"
"v_mfma_f32_16x16x32_bf16 v[204:207], a[88:91], a[192:195], v[204:207]\n"
"v_cvt_pk_bf16_f32 v165, v166, v167\n"
"v_cvt_pk_bf16_f32 v166, v184, v185\n"
"v_mfma_f32_16x16x32_bf16 v[208:211], a[88:91], a[196:199], v[208:211]\n"
"v_cvt_pk_bf16_f32 v167, v186, v187\n"
"v_mfma_f32_16x16x32_bf16 v[212:215], a[88:91], a[200:203], v[212:215]\n"
"buffer_store_dwordx4 v[164:167], v253, s[44:47], s57 offen offset:1024 sc1\n"
"v_mfma_f32_16x16x32_bf16 v[216:219], a[88:91], a[204:207], v[216:219]\n"
"ds_read_b64_tr_b16 a[88:89], v240 offset:45312\n"
"v_mfma_f32_16x16x32_bf16 v[220:223], a[92:95], a[188:191], v[220:223]\n"
"ds_read_b64_tr_b16 a[90:91], v240 offset:47360\n"
"v_cvt_pk_bf16_f32 v168, v168, v169\n"
"v_mfma_f32_16x16x32_bf16 v[224:227], a[92:95], a[192:195], v[224:227]\n"
"v_cvt_pk_bf16_f32 v169, v170, v171\n"
"v_cvt_pk_bf16_f32 v170, v188, v189\n"
"v_mfma_f32_16x16x32_bf16 v[228:231], a[92:95], a[196:199], v[228:231]\n"
"v_cvt_pk_bf16_f32 v171, v190, v191\n"
"v_mfma_f32_16x16x32_bf16 v[232:235], a[92:95], a[200:203], v[232:235]\n"
"buffer_store_dwordx4 v[168:171], v253, s[44:47], s57 offen offset:2048 sc1\n"
"v_mfma_f32_16x16x32_bf16 v[236:239], a[92:95], a[204:207], v[236:239]\n"
"s_waitcnt lgkmcnt(15)\n"
"ds_read_b64_tr_b16 a[92:93], v241 offset:45312\n"
"v_mfma_f32_16x16x32_bf16 v[200:203], a[96:99], a[208:211], v[200:203]\n"
"ds_read_b64_tr_b16 a[94:95], v241 offset:47360\n"
"v_cvt_pk_bf16_f32 v172, v172, v173\n"
"v_mfma_f32_16x16x32_bf16 v[204:207], a[96:99], a[212:215], v[204:207]\n"
"v_cvt_pk_bf16_f32 v173, v174, v175\n"
"v_cvt_pk_bf16_f32 v174, v192, v193\n"
"v_mfma_f32_16x16x32_bf16 v[208:211], a[96:99], a[216:219], v[208:211]\n"
"v_cvt_pk_bf16_f32 v175, v194, v195\n"
"v_mfma_f32_16x16x32_bf16 v[212:215], a[96:99], a[220:223], v[212:215]\n"
"buffer_store_dwordx4 v[172:175], v253, s[44:47], s57 offen offset:3072 sc1\n"
"v_mfma_f32_16x16x32_bf16 v[216:219], a[96:99], a[224:227], v[216:219]\n"
"ds_read_b64_tr_b16 a[96:97], v240 offset:49408\n"
"v_mfma_f32_16x16x32_bf16 v[220:223], a[100:103], a[208:211], v[220:223]\n"
"ds_read_b64_tr_b16 a[98:99], v240 offset:51456\n"
"v_cvt_pk_bf16_f32 v176, v176, v177\n"
"v_mfma_f32_16x16x32_bf16 v[224:227], a[100:103], a[212:215], v[224:227]\n"
"v_cvt_pk_bf16_f32 v177, v178, v179\n"
"v_cvt_pk_bf16_f32 v178, v196, v197\n"
"v_mfma_f32_16x16x32_bf16 v[228:231], a[100:103], a[216:219], v[228:231]\n"
"v_cvt_pk_bf16_f32 v179, v198, v199\n"
"v_mfma_f32_16x16x32_bf16 v[232:235], a[100:103], a[220:223], v[232:235]\n"
"buffer_store_dwordx4 v[176:179], v253, s[44:47], s80 offen sc1\n"
"v_mfma_f32_16x16x32_bf16 v[236:239], a[100:103], a[224:227], v[236:239]\n"
"ds_read_b64_tr_b16 a[100:101], v241 offset:49408\n"
"v_mfma_f32_16x16x32_bf16 v[200:203], a[104:107], a[228:231], v[200:203]\n"
"ds_read_b64_tr_b16 a[102:103], v241 offset:51456\n"
"v_mfma_f32_16x16x32_bf16 v[204:207], a[104:107], a[232:235], v[204:207]\n"
"v_mfma_f32_16x16x32_bf16 v[208:211], a[104:107], a[236:239], v[208:211]\n"
"v_mfma_f32_16x16x32_bf16 v[212:215], a[104:107], a[240:243], v[212:215]\n"
"v_mfma_f32_16x16x32_bf16 v[216:219], a[104:107], a[244:247], v[216:219]\n"
"ds_read_b64_tr_b16 a[104:105], v240 offset:53504\n"
"v_mfma_f32_16x16x32_bf16 v[220:223], a[108:111], a[228:231], v[220:223]\n"
"ds_read_b64_tr_b16 a[106:107], v240 offset:55552\n"
"v_mfma_f32_16x16x32_bf16 v[224:227], a[108:111], a[232:235], v[224:227]\n"
"v_mfma_f32_16x16x32_bf16 v[228:231], a[108:111], a[236:239], v[228:231]\n"
"v_mfma_f32_16x16x32_bf16 v[232:235], a[108:111], a[240:243], v[232:235]\n"
"v_mfma_f32_16x16x32_bf16 v[236:239], a[108:111], a[244:247], v[236:239]\n"
"s_waitcnt lgkmcnt(15)\n"
"ds_read_b64_tr_b16 a[108:109], v241 offset:53504\n"
"v_mfma_f32_16x16x32_bf16 v[200:203], a[112:115], a[0:3], v[200:203]\n"
"ds_read_b64_tr_b16 a[110:111], v241 offset:55552\n"
"v_mfma_f32_16x16x32_bf16 v[204:207], a[112:115], a[4:7], v[204:207]\n"
"v_mfma_f32_16x16x32_bf16 v[208:211], a[112:115], a[8:11], v[208:211]\n"
"v_mfma_f32_16x16x32_bf16 v[212:215], a[112:115], a[12:15], v[212:215]\n"
"v_mfma_f32_16x16x32_bf16 v[216:219], a[112:115], a[16:19], v[216:219]\n"
"ds_read_b64_tr_b16 a[112:113], v242 offset:41216\n"
"v_mfma_f32_16x16x32_bf16 v[220:223], a[116:119], a[0:3], v[220:223]\n"
"ds_read_b64_tr_b16 a[114:115], v242 offset:43264\n"
"v_mfma_f32_16x16x32_bf16 v[224:227], a[116:119], a[4:7], v[224:227]\n"
"v_mfma_f32_16x16x32_bf16 v[228:231], a[116:119], a[8:11], v[228:231]\n"
"v_mfma_f32_16x16x32_bf16 v[232:235], a[116:119], a[12:15], v[232:235]\n"
"v_mfma_f32_16x16x32_bf16 v[236:239], a[116:119], a[16:19], v[236:239]\n"
"ds_read_b64_tr_b16 a[116:117], v243 offset:41216\n"
"v_mfma_f32_16x16x32_bf16 v[200:203], a[120:123], a[20:23], v[200:203]\n"
"ds_read_b64_tr_b16 a[118:119], v243 offset:43264\n"
"v_mfma_f32_16x16x32_bf16 v[204:207], a[120:123], a[24:27], v[204:207]\n"
"v_mfma_f32_16x16x32_bf16 v[208:211], a[120:123], a[28:31], v[208:211]\n"
"v_mfma_f32_16x16x32_bf16 v[212:215], a[120:123], a[32:35], v[212:215]\n"
"v_mfma_f32_16x16x32_bf16 v[216:219], a[120:123], a[36:39], v[216:219]\n"
"ds_read_b64_tr_b16 a[120:121], v242 offset:45312\n"
"v_mfma_f32_16x16x32_bf16 v[220:223], a[124:127], a[20:23], v[220:223]\n"
"ds_read_b64_tr_b16 a[122:123], v242 offset:47360\n"
"v_mfma_f32_16x16x32_bf16 v[224:227], a[124:127], a[24:27], v[224:227]\n"
"v_mfma_f32_16x16x32_bf16 v[228:231], a[124:127], a[28:31], v[228:231]\n"
"v_mfma_f32_16x16x32_bf16 v[232:235], a[124:127], a[32:35], v[232:235]\n"
"v_mfma_f32_16x16x32_bf16 v[236:239], a[124:127], a[36:39], v[236:239]\n"
"s_waitcnt lgkmcnt(15)\n"
"ds_read_b64_tr_b16 a[124:125], v243 offset:45312\n"
"v_mfma_f32_16x16x32_bf16 v[200:203], a[128:131], a[40:43], v[200:203]\n"
"ds_read_b64_tr_b16 a[126:127], v243 offset:47360\n"
"v_mfma_f32_16x16x32_bf16 v[204:207], a[128:131], a[44:47], v[204:207]\n"
"v_mfma_f32_16x16x32_bf16 v[208:211], a[128:131], a[48:51], v[208:211]\n"
"v_mfma_f32_16x16x32_bf16 v[212:215], a[128:131], a[52:55], v[212:215]\n"
"v_mfma_f32_16x16x32_bf16 v[216:219], a[128:131], a[56:59], v[216:219]\n"
"ds_read_b64_tr_b16 a[128:129], v242 offset:49408\n"
"v_mfma_f32_16x16x32_bf16 v[220:223], a[132:135], a[40:43], v[220:223]\n"
"ds_read_b64_tr_b16 a[130:131], v242 offset:51456\n"
"v_mfma_f32_16x16x32_bf16 v[224:227], a[132:135], a[44:47], v[224:227]\n"
"v_mfma_f32_16x16x32_bf16 v[228:231], a[132:135], a[48:51], v[228:231]\n"
"v_mfma_f32_16x16x32_bf16 v[232:235], a[132:135], a[52:55], v[232:235]\n"
"v_mfma_f32_16x16x32_bf16 v[236:239], a[132:135], a[56:59], v[236:239]\n"
"ds_read_b64_tr_b16 a[132:133], v243 offset:49408\n"
"v_mfma_f32_16x16x32_bf16 v[200:203], a[136:139], a[60:63], v[200:203]\n"
"ds_read_b64_tr_b16 a[134:135], v243 offset:51456\n"
"v_mfma_f32_16x16x32_bf16 v[204:207], a[136:139], a[64:67], v[204:207]\n"
"v_mfma_f32_16x16x32_bf16 v[208:211], a[136:139], a[68:71], v[208:211]\n"
"v_mfma_f32_16x16x32_bf16 v[212:215], a[136:139], a[72:75], v[212:215]\n"
"v_mfma_f32_16x16x32_bf16 v[216:219], a[136:139], a[76:79], v[216:219]\n"
"ds_read_b64_tr_b16 a[136:137], v242 offset:53504\n"
"v_mfma_f32_16x16x32_bf16 v[220:223], a[140:143], a[60:63], v[220:223]\n"
"ds_read_b64_tr_b16 a[138:139], v242 offset:55552\n"
"v_mfma_f32_16x16x32_bf16 v[224:227], a[140:143], a[64:67], v[224:227]\n"
"v_mfma_f32_16x16x32_bf16 v[228:231], a[140:143], a[68:71], v[228:231]\n"
"v_mfma_f32_16x16x32_bf16 v[232:235], a[140:143], a[72:75], v[232:235]\n"
"v_mfma_f32_16x16x32_bf16 v[236:239], a[140:143], a[76:79], v[236:239]\n"
"s_waitcnt vmcnt(5)\n"
"s_barrier\n"
"s_waitcnt lgkmcnt(15)\n"
"v_mfma_f32_16x16x32_bf16 v[200:203], a[80:83], v[0:3], v[200:203]\n"
"ds_read_b64_tr_b16 a[140:141], v243 offset:53504\n"
"v_mfma_f32_16x16x32_bf16 v[204:207], a[80:83], v[4:7], v[204:207]\n"
"ds_read_b64_tr_b16 a[142:143], v243 offset:55552\n"
"v_mfma_f32_16x16x32_bf16 v[208:211], a[80:83], v[8:11], v[208:211]\n"
"v_mfma_f32_16x16x32_bf16 v[212:215], a[80:83], v[12:15], v[212:215]\n"
"v_mfma_f32_16x16x32_bf16 v[216:219], a[80:83], v[16:19], v[216:219]\n"
"ds_read_b64_tr_b16 a[80:81], v245 offset:32768\n"
"v_mfma_f32_16x16x32_bf16 v[220:223], a[84:87], v[0:3], v[220:223]\n"
"ds_read_b64_tr_b16 a[82:83], v245 offset:34816\n"
"v_mfma_f32_16x16x32_bf16 v[224:227], a[84:87], v[4:7], v[224:227]\n"
"s_add_u32 m0, s49, 65536\n"
"s_add_u32 s52, s56, 327680\n"
"v_mfma_f32_16x16x32_bf16 v[228:231], a[84:87], v[8:11], v[228:231]\n"
"buffer_load_dwordx4 v249, s[64:67], s52 offen lds\n"
"v_mfma_f32_16x16x32_bf16 v[232:235], a[84:87], v[12:15], v[232:235]\n"
"v_mfma_f32_16x16x32_bf16 v[236:239], a[84:87], v[16:19], v[236:239]\n"
"ds_read_b64_tr_b16 a[84:85], v246 offset:32768\n"
"v_mfma_f32_16x16x32_bf16 v[200:203], a[88:91], v[40:43], v[200:203]\n"
"ds_read_b64_tr_b16 a[86:87], v246 offset:34816\n"
"s_add_u32 m0, s49, 69632\n"
"v_mfma_f32_16x16x32_bf16 v[204:207], a[88:91], v[44:47], v[204:207]\n"
"s_add_u32 s52, s56, 368640\n"
"buffer_load_dwordx4 v249, s[64:67], s52 offen lds\n"
"v_mfma_f32_16x16x32_bf16 v[208:211], a[88:91], v[48:51], v[208:211]\n"
"v_mfma_f32_16x16x32_bf16 v[212:215], a[88:91], v[52:55], v[212:215]\n"
"v_mfma_f32_16x16x32_bf16 v[216:219], a[88:91], v[56:59], v[216:219]\n"
"ds_read_b64_tr_b16 a[88:89], v245 offset:36864\n"
"s_add_u32 m0, s49, 73728\n"
"v_mfma_f32_16x16x32_bf16 v[220:223], a[92:95], v[40:43], v[220:223]\n"
"ds_read_b64_tr_b16 a[90:91], v245 offset:38912\n"
"s_add_u32 s52, s56, 409600\n"
"v_mfma_f32_16x16x32_bf16 v[224:227], a[92:95], v[44:47], v[224:227]\n"
"buffer_load_dwordx4 v249, s[64:67], s52 offen lds\n"
"v_mfma_f32_16x16x32_bf16 v[228:231], a[92:95], v[48:51], v[228:231]\n"
"v_mfma_f32_16x16x32_bf16 v[232:235], a[92:95], v[52:55], v[232:235]\n"
"s_add_u32 m0, s49, 77824\n"
"s_add_u32 s52, s56, 450560\n"
"v_mfma_f32_16x16x32_bf16 v[236:239], a[92:95], v[56:59], v[236:239]\n"
"s_waitcnt lgkmcnt(15)\n"
"ds_read_b64_tr_b16 a[92:93], v246 offset:36864\n"
"v_mfma_f32_16x16x32_bf16 v[200:203], a[96:99], v[80:83], v[200:203]\n"
"ds_read_b64_tr_b16 a[94:95], v246 offset:38912\n"
"v_mfma_f32_16x16x32_bf16 v[204:207], a[96:99], v[84:87], v[204:207]\n"
"buffer_load_dwordx4 v249, s[64:67], s52 offen lds\n"
"v_mfma_f32_16x16x32_bf16 v[208:211], a[96:99], v[88:91], v[208:211]\n"
"s_add_u32 m0, s49, 81920\n"
"s_add_u32 s52, s56, 491520\n"
"v_mfma_f32_16x16x32_bf16 v[212:215], a[96:99], v[92:95], v[212:215]\n"
"buffer_load_dwordx4 v249, s[64:67], s52 offen lds\n"
"v_mfma_f32_16x16x32_bf16 v[216:219], a[96:99], v[96:99], v[216:219]\n"
"ds_read_b64_tr_b16 a[96:97], v245 offset:40960\n"
"v_mfma_f32_16x16x32_bf16 v[220:223], a[100:103], v[80:83], v[220:223]\n"
"ds_read_b64_tr_b16 a[98:99], v245 offset:43008\n"
"v_mfma_f32_16x16x32_bf16 v[224:227], a[100:103], v[84:87], v[224:227]\n"
"s_add_u32 m0, s49, 86016\n"
"s_add_u32 s52, s56, 532480\n"
"v_mfma_f32_16x16x32_bf16 v[228:231], a[100:103], v[88:91], v[228:231]\n"
"buffer_load_dwordx4 v249, s[64:67], s52 offen lds\n"
"v_mfma_f32_16x16x32_bf16 v[232:235], a[100:103], v[92:95], v[232:235]\n"
"v_mfma_f32_16x16x32_bf16 v[236:239], a[100:103], v[96:99], v[236:239]\n"
"ds_read_b64_tr_b16 a[100:101], v246 offset:40960\n"
"v_mfma_f32_16x16x32_bf16 v[200:203], a[104:107], v[120:123], v[200:203]\n"
"ds_read_b64_tr_b16 a[102:103], v246 offset:43008\n"
"s_add_u32 m0, s49, 90112\n"
"v_mfma_f32_16x16x32_bf16 v[204:207], a[104:107], v[124:127], v[204:207]\n"
"s_add_u32 s52, s56, 573440\n"
"buffer_load_dwordx4 v249, s[64:67], s52 offen lds\n"
"v_mfma_f32_16x16x32_bf16 v[208:211], a[104:107], v[128:131], v[208:211]\n"
"v_mfma_f32_16x16x32_bf16 v[212:215], a[104:107], v[132:135], v[212:215]\n"
"v_mfma_f32_16x16x32_bf16 v[216:219], a[104:107], v[136:139], v[216:219]\n"
"ds_read_b64_tr_b16 a[104:105], v245 offset:45056\n"
"s_add_u32 m0, s49, 94208\n"
"v_mfma_f32_16x16x32_bf16 v[220:223], a[108:111], v[120:123], v[220:223]\n"
"ds_read_b64_tr_b16 a[106:107], v245 offset:47104\n"
"s_add_u32 s52, s56, 614400\n"
"v_mfma_f32_16x16x32_bf16 v[224:227], a[108:111], v[124:127], v[224:227]\n"
"buffer_load_dwordx4 v249, s[64:67], s52 offen lds\n"
"s_lshr_b64 s[72:73], s[72:73], 4\n"
"v_mfma_f32_16x16x32_bf16 v[228:231], a[108:111], v[128:131], v[228:231]\n"
"s_and_b32 s56, s72, 15\n"
"s_lshl_b32 s56, s56, 7\n"
"v_mfma_f32_16x16x32_bf16 v[232:235], a[108:111], v[132:135], v[232:235]\n"
"v_mfma_f32_16x16x32_bf16 v[236:239], a[108:111], v[136:139], v[236:239]\n"
"s_waitcnt lgkmcnt(15)\n"
"ds_read_b64_tr_b16 a[108:109], v246 offset:45056\n"
"v_mfma_f32_16x16x32_bf16 v[200:203], a[112:115], v[20:23], v[200:203]\n"
"ds_read_b64_tr_b16 a[110:111], v246 offset:47104\n"
"s_and_b32 s57, s74, 15\n"
"v_mfma_f32_16x16x32_bf16 v[204:207], a[112:115], v[24:27], v[204:207]\n"
"s_mul_i32 s80, s57, 7\n"
"s_add_u32 s80, s80, s79\n"
"s_add_u32 m0, s49, 0\n"
"v_mfma_f32_16x16x32_bf16 v[208:211], a[112:115], v[28:31], v[208:211]\n"
"s_cmp_lt_u32 s57, 8\n"
"s_cselect_b32 s57, s80, s98\n"
"s_mul_i32 s57, s57, 20736\n"
"v_mfma_f32_16x16x32_bf16 v[212:215], a[112:115], v[32:35], v[212:215]\n"
"s_add_u32 s57, s57, s70\n"
"s_add_u32 s80, s57, 4096\n"
"s_lshr_b64 s[74:75], s[74:75], 4\n"
"v_mfma_f32_16x16x32_bf16 v[216:219], a[112:115], v[36:39], v[216:219]\n"
"ds_read_b64_tr_b16 a[112:113], v247 offset:32768\n"
"s_add_u32 s98, s98, 1\n"
"v_mfma_f32_16x16x32_bf16 v[220:223], a[116:119], v[20:23], v[220:223]\n"
"ds_read_b64_tr_b16 a[114:115], v247 offset:34816\n"
"s_add_u32 s79, s79, 1\n"
"v_mfma_f32_16x16x32_bf16 v[224:227], a[116:119], v[24:27], v[224:227]\n"
"s_cmp_eq_u32 s79, 6\n"
"s_cselect_b32 s96, 31, 0x80\n"
"s_add_u32 s52, s56, 0\n"
"v_mfma_f32_16x16x32_bf16 v[228:231], a[116:119], v[28:31], v[228:231]\n"
"buffer_load_dwordx4 v249, s[64:67], s52 offen lds\n"
"s_add_u32 m0, s49, 4096\n"
"v_mfma_f32_16x16x32_bf16 v[232:235], a[116:119], v[32:35], v[232:235]\n"
"s_add_u32 s52, s56, 40960\n"
"buffer_load_dwordx4 v249, s[64:67], s52 offen lds\n"
"v_mfma_f32_16x16x32_bf16 v[236:239], a[116:119], v[36:39], v[236:239]\n"
"ds_read_b64_tr_b16 a[116:117], v248 offset:32768\n"
"s_add_u32 m0, s49, 8192\n"
"v_mfma_f32_16x16x32_bf16 v[200:203], a[120:123], v[60:63], v[200:203]\n"
"ds_read_b64_tr_b16 a[118:119], v248 offset:34816\n"
"s_add_u32 s52, s56, 81920\n"
"v_mfma_f32_16x16x32_bf16 v[204:207], a[120:123], v[64:67], v[204:207]\n"
"buffer_load_dwordx4 v249, s[64:67], s52 offen lds\n"
"s_add_u32 m0, s49, 12288\n"
"v_mfma_f32_16x16x32_bf16 v[208:211], a[120:123], v[68:71], v[208:211]\n"
"s_add_u32 s52, s56, 122880\n"
"buffer_load_dwordx4 v249, s[64:67], s52 offen lds\n"
"v_mfma_f32_16x16x32_bf16 v[212:215], a[120:123], v[72:75], v[212:215]\n"
"v_mfma_f32_16x16x32_bf16 v[216:219], a[120:123], v[76:79], v[216:219]\n"
"ds_read_b64_tr_b16 a[120:121], v247 offset:36864\n"
"v_mfma_f32_16x16x32_bf16 v[220:223], a[124:127], v[60:63], v[220:223]\n"
"ds_read_b64_tr_b16 a[122:123], v247 offset:38912\n"
"s_add_u32 m0, s49, 16384\n"
"v_mfma_f32_16x16x32_bf16 v[224:227], a[124:127], v[64:67], v[224:227]\n"
"s_add_u32 s52, s56, 163840\n"
"buffer_load_dwordx4 v249, s[64:67], s52 offen lds\n"
"v_mfma_f32_16x16x32_bf16 v[228:231], a[124:127], v[68:71], v[228:231]\n"
"v_mfma_f32_16x16x32_bf16 v[232:235], a[124:127], v[72:75], v[232:235]\n"
"v_mfma_f32_16x16x32_bf16 v[236:239], a[124:127], v[76:79], v[236:239]\n"
"s_waitcnt lgkmcnt(15)\n"
"ds_read_b64_tr_b16 a[124:125], v248 offset:36864\n"
"v_mfma_f32_16x16x32_bf16 v[200:203], a[128:131], v[100:103], v[200:203]\n"
"ds_read_b64_tr_b16 a[126:127], v248 offset:38912\n"
"s_add_u32 m0, s49, 20480\n"
"v_mfma_f32_16x16x32_bf16 v[204:207], a[128:131], v[104:107], v[204:207]\n"
"s_add_u32 s52, s56, 204800\n"
"buffer_load_dwordx4 v249, s[64:67], s52 offen lds\n"
"v_mfma_f32_16x16x32_bf16 v[208:211], a[128:131], v[108:111], v[208:211]\n"
"v_mfma_f32_16x16x32_bf16 v[212:215], a[128:131], v[112:115], v[212:215]\n"
"s_add_u32 m0, s49, 24576\n"
"s_add_u32 s52, s56, 245760\n"
"v_mfma_f32_16x16x32_bf16 v[216:219], a[128:131], v[116:119], v[216:219]\n"
"ds_read_b64_tr_b16 a[128:129], v247 offset:40960\n"
"v_mfma_f32_16x16x32_bf16 v[220:223], a[132:135], v[100:103], v[220:223]\n"
"ds_read_b64_tr_b16 a[130:131], v247 offset:43008\n"
"v_mfma_f32_16x16x32_bf16 v[224:227], a[132:135], v[104:107], v[224:227]\n"
"buffer_load_dwordx4 v249, s[64:67], s52 offen lds\n"
"v_mfma_f32_16x16x32_bf16 v[228:231], a[132:135], v[108:111], v[228:231]\n"
"s_add_u32 m0, s49, 28672\n"
"s_add_u32 s52, s56, 286720\n"
"v_mfma_f32_16x16x32_bf16 v[232:235], a[132:135], v[112:115], v[232:235]\n"
"buffer_load_dwordx4 v249, s[64:67], s52 offen lds\n"
"v_mfma_f32_16x16x32_bf16 v[236:239], a[132:135], v[116:119], v[236:239]\n"
"ds_read_b64_tr_b16 a[132:133], v248 offset:40960\n"
"v_mfma_f32_16x16x32_bf16 v[200:203], a[136:139], v[140:143], v[200:203]\n"
"ds_read_b64_tr_b16 a[134:135], v248 offset:43008\n"
"v_mfma_f32_16x16x32_bf16 v[204:207], a[136:139], v[144:147], v[204:207]\n"
"v_mfma_f32_16x16x32_bf16 v[208:211], a[136:139], v[148:151], v[208:211]\n"
"v_mfma_f32_16x16x32_bf16 v[212:215], a[136:139], v[152:155], v[212:215]\n"
"v_mfma_f32_16x16x32_bf16 v[216:219], a[136:139], v[156:159], v[216:219]\n"
"ds_read_b64_tr_b16 a[136:137], v247 offset:45056\n"
"v_mfma_f32_16x16x32_bf16 v[220:223], a[140:143], v[140:143], v[220:223]\n"
"ds_read_b64_tr_b16 a[138:139], v247 offset:47104\n"
"v_mfma_f32_16x16x32_bf16 v[224:227], a[140:143], v[144:147], v[224:227]\n"
"v_mfma_f32_16x16x32_bf16 v[228:231], a[140:143], v[148:151], v[228:231]\n"
"v_mfma_f32_16x16x32_bf16 v[232:235], a[140:143], v[152:155], v[232:235]\n"
"v_mfma_f32_16x16x32_bf16 v[236:239], a[140:143], v[156:159], v[236:239]\n"
"s_waitcnt vmcnt(8)\n"
"s_barrier\n"
"s_waitcnt lgkmcnt(15)\n"
"v_mfma_f32_16x16x32_bf16 v[160:163], a[80:83], a[168:171], 0\n"
"ds_read_b64_tr_b16 a[140:141], v248 offset:45056\n"
"v_mfma_f32_16x16x32_bf16 v[164:167], a[80:83], a[172:175], 0\n"
"ds_read_b64_tr_b16 a[142:143], v248 offset:47104\n"
"v_mfma_f32_16x16x32_bf16 v[168:171], a[80:83], a[176:179], 0\n"
"s_mov_b32 exec_lo, s96\n"
"s_mov_b32 exec_hi, 0\n"
"global_store_dwordx2 v244, a[254:255], s[60:61] sc1\n"
"s_mov_b64 exec, -1\n"
"v_mfma_f32_16x16x32_bf16 v[172:175], a[80:83], a[180:183], 0\n"
"v_mfma_f32_16x16x32_bf16 v[176:179], a[80:83], a[184:187], 0\n"
"ds_read_b64_tr_b16 a[80:81], v240 offset:0\n"
"v_mfma_f32_16x16x32_bf16 v[180:183], a[84:87], a[168:171], 0\n"
"ds_read_b64_tr_b16 a[82:83], v240 offset:2048\n"
"v_cvt_pk_bf16_f32 v200, v200, v201\n"
"v_mfma_f32_16x16x32_bf16 v[184:187], a[84:87], a[172:175], 0\n"
"v_cvt_pk_bf16_f32 v201, v202, v203\n"
"v_cvt_pk_bf16_f32 v202, v220, v221\n"
"v_mfma_f32_16x16x32_bf16 v[188:191], a[84:87], a[176:179], 0\n"
"v_cvt_pk_bf16_f32 v203, v222, v223\n"
"v_mfma_f32_16x16x32_bf16 v[192:195], a[84:87], a[180:183], 0\n"
"buffer_store_dwordx4 v[200:203], v253, s[44:47], s57 offen sc1\n"
"v_mfma_f32_16x16x32_bf16 v[196:199], a[84:87], a[184:187], 0\n"
"ds_read_b64_tr_b16 a[84:85], v241 offset:0\n"
"v_mfma_f32_16x16x32_bf16 v[160:163], a[88:91], a[188:191], v[160:163]\n"
"ds_read_b64_tr_b16 a[86:87], v241 offset:2048\n"
"v_cvt_pk_bf16_f32 v204, v204, v205\n"
"v_mfma_f32_16x16x32_bf16 v[164:167], a[88:91], a[192:195], v[164:167]\n"
"v_cvt_pk_bf16_f32 v205, v206, v207\n"
"v_cvt_pk_bf16_f32 v206, v224, v225\n"
"v_mfma_f32_16x16x32_bf16 v[168:171], a[88:91], a[196:199], v[168:171]\n"
"v_cvt_pk_bf16_f32 v207, v226, v227\n"
"v_mfma_f32_16x16x32_bf16 v[172:175], a[88:91], a[200:203], v[172:175]\n"
"buffer_store_dwordx4 v[204:207], v253, s[44:47], s57 offen offset:1024 sc1\n"
"v_mfma_f32_16x16x32_bf16 v[176:179], a[88:91], a[204:207], v[176:179]\n"
"ds_read_b64_tr_b16 a[88:89], v240 offset:4096\n"
"v_mfma_f32_16x16x32_bf16 v[180:183], a[92:95], a[188:191], v[180:183]\n"
"ds_read_b64_tr_b16 a[90:91], v240 offset:6144\n"
"v_cvt_pk_bf16_f32 v208, v208, v209\n"
"v_mfma_f32_16x16x32_bf16 v[184:187], a[92:95], a[192:195], v[184:187]\n"
"v_cvt_pk_bf16_f32 v209, v210, v211\n"
"v_cvt_pk_bf16_f32 v210, v228, v229\n"
"v_mfma_f32_16x16x32_bf16 v[188:191], a[92:95], a[196:199], v[188:191]\n"
"v_cvt_pk_bf16_f32 v211, v230, v231\n"
"v_mfma_f32_16x16x32_bf16 v[192:195], a[92:95], a[200:203], v[192:195]\n"
"buffer_store_dwordx4 v[208:211], v253, s[44:47], s57 offen offset:2048 sc1\n"
"v_mfma_f32_16x16x32_bf16 v[196:199], a[92:95], a[204:207], v[196:199]\n"
"s_waitcnt lgkmcnt(15)\n"
"ds_read_b64_tr_b16 a[92:93], v241 offset:4096\n"
"v_mfma_f32_16x16x32_bf16 v[160:163], a[96:99], a[208:211], v[160:163]\n"
"ds_read_b64_tr_b16 a[94:95], v241 offset:6144\n"
"v_cvt_pk_bf16_f32 v212, v212, v213\n"
"v_mfma_f32_16x16x32_bf16 v[164:167], a[96:99], a[212:215], v[164:167]\n"
"v_cvt_pk_bf16_f32 v213, v214, v215\n"
"v_cvt_pk_bf16_f32 v214, v232, v233\n"
"v_mfma_f32_16x16x32_bf16 v[168:171], a[96:99], a[216:219], v[168:171]\n"
"v_cvt_pk_bf16_f32 v215, v234, v235\n"
"v_mfma_f32_16x16x32_bf16 v[172:175], a[96:99], a[220:223], v[172:175]\n"
"buffer_store_dwordx4 v[212:215], v253, s[44:47], s57 offen offset:3072 sc1\n"
"v_mfma_f32_16x16x32_bf16 v[176:179], a[96:99], a[224:227], v[176:179]\n"
"ds_read_b64_tr_b16 a[96:97], v240 offset:8192\n"
"v_mfma_f32_16x16x32_bf16 v[180:183], a[100:103], a[208:211], v[180:183]\n"
"ds_read_b64_tr_b16 a[98:99], v240 offset:10240\n"
"v_cvt_pk_bf16_f32 v216, v216, v217\n"
"v_mfma_f32_16x16x32_bf16 v[184:187], a[100:103], a[212:215], v[184:187]\n"
"v_cvt_pk_bf16_f32 v217, v218, v219\n"
"v_cvt_pk_bf16_f32 v218, v236, v237\n"
"v_mfma_f32_16x16x32_bf16 v[188:191], a[100:103], a[216:219], v[188:191]\n"
"v_cvt_pk_bf16_f32 v219, v238, v239\n"
"v_mfma_f32_16x16x32_bf16 v[192:195], a[100:103], a[220:223], v[192:195]\n"
"buffer_store_dwordx4 v[216:219], v253, s[44:47], s80 offen sc1\n"
"v_mfma_f32_16x16x32_bf16 v[196:199], a[100:103], a[224:227], v[196:199]\n"
"ds_read_b64_tr_b16 a[100:101], v241 offset:8192\n"
"v_mfma_f32_16x16x32_bf16 v[160:163], a[104:107], a[228:231], v[160:163]\n"
"ds_read_b64_tr_b16 a[102:103], v241 offset:10240\n"
"v_mfma_f32_16x16x32_bf16 v[164:167], a[104:107], a[232:235], v[164:167]\n"
"v_mfma_f32_16x16x32_bf16 v[168:171], a[104:107], a[236:239], v[168:171]\n"
"v_mfma_f32_16x16x32_bf16 v[172:175], a[104:107], a[240:243], v[172:175]\n"
"v_mfma_f32_16x16x32_bf16 v[176:179], a[104:107], a[244:247], v[176:179]\n"
"ds_read_b64_tr_b16 a[104:105], v240 offset:12288\n"
"v_mfma_f32_16x16x32_bf16 v[180:183], a[108:111], a[228:231], v[180:183]\n"
"ds_read_b64_tr_b16 a[106:107], v240 offset:14336\n"
"v_mfma_f32_16x16x32_bf16 v[184:187], a[108:111], a[232:235], v[184:187]\n"
"v_mfma_f32_16x16x32_bf16 v[188:191], a[108:111], a[236:239], v[188:191]\n"
"v_mfma_f32_16x16x32_bf16 v[192:195], a[108:111], a[240:243], v[192:195]\n"
"v_mfma_f32_16x16x32_bf16 v[196:199], a[108:111], a[244:247], v[196:199]\n"
"s_waitcnt lgkmcnt(15)\n"
"ds_read_b64_tr_b16 a[108:109], v241 offset:12288\n"
"v_mfma_f32_16x16x32_bf16 v[160:163], a[112:115], a[0:3], v[160:163]\n"
"ds_read_b64_tr_b16 a[110:111], v241 offset:14336\n"
"v_mfma_f32_16x16x32_bf16 v[164:167], a[112:115], a[4:7], v[164:167]\n"
"v_mfma_f32_16x16x32_bf16 v[168:171], a[112:115], a[8:11], v[168:171]\n"
"v_mfma_f32_16x16x32_bf16 v[172:175], a[112:115], a[12:15], v[172:175]\n"
"v_mfma_f32_16x16x32_bf16 v[176:179], a[112:115], a[16:19], v[176:179]\n"
"ds_read_b64_tr_b16 a[112:113], v242 offset:0\n"
"v_mfma_f32_16x16x32_bf16 v[180:183], a[116:119], a[0:3], v[180:183]\n"
"ds_read_b64_tr_b16 a[114:115], v242 offset:2048\n"
"v_mfma_f32_16x16x32_bf16 v[184:187], a[116:119], a[4:7], v[184:187]\n"
"v_mfma_f32_16x16x32_bf16 v[188:191], a[116:119], a[8:11], v[188:191]\n"
"v_mfma_f32_16x16x32_bf16 v[192:195], a[116:119], a[12:15], v[192:195]\n"
"v_mfma_f32_16x16x32_bf16 v[196:199], a[116:119], a[16:19], v[196:199]\n"
"ds_read_b64_tr_b16 a[116:117], v243 offset:0\n"
"v_mfma_f32_16x16x32_bf16 v[160:163], a[120:123], a[20:23], v[160:163]\n"
"ds_read_b64_tr_b16 a[118:119], v243 offset:2048\n"
"v_mfma_f32_16x16x32_bf16 v[164:167], a[120:123], a[24:27], v[164:167]\n"
"v_mfma_f32_16x16x32_bf16 v[168:171], a[120:123], a[28:31], v[168:171]\n"
"v_mfma_f32_16x16x32_bf16 v[172:175], a[120:123], a[32:35], v[172:175]\n"
"v_mfma_f32_16x16x32_bf16 v[176:179], a[120:123], a[36:39], v[176:179]\n"
"ds_read_b64_tr_b16 a[120:121], v242 offset:4096\n"
"v_mfma_f32_16x16x32_bf16 v[180:183], a[124:127], a[20:23], v[180:183]\n"
"ds_read_b64_tr_b16 a[122:123], v242 offset:6144\n"
"v_mfma_f32_16x16x32_bf16 v[184:187], a[124:127], a[24:27], v[184:187]\n"
"v_mfma_f32_16x16x32_bf16 v[188:191], a[124:127], a[28:31], v[188:191]\n"
"v_mfma_f32_16x16x32_bf16 v[192:195], a[124:127], a[32:35], v[192:195]\n"
"v_mfma_f32_16x16x32_bf16 v[196:199], a[124:127], a[36:39], v[196:199]\n"
"s_waitcnt lgkmcnt(15)\n"
"ds_read_b64_tr_b16 a[124:125], v243 offset:4096\n"
"v_mfma_f32_16x16x32_bf16 v[160:163], a[128:131], a[40:43], v[160:163]\n"
"ds_read_b64_tr_b16 a[126:127], v243 offset:6144\n"
"v_mfma_f32_16x16x32_bf16 v[164:167], a[128:131], a[44:47], v[164:167]\n"
"v_mfma_f32_16x16x32_bf16 v[168:171], a[128:131], a[48:51], v[168:171]\n"
"v_mfma_f32_16x16x32_bf16 v[172:175], a[128:131], a[52:55], v[172:175]\n"
"v_mfma_f32_16x16x32_bf16 v[176:179], a[128:131], a[56:59], v[176:179]\n"
"ds_read_b64_tr_b16 a[128:129], v242 offset:8192\n"
"v_mfma_f32_16x16x32_bf16 v[180:183], a[132:135], a[40:43], v[180:183]\n"
"ds_read_b64_tr_b16 a[130:131], v242 offset:10240\n"
"v_mfma_f32_16x16x32_bf16 v[184:187], a[132:135], a[44:47], v[184:187]\n"
"v_mfma_f32_16x16x32_bf16 v[188:191], a[132:135], a[48:51], v[188:191]\n"
"v_mfma_f32_16x16x32_bf16 v[192:195], a[132:135], a[52:55], v[192:195]\n"
"v_mfma_f32_16x16x32_bf16 v[196:199], a[132:135], a[56:59], v[196:199]\n"
"ds_read_b64_tr_b16 a[132:133], v243 offset:8192\n"
"v_mfma_f32_16x16x32_bf16 v[160:163], a[136:139], a[60:63], v[160:163]\n"
"ds_read_b64_tr_b16 a[134:135], v243 offset:10240\n"
"v_mfma_f32_16x16x32_bf16 v[164:167], a[136:139], a[64:67], v[164:167]\n"
"v_mfma_f32_16x16x32_bf16 v[168:171], a[136:139], a[68:71], v[168:171]\n"
"v_mfma_f32_16x16x32_bf16 v[172:175], a[136:139], a[72:75], v[172:175]\n"
"v_mfma_f32_16x16x32_bf16 v[176:179], a[136:139], a[76:79], v[176:179]\n"
"ds_read_b64_tr_b16 a[136:137], v242 offset:12288\n"
"v_mfma_f32_16x16x32_bf16 v[180:183], a[140:143], a[60:63], v[180:183]\n"
"ds_read_b64_tr_b16 a[138:139], v242 offset:14336\n"
"v_mfma_f32_16x16x32_bf16 v[184:187], a[140:143], a[64:67], v[184:187]\n"
"v_mfma_f32_16x16x32_bf16 v[188:191], a[140:143], a[68:71], v[188:191]\n"
"v_mfma_f32_16x16x32_bf16 v[192:195], a[140:143], a[72:75], v[192:195]\n"
"v_mfma_f32_16x16x32_bf16 v[196:199], a[140:143], a[76:79], v[196:199]\n"
"s_waitcnt vmcnt(6)\n"
"s_barrier\n"
"s_waitcnt lgkmcnt(15)\n"
"v_mfma_f32_16x16x32_bf16 v[160:163], a[80:83], v[0:3], v[160:163]\n"
"ds_read_b64_tr_b16 a[140:141], v243 offset:12288\n"
"v_mfma_f32_16x16x32_bf16 v[164:167], a[80:83], v[4:7], v[164:167]\n"
"ds_read_b64_tr_b16 a[142:143], v243 offset:14336\n"
"v_mfma_f32_16x16x32_bf16 v[168:171], a[80:83], v[8:11], v[168:171]\n"
"v_mfma_f32_16x16x32_bf16 v[172:175], a[80:83], v[12:15], v[172:175]\n"
"v_mfma_f32_16x16x32_bf16 v[176:179], a[80:83], v[16:19], v[176:179]\n"
"ds_read_b64_tr_b16 a[80:81], v245 offset:0\n"
"v_mfma_f32_16x16x32_bf16 v[180:183], a[84:87], v[0:3], v[180:183]\n"
"ds_read_b64_tr_b16 a[82:83], v245 offset:2048\n"
"v_mfma_f32_16x16x32_bf16 v[184:187], a[84:87], v[4:7], v[184:187]\n"
"s_add_u32 m0, s49, 106752\n"
"s_add_u32 s52, s56, 327680\n"
"v_mfma_f32_16x16x32_bf16 v[188:191], a[84:87], v[8:11], v[188:191]\n"
"buffer_load_dwordx4 v249, s[64:67], s52 offen lds\n"
"v_mfma_f32_16x16x32_bf16 v[192:195], a[84:87], v[12:15], v[192:195]\n"
"v_mfma_f32_16x16x32_bf16 v[196:199], a[84:87], v[16:19], v[196:199]\n"
"ds_read_b64_tr_b16 a[84:85], v246 offset:0\n"
"v_mfma_f32_16x16x32_bf16 v[160:163], a[88:91], v[40:43], v[160:163]\n"
"ds_read_b64_tr_b16 a[86:87], v246 offset:2048\n"
"s_add_u32 m0, s49, 110848\n"
"v_mfma_f32_16x16x32_bf16 v[164:167], a[88:91], v[44:47], v[164:167]\n"
"s_add_u32 s52, s56, 368640\n"
"buffer_load_dwordx4 v249, s[64:67], s52 offen lds\n"
"v_mfma_f32_16x16x32_bf16 v[168:171], a[88:91], v[48:51], v[168:171]\n"
"v_mfma_f32_16x16x32_bf16 v[172:175], a[88:91], v[52:55], v[172:175]\n"
"v_mfma_f32_16x16x32_bf16 v[176:179], a[88:91], v[56:59], v[176:179]\n"
"ds_read_b64_tr_b16 a[88:89], v245 offset:4096\n"
"s_add_u32 m0, s49, 114944\n"
"v_mfma_f32_16x16x32_bf16 v[180:183], a[92:95], v[40:43], v[180:183]\n"
"ds_read_b64_tr_b16 a[90:91], v245 offset:6144\n"
"s_add_u32 s52, s56, 409600\n"
"v_mfma_f32_16x16x32_bf16 v[184:187], a[92:95], v[44:47], v[184:187]\n"
"buffer_load_dwordx4 v249, s[64:67], s52 offen lds\n"
"v_mfma_f32_16x16x32_bf16 v[188:191], a[92:95], v[48:51], v[188:191]\n"
"v_mfma_f32_16x16x32_bf16 v[192:195], a[92:95], v[52:55], v[192:195]\n"
"s_add_u32 m0, s49, 119040\n"
"s_add_u32 s52, s56, 450560\n"
"v_mfma_f32_16x16x32_bf16 v[196:199], a[92:95], v[56:59], v[196:199]\n"
"s_waitcnt lgkmcnt(15)\n"
"ds_read_b64_tr_b16 a[92:93], v246 offset:4096\n"
"v_mfma_f32_16x16x32_bf16 v[160:163], a[96:99], v[80:83], v[160:163]\n"
"ds_read_b64_tr_b16 a[94:95], v246 offset:6144\n"
"v_mfma_f32_16x16x32_bf16 v[164:167], a[96:99], v[84:87], v[164:167]\n"
"buffer_load_dwordx4 v249, s[64:67], s52 offen lds\n"
"v_mfma_f32_16x16x32_bf16 v[168:171], a[96:99], v[88:91], v[168:171]\n"
"s_add_u32 m0, s49, 123136\n"
"s_add_u32 s52, s56, 491520\n"
"v_mfma_f32_16x16x32_bf16 v[172:175], a[96:99], v[92:95], v[172:175]\n"
"buffer_load_dwordx4 v249, s[64:67], s52 offen lds\n"
"v_mfma_f32_16x16x32_bf16 v[176:179], a[96:99], v[96:99], v[176:179]\n"
"ds_read_b64_tr_b16 a[96:97], v245 offset:8192\n"
"v_mfma_f32_16x16x32_bf16 v[180:183], a[100:103], v[80:83], v[180:183]\n"
"ds_read_b64_tr_b16 a[98:99], v245 offset:10240\n"
"v_mfma_f32_16x16x32_bf16 v[184:187], a[100:103], v[84:87], v[184:187]\n"
"s_add_u32 m0, s49, 127232\n"
"s_add_u32 s52, s56, 532480\n"
"v_mfma_f32_16x16x32_bf16 v[188:191], a[100:103], v[88:91], v[188:191]\n"
"buffer_load_dwordx4 v249, s[64:67], s52 offen lds\n"
"v_mfma_f32_16x16x32_bf16 v[192:195], a[100:103], v[92:95], v[192:195]\n"
"v_mfma_f32_16x16x32_bf16 v[196:199], a[100:103], v[96:99], v[196:199]\n"
"ds_read_b64_tr_b16 a[100:101], v246 offset:8192\n"
"v_mfma_f32_16x16x32_bf16 v[160:163], a[104:107], v[120:123], v[160:163]\n"
"ds_read_b64_tr_b16 a[102:103], v246 offset:10240\n"
"s_add_u32 m0, s49, 131328\n"
"v_mfma_f32_16x16x32_bf16 v[164:167], a[104:107], v[124:127], v[164:167]\n"
"s_add_u32 s52, s56, 573440\n"
"buffer_load_dwordx4 v249, s[64:67], s52 offen lds\n"
"v_mfma_f32_16x16x32_bf16 v[168:171], a[104:107], v[128:131], v[168:171]\n"
"v_mfma_f32_16x16x32_bf16 v[172:175], a[104:107], v[132:135], v[172:175]\n"
"v_mfma_f32_16x16x32_bf16 v[176:179], a[104:107], v[136:139], v[176:179]\n"
"ds_read_b64_tr_b16 a[104:105], v245 offset:12288\n"
"s_add_u32 m0, s49, 135424\n"
"v_mfma_f32_16x16x32_bf16 v[180:183], a[108:111], v[120:123], v[180:183]\n"
"ds_read_b64_tr_b16 a[106:107], v245 offset:14336\n"
"s_add_u32 s52, s56, 614400\n"
"v_mfma_f32_16x16x32_bf16 v[184:187], a[108:111], v[124:127], v[184:187]\n"
"buffer_load_dwordx4 v249, s[64:67], s52 offen lds\n"
"s_lshr_b64 s[72:73], s[72:73], 4\n"
"v_mfma_f32_16x16x32_bf16 v[188:191], a[108:111], v[128:131], v[188:191]\n"
"s_and_b32 s56, s72, 15\n"
"s_lshl_b32 s56, s56, 7\n"
"v_mfma_f32_16x16x32_bf16 v[192:195], a[108:111], v[132:135], v[192:195]\n"
"v_mfma_f32_16x16x32_bf16 v[196:199], a[108:111], v[136:139], v[196:199]\n"
"s_waitcnt lgkmcnt(15)\n"
"ds_read_b64_tr_b16 a[108:109], v246 offset:12288\n"
"v_mfma_f32_16x16x32_bf16 v[160:163], a[112:115], v[20:23], v[160:163]\n"
"ds_read_b64_tr_b16 a[110:111], v246 offset:14336\n"
"s_and_b32 s57, s74, 15\n"
"v_mfma_f32_16x16x32_bf16 v[164:167], a[112:115], v[24:27], v[164:167]\n"
"s_mul_i32 s80, s57, 7\n"
"s_add_u32 s80, s80, s79\n"
"s_add_u32 m0, s49, 32768\n"
"v_mfma_f32_16x16x32_bf16 v[168:171], a[112:115], v[28:31], v[168:171]\n"
"s_cmp_lt_u32 s57, 8\n"
"s_cselect_b32 s57, s80, s98\n"
"s_mul_i32 s57, s57, 20736\n"
"v_mfma_f32_16x16x32_bf16 v[172:175], a[112:115], v[32:35], v[172:175]\n"
"s_add_u32 s57, s57, s70\n"
"s_add_u32 s80, s57, 4096\n"
"s_lshr_b64 s[74:75], s[74:75], 4\n"
"v_mfma_f32_16x16x32_bf16 v[176:179], a[112:115], v[36:39], v[176:179]\n"
"ds_read_b64_tr_b16 a[112:113], v247 offset:0\n"
"s_add_u32 s98, s98, 1\n"
"v_mfma_f32_16x16x32_bf16 v[180:183], a[116:119], v[20:23], v[180:183]\n"
"ds_read_b64_tr_b16 a[114:115], v247 offset:2048\n"
"s_add_u32 s79, s79, 1\n"
"v_mfma_f32_16x16x32_bf16 v[184:187], a[116:119], v[24:27], v[184:187]\n"
"s_cmp_eq_u32 s79, 6\n"
"s_cselect_b32 s96, 31, 0x80\n"
"s_add_u32 s52, s56, 0\n"
"v_mfma_f32_16x16x32_bf16 v[188:191], a[116:119], v[28:31], v[188:191]\n"
"buffer_load_dwordx4 v249, s[64:67], s52 offen lds\n"
"s_add_u32 m0, s49, 36864\n"
"v_mfma_f32_16x16x32_bf16 v[192:195], a[116:119], v[32:35], v[192:195]\n"
"s_add_u32 s52, s56, 40960\n"
"buffer_load_dwordx4 v249, s[64:67], s52 offen lds\n"
"v_mfma_f32_16x16x32_bf16 v[196:199], a[116:119], v[36:39], v[196:199]\n"
"ds_read_b64_tr_b16 a[116:117], v248 offset:0\n"
"s_add_u32 m0, s49, 40960\n"
"v_mfma_f32_16x16x32_bf16 v[160:163], a[120:123], v[60:63], v[160:163]\n"
"ds_read_b64_tr_b16 a[118:119], v248 offset:2048\n"
"s_add_u32 s52, s56, 81920\n"
"v_mfma_f32_16x16x32_bf16 v[164:167], a[120:123], v[64:67], v[164:167]\n"
"buffer_load_dwordx4 v249, s[64:67], s52 offen lds\n"
"s_add_u32 m0, s49, 45056\n"
"v_mfma_f32_16x16x32_bf16 v[168:171], a[120:123], v[68:71], v[168:171]\n"
"s_add_u32 s52, s56, 122880\n"
"buffer_load_dwordx4 v249, s[64:67], s52 offen lds\n"
"v_mfma_f32_16x16x32_bf16 v[172:175], a[120:123], v[72:75], v[172:175]\n"
"v_mfma_f32_16x16x32_bf16 v[176:179], a[120:123], v[76:79], v[176:179]\n"
"ds_read_b64_tr_b16 a[120:121], v247 offset:4096\n"
"v_mfma_f32_16x16x32_bf16 v[180:183], a[124:127], v[60:63], v[180:183]\n"
"ds_read_b64_tr_b16 a[122:123], v247 offset:6144\n"
"s_add_u32 m0, s49, 49152\n"
"v_mfma_f32_16x16x32_bf16 v[184:187], a[124:127], v[64:67], v[184:187]\n"
"s_add_u32 s52, s56, 163840\n"
"buffer_load_dwordx4 v249, s[64:67], s52 offen lds\n"
"v_mfma_f32_16x16x32_bf16 v[188:191], a[124:127], v[68:71], v[188:191]\n"
"v_mfma_f32_16x16x32_bf16 v[192:195], a[124:127], v[72:75], v[192:195]\n"
"v_mfma_f32_16x16x32_bf16 v[196:199], a[124:127], v[76:79], v[196:199]\n"
"s_waitcnt lgkmcnt(15)\n"
"ds_read_b64_tr_b16 a[124:125], v248 offset:4096\n"
"v_mfma_f32_16x16x32_bf16 v[160:163], a[128:131], v[100:103], v[160:163]\n"
"ds_read_b64_tr_b16 a[126:127], v248 offset:6144\n"
"s_add_u32 m0, s49, 53248\n"
"v_mfma_f32_16x16x32_bf16 v[164:167], a[128:131], v[104:107], v[164:167]\n"
"s_add_u32 s52, s56, 204800\n"
"buffer_load_dwordx4 v249, s[64:67], s52 offen lds\n"
"v_mfma_f32_16x16x32_bf16 v[168:171], a[128:131], v[108:111], v[168:171]\n"
"v_mfma_f32_16x16x32_bf16 v[172:175], a[128:131], v[112:115], v[172:175]\n"
"s_add_u32 m0, s49, 57344\n"
"s_add_u32 s52, s56, 245760\n"
"v_mfma_f32_16x16x32_bf16 v[176:179], a[128:131], v[116:119], v[176:179]\n"
"ds_read_b64_tr_b16 a[128:129], v247 offset:8192\n"
"v_mfma_f32_16x16x32_bf16 v[180:183], a[132:135], v[100:103], v[180:183]\n"
"ds_read_b64_tr_b16 a[130:131], v247 offset:10240\n"
"v_mfma_f32_16x16x32_bf16 v[184:187], a[132:135], v[104:107], v[184:187]\n"
"buffer_load_dwordx4 v249, s[64:67], s52 offen lds\n"
"v_mfma_f32_16x16x32_bf16 v[188:191], a[132:135], v[108:111], v[188:191]\n"
"s_add_u32 m0, s49, 61440\n"
"s_add_u32 s52, s56, 286720\n"
"v_mfma_f32_16x16x32_bf16 v[192:195], a[132:135], v[112:115], v[192:195]\n"
"buffer_load_dwordx4 v249, s[64:67], s52 offen lds\n"
"v_mfma_f32_16x16x32_bf16 v[196:199], a[132:135], v[116:119], v[196:199]\n"
"ds_read_b64_tr_b16 a[132:133], v248 offset:8192\n"
"v_mfma_f32_16x16x32_bf16 v[160:163], a[136:139], v[140:143], v[160:163]\n"
"ds_read_b64_tr_b16 a[134:135], v248 offset:10240\n"
"v_mfma_f32_16x16x32_bf16 v[164:167], a[136:139], v[144:147], v[164:167]\n"
"v_mfma_f32_16x16x32_bf16 v[168:171], a[136:139], v[148:151], v[168:171]\n"
"v_mfma_f32_16x16x32_bf16 v[172:175], a[136:139], v[152:155], v[172:175]\n"
"v_mfma_f32_16x16x32_bf16 v[176:179], a[136:139], v[156:159], v[176:179]\n"
"ds_read_b64_tr_b16 a[136:137], v247 offset:12288\n"
"v_mfma_f32_16x16x32_bf16 v[180:183], a[140:143], v[140:143], v[180:183]\n"
"ds_read_b64_tr_b16 a[138:139], v247 offset:14336\n"
"v_mfma_f32_16x16x32_bf16 v[184:187], a[140:143], v[144:147], v[184:187]\n"
"v_mfma_f32_16x16x32_bf16 v[188:191], a[140:143], v[148:151], v[188:191]\n"
"global_load_dwordx2 v[254:255], v252, s[60:61] sc1\n"
"v_mfma_f32_16x16x32_bf16 v[192:195], a[140:143], v[152:155], v[192:195]\n"
"v_mfma_f32_16x16x32_bf16 v[196:199], a[140:143], v[156:159], v[196:199]\n"
"ds_read_b64_tr_b16 a[140:141], v248 offset:12288\n"
"ds_read_b64_tr_b16 a[142:143], v248 offset:14336\n"
"s_cmp_lt_u32 s79, 7\n"
"s_cbranch_scc1 L_ol_%=\n"
"s_waitcnt vmcnt(9)\n"
"s_barrier\n"
"s_waitcnt lgkmcnt(15)\n"
"v_mfma_f32_16x16x32_bf16 v[200:203], a[80:83], a[168:171], 0\n"
"v_mfma_f32_16x16x32_bf16 v[204:207], a[80:83], a[172:175], 0\n"
"v_mfma_f32_16x16x32_bf16 v[208:211], a[80:83], a[176:179], 0\n"
"v_mfma_f32_16x16x32_bf16 v[212:215], a[80:83], a[180:183], 0\n"
"v_mfma_f32_16x16x32_bf16 v[216:219], a[80:83], a[184:187], 0\n"
"ds_read_b64_tr_b16 a[80:81], v240 offset:41216\n"
"v_mfma_f32_16x16x32_bf16 v[220:223], a[84:87], a[168:171], 0\n"
"ds_read_b64_tr_b16 a[82:83], v240 offset:43264\n"
"v_cvt_pk_bf16_f32 v160, v160, v161\n"
"v_mfma_f32_16x16x32_bf16 v[224:227], a[84:87], a[172:175], 0\n"
"v_cvt_pk_bf16_f32 v161, v162, v163\n"
"v_cvt_pk_bf16_f32 v162, v180, v181\n"
"v_mfma_f32_16x16x32_bf16 v[228:231], a[84:87], a[176:179], 0\n"
"v_cvt_pk_bf16_f32 v163, v182, v183\n"
"v_mfma_f32_16x16x32_bf16 v[232:235], a[84:87], a[180:183], 0\n"
"buffer_store_dwordx4 v[160:163], v253, s[44:47], s57 offen sc1\n"
"v_mfma_f32_16x16x32_bf16 v[236:239], a[84:87], a[184:187], 0\n"
"ds_read_b64_tr_b16 a[84:85], v241 offset:41216\n"
"v_mfma_f32_16x16x32_bf16 v[200:203], a[88:91], a[188:191], v[200:203]\n"
"ds_read_b64_tr_b16 a[86:87], v241 offset:43264\n"
"v_cvt_pk_bf16_f32 v164, v164, v165\n"
"v_mfma_f32_16x16x32_bf16 v[204:207], a[88:91], a[192:195], v[204:207]\n"
"v_cvt_pk_bf16_f32 v165, v166, v167\n"
"v_cvt_pk_bf16_f32 v166, v184, v185\n"
"v_mfma_f32_16x16x32_bf16 v[208:211], a[88:91], a[196:199], v[208:211]\n"
"v_cvt_pk_bf16_f32 v167, v186, v187\n"
"v_mfma_f32_16x16x32_bf16 v[212:215], a[88:91], a[200:203], v[212:215]\n"
"buffer_store_dwordx4 v[164:167], v253, s[44:47], s57 offen offset:1024 sc1\n"
"v_mfma_f32_16x16x32_bf16 v[216:219], a[88:91], a[204:207], v[216:219]\n"
"ds_read_b64_tr_b16 a[88:89], v240 offset:45312\n"
"v_mfma_f32_16x16x32_bf16 v[220:223], a[92:95], a[188:191], v[220:223]\n"
"ds_read_b64_tr_b16 a[90:91], v240 offset:47360\n"
"v_cvt_pk_bf16_f32 v168, v168, v169\n"
"v_mfma_f32_16x16x32_bf16 v[224:227], a[92:95], a[192:195], v[224:227]\n"
"v_cvt_pk_bf16_f32 v169, v170, v171\n"
"v_cvt_pk_bf16_f32 v170, v188, v189\n"
"v_mfma_f32_16x16x32_bf16 v[228:231], a[92:95], a[196:199], v[228:231]\n"
"v_cvt_pk_bf16_f32 v171, v190, v191\n"
"v_mfma_f32_16x16x32_bf16 v[232:235], a[92:95], a[200:203], v[232:235]\n"
"buffer_store_dwordx4 v[168:171], v253, s[44:47], s57 offen offset:2048 sc1\n"
"v_mfma_f32_16x16x32_bf16 v[236:239], a[92:95], a[204:207], v[236:239]\n"
"s_waitcnt lgkmcnt(15)\n"
"ds_read_b64_tr_b16 a[92:93], v241 offset:45312\n"
"v_mfma_f32_16x16x32_bf16 v[200:203], a[96:99], a[208:211], v[200:203]\n"
"ds_read_b64_tr_b16 a[94:95], v241 offset:47360\n"
"v_cvt_pk_bf16_f32 v172, v172, v173\n"
"v_mfma_f32_16x16x32_bf16 v[204:207], a[96:99], a[212:215], v[204:207]\n"
"v_cvt_pk_bf16_f32 v173, v174, v175\n"
"v_cvt_pk_bf16_f32 v174, v192, v193\n"
"v_mfma_f32_16x16x32_bf16 v[208:211], a[96:99], a[216:219], v[208:211]\n"
"v_cvt_pk_bf16_f32 v175, v194, v195\n"
"v_mfma_f32_16x16x32_bf16 v[212:215], a[96:99], a[220:223], v[212:215]\n"
"buffer_store_dwordx4 v[172:175], v253, s[44:47], s57 offen offset:3072 sc1\n"
"v_mfma_f32_16x16x32_bf16 v[216:219], a[96:99], a[224:227], v[216:219]\n"
"ds_read_b64_tr_b16 a[96:97], v240 offset:49408\n"
"v_mfma_f32_16x16x32_bf16 v[220:223], a[100:103], a[208:211], v[220:223]\n"
"ds_read_b64_tr_b16 a[98:99], v240 offset:51456\n"
"v_cvt_pk_bf16_f32 v176, v176, v177\n"
"v_mfma_f32_16x16x32_bf16 v[224:227], a[100:103], a[212:215], v[224:227]\n"
"v_cvt_pk_bf16_f32 v177, v178, v179\n"
"v_cvt_pk_bf16_f32 v178, v196, v197\n"
"v_mfma_f32_16x16x32_bf16 v[228:231], a[100:103], a[216:219], v[228:231]\n"
"v_cvt_pk_bf16_f32 v179, v198, v199\n"
"v_mfma_f32_16x16x32_bf16 v[232:235], a[100:103], a[220:223], v[232:235]\n"
"buffer_store_dwordx4 v[176:179], v253, s[44:47], s80 offen sc1\n"
"v_mfma_f32_16x16x32_bf16 v[236:239], a[100:103], a[224:227], v[236:239]\n"
"ds_read_b64_tr_b16 a[100:101], v241 offset:49408\n"
"v_mfma_f32_16x16x32_bf16 v[200:203], a[104:107], a[228:231], v[200:203]\n"
"ds_read_b64_tr_b16 a[102:103], v241 offset:51456\n"
"v_mfma_f32_16x16x32_bf16 v[204:207], a[104:107], a[232:235], v[204:207]\n"
"v_mfma_f32_16x16x32_bf16 v[208:211], a[104:107], a[236:239], v[208:211]\n"
"v_mfma_f32_16x16x32_bf16 v[212:215], a[104:107], a[240:243], v[212:215]\n"
"v_mfma_f32_16x16x32_bf16 v[216:219], a[104:107], a[244:247], v[216:219]\n"
"ds_read_b64_tr_b16 a[104:105], v240 offset:53504\n"
"v_mfma_f32_16x16x32_bf16 v[220:223], a[108:111], a[228:231], v[220:223]\n"
"ds_read_b64_tr_b16 a[106:107], v240 offset:55552\n"
"v_mfma_f32_16x16x32_bf16 v[224:227], a[108:111], a[232:235], v[224:227]\n"
"v_mfma_f32_16x16x32_bf16 v[228:231], a[108:111], a[236:239], v[228:231]\n"
"v_mfma_f32_16x16x32_bf16 v[232:235], a[108:111], a[240:243], v[232:235]\n"
"v_mfma_f32_16x16x32_bf16 v[236:239], a[108:111], a[244:247], v[236:239]\n"
"s_waitcnt lgkmcnt(15)\n"
"ds_read_b64_tr_b16 a[108:109], v241 offset:53504\n"
"v_mfma_f32_16x16x32_bf16 v[200:203], a[112:115], a[0:3], v[200:203]\n"
"ds_read_b64_tr_b16 a[110:111], v241 offset:55552\n"
"v_mfma_f32_16x16x32_bf16 v[204:207], a[112:115], a[4:7], v[204:207]\n"
"v_mfma_f32_16x16x32_bf16 v[208:211], a[112:115], a[8:11], v[208:211]\n"
"v_mfma_f32_16x16x32_bf16 v[212:215], a[112:115], a[12:15], v[212:215]\n"
"v_mfma_f32_16x16x32_bf16 v[216:219], a[112:115], a[16:19], v[216:219]\n"
"ds_read_b64_tr_b16 a[112:113], v242 offset:41216\n"
"v_mfma_f32_16x16x32_bf16 v[220:223], a[116:119], a[0:3], v[220:223]\n"
"ds_read_b64_tr_b16 a[114:115], v242 offset:43264\n"
"v_mfma_f32_16x16x32_bf16 v[224:227], a[116:119], a[4:7], v[224:227]\n"
"v_mfma_f32_16x16x32_bf16 v[228:231], a[116:119], a[8:11], v[228:231]\n"
"v_mfma_f32_16x16x32_bf16 v[232:235], a[116:119], a[12:15], v[232:235]\n"
"v_mfma_f32_16x16x32_bf16 v[236:239], a[116:119], a[16:19], v[236:239]\n"
"ds_read_b64_tr_b16 a[116:117], v243 offset:41216\n"
"v_mfma_f32_16x16x32_bf16 v[200:203], a[120:123], a[20:23], v[200:203]\n"
"ds_read_b64_tr_b16 a[118:119], v243 offset:43264\n"
"v_mfma_f32_16x16x32_bf16 v[204:207], a[120:123], a[24:27], v[204:207]\n"
"v_mfma_f32_16x16x32_bf16 v[208:211], a[120:123], a[28:31], v[208:211]\n"
"v_mfma_f32_16x16x32_bf16 v[212:215], a[120:123], a[32:35], v[212:215]\n"
"v_mfma_f32_16x16x32_bf16 v[216:219], a[120:123], a[36:39], v[216:219]\n"
"ds_read_b64_tr_b16 a[120:121], v242 offset:45312\n"
"v_mfma_f32_16x16x32_bf16 v[220:223], a[124:127], a[20:23], v[220:223]\n"
"ds_read_b64_tr_b16 a[122:123], v242 offset:47360\n"
"v_mfma_f32_16x16x32_bf16 v[224:227], a[124:127], a[24:27], v[224:227]\n"
"v_mfma_f32_16x16x32_bf16 v[228:231], a[124:127], a[28:31], v[228:231]\n"
"v_mfma_f32_16x16x32_bf16 v[232:235], a[124:127], a[32:35], v[232:235]\n"
"v_mfma_f32_16x16x32_bf16 v[236:239], a[124:127], a[36:39], v[236:239]\n"
"s_waitcnt lgkmcnt(15)\n"
"ds_read_b64_tr_b16 a[124:125], v243 offset:45312\n"
"v_mfma_f32_16x16x32_bf16 v[200:203], a[128:131], a[40:43], v[200:203]\n"
"ds_read_b64_tr_b16 a[126:127], v243 offset:47360\n"
"v_mfma_f32_16x16x32_bf16 v[204:207], a[128:131], a[44:47], v[204:207]\n"
"v_mfma_f32_16x16x32_bf16 v[208:211], a[128:131], a[48:51], v[208:211]\n"
"v_mfma_f32_16x16x32_bf16 v[212:215], a[128:131], a[52:55], v[212:215]\n"
"v_mfma_f32_16x16x32_bf16 v[216:219], a[128:131], a[56:59], v[216:219]\n"
"ds_read_b64_tr_b16 a[128:129], v242 offset:49408\n"
"v_mfma_f32_16x16x32_bf16 v[220:223], a[132:135], a[40:43], v[220:223]\n"
"ds_read_b64_tr_b16 a[130:131], v242 offset:51456\n"
"v_mfma_f32_16x16x32_bf16 v[224:227], a[132:135], a[44:47], v[224:227]\n"
"v_mfma_f32_16x16x32_bf16 v[228:231], a[132:135], a[48:51], v[228:231]\n"
"v_mfma_f32_16x16x32_bf16 v[232:235], a[132:135], a[52:55], v[232:235]\n"
"v_mfma_f32_16x16x32_bf16 v[236:239], a[132:135], a[56:59], v[236:239]\n"
"ds_read_b64_tr_b16 a[132:133], v243 offset:49408\n"
"v_mfma_f32_16x16x32_bf16 v[200:203], a[136:139], a[60:63], v[200:203]\n"
"ds_read_b64_tr_b16 a[134:135], v243 offset:51456\n"
"v_mfma_f32_16x16x32_bf16 v[204:207], a[136:139], a[64:67], v[204:207]\n"
"v_mfma_f32_16x16x32_bf16 v[208:211], a[136:139], a[68:71], v[208:211]\n"
"v_mfma_f32_16x16x32_bf16 v[212:215], a[136:139], a[72:75], v[212:215]\n"
"v_mfma_f32_16x16x32_bf16 v[216:219], a[136:139], a[76:79], v[216:219]\n"
"ds_read_b64_tr_b16 a[136:137], v242 offset:53504\n"
"v_mfma_f32_16x16x32_bf16 v[220:223], a[140:143], a[60:63], v[220:223]\n"
"ds_read_b64_tr_b16 a[138:139], v242 offset:55552\n"
"v_mfma_f32_16x16x32_bf16 v[224:227], a[140:143], a[64:67], v[224:227]\n"
"v_mfma_f32_16x16x32_bf16 v[228:231], a[140:143], a[68:71], v[228:231]\n"
"v_mfma_f32_16x16x32_bf16 v[232:235], a[140:143], a[72:75], v[232:235]\n"
"v_mfma_f32_16x16x32_bf16 v[236:239], a[140:143], a[76:79], v[236:239]\n"
"s_waitcnt vmcnt(6)\n"
"s_barrier\n"
"s_waitcnt lgkmcnt(15)\n"
"v_mfma_f32_16x16x32_bf16 v[200:203], a[80:83], v[0:3], v[200:203]\n"
"ds_read_b64_tr_b16 a[140:141], v243 offset:53504\n"
"v_mfma_f32_16x16x32_bf16 v[204:207], a[80:83], v[4:7], v[204:207]\n"
"ds_read_b64_tr_b16 a[142:143], v243 offset:55552\n"
"v_mfma_f32_16x16x32_bf16 v[208:211], a[80:83], v[8:11], v[208:211]\n"
"v_mfma_f32_16x16x32_bf16 v[212:215], a[80:83], v[12:15], v[212:215]\n"
"v_mfma_f32_16x16x32_bf16 v[216:219], a[80:83], v[16:19], v[216:219]\n"
"ds_read_b64_tr_b16 a[80:81], v245 offset:32768\n"
"v_mfma_f32_16x16x32_bf16 v[220:223], a[84:87], v[0:3], v[220:223]\n"
"ds_read_b64_tr_b16 a[82:83], v245 offset:34816\n"
"v_mfma_f32_16x16x32_bf16 v[224:227], a[84:87], v[4:7], v[224:227]\n"
"s_add_u32 m0, s49, 65536\n"
"s_add_u32 s52, s56, 327680\n"
"v_mfma_f32_16x16x32_bf16 v[228:231], a[84:87], v[8:11], v[228:231]\n"
"buffer_load_dwordx4 v249, s[64:67], s52 offen lds\n"
"v_mfma_f32_16x16x32_bf16 v[232:235], a[84:87], v[12:15], v[232:235]\n"
"v_mfma_f32_16x16x32_bf16 v[236:239], a[84:87], v[16:19], v[236:239]\n"
"ds_read_b64_tr_b16 a[84:85], v246 offset:32768\n"
"v_mfma_f32_16x16x32_bf16 v[200:203], a[88:91], v[40:43], v[200:203]\n"
"ds_read_b64_tr_b16 a[86:87], v246 offset:34816\n"
"s_add_u32 m0, s49, 69632\n"
"v_mfma_f32_16x16x32_bf16 v[204:207], a[88:91], v[44:47], v[204:207]\n"
"s_add_u32 s52, s56, 368640\n"
"buffer_load_dwordx4 v249, s[64:67], s52 offen lds\n"
"v_mfma_f32_16x16x32_bf16 v[208:211], a[88:91], v[48:51], v[208:211]\n"
"v_mfma_f32_16x16x32_bf16 v[212:215], a[88:91], v[52:55], v[212:215]\n"
"v_mfma_f32_16x16x32_bf16 v[216:219], a[88:91], v[56:59], v[216:219]\n"
"ds_read_b64_tr_b16 a[88:89], v245 offset:36864\n"
"s_add_u32 m0, s49, 73728\n"
"v_mfma_f32_16x16x32_bf16 v[220:223], a[92:95], v[40:43], v[220:223]\n"
"ds_read_b64_tr_b16 a[90:91], v245 offset:38912\n"
"s_add_u32 s52, s56, 409600\n"
"v_mfma_f32_16x16x32_bf16 v[224:227], a[92:95], v[44:47], v[224:227]\n"
"buffer_load_dwordx4 v249, s[64:67], s52 offen lds\n"
"v_mfma_f32_16x16x32_bf16 v[228:231], a[92:95], v[48:51], v[228:231]\n"
"v_mfma_f32_16x16x32_bf16 v[232:235], a[92:95], v[52:55], v[232:235]\n"
"s_add_u32 m0, s49, 77824\n"
"s_add_u32 s52, s56, 450560\n"
"v_mfma_f32_16x16x32_bf16 v[236:239], a[92:95], v[56:59], v[236:239]\n"
"s_waitcnt lgkmcnt(15)\n"
"ds_read_b64_tr_b16 a[92:93], v246 offset:36864\n"
"v_mfma_f32_16x16x32_bf16 v[200:203], a[96:99], v[80:83], v[200:203]\n"
"ds_read_b64_tr_b16 a[94:95], v246 offset:38912\n"
"v_mfma_f32_16x16x32_bf16 v[204:207], a[96:99], v[84:87], v[204:207]\n"
"buffer_load_dwordx4 v249, s[64:67], s52 offen lds\n"
"v_mfma_f32_16x16x32_bf16 v[208:211], a[96:99], v[88:91], v[208:211]\n"
"s_add_u32 m0, s49, 81920\n"
"s_add_u32 s52, s56, 491520\n"
"v_mfma_f32_16x16x32_bf16 v[212:215], a[96:99], v[92:95], v[212:215]\n"
"buffer_load_dwordx4 v249, s[64:67], s52 offen lds\n"
"v_mfma_f32_16x16x32_bf16 v[216:219], a[96:99], v[96:99], v[216:219]\n"
"ds_read_b64_tr_b16 a[96:97], v245 offset:40960\n"
"v_mfma_f32_16x16x32_bf16 v[220:223], a[100:103], v[80:83], v[220:223]\n"
"ds_read_b64_tr_b16 a[98:99], v245 offset:43008\n"
"v_mfma_f32_16x16x32_bf16 v[224:227], a[100:103], v[84:87], v[224:227]\n"
"s_add_u32 m0, s49, 86016\n"
"s_add_u32 s52, s56, 532480\n"
"v_mfma_f32_16x16x32_bf16 v[228:231], a[100:103], v[88:91], v[228:231]\n"
"buffer_load_dwordx4 v249, s[64:67], s52 offen lds\n"
"v_mfma_f32_16x16x32_bf16 v[232:235], a[100:103], v[92:95], v[232:235]\n"
"v_mfma_f32_16x16x32_bf16 v[236:239], a[100:103], v[96:99], v[236:239]\n"
"ds_read_b64_tr_b16 a[100:101], v246 offset:40960\n"
"v_mfma_f32_16x16x32_bf16 v[200:203], a[104:107], v[120:123], v[200:203]\n"
"ds_read_b64_tr_b16 a[102:103], v246 offset:43008\n"
"s_add_u32 m0, s49, 90112\n"
"v_mfma_f32_16x16x32_bf16 v[204:207], a[104:107], v[124:127], v[204:207]\n"
"s_add_u32 s52, s56, 573440\n"
"buffer_load_dwordx4 v249, s[64:67], s52 offen lds\n"
"v_mfma_f32_16x16x32_bf16 v[208:211], a[104:107], v[128:131], v[208:211]\n"
"v_mfma_f32_16x16x32_bf16 v[212:215], a[104:107], v[132:135], v[212:215]\n"
"v_mfma_f32_16x16x32_bf16 v[216:219], a[104:107], v[136:139], v[216:219]\n"
"ds_read_b64_tr_b16 a[104:105], v245 offset:45056\n"
"s_add_u32 m0, s49, 94208\n"
"v_mfma_f32_16x16x32_bf16 v[220:223], a[108:111], v[120:123], v[220:223]\n"
"ds_read_b64_tr_b16 a[106:107], v245 offset:47104\n"
"s_add_u32 s52, s56, 614400\n"
"v_mfma_f32_16x16x32_bf16 v[224:227], a[108:111], v[124:127], v[224:227]\n"
"buffer_load_dwordx4 v249, s[64:67], s52 offen lds\n"
"s_lshr_b64 s[72:73], s[72:73], 4\n"
"v_mfma_f32_16x16x32_bf16 v[228:231], a[108:111], v[128:131], v[228:231]\n"
"s_and_b32 s56, s72, 15\n"
"s_lshl_b32 s56, s56, 7\n"
"v_mfma_f32_16x16x32_bf16 v[232:235], a[108:111], v[132:135], v[232:235]\n"
"v_mfma_f32_16x16x32_bf16 v[236:239], a[108:111], v[136:139], v[236:239]\n"
"s_waitcnt lgkmcnt(15)\n"
"ds_read_b64_tr_b16 a[108:109], v246 offset:45056\n"
"v_mfma_f32_16x16x32_bf16 v[200:203], a[112:115], v[20:23], v[200:203]\n"
"ds_read_b64_tr_b16 a[110:111], v246 offset:47104\n"
"s_and_b32 s57, s74, 15\n"
"v_mfma_f32_16x16x32_bf16 v[204:207], a[112:115], v[24:27], v[204:207]\n"
"s_mul_i32 s80, s57, 7\n"
"s_add_u32 s80, s80, s79\n"
"s_add_u32 m0, s49, 0\n"
"v_mfma_f32_16x16x32_bf16 v[208:211], a[112:115], v[28:31], v[208:211]\n"
"s_cmp_lt_u32 s57, 8\n"
"s_cselect_b32 s57, s80, s98\n"
"s_mul_i32 s57, s57, 20736\n"
"v_mfma_f32_16x16x32_bf16 v[212:215], a[112:115], v[32:35], v[212:215]\n"
"s_add_u32 s57, s57, s70\n"
"s_add_u32 s80, s57, 4096\n"
"s_lshr_b64 s[74:75], s[74:75], 4\n"
"v_mfma_f32_16x16x32_bf16 v[216:219], a[112:115], v[36:39], v[216:219]\n"
"ds_read_b64_tr_b16 a[112:113], v247 offset:32768\n"
"s_add_u32 s98, s98, 1\n"
"v_mfma_f32_16x16x32_bf16 v[220:223], a[116:119], v[20:23], v[220:223]\n"
"ds_read_b64_tr_b16 a[114:115], v247 offset:34816\n"
"s_add_u32 s79, s79, 1\n"
"v_mfma_f32_16x16x32_bf16 v[224:227], a[116:119], v[24:27], v[224:227]\n"
"s_add_u32 s52, s56, 0\n"
"buffer_load_dwordx4 v249, s[64:67], s52 offen lds\n"
"v_mfma_f32_16x16x32_bf16 v[228:231], a[116:119], v[28:31], v[228:231]\n"
"s_add_u32 m0, s49, 4096\n"
"s_add_u32 s52, s56, 40960\n"
"v_mfma_f32_16x16x32_bf16 v[232:235], a[116:119], v[32:35], v[232:235]\n"
"buffer_load_dwordx4 v249, s[64:67], s52 offen lds\n"
"s_add_u32 m0, s49, 8192\n"
"v_mfma_f32_16x16x32_bf16 v[236:239], a[116:119], v[36:39], v[236:239]\n"
"ds_read_b64_tr_b16 a[116:117], v248 offset:32768\n"
"s_add_u32 s52, s56, 81920\n"
"v_mfma_f32_16x16x32_bf16 v[200:203], a[120:123], v[60:63], v[200:203]\n"
"ds_read_b64_tr_b16 a[118:119], v248 offset:34816\n"
"v_mfma_f32_16x16x32_bf16 v[204:207], a[120:123], v[64:67], v[204:207]\n"
"buffer_load_dwordx4 v249, s[64:67], s52 offen lds\n"
"s_add_u32 m0, s49, 12288\n"
"v_mfma_f32_16x16x32_bf16 v[208:211], a[120:123], v[68:71], v[208:211]\n"
"s_add_u32 s52, s56, 122880\n"
"buffer_load_dwordx4 v249, s[64:67], s52 offen lds\n"
"v_mfma_f32_16x16x32_bf16 v[212:215], a[120:123], v[72:75], v[212:215]\n"
"v_mfma_f32_16x16x32_bf16 v[216:219], a[120:123], v[76:79], v[216:219]\n"
"ds_read_b64_tr_b16 a[120:121], v247 offset:36864\n"
"v_mfma_f32_16x16x32_bf16 v[220:223], a[124:127], v[60:63], v[220:223]\n"
"ds_read_b64_tr_b16 a[122:123], v247 offset:38912\n"
"s_add_u32 m0, s49, 16384\n"
"v_mfma_f32_16x16x32_bf16 v[224:227], a[124:127], v[64:67], v[224:227]\n"
"s_add_u32 s52, s56, 163840\n"
"buffer_load_dwordx4 v249, s[64:67], s52 offen lds\n"
"v_mfma_f32_16x16x32_bf16 v[228:231], a[124:127], v[68:71], v[228:231]\n"
"v_mfma_f32_16x16x32_bf16 v[232:235], a[124:127], v[72:75], v[232:235]\n"
"v_mfma_f32_16x16x32_bf16 v[236:239], a[124:127], v[76:79], v[236:239]\n"
"s_waitcnt lgkmcnt(15)\n"
"ds_read_b64_tr_b16 a[124:125], v248 offset:36864\n"
"v_mfma_f32_16x16x32_bf16 v[200:203], a[128:131], v[100:103], v[200:203]\n"
"ds_read_b64_tr_b16 a[126:127], v248 offset:38912\n"
"s_add_u32 m0, s49, 20480\n"
"v_mfma_f32_16x16x32_bf16 v[204:207], a[128:131], v[104:107], v[204:207]\n"
"s_add_u32 s52, s56, 204800\n"
"buffer_load_dwordx4 v249, s[64:67], s52 offen lds\n"
"v_mfma_f32_16x16x32_bf16 v[208:211], a[128:131], v[108:111], v[208:211]\n"
"v_mfma_f32_16x16x32_bf16 v[212:215], a[128:131], v[112:115], v[212:215]\n"
"s_add_u32 m0, s49, 24576\n"
"s_add_u32 s52, s56, 245760\n"
"v_mfma_f32_16x16x32_bf16 v[216:219], a[128:131], v[116:119], v[216:219]\n"
"ds_read_b64_tr_b16 a[128:129], v247 offset:40960\n"
"v_mfma_f32_16x16x32_bf16 v[220:223], a[132:135], v[100:103], v[220:223]\n"
"ds_read_b64_tr_b16 a[130:131], v247 offset:43008\n"
"v_mfma_f32_16x16x32_bf16 v[224:227], a[132:135], v[104:107], v[224:227]\n"
"buffer_load_dwordx4 v249, s[64:67], s52 offen lds\n"
"v_mfma_f32_16x16x32_bf16 v[228:231], a[132:135], v[108:111], v[228:231]\n"
"s_add_u32 m0, s49, 28672\n"
"s_add_u32 s52, s56, 286720\n"
"v_mfma_f32_16x16x32_bf16 v[232:235], a[132:135], v[112:115], v[232:235]\n"
"buffer_load_dwordx4 v249, s[64:67], s52 offen lds\n"
"v_mfma_f32_16x16x32_bf16 v[236:239], a[132:135], v[116:119], v[236:239]\n"
"ds_read_b64_tr_b16 a[132:133], v248 offset:40960\n"
"v_mfma_f32_16x16x32_bf16 v[200:203], a[136:139], v[140:143], v[200:203]\n"
"ds_read_b64_tr_b16 a[134:135], v248 offset:43008\n"
"v_mfma_f32_16x16x32_bf16 v[204:207], a[136:139], v[144:147], v[204:207]\n"
"v_mfma_f32_16x16x32_bf16 v[208:211], a[136:139], v[148:151], v[208:211]\n"
"v_mfma_f32_16x16x32_bf16 v[212:215], a[136:139], v[152:155], v[212:215]\n"
"v_mfma_f32_16x16x32_bf16 v[216:219], a[136:139], v[156:159], v[216:219]\n"
"ds_read_b64_tr_b16 a[136:137], v247 offset:45056\n"
"v_mfma_f32_16x16x32_bf16 v[220:223], a[140:143], v[140:143], v[220:223]\n"
"ds_read_b64_tr_b16 a[138:139], v247 offset:47104\n"
"v_mfma_f32_16x16x32_bf16 v[224:227], a[140:143], v[144:147], v[224:227]\n"
"v_mfma_f32_16x16x32_bf16 v[228:231], a[140:143], v[148:151], v[228:231]\n"
"v_mfma_f32_16x16x32_bf16 v[232:235], a[140:143], v[152:155], v[232:235]\n"
"v_mfma_f32_16x16x32_bf16 v[236:239], a[140:143], v[156:159], v[236:239]\n"
"s_waitcnt vmcnt(8)\n"
"s_barrier\n"
"s_waitcnt lgkmcnt(15)\n"
"v_mfma_f32_16x16x32_bf16 v[160:163], a[80:83], a[168:171], 0\n"
"ds_read_b64_tr_b16 a[140:141], v248 offset:45056\n"
"v_mfma_f32_16x16x32_bf16 v[164:167], a[80:83], a[172:175], 0\n"
"ds_read_b64_tr_b16 a[142:143], v248 offset:47104\n"
"s_add_u32 m0, s49, 106752\n"
"v_mfma_f32_16x16x32_bf16 v[168:171], a[80:83], a[176:179], 0\n"
"s_add_u32 s52, s56, 327680\n"
"buffer_load_dwordx4 v249, s[64:67], s52 offen lds\n"
"v_mfma_f32_16x16x32_bf16 v[172:175], a[80:83], a[180:183], 0\n"
"v_mfma_f32_16x16x32_bf16 v[176:179], a[80:83], a[184:187], 0\n"
"ds_read_b64_tr_b16 a[80:81], v240 offset:0\n"
"s_add_u32 m0, s49, 110848\n"
"v_mfma_f32_16x16x32_bf16 v[180:183], a[84:87], a[168:171], 0\n"
"ds_read_b64_tr_b16 a[82:83], v240 offset:2048\n"
"s_add_u32 s52, s56, 368640\n"
"v_mfma_f32_16x16x32_bf16 v[184:187], a[84:87], a[172:175], 0\n"
"s_waitcnt vmcnt(22)\n"
"v_cmp_gt_i32 s[76:77], 0, v255\n"
"v_and_b32 v255, 2147483647, v255\n"
"v_mfma_f32_16x16x32_bf16 v[188:191], a[84:87], a[176:179], 0\n"
"v_cmp_eq_u64 vcc, s[62:63], v[254:255]\n"
"s_and_b32 s59, s76, 31\n"
"v_mfma_f32_16x16x32_bf16 v[192:195], a[84:87], a[180:183], 0\n"
"s_nop 0\n"
"s_and_b32 s77, vcc_lo, 31\n"
"s_cmp_eq_u32 s77, 31\n"
"s_cbranch_scc0 L_sp0_%=\n"
"L_ok0_%=:\n"
"s_or_b32 s58, s58, s59\n"
"v_mfma_f32_16x16x32_bf16 v[196:199], a[84:87], a[184:187], 0\n"
"ds_read_b64_tr_b16 a[84:85], v241 offset:0\n"
"v_mfma_f32_16x16x32_bf16 v[160:163], a[88:91], a[188:191], v[160:163]\n"
"ds_read_b64_tr_b16 a[86:87], v241 offset:2048\n"
"v_mfma_f32_16x16x32_bf16 v[164:167], a[88:91], a[192:195], v[164:167]\n"
"buffer_load_dwordx4 v249, s[64:67], s52 offen lds\n"
"s_add_u32 m0, s49, 114944\n"
"v_mfma_f32_16x16x32_bf16 v[168:171], a[88:91], a[196:199], v[168:171]\n"
"s_add_u32 s52, s56, 409600\n"
"buffer_load_dwordx4 v249, s[64:67], s52 offen lds\n"
"v_mfma_f32_16x16x32_bf16 v[172:175], a[88:91], a[200:203], v[172:175]\n"
"s_add_u32 m0, s49, 119040\n"
"s_add_u32 s52, s56, 450560\n"
"v_mfma_f32_16x16x32_bf16 v[176:179], a[88:91], a[204:207], v[176:179]\n"
"ds_read_b64_tr_b16 a[88:89], v240 offset:4096\n"
"v_mfma_f32_16x16x32_bf16 v[180:183], a[92:95], a[188:191], v[180:183]\n"
"ds_read_b64_tr_b16 a[90:91], v240 offset:6144\n"
"v_mfma_f32_16x16x32_bf16 v[184:187], a[92:95], a[192:195], v[184:187]\n"
"buffer_load_dwordx4 v249, s[64:67], s52 offen lds\n"
"s_add_u32 m0, s49, 123136\n"
"v_mfma_f32_16x16x32_bf16 v[188:191], a[92:95], a[196:199], v[188:191]\n"
"s_add_u32 s52, s56, 491520\n"
"buffer_load_dwordx4 v249, s[64:67], s52 offen lds\n"
"v_mfma_f32_16x16x32_bf16 v[192:195], a[92:95], a[200:203], v[192:195]\n"
"s_add_u32 m0, s49, 127232\n"
"s_add_u32 s52, s56, 532480\n"
"v_mfma_f32_16x16x32_bf16 v[196:199], a[92:95], a[204:207], v[196:199]\n"
"s_waitcnt lgkmcnt(15)\n"
"ds_read_b64_tr_b16 a[92:93], v241 offset:4096\n"
"v_mfma_f32_16x16x32_bf16 v[160:163], a[96:99], a[208:211], v[160:163]\n"
"ds_read_b64_tr_b16 a[94:95], v241 offset:6144\n"
"v_mfma_f32_16x16x32_bf16 v[164:167], a[96:99], a[212:215], v[164:167]\n"
"buffer_load_dwordx4 v249, s[64:67], s52 offen lds\n"
"s_add_u32 m0, s49, 131328\n"
"v_mfma_f32_16x16x32_bf16 v[168:171], a[96:99], a[216:219], v[168:171]\n"
"s_mov_b64 exec, 96\n"
"s_mov_b32 exec_hi, 0\n"
"global_store_dwordx2 v244, a[254:255], s[60:61] sc1\n"
"s_mov_b64 exec, -1\n"
"v_mfma_f32_16x16x32_bf16 v[172:175], a[96:99], a[220:223], v[172:175]\n"
"s_add_u32 s52, s56, 573440\n"
"buffer_load_dwordx4 v249, s[64:67], s52 offen lds\n"
"v_mfma_f32_16x16x32_bf16 v[176:179], a[96:99], a[224:227], v[176:179]\n"
"ds_read_b64_tr_b16 a[96:97], v240 offset:8192\n"
"s_add_u32 m0, s49, 135424\n"
"v_mfma_f32_16x16x32_bf16 v[180:183], a[100:103], a[208:211], v[180:183]\n"
"ds_read_b64_tr_b16 a[98:99], v240 offset:10240\n"
"s_add_u32 s52, s56, 614400\n"
"v_mfma_f32_16x16x32_bf16 v[184:187], a[100:103], a[212:215], v[184:187]\n"
"s_add_u32 s71, s68, 0\n"
"v_cvt_pk_bf16_f32 v200, v200, v201\n"
"v_cvt_pk_bf16_f32 v201, v202, v203\n"
"v_mfma_f32_16x16x32_bf16 v[188:191], a[100:103], a[216:219], v[188:191]\n"
"v_cvt_pk_bf16_f32 v202, v220, v221\n"
"v_cvt_pk_bf16_f32 v203, v222, v223\n"
"v_mfma_f32_16x16x32_bf16 v[192:195], a[100:103], a[220:223], v[192:195]\n"
"buffer_load_dwordx4 a[144:147], v253, s[44:47], s71 offen sc1\n"
"v_mfma_f32_16x16x32_bf16 v[196:199], a[100:103], a[224:227], v[196:199]\n"
"ds_read_b64_tr_b16 a[100:101], v241 offset:8192\n"
"v_mfma_f32_16x16x32_bf16 v[160:163], a[104:107], a[228:231], v[160:163]\n"
"ds_read_b64_tr_b16 a[102:103], v241 offset:10240\n"
"v_mfma_f32_16x16x32_bf16 v[164:167], a[104:107], a[232:235], v[164:167]\n"
"buffer_load_dwordx4 a[148:151], v253, s[44:47], s71 offen offset:1024 sc1\n"
"v_mfma_f32_16x16x32_bf16 v[168:171], a[104:107], a[236:239], v[168:171]\n"
"buffer_load_dwordx4 a[152:155], v253, s[44:47], s71 offen offset:2048 sc1\n"
"v_mfma_f32_16x16x32_bf16 v[172:175], a[104:107], a[240:243], v[172:175]\n"
"buffer_store_dwordx4 v[200:203], v253, s[44:47], s57 offen sc1\n"
"v_cvt_pk_bf16_f32 v204, v204, v205\n"
"v_mfma_f32_16x16x32_bf16 v[176:179], a[104:107], a[244:247], v[176:179]\n"
"ds_read_b64_tr_b16 a[104:105], v240 offset:12288\n"
"v_cvt_pk_bf16_f32 v205, v206, v207\n"
"v_mfma_f32_16x16x32_bf16 v[180:183], a[108:111], a[228:231], v[180:183]\n"
"ds_read_b64_tr_b16 a[106:107], v240 offset:14336\n"
"v_cvt_pk_bf16_f32 v206, v224, v225\n"
"v_mfma_f32_16x16x32_bf16 v[184:187], a[108:111], a[232:235], v[184:187]\n"
"buffer_load_dwordx4 a[156:159], v253, s[44:47], s71 offen offset:3072 sc1\n"
"s_add_u32 s71, s71, 4096\n"
"v_mfma_f32_16x16x32_bf16 v[188:191], a[108:111], a[236:239], v[188:191]\n"
"buffer_load_dwordx4 v249, s[64:67], s52 offen lds\n"
"v_cvt_pk_bf16_f32 v207, v226, v227\n"
"v_mfma_f32_16x16x32_bf16 v[192:195], a[108:111], a[240:243], v[192:195]\n"
"buffer_load_dwordx4 a[160:163], v253, s[44:47], s71 offen sc1\n"
"v_mfma_f32_16x16x32_bf16 v[196:199], a[108:111], a[244:247], v[196:199]\n"
"s_waitcnt lgkmcnt(15)\n"
"ds_read_b64_tr_b16 a[108:109], v241 offset:12288\n"
"v_mfma_f32_16x16x32_bf16 v[160:163], a[112:115], a[0:3], v[160:163]\n"
"ds_read_b64_tr_b16 a[110:111], v241 offset:14336\n"
"v_mfma_f32_16x16x32_bf16 v[164:167], a[112:115], a[4:7], v[164:167]\n"
"buffer_store_dwordx4 v[204:207], v253, s[44:47], s57 offen offset:1024 sc1\n"
"v_cvt_pk_bf16_f32 v208, v208, v209\n"
"v_mfma_f32_16x16x32_bf16 v[168:171], a[112:115], a[8:11], v[168:171]\n"
"v_cvt_pk_bf16_f32 v209, v210, v211\n"
"v_cvt_pk_bf16_f32 v210, v228, v229\n"
"v_mfma_f32_16x16x32_bf16 v[172:175], a[112:115], a[12:15], v[172:175]\n"
"v_cvt_pk_bf16_f32 v211, v230, v231\n"
"v_mfma_f32_16x16x32_bf16 v[176:179], a[112:115], a[16:19], v[176:179]\n"
"ds_read_b64_tr_b16 a[112:113], v242 offset:0\n"
"v_mfma_f32_16x16x32_bf16 v[180:183], a[116:119], a[0:3], v[180:183]\n"
"ds_read_b64_tr_b16 a[114:115], v242 offset:2048\n"
"v_mfma_f32_16x16x32_bf16 v[184:187], a[116:119], a[4:7], v[184:187]\n"
"buffer_store_dwordx4 v[208:211], v253, s[44:47], s57 offen offset:2048 sc1\n"
"v_cvt_pk_bf16_f32 v212, v212, v213\n"
"v_mfma_f32_16x16x32_bf16 v[188:191], a[116:119], a[8:11], v[188:191]\n"
"v_cvt_pk_bf16_f32 v213, v214, v215\n"
"v_cvt_pk_bf16_f32 v214, v232, v233\n"
"v_mfma_f32_16x16x32_bf16 v[192:195], a[116:119], a[12:15], v[192:195]\n"
"v_cvt_pk_bf16_f32 v215, v234, v235\n"
"v_mfma_f32_16x16x32_bf16 v[196:199], a[116:119], a[16:19], v[196:199]\n"
"ds_read_b64_tr_b16 a[116:117], v243 offset:0\n"
"v_mfma_f32_16x16x32_bf16 v[160:163], a[120:123], a[20:23], v[160:163]\n"
"ds_read_b64_tr_b16 a[118:119], v243 offset:2048\n"
"v_mfma_f32_16x16x32_bf16 v[164:167], a[120:123], a[24:27], v[164:167]\n"
"buffer_store_dwordx4 v[212:215], v253, s[44:47], s57 offen offset:3072 sc1\n"
"v_cvt_pk_bf16_f32 v216, v216, v217\n"
"v_mfma_f32_16x16x32_bf16 v[168:171], a[120:123], a[28:31], v[168:171]\n"
"v_cvt_pk_bf16_f32 v217, v218, v219\n"
"v_cvt_pk_bf16_f32 v218, v236, v237\n"
"v_mfma_f32_16x16x32_bf16 v[172:175], a[120:123], a[32:35], v[172:175]\n"
"v_cvt_pk_bf16_f32 v219, v238, v239\n"
"v_mfma_f32_16x16x32_bf16 v[176:179], a[120:123], a[36:39], v[176:179]\n"
"ds_read_b64_tr_b16 a[120:121], v242 offset:4096\n"
"v_mfma_f32_16x16x32_bf16 v[180:183], a[124:127], a[20:23], v[180:183]\n"
"ds_read_b64_tr_b16 a[122:123], v242 offset:6144\n"
"v_mfma_f32_16x16x32_bf16 v[184:187], a[124:127], a[24:27], v[184:187]\n"
"buffer_store_dwordx4 v[216:219], v253, s[44:47], s80 offen sc1\n"
"v_mfma_f32_16x16x32_bf16 v[188:191], a[124:127], a[28:31], v[188:191]\n"
"v_mfma_f32_16x16x32_bf16 v[192:195], a[124:127], a[32:35], v[192:195]\n"
"v_mfma_f32_16x16x32_bf16 v[196:199], a[124:127], a[36:39], v[196:199]\n"
"s_waitcnt lgkmcnt(15)\n"
"ds_read_b64_tr_b16 a[124:125], v243 offset:4096\n"
"v_mfma_f32_16x16x32_bf16 v[160:163], a[128:131], a[40:43], v[160:163]\n"
"ds_read_b64_tr_b16 a[126:127], v243 offset:6144\n"
"v_mfma_f32_16x16x32_bf16 v[164:167], a[128:131], a[44:47], v[164:167]\n"
"v_mfma_f32_16x16x32_bf16 v[168:171], a[128:131], a[48:51], v[168:171]\n"
"v_mfma_f32_16x16x32_bf16 v[172:175], a[128:131], a[52:55], v[172:175]\n"
"v_mfma_f32_16x16x32_bf16 v[176:179], a[128:131], a[56:59], v[176:179]\n"
"ds_read_b64_tr_b16 a[128:129], v242 offset:8192\n"
"v_mfma_f32_16x16x32_bf16 v[180:183], a[132:135], a[40:43], v[180:183]\n"
"ds_read_b64_tr_b16 a[130:131], v242 offset:10240\n"
"v_mfma_f32_16x16x32_bf16 v[184:187], a[132:135], a[44:47], v[184:187]\n"
"v_mfma_f32_16x16x32_bf16 v[188:191], a[132:135], a[48:51], v[188:191]\n"
"v_mfma_f32_16x16x32_bf16 v[192:195], a[132:135], a[52:55], v[192:195]\n"
"v_mfma_f32_16x16x32_bf16 v[196:199], a[132:135], a[56:59], v[196:199]\n"
"ds_read_b64_tr_b16 a[132:133], v243 offset:8192\n"
"v_mfma_f32_16x16x32_bf16 v[160:163], a[136:139], a[60:63], v[160:163]\n"
"ds_read_b64_tr_b16 a[134:135], v243 offset:10240\n"
"v_mfma_f32_16x16x32_bf16 v[164:167], a[136:139], a[64:67], v[164:167]\n"
"v_mfma_f32_16x16x32_bf16 v[168:171], a[136:139], a[68:71], v[168:171]\n"
"v_mfma_f32_16x16x32_bf16 v[172:175], a[136:139], a[72:75], v[172:175]\n"
"v_mfma_f32_16x16x32_bf16 v[176:179], a[136:139], a[76:79], v[176:179]\n"
"ds_read_b64_tr_b16 a[136:137], v242 offset:12288\n"
"v_mfma_f32_16x16x32_bf16 v[180:183], a[140:143], a[60:63], v[180:183]\n"
"ds_read_b64_tr_b16 a[138:139], v242 offset:14336\n"
"v_mfma_f32_16x16x32_bf16 v[184:187], a[140:143], a[64:67], v[184:187]\n"
"v_mfma_f32_16x16x32_bf16 v[188:191], a[140:143], a[68:71], v[188:191]\n"
"v_mfma_f32_16x16x32_bf16 v[192:195], a[140:143], a[72:75], v[192:195]\n"
"v_mfma_f32_16x16x32_bf16 v[196:199], a[140:143], a[76:79], v[196:199]\n"
"s_waitcnt vmcnt(19)\n"
"s_barrier\n"
"s_waitcnt lgkmcnt(15)\n"
"v_mfma_f32_16x16x32_bf16 v[160:163], a[80:83], v[0:3], v[160:163]\n"
"ds_read_b64_tr_b16 a[140:141], v243 offset:12288\n"
"v_mfma_f32_16x16x32_bf16 v[164:167], a[80:83], v[4:7], v[164:167]\n"
"ds_read_b64_tr_b16 a[142:143], v243 offset:14336\n"
"v_mfma_f32_16x16x32_bf16 v[168:171], a[80:83], v[8:11], v[168:171]\n"
"v_mfma_f32_16x16x32_bf16 v[172:175], a[80:83], v[12:15], v[172:175]\n"
"v_mfma_f32_16x16x32_bf16 v[176:179], a[80:83], v[16:19], v[176:179]\n"
"ds_read_b64_tr_b16 a[80:81], v245 offset:0\n"
"v_mfma_f32_16x16x32_bf16 v[180:183], a[84:87], v[0:3], v[180:183]\n"
"ds_read_b64_tr_b16 a[82:83], v245 offset:2048\n"
"v_mfma_f32_16x16x32_bf16 v[184:187], a[84:87], v[4:7], v[184:187]\n"
"v_mfma_f32_16x16x32_bf16 v[188:191], a[84:87], v[8:11], v[188:191]\n"
"v_mfma_f32_16x16x32_bf16 v[192:195], a[84:87], v[12:15], v[192:195]\n"
"v_mfma_f32_16x16x32_bf16 v[196:199], a[84:87], v[16:19], v[196:199]\n"
"ds_read_b64_tr_b16 a[84:85], v246 offset:0\n"
"v_mfma_f32_16x16x32_bf16 v[160:163], a[88:91], v[40:43], v[160:163]\n"
"ds_read_b64_tr_b16 a[86:87], v246 offset:2048\n"
"v_mfma_f32_16x16x32_bf16 v[164:167], a[88:91], v[44:47], v[164:167]\n"
"v_mfma_f32_16x16x32_bf16 v[168:171], a[88:91], v[48:51], v[168:171]\n"
"v_mfma_f32_16x16x32_bf16 v[172:175], a[88:91], v[52:55], v[172:175]\n"
"v_mfma_f32_16x16x32_bf16 v[176:179], a[88:91], v[56:59], v[176:179]\n"
"ds_read_b64_tr_b16 a[88:89], v245 offset:4096\n"
"v_mfma_f32_16x16x32_bf16 v[180:183], a[92:95], v[40:43], v[180:183]\n"
"ds_read_b64_tr_b16 a[90:91], v245 offset:6144\n"
"v_mfma_f32_16x16x32_bf16 v[184:187], a[92:95], v[44:47], v[184:187]\n"
"v_mfma_f32_16x16x32_bf16 v[188:191], a[92:95], v[48:51], v[188:191]\n"
"v_mfma_f32_16x16x32_bf16 v[192:195], a[92:95], v[52:55], v[192:195]\n"
"v_mfma_f32_16x16x32_bf16 v[196:199], a[92:95], v[56:59], v[196:199]\n"
"s_waitcnt lgkmcnt(15)\n"
"ds_read_b64_tr_b16 a[92:93], v246 offset:4096\n"
"v_mfma_f32_16x16x32_bf16 v[160:163], a[96:99], v[80:83], v[160:163]\n"
"ds_read_b64_tr_b16 a[94:95], v246 offset:6144\n"
"v_mfma_f32_16x16x32_bf16 v[164:167], a[96:99], v[84:87], v[164:167]\n"
"v_mfma_f32_16x16x32_bf16 v[168:171], a[96:99], v[88:91], v[168:171]\n"
"v_mfma_f32_16x16x32_bf16 v[172:175], a[96:99], v[92:95], v[172:175]\n"
"v_mfma_f32_16x16x32_bf16 v[176:179], a[96:99], v[96:99], v[176:179]\n"
"ds_read_b64_tr_b16 a[96:97], v245 offset:8192\n"
"v_mfma_f32_16x16x32_bf16 v[180:183], a[100:103], v[80:83], v[180:183]\n"
"ds_read_b64_tr_b16 a[98:99], v245 offset:10240\n"
"v_mfma_f32_16x16x32_bf16 v[184:187], a[100:103], v[84:87], v[184:187]\n"
"v_mfma_f32_16x16x32_bf16 v[188:191], a[100:103], v[88:91], v[188:191]\n"
"v_mfma_f32_16x16x32_bf16 v[192:195], a[100:103], v[92:95], v[192:195]\n"
"v_mfma_f32_16x16x32_bf16 v[196:199], a[100:103], v[96:99], v[196:199]\n"
"ds_read_b64_tr_b16 a[100:101], v246 offset:8192\n"
"v_mfma_f32_16x16x32_bf16 v[160:163], a[104:107], v[120:123], v[160:163]\n"
"ds_read_b64_tr_b16 a[102:103], v246 offset:10240\n"
"v_mfma_f32_16x16x32_bf16 v[164:167], a[104:107], v[124:127], v[164:167]\n"
"v_mfma_f32_16x16x32_bf16 v[168:171], a[104:107], v[128:131], v[168:171]\n"
"v_mfma_f32_16x16x32_bf16 v[172:175], a[104:107], v[132:135], v[172:175]\n"
"v_mfma_f32_16x16x32_bf16 v[176:179], a[104:107], v[136:139], v[176:179]\n"
"ds_read_b64_tr_b16 a[104:105], v245 offset:12288\n"
"v_mfma_f32_16x16x32_bf16 v[180:183], a[108:111], v[120:123], v[180:183]\n"
"ds_read_b64_tr_b16 a[106:107], v245 offset:14336\n"
"v_mfma_f32_16x16x32_bf16 v[184:187], a[108:111], v[124:127], v[184:187]\n"
"v_mfma_f32_16x16x32_bf16 v[188:191], a[108:111], v[128:131], v[188:191]\n"
"v_mfma_f32_16x16x32_bf16 v[192:195], a[108:111], v[132:135], v[192:195]\n"
"v_mfma_f32_16x16x32_bf16 v[196:199], a[108:111], v[136:139], v[196:199]\n"
"s_waitcnt vmcnt(4)\n"
"ds_read_b64_tr_b16 a[108:109], v246 offset:12288\n"
"v_mfma_f32_16x16x32_bf16 v[200:203], a[250:253], a[144:147], 0\n"
"ds_read_b64_tr_b16 a[110:111], v246 offset:14336\n"
"v_mfma_f32_16x16x32_bf16 v[220:223], a[248:251], a[144:147], 0\n"
"v_mfma_f32_16x16x32_bf16 v[204:207], a[250:253], a[148:151], 0\n"
"v_mfma_f32_16x16x32_bf16 v[224:227], a[248:251], a[148:151], 0\n"
"v_mfma_f32_16x16x32_bf16 v[208:211], a[250:253], a[152:155], 0\n"
"v_mfma_f32_16x16x32_bf16 v[228:231], a[248:251], a[152:155], 0\n"
"v_mfma_f32_16x16x32_bf16 v[212:215], a[250:253], a[156:159], 0\n"
"v_mfma_f32_16x16x32_bf16 v[232:235], a[248:251], a[156:159], 0\n"
"v_mfma_f32_16x16x32_bf16 v[216:219], a[250:253], a[160:163], 0\n"
"v_mfma_f32_16x16x32_bf16 v[236:239], a[248:251], a[160:163], 0\n"
"s_waitcnt lgkmcnt(15)\n"
"v_mfma_f32_16x16x32_bf16 v[160:163], a[112:115], v[20:23], v[160:163]\n"
"s_add_u32 s71, s68, 20736\n"
"s_and_b32 s57, s74, 15\n"
"s_mul_i32 s80, s57, 7\n"
"v_mfma_f32_16x16x32_bf16 v[164:167], a[112:115], v[24:27], v[164:167]\n"
"buffer_load_dwordx4 a[144:147], v253, s[44:47], s71 offen sc1\n"
"s_add_u32 s80, s80, s79\n"
"v_mfma_f32_16x16x32_bf16 v[168:171], a[112:115], v[28:31], v[168:171]\n"
"buffer_load_dwordx4 a[148:151], v253, s[44:47], s71 offen offset:1024 sc1\n"
"v_mfma_f32_16x16x32_bf16 v[172:175], a[112:115], v[32:35], v[172:175]\n"
"buffer_load_dwordx4 a[152:155], v253, s[44:47], s71 offen offset:2048 sc1\n"
"v_mfma_f32_16x16x32_bf16 v[176:179], a[112:115], v[36:39], v[176:179]\n"
"ds_read_b64_tr_b16 a[112:113], v247 offset:0\n"
"v_mfma_f32_16x16x32_bf16 v[180:183], a[116:119], v[20:23], v[180:183]\n"
"ds_read_b64_tr_b16 a[114:115], v247 offset:2048\n"
"v_mfma_f32_16x16x32_bf16 v[184:187], a[116:119], v[24:27], v[184:187]\n"
"buffer_load_dwordx4 a[156:159], v253, s[44:47], s71 offen offset:3072 sc1\n"
"s_add_u32 s71, s71, 4096\n"
"v_mfma_f32_16x16x32_bf16 v[188:191], a[116:119], v[28:31], v[188:191]\n"
"buffer_load_dwordx4 a[160:163], v253, s[44:47], s71 offen sc1\n"
"v_mfma_f32_16x16x32_bf16 v[192:195], a[116:119], v[32:35], v[192:195]\n"
"global_load_dwordx2 v[254:255], v252, s[60:61] sc1\n"
"v_mfma_f32_16x16x32_bf16 v[196:199], a[116:119], v[36:39], v[196:199]\n"
"ds_read_b64_tr_b16 a[116:117], v248 offset:0\n"
"v_mfma_f32_16x16x32_bf16 v[160:163], a[120:123], v[60:63], v[160:163]\n"
"ds_read_b64_tr_b16 a[118:119], v248 offset:2048\n"
"v_mfma_f32_16x16x32_bf16 v[164:167], a[120:123], v[64:67], v[164:167]\n"
"s_cmp_lt_u32 s57, 8\n"
"s_cselect_b32 s57, s80, s98\n"
"s_mul_i32 s57, s57, 20736\n"
"v_mfma_f32_16x16x32_bf16 v[168:171], a[120:123], v[68:71], v[168:171]\n"
"s_add_u32 s57, s57, s70\n"
"s_add_u32 s80, s57, 4096\n"
"s_lshr_b64 s[74:75], s[74:75], 4\n"
"v_mfma_f32_16x16x32_bf16 v[172:175], a[120:123], v[72:75], v[172:175]\n"
"s_add_u32 s98, s98, 1\n"
"s_add_u32 s79, s79, 1\n"
"v_mfma_f32_16x16x32_bf16 v[176:179], a[120:123], v[76:79], v[176:179]\n"
"ds_read_b64_tr_b16 a[120:121], v247 offset:4096\n"
"v_mfma_f32_16x16x32_bf16 v[180:183], a[124:127], v[60:63], v[180:183]\n"
"ds_read_b64_tr_b16 a[122:123], v247 offset:6144\n"
"v_mfma_f32_16x16x32_bf16 v[184:187], a[124:127], v[64:67], v[184:187]\n"
"v_mfma_f32_16x16x32_bf16 v[188:191], a[124:127], v[68:71], v[188:191]\n"
"v_mfma_f32_16x16x32_bf16 v[192:195], a[124:127], v[72:75], v[192:195]\n"
"v_mfma_f32_16x16x32_bf16 v[196:199], a[124:127], v[76:79], v[196:199]\n"
"s_waitcnt lgkmcnt(15)\n"
"ds_read_b64_tr_b16 a[124:125], v248 offset:4096\n"
"v_mfma_f32_16x16x32_bf16 v[160:163], a[128:131], v[100:103], v[160:163]\n"
"ds_read_b64_tr_b16 a[126:127], v248 offset:6144\n"
"v_mfma_f32_16x16x32_bf16 v[164:167], a[128:131], v[104:107], v[164:167]\n"
"v_mfma_f32_16x16x32_bf16 v[168:171], a[128:131], v[108:111], v[168:171]\n"
"v_mfma_f32_16x16x32_bf16 v[172:175], a[128:131], v[112:115], v[172:175]\n"
"v_mfma_f32_16x16x32_bf16 v[176:179], a[128:131], v[116:119], v[176:179]\n"
"ds_read_b64_tr_b16 a[128:129], v247 offset:8192\n"
"v_mfma_f32_16x16x32_bf16 v[180:183], a[132:135], v[100:103], v[180:183]\n"
"ds_read_b64_tr_b16 a[130:131], v247 offset:10240\n"
"v_mfma_f32_16x16x32_bf16 v[184:187], a[132:135], v[104:107], v[184:187]\n"
"v_mfma_f32_16x16x32_bf16 v[188:191], a[132:135], v[108:111], v[188:191]\n"
"v_mfma_f32_16x16x32_bf16 v[192:195], a[132:135], v[112:115], v[192:195]\n"
"v_mfma_f32_16x16x32_bf16 v[196:199], a[132:135], v[116:119], v[196:199]\n"
"ds_read_b64_tr_b16 a[132:133], v248 offset:8192\n"
"v_mfma_f32_16x16x32_bf16 v[160:163], a[136:139], v[140:143], v[160:163]\n"
"ds_read_b64_tr_b16 a[134:135], v248 offset:10240\n"
"v_mfma_f32_16x16x32_bf16 v[164:167], a[136:139], v[144:147], v[164:167]\n"
"v_mfma_f32_16x16x32_bf16 v[168:171], a[136:139], v[148:151], v[168:171]\n"
"v_mfma_f32_16x16x32_bf16 v[172:175], a[136:139], v[152:155], v[172:175]\n"
"v_mfma_f32_16x16x32_bf16 v[176:179], a[136:139], v[156:159], v[176:179]\n"
"ds_read_b64_tr_b16 a[136:137], v247 offset:12288\n"
"v_mfma_f32_16x16x32_bf16 v[180:183], a[140:143], v[140:143], v[180:183]\n"
"ds_read_b64_tr_b16 a[138:139], v247 offset:14336\n"
"v_mfma_f32_16x16x32_bf16 v[184:187], a[140:143], v[144:147], v[184:187]\n"
"v_mfma_f32_16x16x32_bf16 v[188:191], a[140:143], v[148:151], v[188:191]\n"
"v_mfma_f32_16x16x32_bf16 v[192:195], a[140:143], v[152:155], v[192:195]\n"
"v_mfma_f32_16x16x32_bf16 v[196:199], a[140:143], v[156:159], v[196:199]\n"
"s_waitcnt vmcnt(11)\n"
"s_barrier\n"
"s_waitcnt lgkmcnt(15)\n"
"v_mfma_f32_16x16x32_bf16 v[200:203], a[80:83], a[168:171], v[200:203]\n"
"ds_read_b64_tr_b16 a[140:141], v248 offset:12288\n"
"v_mfma_f32_16x16x32_bf16 v[204:207], a[80:83], a[172:175], v[204:207]\n"
"ds_read_b64_tr_b16 a[142:143], v248 offset:14336\n"
"v_mfma_f32_16x16x32_bf16 v[208:211], a[80:83], a[176:179], v[208:211]\n"
"v_mfma_f32_16x16x32_bf16 v[212:215], a[80:83], a[180:183], v[212:215]\n"
"v_mfma_f32_16x16x32_bf16 v[216:219], a[80:83], a[184:187], v[216:219]\n"
"ds_read_b64_tr_b16 a[80:81], v240 offset:41216\n"
"v_mfma_f32_16x16x32_bf16 v[220:223], a[84:87], a[168:171], v[220:223]\n"
"ds_read_b64_tr_b16 a[82:83], v240 offset:43264\n"
"v_cvt_pk_bf16_f32 v160, v160, v161\n"
"v_mfma_f32_16x16x32_bf16 v[224:227], a[84:87], a[172:175], v[224:227]\n"
"v_cvt_pk_bf16_f32 v161, v162, v163\n"
"v_cvt_pk_bf16_f32 v162, v180, v181\n"
"v_mfma_f32_16x16x32_bf16 v[228:231], a[84:87], a[176:179], v[228:231]\n"
"v_cvt_pk_bf16_f32 v163, v182, v183\n"
"v_mfma_f32_16x16x32_bf16 v[232:235], a[84:87], a[180:183], v[232:235]\n"
"buffer_store_dwordx4 v[160:163], v253, s[44:47], s57 offen sc1\n"
"v_cvt_pk_bf16_f32 v164, v164, v165\n"
"v_mfma_f32_16x16x32_bf16 v[236:239], a[84:87], a[184:187], v[236:239]\n"
"ds_read_b64_tr_b16 a[84:85], v241 offset:41216\n"
"v_cvt_pk_bf16_f32 v165, v166, v167\n"
"v_mfma_f32_16x16x32_bf16 v[200:203], a[88:91], a[188:191], v[200:203]\n"
"ds_read_b64_tr_b16 a[86:87], v241 offset:43264\n"
"v_cvt_pk_bf16_f32 v166, v184, v185\n"
"v_mfma_f32_16x16x32_bf16 v[204:207], a[88:91], a[192:195], v[204:207]\n"
"v_cvt_pk_bf16_f32 v167, v186, v187\n"
"v_mfma_f32_16x16x32_bf16 v[208:211], a[88:91], a[196:199], v[208:211]\n"
"buffer_store_dwordx4 v[164:167], v253, s[44:47], s57 offen offset:1024 sc1\n"
"v_cvt_pk_bf16_f32 v168, v168, v169\n"
"v_mfma_f32_16x16x32_bf16 v[212:215], a[88:91], a[200:203], v[212:215]\n"
"v_cvt_pk_bf16_f32 v169, v170, v171\n"
"v_cvt_pk_bf16_f32 v170, v188, v189\n"
"v_mfma_f32_16x16x32_bf16 v[216:219], a[88:91], a[204:207], v[216:219]\n"
"ds_read_b64_tr_b16 a[88:89], v240 offset:45312\n"
"v_cvt_pk_bf16_f32 v171, v190, v191\n"
"v_mfma_f32_16x16x32_bf16 v[220:223], a[92:95], a[188:191], v[220:223]\n"
"ds_read_b64_tr_b16 a[90:91], v240 offset:47360\n"
"v_mfma_f32_16x16x32_bf16 v[224:227], a[92:95], a[192:195], v[224:227]\n"
"buffer_store_dwordx4 v[168:171], v253, s[44:47], s57 offen offset:2048 sc1\n"
"v_cvt_pk_bf16_f32 v172, v172, v173\n"
"v_mfma_f32_16x16x32_bf16 v[228:231], a[92:95], a[196:199], v[228:231]\n"
"v_cvt_pk_bf16_f32 v173, v174, v175\n"
"v_cvt_pk_bf16_f32 v174, v192, v193\n"
"v_mfma_f32_16x16x32_bf16 v[232:235], a[92:95], a[200:203], v[232:235]\n"
"v_cvt_pk_bf16_f32 v175, v194, v195\n"
"v_mfma_f32_16x16x32_bf16 v[236:239], a[92:95], a[204:207], v[236:239]\n"
"s_waitcnt vmcnt(4)\n"
"ds_read_b64_tr_b16 a[92:93], v241 offset:45312\n"
"v_mfma_f32_16x16x32_bf16 v[200:203], a[250:253], a[144:147], v[200:203]\n"
"ds_read_b64_tr_b16 a[94:95], v241 offset:47360\n"
"v_mfma_f32_16x16x32_bf16 v[220:223], a[248:251], a[144:147], v[220:223]\n"
"buffer_store_dwordx4 v[172:175], v253, s[44:47], s57 offen offset:3072 sc1\n"
"v_cvt_pk_bf16_f32 v176, v176, v177\n"
"v_mfma_f32_16x16x32_bf16 v[204:207], a[250:253], a[148:151], v[204:207]\n"
"v_cvt_pk_bf16_f32 v177, v178, v179\n"
"v_cvt_pk_bf16_f32 v178, v196, v197\n"
"v_mfma_f32_16x16x32_bf16 v[224:227], a[248:251], a[148:151], v[224:227]\n"
"v_cvt_pk_bf16_f32 v179, v198, v199\n"
"v_mfma_f32_16x16x32_bf16 v[208:211], a[250:253], a[152:155], v[208:211]\n"
"buffer_store_dwordx4 v[176:179], v253, s[44:47], s80 offen sc1\n"
"v_mfma_f32_16x16x32_bf16 v[228:231], a[248:251], a[152:155], v[228:231]\n"
"v_mfma_f32_16x16x32_bf16 v[212:215], a[250:253], a[156:159], v[212:215]\n"
"v_mfma_f32_16x16x32_bf16 v[232:235], a[248:251], a[156:159], v[232:235]\n"
"v_mfma_f32_16x16x32_bf16 v[216:219], a[250:253], a[160:163], v[216:219]\n"
"v_mfma_f32_16x16x32_bf16 v[236:239], a[248:251], a[160:163], v[236:239]\n"
"s_waitcnt lgkmcnt(15)\n"
"v_mfma_f32_16x16x32_bf16 v[200:203], a[96:99], a[208:211], v[200:203]\n"
"s_add_u32 s71, s68, 41472\n"
"v_mfma_f32_16x16x32_bf16 v[204:207], a[96:99], a[212:215], v[204:207]\n"
"buffer_load_dwordx4 a[144:147], v253, s[44:47], s71 offen sc1\n"
"v_mfma_f32_16x16x32_bf16 v[208:211], a[96:99], a[216:219], v[208:211]\n"
"buffer_load_dwordx4 a[148:151], v253, s[44:47], s71 offen offset:1024 sc1\n"
"v_mfma_f32_16x16x32_bf16 v[212:215], a[96:99], a[220:223], v[212:215]\n"
"buffer_load_dwordx4 a[152:155], v253, s[44:47], s71 offen offset:2048 sc1\n"
"v_mfma_f32_16x16x32_bf16 v[216:219], a[96:99], a[224:227], v[216:219]\n"
"ds_read_b64_tr_b16 a[96:97], v240 offset:49408\n"
"v_mfma_f32_16x16x32_bf16 v[220:223], a[100:103], a[208:211], v[220:223]\n"
"ds_read_b64_tr_b16 a[98:99], v240 offset:51456\n"
"v_mfma_f32_16x16x32_bf16 v[224:227], a[100:103], a[212:215], v[224:227]\n"
"buffer_load_dwordx4 a[156:159], v253, s[44:47], s71 offen offset:3072 sc1\n"
"s_add_u32 s71, s71, 4096\n"
"v_mfma_f32_16x16x32_bf16 v[228:231], a[100:103], a[216:219], v[228:231]\n"
"buffer_load_dwordx4 a[160:163], v253, s[44:47], s71 offen sc1\n"
"v_mfma_f32_16x16x32_bf16 v[232:235], a[100:103], a[220:223], v[232:235]\n"
"v_mfma_f32_16x16x32_bf16 v[236:239], a[100:103], a[224:227], v[236:239]\n"
"ds_read_b64_tr_b16 a[100:101], v241 offset:49408\n"
"v_mfma_f32_16x16x32_bf16 v[200:203], a[104:107], a[228:231], v[200:203]\n"
"ds_read_b64_tr_b16 a[102:103], v241 offset:51456\n"
"v_mfma_f32_16x16x32_bf16 v[204:207], a[104:107], a[232:235], v[204:207]\n"
"v_mfma_f32_16x16x32_bf16 v[208:211], a[104:107], a[236:239], v[208:211]\n"
"v_mfma_f32_16x16x32_bf16 v[212:215], a[104:107], a[240:243], v[212:215]\n"
"v_mfma_f32_16x16x32_bf16 v[216:219], a[104:107], a[244:247], v[216:219]\n"
"ds_read_b64_tr_b16 a[104:105], v240 offset:53504\n"
"v_mfma_f32_16x16x32_bf16 v[220:223], a[108:111], a[228:231], v[220:223]\n"
"ds_read_b64_tr_b16 a[106:107], v240 offset:55552\n"
"v_mfma_f32_16x16x32_bf16 v[224:227], a[108:111], a[232:235], v[224:227]\n"
"v_mfma_f32_16x16x32_bf16 v[228:231], a[108:111], a[236:239], v[228:231]\n"
"v_mfma_f32_16x16x32_bf16 v[232:235], a[108:111], a[240:243], v[232:235]\n"
"v_mfma_f32_16x16x32_bf16 v[236:239], a[108:111], a[244:247], v[236:239]\n"
"s_waitcnt lgkmcnt(15)\n"
"ds_read_b64_tr_b16 a[108:109], v241 offset:53504\n"
"v_mfma_f32_16x16x32_bf16 v[200:203], a[112:115], a[0:3], v[200:203]\n"
"ds_read_b64_tr_b16 a[110:111], v241 offset:55552\n"
"v_mfma_f32_16x16x32_bf16 v[204:207], a[112:115], a[4:7], v[204:207]\n"
"v_mfma_f32_16x16x32_bf16 v[208:211], a[112:115], a[8:11], v[208:211]\n"
"v_mfma_f32_16x16x32_bf16 v[212:215], a[112:115], a[12:15], v[212:215]\n"
"v_mfma_f32_16x16x32_bf16 v[216:219], a[112:115], a[16:19], v[216:219]\n"
"ds_read_b64_tr_b16 a[112:113], v242 offset:41216\n"
"v_mfma_f32_16x16x32_bf16 v[220:223], a[116:119], a[0:3], v[220:223]\n"
"ds_read_b64_tr_b16 a[114:115], v242 offset:43264\n"
"v_mfma_f32_16x16x32_bf16 v[224:227], a[116:119], a[4:7], v[224:227]\n"
"v_mfma_f32_16x16x32_bf16 v[228:231], a[116:119], a[8:11], v[228:231]\n"
"v_mfma_f32_16x16x32_bf16 v[232:235], a[116:119], a[12:15], v[232:235]\n"
"v_mfma_f32_16x16x32_bf16 v[236:239], a[116:119], a[16:19], v[236:239]\n"
"ds_read_b64_tr_b16 a[116:117], v243 offset:41216\n"
"v_mfma_f32_16x16x32_bf16 v[200:203], a[120:123], a[20:23], v[200:203]\n"
"ds_read_b64_tr_b16 a[118:119], v243 offset:43264\n"
"v_lshrrev_b32 v251, 1, v253\n"
"v_mfma_f32_16x16x32_bf16 v[204:207], a[120:123], a[24:27], v[204:207]\n"
"v_sub_u32 v244, v244, v251\n"
"v_mfma_f32_16x16x32_bf16 v[208:211], a[120:123], a[28:31], v[208:211]\n"
"s_lshl_b32 s79, %[hg], 3\n"
"s_add_u32 s79, s79, 64\n"
"v_add_u32 v244, s79, v244\n"
"v_mfma_f32_16x16x32_bf16 v[212:215], a[120:123], a[32:35], v[212:215]\n"
"v_mfma_f32_16x16x32_bf16 v[216:219], a[120:123], a[36:39], v[216:219]\n"
"ds_read_b64_tr_b16 a[120:121], v242 offset:45312\n"
"v_mfma_f32_16x16x32_bf16 v[220:223], a[124:127], a[20:23], v[220:223]\n"
"ds_read_b64_tr_b16 a[122:123], v242 offset:47360\n"
"v_mfma_f32_16x16x32_bf16 v[224:227], a[124:127], a[24:27], v[224:227]\n"
"v_mfma_f32_16x16x32_bf16 v[228:231], a[124:127], a[28:31], v[228:231]\n"
"v_mfma_f32_16x16x32_bf16 v[232:235], a[124:127], a[32:35], v[232:235]\n"
"v_mfma_f32_16x16x32_bf16 v[236:239], a[124:127], a[36:39], v[236:239]\n"
"s_waitcnt lgkmcnt(15)\n"
"ds_read_b64_tr_b16 a[124:125], v243 offset:45312\n"
"v_mfma_f32_16x16x32_bf16 v[200:203], a[128:131], a[40:43], v[200:203]\n"
"ds_read_b64_tr_b16 a[126:127], v243 offset:47360\n"
"v_mfma_f32_16x16x32_bf16 v[204:207], a[128:131], a[44:47], v[204:207]\n"
"v_mfma_f32_16x16x32_bf16 v[208:211], a[128:131], a[48:51], v[208:211]\n"
"v_mfma_f32_16x16x32_bf16 v[212:215], a[128:131], a[52:55], v[212:215]\n"
"v_mfma_f32_16x16x32_bf16 v[216:219], a[128:131], a[56:59], v[216:219]\n"
"ds_read_b64_tr_b16 a[128:129], v242 offset:49408\n"
"v_mfma_f32_16x16x32_bf16 v[220:223], a[132:135], a[40:43], v[220:223]\n"
"ds_read_b64_tr_b16 a[130:131], v242 offset:51456\n"
"v_mfma_f32_16x16x32_bf16 v[224:227], a[132:135], a[44:47], v[224:227]\n"
"v_mfma_f32_16x16x32_bf16 v[228:231], a[132:135], a[48:51], v[228:231]\n"
"v_mfma_f32_16x16x32_bf16 v[232:235], a[132:135], a[52:55], v[232:235]\n"
"s_waitcnt vmcnt(10)\n"
"v_cmp_gt_i32 s[76:77], 0, v255\n"
"v_and_b32 v255, 2147483647, v255\n"
"v_mfma_f32_16x16x32_bf16 v[236:239], a[132:135], a[56:59], v[236:239]\n"
"ds_read_b64_tr_b16 a[132:133], v243 offset:49408\n"
"v_mfma_f32_16x16x32_bf16 v[200:203], a[136:139], a[60:63], v[200:203]\n"
"ds_read_b64_tr_b16 a[134:135], v243 offset:51456\n"
"v_mfma_f32_16x16x32_bf16 v[204:207], a[136:139], a[64:67], v[204:207]\n"
"v_cmp_eq_u64 vcc, s[62:63], v[254:255]\n"
"s_and_b32 s59, s76, 96\n"
"v_mfma_f32_16x16x32_bf16 v[208:211], a[136:139], a[68:71], v[208:211]\n"
"s_nop 0\n"
"s_and_b32 s77, vcc_lo, 96\n"
"s_cmp_eq_u32 s77, 96\n"
"s_cbranch_scc0 L_sp1_%=\n"
"L_ok1_%=:\n"
"s_or_b32 s58, s58, s59\n"
"v_mfma_f32_16x16x32_bf16 v[212:215], a[136:139], a[72:75], v[212:215]\n"
"v_mfma_f32_16x16x32_bf16 v[216:219], a[136:139], a[76:79], v[216:219]\n"
"ds_read_b64_tr_b16 a[136:137], v242 offset:53504\n"
"v_mfma_f32_16x16x32_bf16 v[220:223], a[140:143], a[60:63], v[220:223]\n"
"ds_read_b64_tr_b16 a[138:139], v242 offset:55552\n"
"v_mfma_f32_16x16x32_bf16 v[224:227], a[140:143], a[64:67], v[224:227]\n"
"v_mfma_f32_16x16x32_bf16 v[228:231], a[140:143], a[68:71], v[228:231]\n"
"v_mfma_f32_16x16x32_bf16 v[232:235], a[140:143], a[72:75], v[232:235]\n"
"v_mfma_f32_16x16x32_bf16 v[236:239], a[140:143], a[76:79], v[236:239]\n"
"s_waitcnt vmcnt(5)\n"
"s_barrier\n"
"s_waitcnt lgkmcnt(15)\n"
"v_mfma_f32_16x16x32_bf16 v[200:203], a[80:83], v[0:3], v[200:203]\n"
"s_mov_b64 exec, 255\n"
"s_mov_b32 exec_hi, 0\n"
"global_store_dwordx2 v244, a[254:255], s[60:61] sc1\n"
"s_mov_b64 exec, -1\n"
"v_mfma_f32_16x16x32_bf16 v[204:207], a[80:83], v[4:7], v[204:207]\n"
"ds_read_b64_tr_b16 a[140:141], v243 offset:53504\n"
"v_mfma_f32_16x16x32_bf16 v[208:211], a[80:83], v[8:11], v[208:211]\n"
"ds_read_b64_tr_b16 a[142:143], v243 offset:55552\n"
"v_mfma_f32_16x16x32_bf16 v[212:215], a[80:83], v[12:15], v[212:215]\n"
"s_add_u32 s71, s68, 62208\n"
"v_mfma_f32_16x16x32_bf16 v[216:219], a[80:83], v[16:19], v[216:219]\n"
"s_lshl_b32 s72, %[wv], 10\n"
"v_add_u32 v180, s72, v253\n"
"s_lshl_b32 s72, %[wv], 8\n"
"v_mfma_f32_16x16x32_bf16 v[220:223], a[84:87], v[0:3], v[220:223]\n"
"buffer_load_dwordx4 a[168:171], v253, s[44:47], s71 offen sc1\n"
"s_add_u32 s72, s72, 4096\n"
"v_mfma_f32_16x16x32_bf16 v[224:227], a[84:87], v[4:7], v[224:227]\n"
"v_add_u32 v181, s72, v253\n"
"s_mov_b64 s[74:75], 65535\n"
"v_mfma_f32_16x16x32_bf16 v[228:231], a[84:87], v[8:11], v[228:231]\n"
"buffer_load_dwordx4 a[172:175], v253, s[44:47], s71 offen offset:1024 sc1\n"
"v_mov_b32 v184, 1073741824\n"
"v_mfma_f32_16x16x32_bf16 v[232:235], a[84:87], v[12:15], v[232:235]\n"
"s_nop 0\n"
"v_cndmask_b32 v181, v184, v181, s[74:75]\n"
"v_lshrrev_b32 v184, 10, v180\n"
"v_mfma_f32_16x16x32_bf16 v[236:239], a[84:87], v[16:19], v[236:239]\n"
"buffer_load_dwordx4 a[176:179], v253, s[44:47], s71 offen offset:2048 sc1\n"
"v_lshlrev_b32 v184, 4, v184\n"
"v_mfma_f32_16x16x32_bf16 v[200:203], a[88:91], v[40:43], v[200:203]\n"
"v_bfe_u32 v185, v180, 4, 4\n"
"v_add_u32 v184, v184, v185\n"
"v_mfma_f32_16x16x32_bf16 v[204:207], a[88:91], v[44:47], v[204:207]\n"
"buffer_load_dwordx4 a[180:183], v253, s[44:47], s71 offen offset:3072 sc1\n"
"v_mul_u32_u24 v182, 1280, v184\n"
"v_mfma_f32_16x16x32_bf16 v[208:211], a[88:91], v[48:51], v[208:211]\n"
"v_bfe_u32 v185, v180, 8, 1\n"
"v_lshl_add_u32 v182, v185, 5, v182\n"
"v_mfma_f32_16x16x32_bf16 v[212:215], a[88:91], v[52:55], v[212:215]\n"
"s_add_u32 s71, s71, 4096\n"
"v_bfe_u32 v185, v180, 9, 1\n"
"v_lshl_add_u32 v182, v185, 4, v182\n"
"v_mfma_f32_16x16x32_bf16 v[216:219], a[88:91], v[56:59], v[216:219]\n"
"v_lshrrev_b32 v184, 10, v181\n"
"v_lshlrev_b32 v184, 4, v184\n"
"v_mfma_f32_16x16x32_bf16 v[220:223], a[92:95], v[40:43], v[220:223]\n"
"buffer_load_dwordx4 a[184:187], v253, s[44:47], s71 offen sc1\n"
"v_bfe_u32 v185, v181, 4, 4\n"
"v_mfma_f32_16x16x32_bf16 v[224:227], a[92:95], v[44:47], v[224:227]\n"
"v_add_u32 v184, v184, v185\n"
"v_mul_u32_u24 v183, 1280, v184\n"
"v_mfma_f32_16x16x32_bf16 v[228:231], a[92:95], v[48:51], v[228:231]\n"
"s_add_u32 s71, s68, 82944\n"
"v_bfe_u32 v185, v181, 8, 2\n"
"v_lshl_add_u32 v183, v185, 3, v183\n"
"v_mfma_f32_16x16x32_bf16 v[232:235], a[92:95], v[52:55], v[232:235]\n"
"v_mov_b32 v184, 1073741824\n"
"v_cndmask_b32 v183, v184, v183, s[74:75]\n"
"v_mfma_f32_16x16x32_bf16 v[236:239], a[92:95], v[56:59], v[236:239]\n"
"s_waitcnt lgkmcnt(15)\n"
"buffer_load_dwordx4 a[188:191], v253, s[44:47], s71 offen sc1\n"
"v_mfma_f32_16x16x32_bf16 v[200:203], a[96:99], v[80:83], v[200:203]\n"
"s_lshr_b32 s72, %[hg], 2\n"
"s_and_b32 s73, %[hg], 3\n"
"s_mul_i32 s74, s72, 20736\n"
"v_mfma_f32_16x16x32_bf16 v[204:207], a[96:99], v[84:87], v[204:207]\n"
"buffer_load_dwordx4 a[192:195], v253, s[44:47], s71 offen offset:1024 sc1\n"
"s_mul_i32 s75, s73, 5120\n"
"v_mfma_f32_16x16x32_bf16 v[208:211], a[96:99], v[88:91], v[208:211]\n"
"s_add_u32 s74, s74, s75\n"
"s_add_u32 s74, s74, 1161216\n"
"v_lshrrev_b32 v188, 4, v253\n"
"v_mfma_f32_16x16x32_bf16 v[212:215], a[96:99], v[92:95], v[212:215]\n"
"buffer_load_dwordx4 a[196:199], v253, s[44:47], s71 offen offset:2048 sc1\n"
"v_add_u32 v188, %[hg], v188\n"
"v_mfma_f32_16x16x32_bf16 v[216:219], a[96:99], v[96:99], v[216:219]\n"
"v_add_u32 v188, 1, v188\n"
"v_and_b32 v188, 7, v188\n"
"v_mfma_f32_16x16x32_bf16 v[220:223], a[100:103], v[80:83], v[220:223]\n"
"buffer_load_dwordx4 a[200:203], v253, s[44:47], s71 offen offset:3072 sc1\n"
"v_lshlrev_b32 v188, 3, v188\n"
"v_mfma_f32_16x16x32_bf16 v[224:227], a[100:103], v[84:87], v[224:227]\n"
"s_and_b32 s75, %[wv], 1\n"
"s_mul_i32 s75, s75, 102400\n"
"s_lshr_b32 s96, %[wv], 1\n"
"v_mfma_f32_16x16x32_bf16 v[228:231], a[100:103], v[88:91], v[228:231]\n"
"s_add_u32 s71, s71, 4096\n"
"s_lshl_b32 s96, s96, 6\n"
"s_add_u32 s75, s75, s96\n"
"v_mfma_f32_16x16x32_bf16 v[232:235], a[100:103], v[92:95], v[232:235]\n"
"s_lshl_b32 s96, %[hg], 7\n"
"s_add_u32 s75, s75, s96\n"
"s_and_b32 s97, s73, 1\n"
"v_mfma_f32_16x16x32_bf16 v[236:239], a[100:103], v[96:99], v[236:239]\n"
"buffer_load_dwordx4 a[204:207], v253, s[44:47], s71 offen sc1\n"
"s_mul_i32 s97, s97, 102400\n"
"v_mfma_f32_16x16x32_bf16 v[200:203], a[104:107], v[120:123], v[200:203]\n"
"s_lshr_b32 s96, s73, 1\n"
"s_lshl_b32 s96, s96, 6\n"
"s_add_u32 s97, s97, s96\n"
"v_mfma_f32_16x16x32_bf16 v[204:207], a[104:107], v[124:127], v[204:207]\n"
"s_lshl_b32 s96, s72, 7\n"
"s_add_u32 s97, s97, s96\n"
"s_add_u32 s97, s97, 1024\n"
"v_mfma_f32_16x16x32_bf16 v[208:211], a[104:107], v[128:131], v[208:211]\n"
"v_mfma_f32_16x16x32_bf16 v[212:215], a[104:107], v[132:135], v[212:215]\n"
"v_mfma_f32_16x16x32_bf16 v[216:219], a[104:107], v[136:139], v[216:219]\n"
"v_mfma_f32_16x16x32_bf16 v[220:223], a[108:111], v[120:123], v[220:223]\n"
"v_mfma_f32_16x16x32_bf16 v[224:227], a[108:111], v[124:127], v[224:227]\n"
"v_mfma_f32_16x16x32_bf16 v[228:231], a[108:111], v[128:131], v[228:231]\n"
"v_mfma_f32_16x16x32_bf16 v[232:235], a[108:111], v[132:135], v[232:235]\n"
"v_mfma_f32_16x16x32_bf16 v[236:239], a[108:111], v[136:139], v[236:239]\n"
"s_waitcnt lgkmcnt(8)\n"
"v_mfma_f32_16x16x32_bf16 v[200:203], a[112:115], v[20:23], v[200:203]\n"
"v_mfma_f32_16x16x32_bf16 v[204:207], a[112:115], v[24:27], v[204:207]\n"
"v_mfma_f32_16x16x32_bf16 v[208:211], a[112:115], v[28:31], v[208:211]\n"
"v_mfma_f32_16x16x32_bf16 v[212:215], a[112:115], v[32:35], v[212:215]\n"
"v_mfma_f32_16x16x32_bf16 v[216:219], a[112:115], v[36:39], v[216:219]\n"
"v_mfma_f32_16x16x32_bf16 v[220:223], a[116:119], v[20:23], v[220:223]\n"
"v_mfma_f32_16x16x32_bf16 v[224:227], a[116:119], v[24:27], v[224:227]\n"
"v_mfma_f32_16x16x32_bf16 v[228:231], a[116:119], v[28:31], v[228:231]\n"
"v_mfma_f32_16x16x32_bf16 v[232:235], a[116:119], v[32:35], v[232:235]\n"
"v_mfma_f32_16x16x32_bf16 v[236:239], a[116:119], v[36:39], v[236:239]\n"
"s_waitcnt vmcnt(11)\n"
"v_mfma_f32_16x16x32_bf16 v[200:203], a[250:253], a[144:147], v[200:203]\n"
"v_mfma_f32_16x16x32_bf16 v[220:223], a[248:251], a[144:147], v[220:223]\n"
"v_mfma_f32_16x16x32_bf16 v[204:207], a[250:253], a[148:151], v[204:207]\n"
"v_mfma_f32_16x16x32_bf16 v[224:227], a[248:251], a[148:151], v[224:227]\n"
"v_mfma_f32_16x16x32_bf16 v[208:211], a[250:253], a[152:155], v[208:211]\n"
"v_mfma_f32_16x16x32_bf16 v[228:231], a[248:251], a[152:155], v[228:231]\n"
"v_mfma_f32_16x16x32_bf16 v[212:215], a[250:253], a[156:159], v[212:215]\n"
"v_mfma_f32_16x16x32_bf16 v[232:235], a[248:251], a[156:159], v[232:235]\n"
"v_mfma_f32_16x16x32_bf16 v[216:219], a[250:253], a[160:163], v[216:219]\n"
"v_mfma_f32_16x16x32_bf16 v[236:239], a[248:251], a[160:163], v[236:239]\n"
"v_mfma_f32_16x16x32_bf16 v[200:203], a[120:123], v[60:63], v[200:203]\n"
"v_mfma_f32_16x16x32_bf16 v[204:207], a[120:123], v[64:67], v[204:207]\n"
"v_mfma_f32_16x16x32_bf16 v[208:211], a[120:123], v[68:71], v[208:211]\n"
"v_mfma_f32_16x16x32_bf16 v[212:215], a[120:123], v[72:75], v[212:215]\n"
"v_mfma_f32_16x16x32_bf16 v[216:219], a[120:123], v[76:79], v[216:219]\n"
"v_mfma_f32_16x16x32_bf16 v[220:223], a[124:127], v[60:63], v[220:223]\n"
"v_mfma_f32_16x16x32_bf16 v[224:227], a[124:127], v[64:67], v[224:227]\n"
"v_mfma_f32_16x16x32_bf16 v[228:231], a[124:127], v[68:71], v[228:231]\n"
"v_mfma_f32_16x16x32_bf16 v[232:235], a[124:127], v[72:75], v[232:235]\n"
"v_mfma_f32_16x16x32_bf16 v[236:239], a[124:127], v[76:79], v[236:239]\n"
"s_waitcnt lgkmcnt(0)\n"
"v_mfma_f32_16x16x32_bf16 v[200:203], a[128:131], v[100:103], v[200:203]\n"
"v_mfma_f32_16x16x32_bf16 v[204:207], a[128:131], v[104:107], v[204:207]\n"
"v_mfma_f32_16x16x32_bf16 v[208:211], a[128:131], v[108:111], v[208:211]\n"
"v_mfma_f32_16x16x32_bf16 v[212:215], a[128:131], v[112:115], v[212:215]\n"
"v_mfma_f32_16x16x32_bf16 v[216:219], a[128:131], v[116:119], v[216:219]\n"
"v_mfma_f32_16x16x32_bf16 v[220:223], a[132:135], v[100:103], v[220:223]\n"
"v_mfma_f32_16x16x32_bf16 v[224:227], a[132:135], v[104:107], v[224:227]\n"
"v_mfma_f32_16x16x32_bf16 v[228:231], a[132:135], v[108:111], v[228:231]\n"
"v_mfma_f32_16x16x32_bf16 v[232:235], a[132:135], v[112:115], v[232:235]\n"
"v_mfma_f32_16x16x32_bf16 v[236:239], a[132:135], v[116:119], v[236:239]\n"
"v_mfma_f32_16x16x32_bf16 v[200:203], a[136:139], v[140:143], v[200:203]\n"
"v_mfma_f32_16x16x32_bf16 v[204:207], a[136:139], v[144:147], v[204:207]\n"
"v_mfma_f32_16x16x32_bf16 v[208:211], a[136:139], v[148:151], v[208:211]\n"
"v_mfma_f32_16x16x32_bf16 v[212:215], a[136:139], v[152:155], v[212:215]\n"
"v_mfma_f32_16x16x32_bf16 v[216:219], a[136:139], v[156:159], v[216:219]\n"
"v_mfma_f32_16x16x32_bf16 v[220:223], a[140:143], v[140:143], v[220:223]\n"
"v_mfma_f32_16x16x32_bf16 v[224:227], a[140:143], v[144:147], v[224:227]\n"
"v_mfma_f32_16x16x32_bf16 v[228:231], a[140:143], v[148:151], v[228:231]\n"
"v_mfma_f32_16x16x32_bf16 v[232:235], a[140:143], v[152:155], v[232:235]\n"
"v_mfma_f32_16x16x32_bf16 v[236:239], a[140:143], v[156:159], v[236:239]\n"
"s_add_u32 s71, s68, 103680\n"
"buffer_load_dwordx4 a[208:211], v253, s[44:47], s71 offen sc1\n"
"buffer_load_dwordx4 a[212:215], v253, s[44:47], s71 offen offset:1024 sc1\n"
"buffer_load_dwordx4 a[216:219], v253, s[44:47], s71 offen offset:2048 sc1\n"
"buffer_load_dwordx4 a[220:223], v253, s[44:47], s71 offen offset:3072 sc1\n"
"s_add_u32 s71, s71, 4096\n"
"buffer_load_dwordx4 a[224:227], v253, s[44:47], s71 offen sc1\n"
"s_add_u32 s71, s68, 124416\n"
"buffer_load_dwordx4 a[228:231], v253, s[44:47], s71 offen sc1\n"
"buffer_load_dwordx4 a[232:235], v253, s[44:47], s71 offen offset:1024 sc1\n"
"buffer_load_dwordx4 a[236:239], v253, s[44:47], s71 offen offset:2048 sc1\n"
"buffer_load_dwordx4 a[240:243], v253, s[44:47], s71 offen offset:3072 sc1\n"
"s_add_u32 s71, s71, 4096\n"
"buffer_load_dwordx4 a[244:247], v253, s[44:47], s71 offen sc1\n"
"global_load_dwordx2 v[190:191], v252, s[60:61] sc1\n"
"s_waitcnt vmcnt(16)\n"
"v_mfma_f32_16x16x32_bf16 v[200:203], a[250:253], a[168:171], v[200:203]\n"
"v_mfma_f32_16x16x32_bf16 v[220:223], a[248:251], a[168:171], v[220:223]\n"
"v_mfma_f32_16x16x32_bf16 v[204:207], a[250:253], a[172:175], v[204:207]\n"
"v_mfma_f32_16x16x32_bf16 v[224:227], a[248:251], a[172:175], v[224:227]\n"
"v_mfma_f32_16x16x32_bf16 v[208:211], a[250:253], a[176:179], v[208:211]\n"
"v_mfma_f32_16x16x32_bf16 v[228:231], a[248:251], a[176:179], v[228:231]\n"
"v_mfma_f32_16x16x32_bf16 v[212:215], a[250:253], a[180:183], v[212:215]\n"
"v_mfma_f32_16x16x32_bf16 v[232:235], a[248:251], a[180:183], v[232:235]\n"
"v_mfma_f32_16x16x32_bf16 v[216:219], a[250:253], a[184:187], v[216:219]\n"
"v_mfma_f32_16x16x32_bf16 v[236:239], a[248:251], a[184:187], v[236:239]\n"
"global_load_dwordx2 v[192:193], v252, s[60:61] sc1\n"
"s_waitcnt vmcnt(12)\n"
"v_mfma_f32_16x16x32_bf16 v[200:203], a[250:253], a[188:191], v[200:203]\n"
"v_mfma_f32_16x16x32_bf16 v[220:223], a[248:251], a[188:191], v[220:223]\n"
"v_mfma_f32_16x16x32_bf16 v[204:207], a[250:253], a[192:195], v[204:207]\n"
"v_mfma_f32_16x16x32_bf16 v[224:227], a[248:251], a[192:195], v[224:227]\n"
"v_mfma_f32_16x16x32_bf16 v[208:211], a[250:253], a[196:199], v[208:211]\n"
"v_mfma_f32_16x16x32_bf16 v[228:231], a[248:251], a[196:199], v[228:231]\n"
"v_mfma_f32_16x16x32_bf16 v[212:215], a[250:253], a[200:203], v[212:215]\n"
"v_mfma_f32_16x16x32_bf16 v[232:235], a[248:251], a[200:203], v[232:235]\n"
"v_mfma_f32_16x16x32_bf16 v[216:219], a[250:253], a[204:207], v[216:219]\n"
"v_mfma_f32_16x16x32_bf16 v[236:239], a[248:251], a[204:207], v[236:239]\n"
"global_load_dwordx2 v[194:195], v252, s[60:61] sc1\n"
"s_waitcnt vmcnt(2)\n"
"v_cmp_gt_i32 s[76:77], 0, v191\n"
"v_and_b32 v191, 2147483647, v191\n"
"v_cmp_eq_u64 vcc, s[62:63], v[190:191]\n"
"s_and_b32 s59, s76, 65280\n"
"s_nop 0\n"
"s_and_b32 s77, vcc_lo, 65280\n"
"s_cmp_eq_u32 s77, 65280\n"
"s_cbranch_scc0 L_lsp_%=\n"
"L_lgo_%=:\n"
"s_or_b32 s58, s58, s59\n"
"buffer_load_dwordx4 a[0:3], v180, s[44:47], s74 offen sc1\n"
"buffer_load_dwordx4 a[4:7], v181, s[44:47], s74 offen sc1\n"
"s_add_u32 s74, s74, 41472\n"
"buffer_load_dwordx4 a[8:11], v180, s[44:47], s74 offen sc1\n"
"buffer_load_dwordx4 a[12:15], v181, s[44:47], s74 offen sc1\n"
"s_add_u32 s74, s74, 41472\n"
"buffer_load_dwordx4 a[16:19], v180, s[44:47], s74 offen sc1\n"
"buffer_load_dwordx4 a[20:23], v181, s[44:47], s74 offen sc1\n"
"s_add_u32 s74, s74, 41472\n"
"buffer_load_dwordx4 a[24:27], v180, s[44:47], s74 offen sc1\n"
"buffer_load_dwordx4 a[28:31], v181, s[44:47], s74 offen sc1\n"
"s_add_u32 s74, s74, 41472\n"
"buffer_load_dwordx4 a[32:35], v180, s[44:47], s74 offen sc1\n"
"buffer_load_dwordx4 a[36:39], v181, s[44:47], s74 offen sc1\n"
"s_add_u32 s74, s74, 41472\n"
"buffer_load_dwordx4 a[40:43], v180, s[44:47], s74 offen sc1\n"
"buffer_load_dwordx4 a[44:47], v181, s[44:47], s74 offen sc1\n"
"s_add_u32 s74, s74, 41472\n"
"buffer_load_dwordx4 a[48:51], v180, s[44:47], s74 offen sc1\n"
"buffer_load_dwordx4 a[52:55], v181, s[44:47], s74 offen sc1\n"
"s_add_u32 s74, s74, 41472\n"
"buffer_load_dwordx4 a[56:59], v180, s[44:47], s74 offen sc1\n"
"buffer_load_dwordx4 a[60:63], v181, s[44:47], s74 offen sc1\n"
"s_add_u32 s74, s74, 41472\n"
"global_load_dwordx2 v[186:187], v188, s[60:61] sc1\n"
"s_or_b32 s52, s58, s69\n"
"s_cmp_eq_u32 s52, 0\n"
"s_cselect_b32 s42, s42, 0\n"
"s_waitcnt vmcnt(25)\n"
"v_mfma_f32_16x16x32_bf16 v[200:203], a[250:253], a[208:211], v[200:203]\n"
"v_mfma_f32_16x16x32_bf16 v[220:223], a[248:251], a[208:211], v[220:223]\n"
"v_mfma_f32_16x16x32_bf16 v[204:207], a[250:253], a[212:215], v[204:207]\n"
"v_mfma_f32_16x16x32_bf16 v[224:227], a[248:251], a[212:215], v[224:227]\n"
"v_mfma_f32_16x16x32_bf16 v[208:211], a[250:253], a[216:219], v[208:211]\n"
"v_mfma_f32_16x16x32_bf16 v[228:231], a[248:251], a[216:219], v[228:231]\n"
"v_mfma_f32_16x16x32_bf16 v[212:215], a[250:253], a[220:223], v[212:215]\n"
"v_mfma_f32_16x16x32_bf16 v[232:235], a[248:251], a[220:223], v[232:235]\n"
"v_mfma_f32_16x16x32_bf16 v[216:219], a[250:253], a[224:227], v[216:219]\n"
"v_mfma_f32_16x16x32_bf16 v[236:239], a[248:251], a[224:227], v[236:239]\n"
"s_waitcnt vmcnt(20)\n"
"v_mfma_f32_16x16x32_bf16 v[200:203], a[250:253], a[228:231], v[200:203]\n"
"v_mfma_f32_16x16x32_bf16 v[220:223], a[248:251], a[228:231], v[220:223]\n"
"v_mfma_f32_16x16x32_bf16 v[204:207], a[250:253], a[232:235], v[204:207]\n"
"v_mfma_f32_16x16x32_bf16 v[224:227], a[248:251], a[232:235], v[224:227]\n"
"v_mfma_f32_16x16x32_bf16 v[208:211], a[250:253], a[236:239], v[208:211]\n"
"v_mfma_f32_16x16x32_bf16 v[228:231], a[248:251], a[236:239], v[228:231]\n"
"v_mfma_f32_16x16x32_bf16 v[212:215], a[250:253], a[240:243], v[212:215]\n"
"v_mfma_f32_16x16x32_bf16 v[232:235], a[248:251], a[240:243], v[232:235]\n"
"v_mfma_f32_16x16x32_bf16 v[216:219], a[250:253], a[244:247], v[216:219]\n"
"v_mfma_f32_16x16x32_bf16 v[236:239], a[248:251], a[244:247], v[236:239]\n"
"v_cvt_pk_bf16_f32 v200, v200, v201\n"
"v_cvt_pk_bf16_f32 v201, v202, v203\n"
"v_cvt_pk_bf16_f32 v202, v220, v221\n"
"v_cvt_pk_bf16_f32 v203, v222, v223\n"
"s_nop 0\n"
"v_permlane16_swap_b32 v200, v202\n"
"v_permlane16_swap_b32 v201, v203\n"
"buffer_store_dwordx4 v[200:203], v250, s[40:43], s75 offen\n"
"s_add_u32 s75, s75, 20480\n"
"v_cvt_pk_bf16_f32 v204, v204, v205\n"
"v_cvt_pk_bf16_f32 v205, v206, v207\n"
"v_cvt_pk_bf16_f32 v206, v224, v225\n"
"v_cvt_pk_bf16_f32 v207, v226, v227\n"
"s_nop 0\n"
"v_permlane16_swap_b32 v204, v206\n"
"v_permlane16_swap_b32 v205, v207\n"
"buffer_store_dwordx4 v[204:207], v250, s[40:43], s75 offen\n"
"s_add_u32 s75, s75, 20480\n"
"v_cvt_pk_bf16_f32 v208, v208, v209\n"
"v_cvt_pk_bf16_f32 v209, v210, v211\n"
"v_cvt_pk_bf16_f32 v210, v228, v229\n"
"v_cvt_pk_bf16_f32 v211, v230, v231\n"
"s_nop 0\n"
"v_permlane16_swap_b32 v208, v210\n"
"v_permlane16_swap_b32 v209, v211\n"
"buffer_store_dwordx4 v[208:211], v250, s[40:43], s75 offen\n"
"s_add_u32 s75, s75, 20480\n"
"v_cvt_pk_bf16_f32 v212, v212, v213\n"
"v_cvt_pk_bf16_f32 v213, v214, v215\n"
"v_cvt_pk_bf16_f32 v214, v232, v233\n"
"v_cvt_pk_bf16_f32 v215, v234, v235\n"
"s_nop 0\n"
"v_permlane16_swap_b32 v212, v214\n"
"v_permlane16_swap_b32 v213, v215\n"
"buffer_store_dwordx4 v[212:215], v250, s[40:43], s75 offen\n"
"s_add_u32 s75, s75, 20480\n"
"v_cvt_pk_bf16_f32 v216, v216, v217\n"
"v_cvt_pk_bf16_f32 v217, v218, v219\n"
"v_cvt_pk_bf16_f32 v218, v236, v237\n"
"v_cvt_pk_bf16_f32 v219, v238, v239\n"
"s_nop 0\n"
"v_permlane16_swap_b32 v216, v218\n"
"v_permlane16_swap_b32 v217, v219\n"
"buffer_store_dwordx4 v[216:219], v250, s[40:43], s75 offen\n"
"s_add_u32 s75, s75, 20480\n"
"s_waitcnt vmcnt(20)\n"
"v_mfma_f32_16x16x32_bf16 v[160:163], a[250:253], a[0:3], 0\n"
"v_mfma_f32_16x16x32_bf16 v[164:167], a[248:251], a[0:3], 0\n"
"v_mfma_f32_16x16x32_bf16 v[168:171], a[250:253], a[4:7], 0\n"
"v_mfma_f32_16x16x32_bf16 v[172:175], a[248:251], a[4:7], 0\n"
"s_waitcnt vmcnt(18)\n"
"v_mfma_f32_16x16x32_bf16 v[160:163], a[250:253], a[8:11], v[160:163]\n"
"v_mfma_f32_16x16x32_bf16 v[164:167], a[248:251], a[8:11], v[164:167]\n"
"v_mfma_f32_16x16x32_bf16 v[168:171], a[250:253], a[12:15], v[168:171]\n"
"v_mfma_f32_16x16x32_bf16 v[172:175], a[248:251], a[12:15], v[172:175]\n"
"s_waitcnt vmcnt(16)\n"
"v_mfma_f32_16x16x32_bf16 v[160:163], a[250:253], a[16:19], v[160:163]\n"
"v_mfma_f32_16x16x32_bf16 v[164:167], a[248:251], a[16:19], v[164:167]\n"
"v_mfma_f32_16x16x32_bf16 v[168:171], a[250:253], a[20:23], v[168:171]\n"
"v_mfma_f32_16x16x32_bf16 v[172:175], a[248:251], a[20:23], v[172:175]\n"
"s_waitcnt vmcnt(14)\n"
"v_mfma_f32_16x16x32_bf16 v[160:163], a[250:253], a[24:27], v[160:163]\n"
"v_mfma_f32_16x16x32_bf16 v[164:167], a[248:251], a[24:27], v[164:167]\n"
"v_mfma_f32_16x16x32_bf16 v[168:171], a[250:253], a[28:31], v[168:171]\n"
"v_mfma_f32_16x16x32_bf16 v[172:175], a[248:251], a[28:31], v[172:175]\n"
"s_waitcnt vmcnt(12)\n"
"v_mfma_f32_16x16x32_bf16 v[160:163], a[250:253], a[32:35], v[160:163]\n"
"v_mfma_f32_16x16x32_bf16 v[164:167], a[248:251], a[32:35], v[164:167]\n"
"v_mfma_f32_16x16x32_bf16 v[168:171], a[250:253], a[36:39], v[168:171]\n"
"v_mfma_f32_16x16x32_bf16 v[172:175], a[248:251], a[36:39], v[172:175]\n"
"s_waitcnt vmcnt(10)\n"
"v_mfma_f32_16x16x32_bf16 v[160:163], a[250:253], a[40:43], v[160:163]\n"
"v_mfma_f32_16x16x32_bf16 v[164:167], a[248:251], a[40:43], v[164:167]\n"
"v_mfma_f32_16x16x32_bf16 v[168:171], a[250:253], a[44:47], v[168:171]\n"
"v_mfma_f32_16x16x32_bf16 v[172:175], a[248:251], a[44:47], v[172:175]\n"
"s_waitcnt vmcnt(8)\n"
"v_mfma_f32_16x16x32_bf16 v[160:163], a[250:253], a[48:51], v[160:163]\n"
"v_mfma_f32_16x16x32_bf16 v[164:167], a[248:251], a[48:51], v[164:167]\n"
"v_mfma_f32_16x16x32_bf16 v[168:171], a[250:253], a[52:55], v[168:171]\n"
"v_mfma_f32_16x16x32_bf16 v[172:175], a[248:251], a[52:55], v[172:175]\n"
"s_waitcnt vmcnt(6)\n"
"v_mfma_f32_16x16x32_bf16 v[160:163], a[250:253], a[56:59], v[160:163]\n"
"v_mfma_f32_16x16x32_bf16 v[164:167], a[248:251], a[56:59], v[164:167]\n"
"v_mfma_f32_16x16x32_bf16 v[168:171], a[250:253], a[60:63], v[168:171]\n"
"v_mfma_f32_16x16x32_bf16 v[172:175], a[248:251], a[60:63], v[172:175]\n"
"s_waitcnt vmcnt(5)\n"
"v_accvgpr_read_b32 v254, a254\n"
"v_accvgpr_read_b32 v255, a255\n"
"v_and_b32 v187, 2147483647, v187\n"
"v_and_b32 v255, 2147483647, v255\n"
"v_cmp_ne_u64 vcc, v[186:187], v[254:255]\n"
"s_nop 3\n"
"s_and_b32 s52, vcc_lo, 255\n"
"s_cmp_lg_u32 s52, 0\n"
"s_cselect_b32 s52, 4, 0\n"
"s_or_b32 s69, s69, s52\n"
"s_cmp_lg_u32 s58, 0\n"
"s_cselect_b32 s52, 1, 0\n"
"s_or_b32 %[stat], s69, s52\n"
"v_cvt_pk_bf16_f32 v160, v160, v161\n"
"v_cvt_pk_bf16_f32 v161, v162, v163\n"
"v_cvt_pk_bf16_f32 v162, v164, v165\n"
"v_cvt_pk_bf16_f32 v163, v166, v167\n"
"s_nop 0\n"
"v_permlane16_swap_b32 v160, v162\n"
"v_permlane16_swap_b32 v161, v163\n"
"buffer_store_dwordx4 v[160:163], v182, s[40:43], s97 offen\n"
"v_cvt_pk_bf16_f32 v168, v168, v169\n"
"v_cvt_pk_bf16_f32 v169, v170, v171\n"
"buffer_store_dwordx2 v[168:169], v183, s[40:43], s97 offen\n"
"v_cvt_pk_bf16_f32 v172, v172, v173\n"
"v_cvt_pk_bf16_f32 v173, v174, v175\n"
"buffer_store_dwordx2 v[172:173], v183, s[40:43], s97 offen offset:32\n"
"s_waitcnt lgkmcnt(0)\n"
"L_end_%=:\n"
  : [stat] "=&s"(stat), "+{v240}"(vKr), "+{v241}"(vV0), "+{v242}"(vV1), "+{v243}"(gK), "+{v244}"(gV), "+{v245}"(gW), "+{v246}"(gX), "+{v247}"(vW0), "+{v248}"(vXr),
    "+{v249}"(gWo), "+{v250}"(vWo), "+{v251}"(tXs), "+{v252}"(tWs), "+{v253}"(tL), "+{v254}"(dum), "+{v255}"(tmp), "+{s[64:71]}"(ka8), "+{s[72:75]}"(ka4)
  : [kp] "s"(__builtin_amdgcn_kernarg_segment_ptr()), [bb] "s"(b), [hg] "s"(hg), [wv] "s"(wv), [rr] "s"(r_), [ty] "s"(ty)
  : "v0","v1","v2","v3","v4","v5","v6","v7","v8","v9","v10","v11","v12","v13","v14","v15","v16","v17","v18","v19","v20","v21","v22","v23","v24","v25","v26","v27","v28","v29","v30","v31","v32","v33","v34","v35","v36","v37","v38","v39","v40","v41","v42","v43","v44","v45","v46","v47","v48","v49","v50","v51","v52","v53","v54","v55","v56","v57","v58","v59","v60","v61","v62","v63","v64","v65","v66","v67","v68","v69","v70","v71","v72","v73","v74","v75","v76","v77","v78","v79","v80","v81","v82","v83","v84","v85","v86","v87","v88","v89","v90","v91","v92","v93","v94","v95","v96","v97","v98","v99","v100","v101","v102","v103","v104","v105","v106","v107","v108","v109","v110","v111","v112","v113","v114","v115","v116","v117","v118","v119","v120","v121","v122","v123","v124","v125","v126","v127","v128","v129","v130","v131","v132","v133","v134","v135","v136","v137","v138","v139","v140","v141","v142","v143","v144","v145","v146","v147","v148","v149","v150","v151","v152","v153","v154","v155","v156","v157","v158","v159","v160","v161","v162","v163","v164","v165","v166","v167","v168","v169","v170","v171","v172","v173","v174","v175","v176","v177","v178","v179","v180","v181","v182","v183","v184","v185","v186","v187","v188","v189","v190","v191","v192","v193","v194","v195","v196","v197","v198","v199","v200","v201","v202","v203","v204","v205","v206","v207","v208","v209","v210","v211","v212","v213","v214","v215","v216","v217","v218","v219","v220","v221","v222","v223","v224","v225","v226","v227","v228","v229","v230","v231","v232","v233","v234","v235","v236","v237","v238","v239","a0","a1","a2","a3","a4","a5","a6","a7","a8","a9","a10","a11","a12","a13","a14","a15","a16","a17","a18","a19","a20","a21","a22","a23","a24","a25","a26","a27","a28","a29","a30","a31","a32","a33","a34","a35","a36","a37","a38","a39","a40","a41","a42","a43","a44","a45","a46","a47","a48","a49","a50","a51","a52","a53","a54","a55","a56","a57","a58","a59","a60","a61","a62","a63","a64","a65","a66","a67","a68","a69","a70","a71","a72","a73","a74","a75","a76","a77","a78","a79","a80","a81","a82","a83","a84","a85","a86","a87","a88","a89","a90","a91","a92","a93","a94","a95","a96","a97","a98","a99","a100","a101","a102","a103","a104","a105","a106","a107","a108","a109","a110","a111","a112","a113","a114","a115","a116","a117","a118","a119","a120","a121","a122","a123","a124","a125","a126","a127","a128","a129","a130","a131","a132","a133","a134","a135","a136","a137","a138","a139","a140","a141","a142","a143","a144","a145","a146","a147","a148","a149","a150","a151","a152","a153","a154","a155","a156","a157","a158","a159","a160","a161","a162","a163","a164","a165","a166","a167","a168","a169","a170","a171","a172","a173","a174","a175","a176","a177","a178","a179","a180","a181","a182","a183","a184","a185","a186","a187","a188","a189","a190","a191","a192","a193","a194","a195","a196","a197","a198","a199","a200","a201","a202","a203","a204","a205","a206","a207","a208","a209","a210","a211","a212","a213","a214","a215","a216","a217","a218","a219","a220","a221","a222","a223","a224","a225","a226","a227","a228","a229","a230","a231","a232","a233","a234","a235","a236","a237","a238","a239","a240","a241","a242","a243","a244","a245","a246","a247","a248","a249","a250","a251","a252","a253","a254","a255","s36","s37","s38","s39","s40","s41","s42","s43","s44","s45","s46","s47","s48","s49","s50","s51","s52","s53","s54","s55","s56","s57","s58","s59","s60","s61","s62","s63","s76","s77","s78","s79","s80","s81","s82","s83","s84","s85","s86","s87","s88","s89","s90","s91","s92","s93","s94","s95","s96","s97","s98","s99","vcc","scc","memory");
  unsigned ln; asm volatile("v_mbcnt_lo_u32_b32 %0, -1, 0\n\tv_mbcnt_hi_u32_b32 %0, -1, %0" : "=v"(ln));
  volatile unsigned* lw = (volatile unsigned*)lds;
  __syncthreads();
  if (ln == 0) lw[wv] = stat;
  __syncthreads();
  const unsigned st = lw[0] | lw[1] | lw[2] | lw[3];
  if (st == 0u) return;
  typedef const __attribute__((address_space(4))) unsigned long long* kargp;
  kargp ka = (kargp)__builtin_amdgcn_kernarg_segment_ptr(); asm volatile("" : "+s"(ka));
  ws = (char*)ka[5];
  if ((st & 6u) && wv == 0 && ln < 8u) {
    gu64* nw = (gu64*)(ws + WS_FLAGS) + b * 256u;
    const unsigned cc = (hg + 1u + ln) & 7u;
    const unsigned long long e = (__hip_atomic_load(nw + cc, RLX_AGENT) & 0x7fffffffffffffffull) | ((unsigned long long)(st & 1u) << 63);
    if (ln < 7u) __hip_atomic_store(nw + 16u + 16u * cc + ln, e, RLX_AGENT);
    __hip_atomic_store(nw + 16u + 16u * cc + 8u + hg, e, RLX_AGENT);
  }
  if (st & 3u) {
    __syncthreads();
    slow_rows((const unsigned short*)ka[0], (const unsigned short*)ka[1], (const unsigned short*)ka[2], (const unsigned short*)ka[3], (const unsigned short*)ka[4],
              (unsigned short*)ka[6], b, hg, (st & 2u) != 0u, wv * 64u + ln, (float*)lds);
  }
}

extern "C" void kernel_launch(void* const* d_in, const int* in_sizes, int n_in, void* d_out, int out_size, void* d_ws, size_t ws_size, hipStream_t stream) {
  (void)hipFuncSetAttribute((const void*)fused_mha, hipFuncAttributeMaxDynamicSharedMemorySize, 163840);
  fused_mha<<<256, 256, 163840, stream>>>((const char*)d_in[0], (const char*)d_in[1], (const char*)d_in[2], (const char*)d_in[3], (const char*)d_in[4], (char*)d_ws, (unsigned short*)d_out);
}
```
